```python
import torch

def _softmax(x, axis=-1):
    return torch.softmax(x, dim=axis)

def _gelu(x):
    return torch.nn.functional.gelu(x, approximate='tanh')

def _silu(x):
    return torch.nn.functional.silu(x)

def _sigmoid(x):
    return torch.sigmoid(x)

def _layernorm(x, axis=-1, eps=1e-05):
    m = x.mean(dim=axis, keepdim=True)
    v = x.var(dim=axis, keepdim=True, unbiased=False)
    return (x - m) / torch.sqrt(v + eps)

def _scatter_add(tgt, idx, src, axis=0):
    return tgt.clone().index_add_(axis, idx, src)

def _segment_sum(src, seg, n):
    out = torch.zeros((n,) + src.shape[1:], dtype=src.dtype)
    return out.index_add_(0, seg, src)

def _pad(x, pw):
    flat = tuple((v for pair in reversed(pw) for v in pair))
    return torch.nn.functional.pad(x, flat)
kernel_name = 'concat_heads__memory_aug_recurrent_mem_tr__compressed_kv_cla__prompt'

def setup_inputs():

    _w = lambda a: (lambda t: t.to(torch.bfloat16) if t.is_floating_point() else t)(torch.as_tensor(a))

    def _A_setup():
        B, S, Dm = (1, 2048, 448)
        H, Dh = (16, 256)
        M, seg = (8, 32)
        seg = min(seg, S)
        S = S // seg * seg
        return {'x': _w((lambda *s: torch.randn(*s, dtype=torch.bfloat16) * 0.05)(B, S, Dm)), 'mem0': _w((lambda *s: torch.randn(*s, dtype=torch.bfloat16) * 0.4)(B, M, Dm)), 'Wq': _w((lambda *s: torch.randn(*s, dtype=torch.bfloat16) * 6.4)(Dm, H, Dh) * Dm ** (-0.5)), 'Wk': _w((lambda *s: torch.randn(*s, dtype=torch.bfloat16) * 6.4)(Dm, H, Dh) * Dm ** (-0.5)), 'Wv': _w((lambda *s: torch.randn(*s, dtype=torch.bfloat16) * 0.4)(Dm, H, Dh) * Dm ** (-0.5)), 'Wo': _w((lambda *s: torch.randn(*s, dtype=torch.bfloat16) * 0.4)(H, Dh, Dm) * (H * Dh) ** (-0.5)), 'seg_len': seg}
    _A_in = _A_setup()

    def _B_setup():
        B, H, Sq, Sk, D, M = (1, 16, 2048, 2048, 256, 448)
        Dc = 4 * D
        return {'x': _w((lambda *s: torch.randn(*s, dtype=torch.bfloat16) * 1.6)(B, Sq, M)), 'c_kv': _w((lambda *s: torch.randn(*s, dtype=torch.bfloat16) * 0.2)(B, Sk, Dc)), 'w_q': _w((lambda *s: torch.randn(*s, dtype=torch.bfloat16) * 3.2)(M, H, D) * M ** (-0.5)), 'w_uk': _w((lambda *s: torch.randn(*s, dtype=torch.bfloat16) * 3.2)(Dc, H, D) * Dc ** (-0.5)), 'w_uv': _w((lambda *s: torch.randn(*s, dtype=torch.bfloat16) * 0.2)(Dc, H, D) * Dc ** (-0.5)), 'w_o': _w((lambda *s: torch.randn(*s, dtype=torch.bfloat16) * 0.2)(H, D, M) * (H * D) ** (-0.5))}
    _B_in = _B_setup()
    return {'A_x': _A_in['x'], 'A_mem0': _A_in['mem0'], 'A_Wq': _A_in['Wq'], 'A_Wk': _A_in['Wk'], 'A_Wv': _A_in['Wv'], 'A_Wo': _A_in['Wo'], 'A_seg_len': _A_in['seg_len'], 'B_x': _B_in['x'], 'B_c_kv': _B_in['c_kv'], 'B_w_q': _B_in['w_q'], 'B_w_uk': _B_in['w_uk'], 'B_w_uv': _B_in['w_uv'], 'B_w_o': _B_in['w_o']}

def reference(A_x, A_mem0, A_Wq, A_Wk, A_Wv, A_Wo, A_seg_len, B_x, B_c_kv, B_w_q, B_w_uk, B_w_uv, B_w_o):

    def _A_ref(x, mem0, Wq, Wk, Wv, Wo, seg_len):
        B, S, Dm = x.shape
        H, Dh = (Wq.shape[1], Wq.shape[2])
        M = mem0.shape[1]
        scale = Dh ** (-0.5)
        n_seg = S // seg_len
        L = M + seg_len
        idx = torch.arange(L)
        mask = (idx[:, None] < M) | (idx[:, None] >= idx[None, :])
        mem = mem0
        outs = []
        for s in range(n_seg):
            xs = x[:, s * seg_len:(s + 1) * seg_len, :].clone()
            h = torch.concatenate([mem, xs], axis=1)
            q = torch.einsum('btd,dhe->bhte', h, Wq)
            k = torch.einsum('btd,dhe->bhte', h, Wk)
            v = torch.einsum('btd,dhe->bhte', h, Wv)
            scores = torch.einsum('bhqe,bhke->bhqk', q, k) * scale
            scores = torch.where(mask[None, None], scores, -1000000000.0)
            p = _softmax(scores, axis=-1)
            o = torch.einsum('bhqk,bhke->bhqe', p, v)
            o = torch.einsum('bhte,hed->btd', o, Wo)
            mem = o[:, :M, :].clone()
            outs.append(o[:, M:, :])
        return torch.concatenate(outs, axis=1)

    def _B_ref(x, c_kv, w_q, w_uk, w_uv, w_o):
        B, Sq, M = (x.shape[0], x.shape[1], x.shape[2])
        Sk = c_kv.shape[1]
        H, D = (w_q.shape[1], w_q.shape[2])
        q = torch.einsum('bqm,mhd->bhqd', x, w_q)
        k = torch.einsum('bkc,chd->bhkd', c_kv, w_uk)
        v = torch.einsum('bkc,chd->bhkd', c_kv, w_uv)
        scores = torch.einsum('bhqd,bhkd->bhqk', q, k) * D ** (-0.5)
        off = Sk - Sq
        qi = torch.arange(Sq)[:, None]
        kj = torch.arange(Sk)[None, :]
        cmask = kj <= qi + off
        scores = torch.where(cmask[None, None], scores, -1000000000.0)
        p = _softmax(scores, axis=-1)
        ctx = torch.einsum('bhqk,bhkd->bhqd', p, v)
        return torch.einsum('bhqd,hdm->bqm', ctx, w_o)
    a = _A_ref(A_x, A_mem0, A_Wq, A_Wk, A_Wv, A_Wo, A_seg_len)
    b = _B_ref(B_x, B_c_kv, B_w_q, B_w_uk, B_w_uv, B_w_o)
    return torch.concatenate([a, b], axis=-1)
```

```cpp
#include <hip/hip_runtime.h>
#include <hip/hip_bf16.h>
#include <stdint.h>

typedef unsigned short u16;
typedef __attribute__((ext_vector_type(8))) short bf16x8;
typedef __attribute__((ext_vector_type(4))) short s16x4;
typedef __attribute__((ext_vector_type(16))) float f32x16;
typedef __attribute__((ext_vector_type(4))) float f32x4;
typedef __attribute__((ext_vector_type(4))) unsigned u32x4;
typedef __attribute__((ext_vector_type(2))) unsigned u32x2;
typedef short v4i16_t __attribute__((ext_vector_type(4)));

#define LAS __attribute__((address_space(3)))

__device__ __forceinline__ float bf2f(u16 x) { return __uint_as_float(((unsigned)x) << 16); }
__device__ __forceinline__ u16 f2bf(float f) {
    unsigned u = __float_as_uint(f);
    u += 0x7fffu + ((u >> 16) & 1u);
    return (u16)(u >> 16);
}
__device__ __forceinline__ unsigned pack2(float lo, float hi) {
    return (unsigned)f2bf(lo) | ((unsigned)f2bf(hi) << 16);
}
__device__ __forceinline__ s16x4 tr_read(const void* p) {
    return __builtin_bit_cast(s16x4, __builtin_amdgcn_ds_read_tr16_b64_v4i16((LAS v4i16_t*)p));
}
#define MFMA32(a, b, c) __builtin_amdgcn_mfma_f32_32x32x16_bf16(a, b, c, 0, 0, 0)
#define MFMA16(a, b, c) __builtin_amdgcn_mfma_f32_16x16x32_bf16(a, b, c, 0, 0, 0)

__device__ __forceinline__ int crow(int r, int hi) { return (r & 3) + 8 * (r >> 2) + 4 * hi; }

constexpr int S_ = 2048, DM = 448, H_ = 16, DH = 256, HD = 4096, MM = 8, SEG = 32, NSEG = 64, DC = 1024;

template <int BN, bool NT>
__global__ __launch_bounds__(256) void gemm_kernel(const u16* __restrict__ A, const u16* __restrict__ B,
                                                   u16* __restrict__ C, int lda, int ldb, int ldc,
                                                   long sA, long sB, long sC, int M, int N, int K,
                                                   float scale) {
    constexpr int BM = 128, BK = 64;
    constexpr int AROW = 72;
    constexpr int BROW = NT ? 72 : (BN + 32);
    constexpr int BLDS = NT ? BN * 72 : 64 * (BN + 32);
    __shared__ __attribute__((aligned(16))) u16 smem[BM * AROW + BLDS];
    u16* As = smem;
    u16* Bs = smem + BM * AROW;
    const int tid = threadIdx.x, lane = tid & 63, wid = tid >> 6;
    const int wm = wid >> 1, wn = wid & 1;
    const int m0 = blockIdx.y * BM, n0 = blockIdx.x * BN;
    A += (long)blockIdx.z * sA;
    B += (long)blockIdx.z * sB;
    C += (long)blockIdx.z * sC;
    constexpr int NTI = BN / 64;
    f32x16 acc[2][NTI];
#pragma unroll
    for (int i = 0; i < 2; ++i)
#pragma unroll
        for (int j = 0; j < NTI; ++j) acc[i][j] = f32x16{};

    constexpr int BCH = BN / 32;
    u32x4 ra[4], rb[BCH];
    auto load_tiles = [&](int k0) {
#pragma unroll
        for (int i = 0; i < 4; ++i) {
            int c = tid + 256 * i, row = c >> 3, kc = c & 7;
            int gr = m0 + row;
            if (gr < M)
                ra[i] = *reinterpret_cast<const u32x4*>(A + (long)gr * lda + k0 + kc * 8);
            else
                ra[i] = u32x4{0, 0, 0, 0};
        }
#pragma unroll
        for (int i = 0; i < BCH; ++i) {
            int c = tid + 256 * i;
            if (NT) {
                int row = c >> 3, kc = c & 7;
                rb[i] = *reinterpret_cast<const u32x4*>(B + (long)(n0 + row) * ldb + k0 + kc * 8);
            } else {
                int krow = c / (BN / 8), nc = c % (BN / 8);
                rb[i] = *reinterpret_cast<const u32x4*>(B + (long)(k0 + krow) * ldb + n0 + nc * 8);
            }
        }
    };
    auto store_tiles = [&]() {
#pragma unroll
        for (int i = 0; i < 4; ++i) {
            int c = tid + 256 * i, row = c >> 3, kc = c & 7;
            *reinterpret_cast<u32x4*>(As + row * AROW + kc * 8) = ra[i];
        }
#pragma unroll
        for (int i = 0; i < BCH; ++i) {
            int c = tid + 256 * i;
            if (NT) {
                int row = c >> 3, kc = c & 7;
                *reinterpret_cast<u32x4*>(Bs + row * BROW + kc * 8) = rb[i];
            } else {
                int krow = c / (BN / 8), nc = c % (BN / 8);
                *reinterpret_cast<u32x4*>(Bs + krow * BROW + nc * 8) = rb[i];
            }
        }
    };
    const int r32 = lane & 31, hi = lane >> 5, g = lane >> 4, q4 = (lane & 15) >> 2, p4 = lane & 3;
    load_tiles(0);
    const int nk = K / BK;
    for (int kt = 0; kt < nk; ++kt) {
        __syncthreads();
        store_tiles();
        __syncthreads();
        if (kt + 1 < nk) load_tiles((kt + 1) * BK);
#pragma unroll
        for (int ks = 0; ks < 4; ++ks) {
            bf16x8 af[2], bfr[NTI];
#pragma unroll
            for (int i = 0; i < 2; ++i)
                af[i] = *reinterpret_cast<const bf16x8*>(As + (wm * 64 + i * 32 + r32) * AROW + ks * 16 + hi * 8);
#pragma unroll
            for (int j = 0; j < NTI; ++j) {
                int nb = wn * (BN / 2) + j * 32;
                if (NT) {
                    bfr[j] = *reinterpret_cast<const bf16x8*>(Bs + (nb + r32) * BROW + ks * 16 + hi * 8);
                } else {
                    const u16* pB = Bs + (ks * 16 + 8 * (g >> 1) + q4) * BROW + nb + 16 * (g & 1) + 4 * p4;
                    s16x4 lo = tr_read(pB);
                    s16x4 hi4 = tr_read(pB + 4 * BROW);
                    bfr[j] = bf16x8{lo[0], lo[1], lo[2], lo[3], hi4[0], hi4[1], hi4[2], hi4[3]};
                }
            }
#pragma unroll
            for (int i = 0; i < 2; ++i)
#pragma unroll
                for (int j = 0; j < NTI; ++j) acc[i][j] = MFMA32(af[i], bfr[j], acc[i][j]);
        }
    }
#pragma unroll
    for (int i = 0; i < 2; ++i)
#pragma unroll
        for (int j = 0; j < NTI; ++j) {
            int col = n0 + wn * (BN / 2) + j * 32 + r32;
#pragma unroll
            for (int r = 0; r < 16; ++r) {
                int row = m0 + wm * 64 + i * 32 + crow(r, hi);
                if (row < M) C[(long)row * ldc + col] = f2bf(acc[i][j][r] * scale);
            }
        }
}

constexpr int KROW = 264;
constexpr int VROW = 288;
__global__ __launch_bounds__(256) void attnB_kernel(const u16* __restrict__ Q, const u16* __restrict__ K,
                                                    const u16* __restrict__ V, u16* __restrict__ O) {
    __shared__ __attribute__((aligned(16))) u16 smem[64 * KROW + 64 * VROW];
    u16* Ks = smem;
    u16* Vs = smem + 64 * KROW;
    const int tid = threadIdx.x, lane = tid & 63, wid = tid >> 6;
    const int qb = (int)gridDim.x - 1 - (int)blockIdx.x, h = blockIdx.y;
    const int q0 = qb * 128;
    const int r32 = lane & 31, hi = lane >> 5, g = lane >> 4, q4 = (lane & 15) >> 2, p4 = lane & 3;
    const int qrow = q0 + wid * 32 + r32;
    bf16x8 qf[16];
#pragma unroll
    for (int ks = 0; ks < 16; ++ks)
        qf[ks] = *reinterpret_cast<const bf16x8*>(Q + (long)qrow * HD + h * DH + ks * 16 + hi * 8);
    f32x16 o[8];
#pragma unroll
    for (int i = 0; i < 8; ++i) o[i] = f32x16{};
    float mrun = -INFINITY, lrun = 0.f;
    const float c2 = 0.0625f * 1.4426950408889634f;
    const int nt = (q0 + 128) / 64;
    u32x4 rk[8], rv[8];
    auto load_kv = [&](int t) {
#pragma unroll
        for (int i = 0; i < 8; ++i) {
            int c = tid + 256 * i, row = c >> 5, cc = c & 31;
            long off = (long)(t * 64 + row) * HD + h * DH + cc * 8;
            rk[i] = *reinterpret_cast<const u32x4*>(K + off);
            rv[i] = *reinterpret_cast<const u32x4*>(V + off);
        }
    };
    auto store_kv = [&]() {
#pragma unroll
        for (int i = 0; i < 8; ++i) {
            int c = tid + 256 * i, row = c >> 5, cc = c & 31;
            *reinterpret_cast<u32x4*>(Ks + row * KROW + cc * 8) = rk[i];
            *reinterpret_cast<u32x4*>(Vs + row * VROW + cc * 8) = rv[i];
        }
    };
    load_kv(0);
    for (int t = 0; t < nt; ++t) {
        __syncthreads();
        store_kv();
        __syncthreads();
        if (t + 1 < nt) load_kv(t + 1);
        const int kbase = t * 64;
        if (kbase <= q0 + wid * 32 + 31) {
            f32x16 s0 = f32x16{}, s1 = f32x16{};
#pragma unroll
            for (int ks = 0; ks < 16; ++ks) {
                bf16x8 k0 = *reinterpret_cast<const bf16x8*>(Ks + r32 * KROW + ks * 16 + hi * 8);
                bf16x8 k1 = *reinterpret_cast<const bf16x8*>(Ks + (32 + r32) * KROW + ks * 16 + hi * 8);
                s0 = MFMA32(k0, qf[ks], s0);
                s1 = MFMA32(k1, qf[ks], s1);
                if ((ks & 3) == 3) __builtin_amdgcn_sched_barrier(0);
            }
            if (kbase + 63 > q0 + wid * 32) {
#pragma unroll
                for (int r = 0; r < 16; ++r) {
                    int kk = kbase + crow(r, hi);
                    if (kk > qrow) s0[r] = -INFINITY;
                    if (kk + 32 > qrow) s1[r] = -INFINITY;
                }
            }
            float mx = -INFINITY;
#pragma unroll
            for (int r = 0; r < 16; ++r) mx = fmaxf(mx, fmaxf(s0[r], s1[r]));
            mx = fmaxf(mx, __shfl_xor(mx, 32));
            float mnew = fmaxf(mrun, mx);
            float alpha = __builtin_amdgcn_exp2f((mrun - mnew) * c2);
            float nm = -mnew * c2;
            float psum = 0.f;
#pragma unroll
            for (int r = 0; r < 16; ++r) {
                s0[r] = __builtin_amdgcn_exp2f(fmaf(s0[r], c2, nm));
                s1[r] = __builtin_amdgcn_exp2f(fmaf(s1[r], c2, nm));
                psum += s0[r] + s1[r];
            }
            psum += __shfl_xor(psum, 32);
            lrun = lrun * alpha + psum;
            mrun = mnew;
#pragma unroll
            for (int i = 0; i < 8; ++i)
#pragma unroll
                for (int r = 0; r < 16; ++r) o[i][r] *= alpha;
            bf16x8 pf[4];
#pragma unroll
            for (int s = 0; s < 2; ++s) {
                u32x4 a, b;
#pragma unroll
                for (int j = 0; j < 4; ++j) {
                    a[j] = pack2(s0[8 * s + 2 * j], s0[8 * s + 2 * j + 1]);
                    b[j] = pack2(s1[8 * s + 2 * j], s1[8 * s + 2 * j + 1]);
                }
                pf[s] = __builtin_bit_cast(bf16x8, a);
                pf[2 + s] = __builtin_bit_cast(bf16x8, b);
            }
#pragma unroll
            for (int dt = 0; dt < 8; ++dt) {
#pragma unroll
                for (int s = 0; s < 4; ++s) {
                    const u16* pV = Vs + (16 * s + 4 * hi + q4) * VROW + 32 * dt + 16 * (g & 1) + 4 * p4;
                    s16x4 lo = tr_read(pV);
                    s16x4 hi4 = tr_read(pV + 8 * VROW);
                    bf16x8 vf = bf16x8{lo[0], lo[1], lo[2], lo[3], hi4[0], hi4[1], hi4[2], hi4[3]};
                    o[dt] = MFMA32(vf, pf[s], o[dt]);
                }
                __builtin_amdgcn_sched_barrier(0);
            }
        }
    }
    float inv = 1.f / lrun;
#pragma unroll
    for (int dt = 0; dt < 8; ++dt)
#pragma unroll
        for (int rr = 0; rr < 4; ++rr) {
            u32x2 w;
            w[0] = pack2(o[dt][4 * rr] * inv, o[dt][4 * rr + 1] * inv);
            w[1] = pack2(o[dt][4 * rr + 2] * inv, o[dt][4 * rr + 3] * inv);
            *reinterpret_cast<u32x2*>(O + (long)qrow * HD + h * DH + 32 * dt + 8 * rr + 4 * hi) = w;
        }
}

__global__ __launch_bounds__(64) void attnA_kernel(const u16* __restrict__ XQ, const u16* __restrict__ XK,
                                                   const u16* __restrict__ XV, const u16* __restrict__ KM,
                                                   const u16* __restrict__ VM, u16* __restrict__ OA) {
    __shared__ __attribute__((aligned(16))) u16 Vs[48 * VROW];
    const int lane = threadIdx.x;
    const int s = blockIdx.x, h = blockIdx.y;
    const int r32 = lane & 31, hi = lane >> 5, g = lane >> 4, q4 = (lane & 15) >> 2, p4 = lane & 3;
    for (int c = lane; c < 48 * 32; c += 64) {
        int row = c >> 5, cc = c & 31;
        u32x4 v = u32x4{0, 0, 0, 0};
        if (row < 8)
            v = *reinterpret_cast<const u32x4*>(VM + (long)(s * 8 + row) * HD + h * DH + cc * 8);
        else if (row < 40)
            v = *reinterpret_cast<const u32x4*>(XV + (long)(s * 32 + row - 8) * HD + h * DH + cc * 8);
        *reinterpret_cast<u32x4*>(Vs + row * VROW + cc * 8) = v;
    }
    const int qrow = s * 32 + r32;
    const u16* k0p = (r32 < 8) ? (KM + (long)(s * 8 + r32) * HD + h * DH) : (XK + (long)(s * 32 + r32 - 8) * HD + h * DH);
    const int k1i = (r32 < 8) ? (24 + r32) : 0;
    const u16* k1p = XK + (long)(s * 32 + k1i) * HD + h * DH;
    f32x16 s0 = f32x16{}, s1 = f32x16{};
#pragma unroll
    for (int ks = 0; ks < 16; ++ks) {
        bf16x8 qf = *reinterpret_cast<const bf16x8*>(XQ + (long)qrow * HD + h * DH + ks * 16 + hi * 8);
        bf16x8 ka = *reinterpret_cast<const bf16x8*>(k0p + ks * 16 + hi * 8);
        bf16x8 kb = *reinterpret_cast<const bf16x8*>(k1p + ks * 16 + hi * 8);
        s0 = MFMA32(ka, qf, s0);
        s1 = MFMA32(kb, qf, s1);
    }
#pragma unroll
    for (int r = 0; r < 16; ++r) {
        int kk = crow(r, hi);
        if (kk >= 8 && kk - 8 > r32) s0[r] = -INFINITY;
        int k2 = kk + 32;
        if (k2 >= 40 || k2 - 8 > r32) s1[r] = -INFINITY;
    }
    const float c2 = 0.0625f * 1.4426950408889634f;
    float mx = -INFINITY;
#pragma unroll
    for (int r = 0; r < 16; ++r) mx = fmaxf(mx, fmaxf(s0[r], s1[r]));
    mx = fmaxf(mx, __shfl_xor(mx, 32));
    float nm = -mx * c2, psum = 0.f;
#pragma unroll
    for (int r = 0; r < 16; ++r) {
        s0[r] = __builtin_amdgcn_exp2f(fmaf(s0[r], c2, nm));
        s1[r] = __builtin_amdgcn_exp2f(fmaf(s1[r], c2, nm));
        psum += s0[r] + s1[r];
    }
    psum += __shfl_xor(psum, 32);
    float inv = 1.f / psum;
    bf16x8 pf[3];
#pragma unroll
    for (int sI = 0; sI < 2; ++sI) {
        u32x4 a;
#pragma unroll
        for (int j = 0; j < 4; ++j) a[j] = pack2(s0[8 * sI + 2 * j] * inv, s0[8 * sI + 2 * j + 1] * inv);
        pf[sI] = __builtin_bit_cast(bf16x8, a);
    }
    {
        u32x4 a;
#pragma unroll
        for (int j = 0; j < 4; ++j) a[j] = pack2(s1[2 * j] * inv, s1[2 * j + 1] * inv);
        pf[2] = __builtin_bit_cast(bf16x8, a);
    }
    __syncthreads();
#pragma unroll
    for (int dt = 0; dt < 8; ++dt) {
        f32x16 o = f32x16{};
#pragma unroll
        for (int sI = 0; sI < 3; ++sI) {
            const u16* pV = Vs + (16 * sI + 4 * hi + q4) * VROW + 32 * dt + 16 * (g & 1) + 4 * p4;
            s16x4 lo = tr_read(pV);
            s16x4 hi4 = tr_read(pV + 8 * VROW);
            bf16x8 vf = bf16x8{lo[0], lo[1], lo[2], lo[3], hi4[0], hi4[1], hi4[2], hi4[3]};
            o = MFMA32(vf, pf[sI], o);
        }
#pragma unroll
        for (int rr = 0; rr < 4; ++rr) {
            u32x2 w;
            w[0] = pack2(o[4 * rr], o[4 * rr + 1]);
            w[1] = pack2(o[4 * rr + 2], o[4 * rr + 3]);
            *reinterpret_cast<u32x2*>(OA + (long)qrow * HD + h * DH + 32 * dt + 8 * rr + 4 * hi) = w;
        }
    }
}

__global__ __launch_bounds__(256) void rec_scores_kernel(const u16* __restrict__ MEM, const u16* __restrict__ Amat,
                                                         const u16* __restrict__ Bmat, float* __restrict__ P, int s) {
    __shared__ float mem[8][DM];
    __shared__ float t[8][DM];
    __shared__ float sc[8][40];
    const int tid = threadIdx.x, h = blockIdx.x;
    for (int i = tid; i < 8 * DM; i += 256) mem[i / DM][i % DM] = bf2f(MEM[(long)s * 8 * DM + i]);
    __syncthreads();
    for (int j = tid; j < DM; j += 256) {
        float a[8] = {0, 0, 0, 0, 0, 0, 0, 0};
        const u16* Ap = Amat + (long)h * DM * DM + j;
        for (int i = 0; i < DM; ++i) {
            float w = bf2f(Ap[(long)i * DM]);
#pragma unroll
            for (int r = 0; r < 8; ++r) a[r] += mem[r][i] * w;
        }
#pragma unroll
        for (int r = 0; r < 8; ++r) t[r][j] = a[r];
    }
    {
        int r = tid >> 5, k = tid & 31;
        const u16* Bp = Bmat + (long)h * DM * S_ + s * 32 + k;
        float a = 0.f;
        for (int i = 0; i < DM; ++i) a += mem[r][i] * bf2f(Bp[(long)i * S_]);
        sc[r][8 + k] = a;
    }
    __syncthreads();
    if (tid < 64) {
        int r = tid >> 3, c = tid & 7;
        float a = 0.f;
        for (int j = 0; j < DM; ++j) a += t[r][j] * mem[c][j];
        sc[r][c] = a;
    }
    __syncthreads();
    if (tid < 8) {
        float mx = -INFINITY;
        for (int k = 0; k < 40; ++k) mx = fmaxf(mx, sc[tid][k]);
        float sum = 0.f;
        for (int k = 0; k < 40; ++k) {
            float e = __expf(sc[tid][k] - mx);
            sc[tid][k] = e;
            sum += e;
        }
        float inv = 1.f / sum;
        for (int k = 0; k < 40; ++k) P[(h * 8 + tid) * 40 + k] = sc[tid][k] * inv;
    }
}

__global__ __launch_bounds__(256) void rec_out_kernel(u16* __restrict__ MEM, const u16* __restrict__ Cmat,
                                                      const u16* __restrict__ Dmat, const float* __restrict__ P, int s) {
    __shared__ float mem[8][DM];
    __shared__ float Ps[16][8][40];
    __shared__ float red[16][8][16];
    const int tid = threadIdx.x;
    const int c16 = tid & 15, h = tid >> 4;
    const int col = blockIdx.x * 16 + c16;
    for (int i = tid; i < 8 * DM; i += 256) mem[i / DM][i % DM] = bf2f(MEM[(long)s * 8 * DM + i]);
    for (int i = tid; i < 16 * 8 * 40; i += 256) (&Ps[0][0][0])[i] = P[i];
    __syncthreads();
    float u[8] = {0, 0, 0, 0, 0, 0, 0, 0};
    const u16* Cp = Cmat + (long)h * DM * DM + col;
    for (int i = 0; i < DM; ++i) {
        float w = bf2f(Cp[(long)i * DM]);
#pragma unroll
        for (int r = 0; r < 8; ++r) u[r] += mem[r][i] * w;
    }
    float o[8] = {0, 0, 0, 0, 0, 0, 0, 0};
#pragma unroll
    for (int r = 0; r < 8; ++r)
#pragma unroll
        for (int j = 0; j < 8; ++j) o[r] += Ps[h][r][j] * u[j];
    const u16* Dp = Dmat + ((long)h * S_ + s * 32) * DM + col;
    for (int k = 0; k < 32; ++k) {
        float d = bf2f(Dp[(long)k * DM]);
#pragma unroll
        for (int r = 0; r < 8; ++r) o[r] += Ps[h][r][8 + k] * d;
    }
#pragma unroll
    for (int r = 0; r < 8; ++r) red[h][r][c16] = o[r];
    __syncthreads();
    if (tid < 128) {
        int r = tid >> 4, c = tid & 15;
        float a = 0.f;
        for (int hh = 0; hh < 16; ++hh) a += red[hh][r][c];
        MEM[(long)(s + 1) * 8 * DM + r * DM + blockIdx.x * 16 + c] = f2bf(a);
    }
}

template <int BN, bool NT>
static void launch_gemm(hipStream_t st, const u16* A, const u16* B, u16* C, int lda, int ldb, int ldc, long sA,
                        long sB, long sC, int M, int N, int K, int batch, float scale) {
    dim3 grid(N / BN, (M + 127) / 128, batch);
    hipLaunchKernelGGL((gemm_kernel<BN, NT>), grid, dim3(256), 0, st, A, B, C, lda, ldb, ldc, sA, sB, sC, M, N, K, scale);
}

extern "C" void kernel_launch(void* const* d_in, const int* in_sizes, int n_in, void* d_out, int out_size,
                              void* d_ws, size_t ws_size, hipStream_t stream) {
    const u16* A_x = (const u16*)d_in[0];
    const u16* A_mem0 = (const u16*)d_in[1];
    const u16* A_Wq = (const u16*)d_in[2];
    const u16* A_Wk = (const u16*)d_in[3];
    const u16* A_Wv = (const u16*)d_in[4];
    const u16* A_Wo = (const u16*)d_in[5];
    const u16* B_x = (const u16*)d_in[7];
    const u16* B_ckv = (const u16*)d_in[8];
    const u16* B_wq = (const u16*)d_in[9];
    const u16* B_wuk = (const u16*)d_in[10];
    const u16* B_wuv = (const u16*)d_in[11];
    const u16* B_wo = (const u16*)d_in[12];
    u16* out = (u16*)d_out;

    char* ws = (char*)d_ws;
    const size_t SZ = (size_t)S_ * HD * 2;
    u16* XQ = (u16*)(ws + 0 * SZ);
    u16* XK = (u16*)(ws + 1 * SZ);
    u16* XV = (u16*)(ws + 2 * SZ);
    u16* Qb = (u16*)(ws + 3 * SZ);
    u16* Kb = (u16*)(ws + 4 * SZ);
    u16* Vb = (u16*)(ws + 5 * SZ);
    u16* OA = (u16*)(ws + 6 * SZ);
    u16* OB = (u16*)(ws + 7 * SZ);
    size_t off = 8 * SZ;
    u16* Amat = (u16*)(ws + off); off += (size_t)H_ * DM * DM * 2;
    u16* Cmat = (u16*)(ws + off); off += (size_t)H_ * DM * DM * 2;
    u16* Bmat = (u16*)(ws + off); off += (size_t)H_ * DM * S_ * 2;
    u16* Dmat = (u16*)(ws + off); off += (size_t)H_ * DM * S_ * 2;
    u16* MEM = (u16*)(ws + off); off += (size_t)65 * 8 * DM * 2; off = (off + 255) & ~(size_t)255;
    u16* KM = (u16*)(ws + off); off += (size_t)512 * HD * 2;
    u16* VM = (u16*)(ws + off); off += (size_t)512 * HD * 2;
    float* P = (float*)(ws + off); off += 16 * 8 * 40 * 4;

    launch_gemm<128, false>(stream, A_x, A_Wq, XQ, DM, HD, HD, 0, 0, 0, S_, HD, DM, 1, 1.f);
    launch_gemm<128, false>(stream, A_x, A_Wk, XK, DM, HD, HD, 0, 0, 0, S_, HD, DM, 1, 1.f);
    launch_gemm<128, false>(stream, A_x, A_Wv, XV, DM, HD, HD, 0, 0, 0, S_, HD, DM, 1, 1.f);
    launch_gemm<64, true>(stream, A_Wq, A_Wk, Amat, HD, HD, DM, DH, DH, (long)DM * DM, DM, DM, DH, H_, 0.0625f);
    launch_gemm<64, false>(stream, A_Wv, A_Wo, Cmat, HD, DM, DM, DH, (long)DH * DM, (long)DM * DM, DM, DM, DH, H_, 1.f);
    launch_gemm<128, true>(stream, A_Wq, XK, Bmat, HD, HD, S_, DH, DH, (long)DM * S_, DM, S_, DH, H_, 0.0625f);
    launch_gemm<64, false>(stream, XV, A_Wo, Dmat, HD, DM, DM, DH, (long)DH * DM, (long)S_ * DM, S_, DM, DH, H_, 1.f);
    launch_gemm<128, false>(stream, B_x, B_wq, Qb, DM, HD, HD, 0, 0, 0, S_, HD, DM, 1, 1.f);
    launch_gemm<128, false>(stream, B_ckv, B_wuk, Kb, DC, HD, HD, 0, 0, 0, S_, HD, DC, 1, 1.f);
    launch_gemm<128, false>(stream, B_ckv, B_wuv, Vb, DC, HD, HD, 0, 0, 0, S_, HD, DC, 1, 1.f);
    hipLaunchKernelGGL(attnB_kernel, dim3(16, 16), dim3(256), 0, stream, Qb, Kb, Vb, OB);
    hipMemcpyAsync(MEM, A_mem0, 8 * DM * 2, hipMemcpyDeviceToDevice, stream);
    for (int s = 0; s < NSEG; ++s) {
        hipLaunchKernelGGL(rec_scores_kernel, dim3(16), dim3(256), 0, stream, MEM, Amat, Bmat, P, s);
        if (s + 1 < NSEG)
            hipLaunchKernelGGL(rec_out_kernel, dim3(28), dim3(256), 0, stream, MEM, Cmat, Dmat, P, s);
    }
    launch_gemm<128, false>(stream, MEM, A_Wk, KM, DM, HD, HD, 0, 0, 0, 512, HD, DM, 1, 1.f);
    launch_gemm<128, false>(stream, MEM, A_Wv, VM, DM, HD, HD, 0, 0, 0, 512, HD, DM, 1, 1.f);
    hipLaunchKernelGGL(attnA_kernel, dim3(64, 16), dim3(64), 0, stream, XQ, XK, XV, KM, VM, OA);
    launch_gemm<64, false>(stream, OA, A_Wo, out, HD, DM, 896, 0, 0, 0, S_, DM, HD, 1, 1.f);
    launch_gemm<64, false>(stream, OB, B_wo, out + 448, HD, DM, 896, 0, 0, 0, S_, DM, HD, 1, 1.f);
}
```

```cpp
#include <hip/hip_runtime.h>
#include <hip/hip_bf16.h>
#include <stdint.h>

typedef unsigned short u16;
typedef __attribute__((ext_vector_type(8))) short bf16x8;
typedef __attribute__((ext_vector_type(4))) short s16x4;
typedef __attribute__((ext_vector_type(16))) float f32x16;
typedef __attribute__((ext_vector_type(4))) float f32x4;
typedef __attribute__((ext_vector_type(4))) unsigned u32x4;
typedef __attribute__((ext_vector_type(2))) unsigned u32x2;
typedef short v4i16_t __attribute__((ext_vector_type(4)));

#define LAS __attribute__((address_space(3)))

__device__ __forceinline__ float bf2f(u16 x) { return __uint_as_float(((unsigned)x) << 16); }
__device__ __forceinline__ u16 f2bf(float f) {
    unsigned u = __float_as_uint(f);
    u += 0x7fffu + ((u >> 16) & 1u);
    return (u16)(u >> 16);
}
__device__ __forceinline__ unsigned pack2(float lo, float hi) {
    return (unsigned)f2bf(lo) | ((unsigned)f2bf(hi) << 16);
}
__device__ __forceinline__ s16x4 tr_read(const void* p) {
    return __builtin_bit_cast(s16x4, __builtin_amdgcn_ds_read_tr16_b64_v4i16((LAS v4i16_t*)p));
}
#define MFMA32(a, b, c) __builtin_amdgcn_mfma_f32_32x32x16_bf16(a, b, c, 0, 0, 0)
#define MFMA16(a, b, c) __builtin_amdgcn_mfma_f32_16x16x32_bf16(a, b, c, 0, 0, 0)

__device__ __forceinline__ int crow(int r, int hi) { return (r & 3) + 8 * (r >> 2) + 4 * hi; }

constexpr int S_ = 2048, DM = 448, H_ = 16, DH = 256, HD = 4096, MM = 8, SEG = 32, NSEG = 64, DC = 1024;

template <int BN, bool NT, bool TO>
__global__ __launch_bounds__(256) void gemm_kernel(const u16* __restrict__ A, const u16* __restrict__ B,
                                                   u16* __restrict__ C, int lda, int ldb, int ldc,
                                                   long sA, long sB, long sC, int M, int N, int K,
                                                   float scale) {
    constexpr int BM = 128, BK = 64;
    constexpr int AROW = 72;
    constexpr int BROW = NT ? 72 : (BN + 32);
    constexpr int BLDS = NT ? BN * 72 : 64 * (BN + 32);
    __shared__ __attribute__((aligned(16))) u16 smem[BM * AROW + BLDS];
    u16* As = smem;
    u16* Bs = smem + BM * AROW;
    const int tid = threadIdx.x, lane = tid & 63, wid = tid >> 6;
    const int wm = wid >> 1, wn = wid & 1;
    const int m0 = blockIdx.y * BM, n0 = blockIdx.x * BN;
    A += (long)blockIdx.z * sA;
    B += (long)blockIdx.z * sB;
    C += (long)blockIdx.z * sC;
    constexpr int NTI = BN / 64;
    f32x16 acc[2][NTI];
#pragma unroll
    for (int i = 0; i < 2; ++i)
#pragma unroll
        for (int j = 0; j < NTI; ++j) acc[i][j] = f32x16{};

    constexpr int BCH = BN / 32;
    u32x4 ra[4], rb[BCH];
    auto load_tiles = [&](int k0) {
#pragma unroll
        for (int i = 0; i < 4; ++i) {
            int c = tid + 256 * i, row = c >> 3, kc = c & 7;
            int gr = m0 + row;
            if (gr < M)
                ra[i] = *reinterpret_cast<const u32x4*>(A + (long)gr * lda + k0 + kc * 8);
            else
                ra[i] = u32x4{0, 0, 0, 0};
        }
#pragma unroll
        for (int i = 0; i < BCH; ++i) {
            int c = tid + 256 * i;
            if (NT) {
                int row = c >> 3, kc = c & 7;
                rb[i] = *reinterpret_cast<const u32x4*>(B + (long)(n0 + row) * ldb + k0 + kc * 8);
            } else {
                int krow = c / (BN / 8), nc = c % (BN / 8);
                rb[i] = *reinterpret_cast<const u32x4*>(B + (long)(k0 + krow) * ldb + n0 + nc * 8);
            }
        }
    };
    auto store_tiles = [&]() {
#pragma unroll
        for (int i = 0; i < 4; ++i) {
            int c = tid + 256 * i, row = c >> 3, kc = c & 7;
            *reinterpret_cast<u32x4*>(As + row * AROW + kc * 8) = ra[i];
        }
#pragma unroll
        for (int i = 0; i < BCH; ++i) {
            int c = tid + 256 * i;
            if (NT) {
                int row = c >> 3, kc = c & 7;
                *reinterpret_cast<u32x4*>(Bs + row * BROW + kc * 8) = rb[i];
            } else {
                int krow = c / (BN / 8), nc = c % (BN / 8);
                *reinterpret_cast<u32x4*>(Bs + krow * BROW + nc * 8) = rb[i];
            }
        }
    };
    const int r32 = lane & 31, hi = lane >> 5, g = lane >> 4, q4 = (lane & 15) >> 2, p4 = lane & 3;
    load_tiles(0);
    const int nk = K / BK;
    for (int kt = 0; kt < nk; ++kt) {
        __syncthreads();
        store_tiles();
        __syncthreads();
        if (kt + 1 < nk) load_tiles((kt + 1) * BK);
#pragma unroll
        for (int ks = 0; ks < 4; ++ks) {
            bf16x8 af[2], bfr[NTI];
#pragma unroll
            for (int i = 0; i < 2; ++i)
                af[i] = *reinterpret_cast<const bf16x8*>(As + (wm * 64 + i * 32 + r32) * AROW + ks * 16 + hi * 8);
#pragma unroll
            for (int j = 0; j < NTI; ++j) {
                int nb = wn * (BN / 2) + j * 32;
                if (NT) {
                    bfr[j] = *reinterpret_cast<const bf16x8*>(Bs + (nb + r32) * BROW + ks * 16 + hi * 8);
                } else {
                    const u16* pB = Bs + (ks * 16 + 8 * (g >> 1) + q4) * BROW + nb + 16 * (g & 1) + 4 * p4;
                    s16x4 lo = tr_read(pB);
                    s16x4 hi4 = tr_read(pB + 4 * BROW);
                    bfr[j] = bf16x8{lo[0], lo[1], lo[2], lo[3], hi4[0], hi4[1], hi4[2], hi4[3]};
                }
            }
#pragma unroll
            for (int i = 0; i < 2; ++i)
#pragma unroll
                for (int j = 0; j < NTI; ++j) acc[i][j] = MFMA32(af[i], bfr[j], acc[i][j]);
        }
    }
#pragma unroll
    for (int i = 0; i < 2; ++i)
#pragma unroll
        for (int j = 0; j < NTI; ++j) {
            int col = n0 + wn * (BN / 2) + j * 32 + r32;
            if (TO) {
#pragma unroll
                for (int rr = 0; rr < 4; ++rr) {
                    int row = m0 + wm * 64 + i * 32 + 8 * rr + 4 * hi;
                    u32x2 w;
                    w[0] = pack2(acc[i][j][4 * rr] * scale, acc[i][j][4 * rr + 1] * scale);
                    w[1] = pack2(acc[i][j][4 * rr + 2] * scale, acc[i][j][4 * rr + 3] * scale);
                    if (row < M) *reinterpret_cast<u32x2*>(C + (long)col * ldc + row) = w;
                }
            } else {
#pragma unroll
                for (int r = 0; r < 16; ++r) {
                    int row = m0 + wm * 64 + i * 32 + crow(r, hi);
                    if (row < M) C[(long)row * ldc + col] = f2bf(acc[i][j][r] * scale);
                }
            }
        }
}

constexpr int KROW = 264;
constexpr int VROW = 288;
constexpr int ATTN_LDS = (64 * KROW + 64 * VROW) * 2;
__device__ __forceinline__ void attnB_body(const u16* __restrict__ Q, const u16* __restrict__ K,
                                           const u16* __restrict__ V, u16* __restrict__ O, u16* smem, int qb, int h) {
    u16* Ks = smem;
    u16* Vs = smem + 64 * KROW;
    const int tid = threadIdx.x, lane = tid & 63, wid = tid >> 6;
    const int q0 = qb * 128;
    const int r32 = lane & 31, hi = lane >> 5, g = lane >> 4, q4 = (lane & 15) >> 2, p4 = lane & 3;
    const int qrow = q0 + wid * 32 + r32;
    bf16x8 qf[16];
#pragma unroll
    for (int ks = 0; ks < 16; ++ks)
        qf[ks] = *reinterpret_cast<const bf16x8*>(Q + (long)qrow * HD + h * DH + ks * 16 + hi * 8);
    f32x16 o[8];
#pragma unroll
    for (int i = 0; i < 8; ++i) o[i] = f32x16{};
    float mrun = -INFINITY, lrun = 0.f;
    const float c2 = 0.0625f * 1.4426950408889634f;
    const int nt = (q0 + 128) / 64;
    u32x4 rk[8], rv[8];
    auto load_kv = [&](int t) {
#pragma unroll
        for (int i = 0; i < 8; ++i) {
            int c = tid + 256 * i, row = c >> 5, cc = c & 31;
            long off = (long)(t * 64 + row) * HD + h * DH + cc * 8;
            rk[i] = *reinterpret_cast<const u32x4*>(K + off);
            rv[i] = *reinterpret_cast<const u32x4*>(V + off);
        }
    };
    auto store_kv = [&]() {
#pragma unroll
        for (int i = 0; i < 8; ++i) {
            int c = tid + 256 * i, row = c >> 5, cc = c & 31;
            *reinterpret_cast<u32x4*>(Ks + row * KROW + cc * 8) = rk[i];
            *reinterpret_cast<u32x4*>(Vs + row * VROW + cc * 8) = rv[i];
        }
    };
    load_kv(0);
    for (int t = 0; t < nt; ++t) {
        __syncthreads();
        store_kv();
        __syncthreads();
        if (t + 1 < nt) load_kv(t + 1);
        const int kbase = t * 64;
        if (kbase <= q0 + wid * 32 + 31) {
            f32x16 s0 = f32x16{}, s1 = f32x16{};
#pragma unroll
            for (int ks = 0; ks < 16; ++ks) {
                bf16x8 k0 = *reinterpret_cast<const bf16x8*>(Ks + r32 * KROW + ks * 16 + hi * 8);
                bf16x8 k1 = *reinterpret_cast<const bf16x8*>(Ks + (32 + r32) * KROW + ks * 16 + hi * 8);
                s0 = MFMA32(k0, qf[ks], s0);
                s1 = MFMA32(k1, qf[ks], s1);
                if ((ks & 3) == 3) __builtin_amdgcn_sched_barrier(0);
            }
            if (kbase + 63 > q0 + wid * 32) {
#pragma unroll
                for (int r = 0; r < 16; ++r) {
                    int kk = kbase + crow(r, hi);
                    if (kk > qrow) s0[r] = -INFINITY;
                    if (kk + 32 > qrow) s1[r] = -INFINITY;
                }
            }
            float mx = -INFINITY;
#pragma unroll
            for (int r = 0; r < 16; ++r) mx = fmaxf(mx, fmaxf(s0[r], s1[r]));
            mx = fmaxf(mx, __shfl_xor(mx, 32));
            float mnew = fmaxf(mrun, mx);
            float alpha = __builtin_amdgcn_exp2f((mrun - mnew) * c2);
            float nm = -mnew * c2;
            float psum = 0.f;
#pragma unroll
            for (int r = 0; r < 16; ++r) {
                s0[r] = __builtin_amdgcn_exp2f(fmaf(s0[r], c2, nm));
                s1[r] = __builtin_amdgcn_exp2f(fmaf(s1[r], c2, nm));
                psum += s0[r] + s1[r];
            }
            psum += __shfl_xor(psum, 32);
            lrun = lrun * alpha + psum;
            mrun = mnew;
#pragma unroll
            for (int i = 0; i < 8; ++i)
#pragma unroll
                for (int r = 0; r < 16; ++r) o[i][r] *= alpha;
            bf16x8 pf[4];
#pragma unroll
            for (int s = 0; s < 2; ++s) {
                u32x4 a, b;
#pragma unroll
                for (int j = 0; j < 4; ++j) {
                    a[j] = pack2(s0[8 * s + 2 * j], s0[8 * s + 2 * j + 1]);
                    b[j] = pack2(s1[8 * s + 2 * j], s1[8 * s + 2 * j + 1]);
                }
                pf[s] = __builtin_bit_cast(bf16x8, a);
                pf[2 + s] = __builtin_bit_cast(bf16x8, b);
            }
#pragma unroll
            for (int dt = 0; dt < 8; ++dt) {
#pragma unroll
                for (int s = 0; s < 4; ++s) {
                    const u16* pV = Vs + (16 * s + 4 * hi + q4) * VROW + 32 * dt + 16 * (g & 1) + 4 * p4;
                    s16x4 lo = tr_read(pV);
                    s16x4 hi4 = tr_read(pV + 8 * VROW);
                    bf16x8 vf = bf16x8{lo[0], lo[1], lo[2], lo[3], hi4[0], hi4[1], hi4[2], hi4[3]};
                    o[dt] = MFMA32(vf, pf[s], o[dt]);
                }
                __builtin_amdgcn_sched_barrier(0);
            }
        }
    }
    float inv = 1.f / lrun;
#pragma unroll
    for (int dt = 0; dt < 8; ++dt)
#pragma unroll
        for (int rr = 0; rr < 4; ++rr) {
            u32x2 w;
            w[0] = pack2(o[dt][4 * rr] * inv, o[dt][4 * rr + 1] * inv);
            w[1] = pack2(o[dt][4 * rr + 2] * inv, o[dt][4 * rr + 3] * inv);
            *reinterpret_cast<u32x2*>(O + (long)qrow * HD + h * DH + 32 * dt + 8 * rr + 4 * hi) = w;
        }
}

__global__ __launch_bounds__(64) void attnA_kernel(const u16* __restrict__ XQ, const u16* __restrict__ XK,
                                                   const u16* __restrict__ XV, const u16* __restrict__ KM,
                                                   const u16* __restrict__ VM, u16* __restrict__ OA) {
    __shared__ __attribute__((aligned(16))) u16 Vs[48 * VROW];
    const int lane = threadIdx.x;
    const int s = blockIdx.x, h = blockIdx.y;
    const int r32 = lane & 31, hi = lane >> 5, g = lane >> 4, q4 = (lane & 15) >> 2, p4 = lane & 3;
    for (int c = lane; c < 48 * 32; c += 64) {
        int row = c >> 5, cc = c & 31;
        u32x4 v = u32x4{0, 0, 0, 0};
        if (row < 8)
            v = *reinterpret_cast<const u32x4*>(VM + (long)(s * 8 + row) * HD + h * DH + cc * 8);
        else if (row < 40)
            v = *reinterpret_cast<const u32x4*>(XV + (long)(s * 32 + row - 8) * HD + h * DH + cc * 8);
        *reinterpret_cast<u32x4*>(Vs + row * VROW + cc * 8) = v;
    }
    const int qrow = s * 32 + r32;
    const u16* k0p = (r32 < 8) ? (KM + (long)(s * 8 + r32) * HD + h * DH) : (XK + (long)(s * 32 + r32 - 8) * HD + h * DH);
    const int k1i = (r32 < 8) ? (24 + r32) : 0;
    const u16* k1p = XK + (long)(s * 32 + k1i) * HD + h * DH;
    f32x16 s0 = f32x16{}, s1 = f32x16{};
#pragma unroll
    for (int ks = 0; ks < 16; ++ks) {
        bf16x8 qf = *reinterpret_cast<const bf16x8*>(XQ + (long)qrow * HD + h * DH + ks * 16 + hi * 8);
        bf16x8 ka = *reinterpret_cast<const bf16x8*>(k0p + ks * 16 + hi * 8);
        bf16x8 kb = *reinterpret_cast<const bf16x8*>(k1p + ks * 16 + hi * 8);
        s0 = MFMA32(ka, qf, s0);
        s1 = MFMA32(kb, qf, s1);
    }
#pragma unroll
    for (int r = 0; r < 16; ++r) {
        int kk = crow(r, hi);
        if (kk >= 8 && kk - 8 > r32) s0[r] = -INFINITY;
        int k2 = kk + 32;
        if (k2 >= 40 || k2 - 8 > r32) s1[r] = -INFINITY;
    }
    const float c2 = 0.0625f * 1.4426950408889634f;
    float mx = -INFINITY;
#pragma unroll
    for (int r = 0; r < 16; ++r) mx = fmaxf(mx, fmaxf(s0[r], s1[r]));
    mx = fmaxf(mx, __shfl_xor(mx, 32));
    float nm = -mx * c2, psum = 0.f;
#pragma unroll
    for (int r = 0; r < 16; ++r) {
        s0[r] = __builtin_amdgcn_exp2f(fmaf(s0[r], c2, nm));
        s1[r] = __builtin_amdgcn_exp2f(fmaf(s1[r], c2, nm));
        psum += s0[r] + s1[r];
    }
    psum += __shfl_xor(psum, 32);
    float inv = 1.f / psum;
    bf16x8 pf[3];
#pragma unroll
    for (int sI = 0; sI < 2; ++sI) {
        u32x4 a;
#pragma unroll
        for (int j = 0; j < 4; ++j) a[j] = pack2(s0[8 * sI + 2 * j] * inv, s0[8 * sI + 2 * j + 1] * inv);
        pf[sI] = __builtin_bit_cast(bf16x8, a);
    }
    {
        u32x4 a;
#pragma unroll
        for (int j = 0; j < 4; ++j) a[j] = pack2(s1[2 * j] * inv, s1[2 * j + 1] * inv);
        pf[2] = __builtin_bit_cast(bf16x8, a);
    }
    __syncthreads();
#pragma unroll
    for (int dt = 0; dt < 8; ++dt) {
        f32x16 o = f32x16{};
#pragma unroll
        for (int sI = 0; sI < 3; ++sI) {
            const u16* pV = Vs + (16 * sI + 4 * hi + q4) * VROW + 32 * dt + 16 * (g & 1) + 4 * p4;
            s16x4 lo = tr_read(pV);
            s16x4 hi4 = tr_read(pV + 8 * VROW);
            bf16x8 vf = bf16x8{lo[0], lo[1], lo[2], lo[3], hi4[0], hi4[1], hi4[2], hi4[3]};
            o = MFMA32(vf, pf[sI], o);
        }
#pragma unroll
        for (int rr = 0; rr < 4; ++rr) {
            u32x2 w;
            w[0] = pack2(o[4 * rr], o[4 * rr + 1]);
            w[1] = pack2(o[4 * rr + 2], o[4 * rr + 3]);
            *reinterpret_cast<u32x2*>(OA + (long)qrow * HD + h * DH + 32 * dt + 8 * rr + 4 * hi) = w;
        }
    }
}

typedef unsigned long long u64;
__device__ __forceinline__ u64 ld_gran(const u64* p) {
    return __hip_atomic_load(p, __ATOMIC_RELAXED, __HIP_MEMORY_SCOPE_AGENT);
}
__device__ __forceinline__ void st_gran(u64* p, unsigned tag, unsigned val) {
    __hip_atomic_store(p, ((u64)tag << 32) | (u64)val, __ATOMIC_RELAXED, __HIP_MEMORY_SCOPE_AGENT);
}
constexpr int MROW = 456;
constexpr int G1_PER_HEAD = 384;
constexpr int G1_STEP = 16 * G1_PER_HEAD;
constexpr int G2_STEP = 8 * 224;
constexpr unsigned SPIN_CAP = 400000u;

struct RecParams {
    const u16* mem0;
    const u16* AmatT;
    const u16* CmatT;
    const u16* Bt;
    const u16* Dt;
    u16* MEM;
    u64* G1;
    u64* G2;
};

__device__ __forceinline__ void gather_mem(const RecParams& p, int s, u16* memL, int wid, int lane, bool& dead) {
    unsigned v[7];
    if (s == 0) {
#pragma unroll
        for (int k = 0; k < 7; ++k) {
            int gi = wid * 448 + k * 64 + lane;
            v[k] = *reinterpret_cast<const unsigned*>(p.mem0 + 2 * gi);
        }
    } else {
        const u64* g = p.G2 + (long)s * G2_STEP + wid * 448 + lane;
        const unsigned tag = (unsigned)s;
        for (unsigned spin = 0;; ++spin) {
            bool ok = true;
#pragma unroll
            for (int k = 0; k < 7; ++k) {
                u64 x = ld_gran(g + k * 64);
                v[k] = (unsigned)x;
                ok &= ((unsigned)(x >> 32) == tag);
            }
            if (__all(ok) || dead) break;
            if (spin > SPIN_CAP) { dead = true; break; }
        }
    }
#pragma unroll
    for (int k = 0; k < 7; ++k) {
        int gi = wid * 448 + k * 64 + lane;
        int j = gi / 224, ip = gi - j * 224;
        *reinterpret_cast<unsigned*>(memL + j * MROW + 2 * ip) = v[k];
    }
    __syncthreads();
}

__device__ __forceinline__ void rec_A_body(const RecParams& p, u16* smem, int b) {
    u16* memL = smem;
    float* red = reinterpret_cast<float*>(smem + 16 * MROW);
    const int tid = threadIdx.x, lane = tid & 63, wid = __builtin_amdgcn_readfirstlane(tid >> 6);
    const int h = b >> 1, half = b & 1;
    const int c16 = lane & 15, g = lane >> 4;
    for (int i = tid; i < 8 * MROW / 2; i += 256) reinterpret_cast<unsigned*>(memL + 8 * MROW)[i] = 0u;
    bf16x8 W[4][14];
    const int tile0 = wid * 4;
#pragma unroll
    for (int sl = 0; sl < 4; ++sl) {
        int tile = tile0 + sl;
        bool valid = tile < 14;
        int n = half * 224 + (valid ? tile : 0) * 16 + c16;
        const u16* ap = p.AmatT + ((long)h * DM + n) * DM + 8 * g;
#pragma unroll
        for (int ks = 0; ks < 14; ++ks) W[sl][ks] = *reinterpret_cast<const bf16x8*>(ap + 32 * ks);
    }
    const u16* btp = p.Bt + ((long)h * S_ + half * 16 + c16) * DM + 8 * g;
    if (wid == 3) {
#pragma unroll
        for (int ks = 0; ks < 14; ++ks) W[2][ks] = *reinterpret_cast<const bf16x8*>(btp + 32 * ks);
    }
    bool dead = false;
    for (int s = 0; s < NSEG - 1; ++s) {
        gather_mem(p, s, memL, wid, lane, dead);
        f32x4 acc[4];
#pragma unroll
        for (int sl = 0; sl < 4; ++sl) acc[sl] = f32x4{0.f, 0.f, 0.f, 0.f};
#pragma unroll
        for (int ks = 0; ks < 14; ++ks) {
            bf16x8 mf = *reinterpret_cast<const bf16x8*>(memL + c16 * MROW + 32 * ks + 8 * g);
#pragma unroll
            for (int sl = 0; sl < 4; ++sl) acc[sl] = MFMA16(W[sl][ks], mf, acc[sl]);
        }
        u64* g1 = p.G1 + (long)(s & 1) * G1_STEP + h * G1_PER_HEAD + half * 192;
        const unsigned tag = (unsigned)(s + 1);
        if (wid == 3) {
            if (c16 < 8) {
#pragma unroll
                for (int r = 0; r < 4; ++r) st_gran(g1 + 64 + c16 * 16 + 4 * g + r, tag, __float_as_uint(acc[2][r]));
            }
            if (s + 1 < NSEG - 1) {
#pragma unroll
                for (int ks = 0; ks < 14; ++ks)
                    W[2][ks] = *reinterpret_cast<const bf16x8*>(btp + (long)(s + 1) * 32 * DM + 32 * ks);
            }
        }
        f32x4 sacc = f32x4{0.f, 0.f, 0.f, 0.f};
#pragma unroll
        for (int pr = 0; pr < 2; ++pr) {
            if (pr == 1 && wid == 3) break;
            int ta = tile0 + 2 * pr, tb = ta + 1;
            u32x2 ma = *reinterpret_cast<const u32x2*>(memL + c16 * MROW + half * 224 + 16 * ta + 4 * g);
            u32x2 mb = *reinterpret_cast<const u32x2*>(memL + c16 * MROW + half * 224 + 16 * tb + 4 * g);
            u32x4 av = u32x4{ma[0], ma[1], mb[0], mb[1]};
            u32x4 bv = u32x4{pack2(acc[2 * pr][0], acc[2 * pr][1]), pack2(acc[2 * pr][2], acc[2 * pr][3]),
                             pack2(acc[2 * pr + 1][0], acc[2 * pr + 1][1]), pack2(acc[2 * pr + 1][2], acc[2 * pr + 1][3])};
            sacc = MFMA16(__builtin_bit_cast(bf16x8, av), __builtin_bit_cast(bf16x8, bv), sacc);
        }
        if (c16 < 8 && g < 2) {
#pragma unroll
            for (int r = 0; r < 4; ++r) red[wid * 64 + c16 * 8 + 4 * g + r] = sacc[r];
        }
        __syncthreads();
        if (tid < 64) {
            float v = red[tid] + red[64 + tid] + red[128 + tid] + red[192 + tid];
            st_gran(g1 + tid, tag, __float_as_uint(v));
        }
    }
}

__device__ __forceinline__ void rec_C_body(const RecParams& p, u16* smem, int c) {
    u16* memL = smem;
    float* red = reinterpret_cast<float*>(smem + 16 * MROW);
    float* Sc = red + 512;
    u16* Pl = reinterpret_cast<u16*>(Sc + 4 * 4 * 320);
    const int tid = threadIdx.x, lane = tid & 63, wid = __builtin_amdgcn_readfirstlane(tid >> 6);
    const int c16 = lane & 15, g = lane >> 4;
    for (int i = tid; i < 8 * MROW / 2; i += 256) reinterpret_cast<unsigned*>(memL + 8 * MROW)[i] = 0u;
    const bool cvalid = c16 < 14;
    const int col = c * 14 + (cvalid ? c16 : 0);
    bf16x8 W[4][14];
#pragma unroll
    for (int hh = 0; hh < 4; ++hh) {
        int h = wid * 4 + hh;
        const u16* cp = p.CmatT + ((long)h * DM + col) * DM + 8 * g;
#pragma unroll
        for (int ks = 0; ks < 14; ++ks) {
            bf16x8 w = *reinterpret_cast<const bf16x8*>(cp + 32 * ks);
            W[hh][ks] = cvalid ? w : bf16x8{0, 0, 0, 0, 0, 0, 0, 0};
        }
    }
    u32x2 dlo[4], dhi[4];
    u32x4 dy[2];
    const int klo = (g >= 2) ? (8 * (g - 1)) : 0;
    const int khi = (g >= 2) ? (8 * (g - 1) + 4) : 4 * g;
    auto load_D = [&](int s) {
#pragma unroll
        for (int hh = 0; hh < 4; ++hh) {
            const u16* dp = p.Dt + ((long)(wid * 4 + hh) * DM + col) * S_ + s * 32;
            dlo[hh] = *reinterpret_cast<const u32x2*>(dp + klo);
            dhi[hh] = *reinterpret_cast<const u32x2*>(dp + khi);
        }
#pragma unroll
        for (int pr = 0; pr < 2; ++pr) {
            int hsel = wid * 4 + 2 * pr + (g & 1);
            const u16* dp = p.Dt + ((long)hsel * DM + col) * S_ + s * 32 + 24;
            dy[pr] = *reinterpret_cast<const u32x4*>(dp);
        }
    };
    load_D(0);
    float* Scw = Sc + wid * 4 * 320;
    u16* Plw = Pl + wid * 4 * 8 * 48;
    bool dead = false;
    for (int s = 0; s < NSEG - 1; ++s) {
        gather_mem(p, s, memL, wid, lane, dead);
        f32x4 U[4];
#pragma unroll
        for (int hh = 0; hh < 4; ++hh) U[hh] = f32x4{0.f, 0.f, 0.f, 0.f};
#pragma unroll
        for (int ks = 0; ks < 14; ++ks) {
            bf16x8 mf = *reinterpret_cast<const bf16x8*>(memL + c16 * MROW + 32 * ks + 8 * g);
#pragma unroll
            for (int hh = 0; hh < 4; ++hh) U[hh] = MFMA16(mf, W[hh][ks], U[hh]);
        }
        {
            const u64* g1 = p.G1 + (long)(s & 1) * G1_STEP + (wid * 4) * G1_PER_HEAD;
            const unsigned tag = (unsigned)(s + 1);
            const int mxo = (lane >> 5) * 192 + 64 + ((lane >> 2) & 7) * 16 + 4 * (lane & 3);
            unsigned v[4][6];
            for (unsigned spin = 0;; ++spin) {
                bool ok = true;
#pragma unroll
                for (int hh = 0; hh < 4; ++hh) {
                    const u64* gh = g1 + hh * G1_PER_HEAD;
                    u64 x0 = ld_gran(gh + lane), x1 = ld_gran(gh + 192 + lane);
                    u64 x2 = ld_gran(gh + mxo), x3 = ld_gran(gh + mxo + 1), x4 = ld_gran(gh + mxo + 2), x5 = ld_gran(gh + mxo + 3);
                    v[hh][0] = (unsigned)x0; v[hh][1] = (unsigned)x1; v[hh][2] = (unsigned)x2;
                    v[hh][3] = (unsigned)x3; v[hh][4] = (unsigned)x4; v[hh][5] = (unsigned)x5;
                    ok &= ((unsigned)(x0 >> 32) == tag) & ((unsigned)(x1 >> 32) == tag) & ((unsigned)(x2 >> 32) == tag) &
                          ((unsigned)(x3 >> 32) == tag) & ((unsigned)(x4 >> 32) == tag) & ((unsigned)(x5 >> 32) == tag);
                }
                if (__all(ok) || dead) break;
                if (spin > SPIN_CAP) { dead = true; break; }
            }
#pragma unroll
            for (int hh = 0; hh < 4; ++hh) {
                float* sh = Scw + hh * 320;
                sh[(lane >> 3) * 40 + (lane & 7)] = __uint_as_float(v[hh][0]) + __uint_as_float(v[hh][1]);
                float* sx = sh + ((lane >> 2) & 7) * 40 + 8 + (lane >> 5) * 16 + 4 * (lane & 3);
                sx[0] = __uint_as_float(v[hh][2]); sx[1] = __uint_as_float(v[hh][3]);
                sx[2] = __uint_as_float(v[hh][4]); sx[3] = __uint_as_float(v[hh][5]);
            }
        }
        __builtin_amdgcn_wave_barrier();
        {
            const int row = lane >> 1, part = lane & 1;
            const float* sr = Scw + row * 40 + part * 20;
            float x[20];
            float mx = -INFINITY;
#pragma unroll
            for (int k = 0; k < 20; ++k) { x[k] = sr[k]; mx = fmaxf(mx, x[k]); }
            mx = fmaxf(mx, __shfl_xor(mx, 1));
            float sum = 0.f;
#pragma unroll
            for (int k = 0; k < 20; ++k) { x[k] = __expf(x[k] - mx); sum += x[k]; }
            sum += __shfl_xor(sum, 1);
            float inv = 1.f / sum;
            u16* pr = Plw + row * 48 + part * 20;
#pragma unroll
            for (int k = 0; k < 20; k += 2)
                *reinterpret_cast<unsigned*>(pr + k) = pack2(x[k] * inv, x[k + 1] * inv);
        }
        __builtin_amdgcn_wave_barrier();
        f32x4 oacc = f32x4{0.f, 0.f, 0.f, 0.f};
        const int irow = c16 & 7;
#pragma unroll
        for (int hh = 0; hh < 4; ++hh) {
            const u16* pp = Plw + (hh * 8 + irow) * 48;
            int alo = (g == 0) ? 0 : (g == 1) ? 4 : (g == 2) ? 16 : 24;
            int ahi = (g == 0) ? 8 : (g == 1) ? 12 : (g == 2) ? 20 : 28;
            u32x2 a0 = *reinterpret_cast<const u32x2*>(pp + alo);
            u32x2 a1 = *reinterpret_cast<const u32x2*>(pp + ahi);
            u32x4 av = u32x4{a0[0], a0[1], a1[0], a1[1]};
            u32x2 blo;
            if (g < 2) { blo[0] = pack2(U[hh][0], U[hh][1]); blo[1] = pack2(U[hh][2], U[hh][3]); }
            else blo = dlo[hh];
            u32x4 bv = u32x4{blo[0], blo[1], dhi[hh][0], dhi[hh][1]};
            if (!cvalid) bv = u32x4{0, 0, 0, 0};
            oacc = MFMA16(__builtin_bit_cast(bf16x8, av), __builtin_bit_cast(bf16x8, bv), oacc);
        }
#pragma unroll
        for (int pr = 0; pr < 2; ++pr) {
            const u16* pp = Plw + ((2 * pr + (g & 1)) * 8 + irow) * 48 + 32;
            u32x4 av = *reinterpret_cast<const u32x4*>(pp);
            u32x4 bv = dy[pr];
            if (g >= 2) av = u32x4{0, 0, 0, 0};
            if (g >= 2 || !cvalid) bv = u32x4{0, 0, 0, 0};
            oacc = MFMA16(__builtin_bit_cast(bf16x8, av), __builtin_bit_cast(bf16x8, bv), oacc);
        }
        if (s + 1 < NSEG - 1) load_D(s + 1);
        if (g < 2) {
#pragma unroll
            for (int r = 0; r < 4; ++r) red[wid * 128 + (4 * g + r) * 16 + c16] = oacc[r];
        }
        __syncthreads();
        if (tid < 56) {
            int i = tid / 7, pp = tid - i * 7;
            float v0 = 0.f, v1 = 0.f;
#pragma unroll
            for (int w = 0; w < 4; ++w) {
                v0 += red[w * 128 + i * 16 + 2 * pp];
                v1 += red[w * 128 + i * 16 + 2 * pp + 1];
            }
            unsigned pk = pack2(v0, v1);
            st_gran(p.G2 + (long)(s + 1) * G2_STEP + i * 224 + 7 * c + pp, (unsigned)(s + 1), pk);
            *reinterpret_cast<unsigned*>(p.MEM + (long)(s + 1) * 8 * DM + i * DM + 14 * c + 2 * pp) = pk;
        }
    }
}

constexpr int REC_LDS = 16 * MROW * 2 + 512 * 4 + 4 * 4 * 320 * 4 + 4 * 4 * 8 * 48 * 2;
constexpr int FUSED_LDS = (ATTN_LDS > REC_LDS) ? ATTN_LDS : REC_LDS;

__global__ __launch_bounds__(256) void rec_attn_kernel(RecParams p, const u16* __restrict__ Q, const u16* __restrict__ K,
                                                       const u16* __restrict__ V, u16* __restrict__ O) {
    __shared__ __attribute__((aligned(16))) u16 smem[FUSED_LDS / 2];
    const int b = blockIdx.x;
    if (b < 32) {
        rec_A_body(p, smem, b);
    } else if (b < 64) {
        rec_C_body(p, smem, b - 32);
    } else {
        int a = b - 64;
        int qb = 15 - (a >> 4), h = a & 15;
        attnB_body(Q, K, V, O, smem, qb, h);
    }
}

template <int BN, bool NT, bool TO = false>
static void launch_gemm(hipStream_t st, const u16* A, const u16* B, u16* C, int lda, int ldb, int ldc, long sA,
                        long sB, long sC, int M, int N, int K, int batch, float scale) {
    dim3 grid(N / BN, (M + 127) / 128, batch);
    hipLaunchKernelGGL((gemm_kernel<BN, NT, TO>), grid, dim3(256), 0, st, A, B, C, lda, ldb, ldc, sA, sB, sC, M, N, K, scale);
}

extern "C" void kernel_launch(void* const* d_in, const int* in_sizes, int n_in, void* d_out, int out_size,
                              void* d_ws, size_t ws_size, hipStream_t stream) {
    const u16* A_x = (const u16*)d_in[0];
    const u16* A_mem0 = (const u16*)d_in[1];
    const u16* A_Wq = (const u16*)d_in[2];
    const u16* A_Wk = (const u16*)d_in[3];
    const u16* A_Wv = (const u16*)d_in[4];
    const u16* A_Wo = (const u16*)d_in[5];
    const u16* B_x = (const u16*)d_in[7];
    const u16* B_ckv = (const u16*)d_in[8];
    const u16* B_wq = (const u16*)d_in[9];
    const u16* B_wuk = (const u16*)d_in[10];
    const u16* B_wuv = (const u16*)d_in[11];
    const u16* B_wo = (const u16*)d_in[12];
    u16* out = (u16*)d_out;

    char* ws = (char*)d_ws;
    const size_t SZ = (size_t)S_ * HD * 2;
    u16* XQ = (u16*)(ws + 0 * SZ);
    u16* XK = (u16*)(ws + 1 * SZ);
    u16* XV = (u16*)(ws + 2 * SZ);
    u16* Qb = (u16*)(ws + 3 * SZ);
    u16* Kb = (u16*)(ws + 4 * SZ);
    u16* Vb = (u16*)(ws + 5 * SZ);
    u16* OA = (u16*)(ws + 6 * SZ);
    u16* OB = (u16*)(ws + 7 * SZ);
    size_t off = 8 * SZ;
    u16* AmatT = (u16*)(ws + off); off += (size_t)H_ * DM * DM * 2;
    u16* CmatT = (u16*)(ws + off); off += (size_t)H_ * DM * DM * 2;
    u16* Bt = (u16*)(ws + off); off += (size_t)H_ * DM * S_ * 2;
    u16* Dt = (u16*)(ws + off); off += (size_t)H_ * DM * S_ * 2;
    u16* MEM = (u16*)(ws + off); off += (size_t)65 * 8 * DM * 2; off = (off + 255) & ~(size_t)255;
    u16* KM = (u16*)(ws + off); off += (size_t)512 * HD * 2;
    u16* VM = (u16*)(ws + off); off += (size_t)512 * HD * 2;
    char* gbase = ws + off;
    u64* G1 = (u64*)gbase;
    u64* G2 = G1 + 2 * G1_STEP;
    size_t gbytes = (size_t)(2 * G1_STEP + 64 * G2_STEP) * 8;
    off += gbytes;

    hipMemsetAsync(gbase, 0, gbytes, stream);
    launch_gemm<128, false>(stream, A_x, A_Wq, XQ, DM, HD, HD, 0, 0, 0, S_, HD, DM, 1, 1.f);
    launch_gemm<128, false>(stream, A_x, A_Wk, XK, DM, HD, HD, 0, 0, 0, S_, HD, DM, 1, 1.f);
    launch_gemm<128, false>(stream, A_x, A_Wv, XV, DM, HD, HD, 0, 0, 0, S_, HD, DM, 1, 1.f);
    launch_gemm<64, true>(stream, A_Wk, A_Wq, AmatT, HD, HD, DM, DH, DH, (long)DM * DM, DM, DM, DH, H_, 0.0625f);
    launch_gemm<64, false, true>(stream, A_Wv, A_Wo, CmatT, HD, DM, DM, DH, (long)DH * DM, (long)DM * DM, DM, DM, DH, H_, 1.f);
    launch_gemm<64, true>(stream, XK, A_Wq, Bt, HD, HD, DM, DH, DH, (long)DM * S_, S_, DM, DH, H_, 0.0625f);
    launch_gemm<64, false, true>(stream, XV, A_Wo, Dt, HD, DM, S_, DH, (long)DH * DM, (long)S_ * DM, S_, DM, DH, H_, 1.f);
    launch_gemm<128, false>(stream, B_x, B_wq, Qb, DM, HD, HD, 0, 0, 0, S_, HD, DM, 1, 1.f);
    launch_gemm<128, false>(stream, B_ckv, B_wuk, Kb, DC, HD, HD, 0, 0, 0, S_, HD, DC, 1, 1.f);
    launch_gemm<128, false>(stream, B_ckv, B_wuv, Vb, DC, HD, HD, 0, 0, 0, S_, HD, DC, 1, 1.f);
    hipMemcpyAsync(MEM, A_mem0, 8 * DM * 2, hipMemcpyDeviceToDevice, stream);
    RecParams rp;
    rp.mem0 = A_mem0; rp.AmatT = AmatT; rp.CmatT = CmatT; rp.Bt = Bt; rp.Dt = Dt; rp.MEM = MEM; rp.G1 = G1; rp.G2 = G2;
    hipLaunchKernelGGL(rec_attn_kernel, dim3(64 + 256), dim3(256), 0, stream, rp, Qb, Kb, Vb, OB);
    launch_gemm<128, false>(stream, MEM, A_Wk, KM, DM, HD, HD, 0, 0, 0, 512, HD, DM, 1, 1.f);
    launch_gemm<128, false>(stream, MEM, A_Wv, VM, DM, HD, HD, 0, 0, 0, 512, HD, DM, 1, 1.f);
    hipLaunchKernelGGL(attnA_kernel, dim3(64, 16), dim3(64), 0, stream, XQ, XK, XV, KM, VM, OA);
    launch_gemm<64, false>(stream, OA, A_Wo, out, HD, DM, 896, 0, 0, 0, S_, DM, HD, 1, 1.f);
    launch_gemm<64, false>(stream, OB, B_wo, out + 448, HD, DM, 896, 0, 0, 0, S_, DM, HD, 1, 1.f);
}
```

```cpp
#include <hip/hip_runtime.h>
#include <hip/hip_bf16.h>
#include <stdint.h>

typedef unsigned short u16;
typedef __attribute__((ext_vector_type(8))) short bf16x8;
typedef __attribute__((ext_vector_type(4))) short s16x4;
typedef __attribute__((ext_vector_type(16))) float f32x16;
typedef __attribute__((ext_vector_type(4))) float f32x4;
typedef __attribute__((ext_vector_type(4))) unsigned u32x4;
typedef __attribute__((ext_vector_type(2))) unsigned u32x2;
typedef short v4i16_t __attribute__((ext_vector_type(4)));

#define LAS __attribute__((address_space(3)))

__device__ __forceinline__ float bf2f(u16 x) { return __uint_as_float(((unsigned)x) << 16); }
__device__ __forceinline__ u16 f2bf(float f) {
    unsigned u = __float_as_uint(f);
    u += 0x7fffu + ((u >> 16) & 1u);
    return (u16)(u >> 16);
}
typedef __bf16 bf16x2_t __attribute__((ext_vector_type(2)));
__device__ __forceinline__ unsigned pack2(float lo, float hi) {
    bf16x2_t r = {(__bf16)lo, (__bf16)hi};
    return __builtin_bit_cast(unsigned, r);
}
__device__ __forceinline__ s16x4 tr_read(const void* p) {
    return __builtin_bit_cast(s16x4, __builtin_amdgcn_ds_read_tr16_b64_v4i16((LAS v4i16_t*)p));
}
#define MFMA32(a, b, c) __builtin_amdgcn_mfma_f32_32x32x16_bf16(a, b, c, 0, 0, 0)
#define MFMA16(a, b, c) __builtin_amdgcn_mfma_f32_16x16x32_bf16(a, b, c, 0, 0, 0)

__device__ __forceinline__ int crow(int r, int hi) { return (r & 3) + 8 * (r >> 2) + 4 * hi; }

constexpr int S_ = 2048, DM = 448, H_ = 16, DH = 256, HD = 4096, MM = 8, SEG = 32, NSEG = 64, DC = 1024;

template <int BN, bool NT, bool TO>
__global__ __launch_bounds__(256) void gemm_kernel(const u16* __restrict__ A, const u16* __restrict__ B,
                                                   u16* __restrict__ C, int lda, int ldb, int ldc,
                                                   long sA, long sB, long sC, int M, int N, int K,
                                                   float scale) {
    constexpr int BM = 128, BK = 64;
    constexpr int AROW = 72;
    constexpr int BROW = NT ? 72 : (BN + 32);
    constexpr int BLDS = NT ? BN * 72 : 64 * (BN + 32);
    __shared__ __attribute__((aligned(16))) u16 smem[BM * AROW + BLDS];
    u16* As = smem;
    u16* Bs = smem + BM * AROW;
    const int tid = threadIdx.x, lane = tid & 63, wid = tid >> 6;
    const int wm = wid >> 1, wn = wid & 1;
    const int m0 = blockIdx.y * BM, n0 = blockIdx.x * BN;
    A += (long)blockIdx.z * sA;
    B += (long)blockIdx.z * sB;
    C += (long)blockIdx.z * sC;
    constexpr int NTI = BN / 64;
    f32x16 acc[2][NTI];
#pragma unroll
    for (int i = 0; i < 2; ++i)
#pragma unroll
        for (int j = 0; j < NTI; ++j) acc[i][j] = f32x16{};

    constexpr int BCH = BN / 32;
    u32x4 ra[4], rb[BCH];
    auto load_tiles = [&](int k0) {
#pragma unroll
        for (int i = 0; i < 4; ++i) {
            int c = tid + 256 * i, row = c >> 3, kc = c & 7;
            int gr = m0 + row;
            if (gr < M)
                ra[i] = *reinterpret_cast<const u32x4*>(A + (long)gr * lda + k0 + kc * 8);
            else
                ra[i] = u32x4{0, 0, 0, 0};
        }
#pragma unroll
        for (int i = 0; i < BCH; ++i) {
            int c = tid + 256 * i;
            if (NT) {
                int row = c >> 3, kc = c & 7;
                rb[i] = *reinterpret_cast<const u32x4*>(B + (long)(n0 + row) * ldb + k0 + kc * 8);
            } else {
                int krow = c / (BN / 8), nc = c % (BN / 8);
                rb[i] = *reinterpret_cast<const u32x4*>(B + (long)(k0 + krow) * ldb + n0 + nc * 8);
            }
        }
    };
    auto store_tiles = [&]() {
#pragma unroll
        for (int i = 0; i < 4; ++i) {
            int c = tid + 256 * i, row = c >> 3, kc = c & 7;
            *reinterpret_cast<u32x4*>(As + row * AROW + kc * 8) = ra[i];
        }
#pragma unroll
        for (int i = 0; i < BCH; ++i) {
            int c = tid + 256 * i;
            if (NT) {
                int row = c >> 3, kc = c & 7;
                *reinterpret_cast<u32x4*>(Bs + row * BROW + kc * 8) = rb[i];
            } else {
                int krow = c / (BN / 8), nc = c % (BN / 8);
                *reinterpret_cast<u32x4*>(Bs + krow * BROW + nc * 8) = rb[i];
            }
        }
    };
    const int r32 = lane & 31, hi = lane >> 5, g = lane >> 4, q4 = (lane & 15) >> 2, p4 = lane & 3;
    load_tiles(0);
    const int nk = K / BK;
    for (int kt = 0; kt < nk; ++kt) {
        __syncthreads();
        store_tiles();
        __syncthreads();
        if (kt + 1 < nk) load_tiles((kt + 1) * BK);
#pragma unroll
        for (int ks = 0; ks < 4; ++ks) {
            bf16x8 af[2], bfr[NTI];
#pragma unroll
            for (int i = 0; i < 2; ++i)
                af[i] = *reinterpret_cast<const bf16x8*>(As + (wm * 64 + i * 32 + r32) * AROW + ks * 16 + hi * 8);
#pragma unroll
            for (int j = 0; j < NTI; ++j) {
                int nb = wn * (BN / 2) + j * 32;
                if (NT) {
                    bfr[j] = *reinterpret_cast<const bf16x8*>(Bs + (nb + r32) * BROW + ks * 16 + hi * 8);
                } else {
                    const u16* pB = Bs + (ks * 16 + 8 * (g >> 1) + q4) * BROW + nb + 16 * (g & 1) + 4 * p4;
                    s16x4 lo = tr_read(pB);
                    s16x4 hi4 = tr_read(pB + 4 * BROW);
                    bfr[j] = bf16x8{lo[0], lo[1], lo[2], lo[3], hi4[0], hi4[1], hi4[2], hi4[3]};
                }
            }
#pragma unroll
            for (int i = 0; i < 2; ++i)
#pragma unroll
                for (int j = 0; j < NTI; ++j) acc[i][j] = MFMA32(af[i], bfr[j], acc[i][j]);
        }
    }
#pragma unroll
    for (int i = 0; i < 2; ++i)
#pragma unroll
        for (int j = 0; j < NTI; ++j) {
            int col = n0 + wn * (BN / 2) + j * 32 + r32;
            if (TO) {
#pragma unroll
                for (int rr = 0; rr < 4; ++rr) {
                    int row = m0 + wm * 64 + i * 32 + 8 * rr + 4 * hi;
                    u32x2 w;
                    w[0] = pack2(acc[i][j][4 * rr] * scale, acc[i][j][4 * rr + 1] * scale);
                    w[1] = pack2(acc[i][j][4 * rr + 2] * scale, acc[i][j][4 * rr + 3] * scale);
                    if (row < M) *reinterpret_cast<u32x2*>(C + (long)col * ldc + row) = w;
                }
            } else {
#pragma unroll
                for (int r = 0; r < 16; ++r) {
                    int row = m0 + wm * 64 + i * 32 + crow(r, hi);
                    if (row < M) C[(long)row * ldc + col] = f2bf(acc[i][j][r] * scale);
                }
            }
        }
}

constexpr int KROW = 264;
constexpr int VROW = 288;
constexpr int ATTN_LDS = (64 * KROW + 64 * VROW) * 2;
__device__ __forceinline__ void attnB_body(const u16* __restrict__ Q, const u16* __restrict__ K,
                                           const u16* __restrict__ V, u16* __restrict__ O, u16* smem, int qb, int h) {
    u16* Ks = smem;
    u16* Vs = smem + 64 * KROW;
    const int tid = threadIdx.x, lane = tid & 63, wid = tid >> 6;
    const int q0 = qb * 128;
    const int r32 = lane & 31, hi = lane >> 5, g = lane >> 4, q4 = (lane & 15) >> 2, p4 = lane & 3;
    const int qrow = q0 + wid * 32 + r32;
    bf16x8 qf[16];
#pragma unroll
    for (int ks = 0; ks < 16; ++ks)
        qf[ks] = *reinterpret_cast<const bf16x8*>(Q + (long)qrow * HD + h * DH + ks * 16 + hi * 8);
    f32x16 o[8];
#pragma unroll
    for (int i = 0; i < 8; ++i) o[i] = f32x16{};
    float mrun = -INFINITY, lrun = 0.f;
    const float c2 = 0.0625f * 1.4426950408889634f;
    const int nt = (q0 + 128) / 64;
    u32x4 rk[8], rv[8];
    auto load_kv = [&](int t) {
#pragma unroll
        for (int i = 0; i < 8; ++i) {
            int c = tid + 256 * i, row = c >> 5, cc = c & 31;
            long off = (long)(t * 64 + row) * HD + h * DH + cc * 8;
            rk[i] = *reinterpret_cast<const u32x4*>(K + off);
            rv[i] = *reinterpret_cast<const u32x4*>(V + off);
        }
    };
    auto store_kv = [&]() {
#pragma unroll
        for (int i = 0; i < 8; ++i) {
            int c = tid + 256 * i, row = c >> 5, cc = c & 31;
            *reinterpret_cast<u32x4*>(Ks + row * KROW + cc * 8) = rk[i];
            *reinterpret_cast<u32x4*>(Vs + row * VROW + cc * 8) = rv[i];
        }
    };
    load_kv(0);
    for (int t = 0; t < nt; ++t) {
        __syncthreads();
        store_kv();
        __syncthreads();
        if (t + 1 < nt) load_kv(t + 1);
        const int kbase = t * 64;
        if (kbase <= q0 + wid * 32 + 31) {
            f32x16 s0 = f32x16{}, s1 = f32x16{};
#pragma unroll
            for (int ks = 0; ks < 16; ++ks) {
                bf16x8 k0 = *reinterpret_cast<const bf16x8*>(Ks + r32 * KROW + ks * 16 + hi * 8);
                bf16x8 k1 = *reinterpret_cast<const bf16x8*>(Ks + (32 + r32) * KROW + ks * 16 + hi * 8);
                s0 = MFMA32(k0, qf[ks], s0);
                s1 = MFMA32(k1, qf[ks], s1);
                if ((ks & 3) == 3) __builtin_amdgcn_sched_barrier(0);
            }
            if (kbase + 63 > q0 + wid * 32) {
#pragma unroll
                for (int r = 0; r < 16; ++r) {
                    int kk = kbase + crow(r, hi);
                    if (kk > qrow) s0[r] = -INFINITY;
                    if (kk + 32 > qrow) s1[r] = -INFINITY;
                }
            }
            float mx = -INFINITY;
#pragma unroll
            for (int r = 0; r < 16; ++r) mx = fmaxf(mx, fmaxf(s0[r], s1[r]));
            mx = fmaxf(mx, __shfl_xor(mx, 32));
            float mnew = fmaxf(mrun, mx);
            float alpha = __builtin_amdgcn_exp2f((mrun - mnew) * c2);
            float nm = -mnew * c2;
            float psum = 0.f;
#pragma unroll
            for (int r = 0; r < 16; ++r) {
                s0[r] = __builtin_amdgcn_exp2f(fmaf(s0[r], c2, nm));
                s1[r] = __builtin_amdgcn_exp2f(fmaf(s1[r], c2, nm));
                psum += s0[r] + s1[r];
            }
            psum += __shfl_xor(psum, 32);
            lrun = lrun * alpha + psum;
            mrun = mnew;
#pragma unroll
            for (int i = 0; i < 8; ++i)
#pragma unroll
                for (int r = 0; r < 16; ++r) o[i][r] *= alpha;
            bf16x8 pf[4];
#pragma unroll
            for (int s = 0; s < 2; ++s) {
                u32x4 a, b;
#pragma unroll
                for (int j = 0; j < 4; ++j) {
                    a[j] = pack2(s0[8 * s + 2 * j], s0[8 * s + 2 * j + 1]);
                    b[j] = pack2(s1[8 * s + 2 * j], s1[8 * s + 2 * j + 1]);
                }
                pf[s] = __builtin_bit_cast(bf16x8, a);
                pf[2 + s] = __builtin_bit_cast(bf16x8, b);
            }
#pragma unroll
            for (int dt = 0; dt < 8; ++dt) {
#pragma unroll
                for (int s = 0; s < 4; ++s) {
                    const u16* pV = Vs + (16 * s + 4 * hi + q4) * VROW + 32 * dt + 16 * (g & 1) + 4 * p4;
                    s16x4 lo = tr_read(pV);
                    s16x4 hi4 = tr_read(pV + 8 * VROW);
                    bf16x8 vf = bf16x8{lo[0], lo[1], lo[2], lo[3], hi4[0], hi4[1], hi4[2], hi4[3]};
                    o[dt] = MFMA32(vf, pf[s], o[dt]);
                }
                __builtin_amdgcn_sched_barrier(0);
            }
        }
    }
    float inv = 1.f / lrun;
#pragma unroll
    for (int dt = 0; dt < 8; ++dt)
#pragma unroll
        for (int rr = 0; rr < 4; ++rr) {
            u32x2 w;
            w[0] = pack2(o[dt][4 * rr] * inv, o[dt][4 * rr + 1] * inv);
            w[1] = pack2(o[dt][4 * rr + 2] * inv, o[dt][4 * rr + 3] * inv);
            *reinterpret_cast<u32x2*>(O + (long)qrow * HD + h * DH + 32 * dt + 8 * rr + 4 * hi) = w;
        }
}

__global__ __launch_bounds__(64) void attnA_kernel(const u16* __restrict__ XQ, const u16* __restrict__ XK,
                                                   const u16* __restrict__ XV, const u16* __restrict__ KM,
                                                   const u16* __restrict__ VM, u16* __restrict__ OA) {
    __shared__ __attribute__((aligned(16))) u16 Vs[48 * VROW];
    const int lane = threadIdx.x;
    const int s = blockIdx.x, h = blockIdx.y;
    const int r32 = lane & 31, hi = lane >> 5, g = lane >> 4, q4 = (lane & 15) >> 2, p4 = lane & 3;
    for (int c = lane; c < 48 * 32; c += 64) {
        int row = c >> 5, cc = c & 31;
        u32x4 v = u32x4{0, 0, 0, 0};
        if (row < 8)
            v = *reinterpret_cast<const u32x4*>(VM + (long)(s * 8 + row) * HD + h * DH + cc * 8);
        else if (row < 40)
            v = *reinterpret_cast<const u32x4*>(XV + (long)(s * 32 + row - 8) * HD + h * DH + cc * 8);
        *reinterpret_cast<u32x4*>(Vs + row * VROW + cc * 8) = v;
    }
    const int qrow = s * 32 + r32;
    const u16* k0p = (r32 < 8) ? (KM + (long)(s * 8 + r32) * HD + h * DH) : (XK + (long)(s * 32 + r32 - 8) * HD + h * DH);
    const int k1i = (r32 < 8) ? (24 + r32) : 0;
    const u16* k1p = XK + (long)(s * 32 + k1i) * HD + h * DH;
    f32x16 s0 = f32x16{}, s1 = f32x16{};
#pragma unroll
    for (int ks = 0; ks < 16; ++ks) {
        bf16x8 qf = *reinterpret_cast<const bf16x8*>(XQ + (long)qrow * HD + h * DH + ks * 16 + hi * 8);
        bf16x8 ka = *reinterpret_cast<const bf16x8*>(k0p + ks * 16 + hi * 8);
        bf16x8 kb = *reinterpret_cast<const bf16x8*>(k1p + ks * 16 + hi * 8);
        s0 = MFMA32(ka, qf, s0);
        s1 = MFMA32(kb, qf, s1);
    }
#pragma unroll
    for (int r = 0; r < 16; ++r) {
        int kk = crow(r, hi);
        if (kk >= 8 && kk - 8 > r32) s0[r] = -INFINITY;
        int k2 = kk + 32;
        if (k2 >= 40 || k2 - 8 > r32) s1[r] = -INFINITY;
    }
    const float c2 = 0.0625f * 1.4426950408889634f;
    float mx = -INFINITY;
#pragma unroll
    for (int r = 0; r < 16; ++r) mx = fmaxf(mx, fmaxf(s0[r], s1[r]));
    mx = fmaxf(mx, __shfl_xor(mx, 32));
    float nm = -mx * c2, psum = 0.f;
#pragma unroll
    for (int r = 0; r < 16; ++r) {
        s0[r] = __builtin_amdgcn_exp2f(fmaf(s0[r], c2, nm));
        s1[r] = __builtin_amdgcn_exp2f(fmaf(s1[r], c2, nm));
        psum += s0[r] + s1[r];
    }
    psum += __shfl_xor(psum, 32);
    float inv = 1.f / psum;
    bf16x8 pf[3];
#pragma unroll
    for (int sI = 0; sI < 2; ++sI) {
        u32x4 a;
#pragma unroll
        for (int j = 0; j < 4; ++j) a[j] = pack2(s0[8 * sI + 2 * j] * inv, s0[8 * sI + 2 * j + 1] * inv);
        pf[sI] = __builtin_bit_cast(bf16x8, a);
    }
    {
        u32x4 a;
#pragma unroll
        for (int j = 0; j < 4; ++j) a[j] = pack2(s1[2 * j] * inv, s1[2 * j + 1] * inv);
        pf[2] = __builtin_bit_cast(bf16x8, a);
    }
    __syncthreads();
#pragma unroll
    for (int dt = 0; dt < 8; ++dt) {
        f32x16 o = f32x16{};
#pragma unroll
        for (int sI = 0; sI < 3; ++sI) {
            const u16* pV = Vs + (16 * sI + 4 * hi + q4) * VROW + 32 * dt + 16 * (g & 1) + 4 * p4;
            s16x4 lo = tr_read(pV);
            s16x4 hi4 = tr_read(pV + 8 * VROW);
            bf16x8 vf = bf16x8{lo[0], lo[1], lo[2], lo[3], hi4[0], hi4[1], hi4[2], hi4[3]};
            o = MFMA32(vf, pf[sI], o);
        }
#pragma unroll
        for (int rr = 0; rr < 4; ++rr) {
            u32x2 w;
            w[0] = pack2(o[4 * rr], o[4 * rr + 1]);
            w[1] = pack2(o[4 * rr + 2], o[4 * rr + 3]);
            *reinterpret_cast<u32x2*>(OA + (long)qrow * HD + h * DH + 32 * dt + 8 * rr + 4 * hi) = w;
        }
    }
}

typedef unsigned long long u64;
__device__ __forceinline__ u64 ld_gran(const u64* p) {
    return __hip_atomic_load(p, __ATOMIC_RELAXED, __HIP_MEMORY_SCOPE_AGENT);
}
__device__ __forceinline__ void st_gran(u64* p, unsigned tag, unsigned val) {
    __hip_atomic_store(p, ((u64)tag << 32) | (u64)val, __ATOMIC_RELAXED, __HIP_MEMORY_SCOPE_AGENT);
}
constexpr int NA_BLK = 16, NC_BLK = 28, NREC = NA_BLK + NC_BLK;
constexpr int MROW = 456;
constexpr int G1_STEP = 16 * 160;
constexpr int G2_STEP = 8 * 224;
constexpr unsigned SPIN_CAP = 400000u;

struct RecParams {
    const u16* mem0;
    const u16* AmatT;
    const u16* CmatT;
    const u16* Bt;
    const u16* Dt;
    u16* MEM;
    u64* G1;
    u64* G2;
};

__device__ __forceinline__ float red32_max(float x) {
    float t;
    asm volatile(
        "s_nop 1\n\tv_max_f32_dpp %0, %0, %0 quad_perm:[1,0,3,2] row_mask:0xf bank_mask:0xf\n\t"
        "s_nop 1\n\tv_max_f32_dpp %0, %0, %0 quad_perm:[2,3,0,1] row_mask:0xf bank_mask:0xf\n\t"
        "s_nop 1\n\tv_max_f32_dpp %0, %0, %0 row_half_mirror row_mask:0xf bank_mask:0xf\n\t"
        "s_nop 1\n\tv_max_f32_dpp %0, %0, %0 row_mirror row_mask:0xf bank_mask:0xf\n\t"
        "v_mov_b32 %1, %0\n\t"
        "s_nop 1\n\tv_permlane16_swap_b32 %0, %1\n\t"
        "s_nop 1\n\tv_max_f32 %0, %0, %1"
        : "+v"(x), "=&v"(t));
    return x;
}
__device__ __forceinline__ float red32_sum(float x) {
    float t;
    asm volatile(
        "s_nop 1\n\tv_add_f32_dpp %0, %0, %0 quad_perm:[1,0,3,2] row_mask:0xf bank_mask:0xf\n\t"
        "s_nop 1\n\tv_add_f32_dpp %0, %0, %0 quad_perm:[2,3,0,1] row_mask:0xf bank_mask:0xf\n\t"
        "s_nop 1\n\tv_add_f32_dpp %0, %0, %0 row_half_mirror row_mask:0xf bank_mask:0xf\n\t"
        "s_nop 1\n\tv_add_f32_dpp %0, %0, %0 row_mirror row_mask:0xf bank_mask:0xf\n\t"
        "v_mov_b32 %1, %0\n\t"
        "s_nop 1\n\tv_permlane16_swap_b32 %0, %1\n\t"
        "s_nop 1\n\tv_add_f32 %0, %0, %1"
        : "+v"(x), "=&v"(t));
    return x;
}

template <int N>
__device__ __forceinline__ void sweep(const u64* const (&g)[N], unsigned tag, unsigned (&v)[N], bool& dead) {
    u64 a[N], b[N];
#pragma unroll
    for (int k = 0; k < N; ++k) a[k] = ld_gran(g[k]);
    for (unsigned spin = 0;; ++spin) {
#pragma unroll
        for (int k = 0; k < N; ++k) b[k] = ld_gran(g[k]);
        bool ok = true;
#pragma unroll
        for (int k = 0; k < N; ++k) ok &= ((unsigned)(a[k] >> 32) == tag);
        if (__all(ok) || dead) {
#pragma unroll
            for (int k = 0; k < N; ++k) v[k] = (unsigned)a[k];
            break;
        }
#pragma unroll
        for (int k = 0; k < N; ++k) a[k] = ld_gran(g[k]);
        ok = true;
#pragma unroll
        for (int k = 0; k < N; ++k) ok &= ((unsigned)(b[k] >> 32) == tag);
        if (__all(ok)) {
#pragma unroll
            for (int k = 0; k < N; ++k) v[k] = (unsigned)b[k];
            break;
        }
        if (spin > SPIN_CAP) dead = true;
    }
}

__device__ __forceinline__ void gather_mem(const RecParams& p, int s, u16* memL, const int (&loff)[7], int wid,
                                           int lane, bool& dead) {
    unsigned v[7];
    if (s == 0) {
#pragma unroll
        for (int k = 0; k < 7; ++k) {
            int gi = wid * 448 + k * 64 + lane;
            v[k] = *reinterpret_cast<const unsigned*>(p.mem0 + 2 * gi);
        }
    } else {
        const u64* g0 = p.G2 + (long)s * G2_STEP + wid * 448 + lane;
        const u64* const g[7] = {g0, g0 + 64, g0 + 128, g0 + 192, g0 + 256, g0 + 320, g0 + 384};
        sweep<7>(g, (unsigned)s, v, dead);
    }
#pragma unroll
    for (int k = 0; k < 7; ++k) *reinterpret_cast<unsigned*>(memL + loff[k]) = v[k];
    __syncthreads();
}

__device__ __forceinline__ void rec_A_body(const RecParams& p, u16* smem, int h) {
    u16* memL = smem;
    float* Sc = reinterpret_cast<float*>(smem + 16 * MROW);
    const int tid = threadIdx.x, lane = tid & 63, wid = __builtin_amdgcn_readfirstlane(tid >> 6);
    const int c16 = lane & 15, g = lane >> 4;
    for (int i = tid; i < 8 * MROW / 2; i += 256) reinterpret_cast<unsigned*>(memL + 8 * MROW)[i] = 0u;
    int loff[7];
#pragma unroll
    for (int k = 0; k < 7; ++k) {
        int gi = wid * 448 + k * 64 + lane;
        int j = gi / 224, ip = gi - j * 224;
        loff[k] = j * MROW + 2 * ip;
    }
    bf16x8 W[6][14];
    u16* WL = reinterpret_cast<u16*>(Sc + 4 * 320) + (wid * 14 * 64 + lane) * 8;
#pragma unroll
    for (int t = 0; t < 7; ++t) {
        const u16* ap = p.AmatT + ((long)h * DM + (7 * wid + t) * 16 + c16) * DM + 8 * g;
#pragma unroll
        for (int ks = 0; ks < 14; ++ks) {
            bf16x8 w = *reinterpret_cast<const bf16x8*>(ap + 32 * ks);
            if (t < 6)
                W[t][ks] = w;
            else
                *reinterpret_cast<bf16x8*>(WL + ks * 512) = w;
        }
    }
    bf16x8 X[2][4];
    int ksq[4];
#pragma unroll
    for (int q = 0; q < 4; ++q) ksq[q] = (wid + 4 * q < 14) ? (wid + 4 * q) : 0;
    const bool q3ok = (wid + 12) < 14;
    const u16* btp = p.Bt + ((long)h * S_ + c16) * DM + 8 * g;
    auto load_X = [&](int s) {
#pragma unroll
        for (int tl = 0; tl < 2; ++tl)
#pragma unroll
            for (int q = 0; q < 4; ++q) {
                bf16x8 w = *reinterpret_cast<const bf16x8*>(btp + (long)(s * 32 + tl * 16) * DM + 32 * ksq[q]);
                if (q == 3 && !q3ok) w = bf16x8{0, 0, 0, 0, 0, 0, 0, 0};
                X[tl][q] = w;
            }
    };
    load_X(0);
    const int srow = 2 * wid + (lane >> 5), sub = lane & 31;
    const bool sact = sub < 20;
    const float* sread = Sc + srow * 40 + (sact ? 2 * sub : 0);
    float* swr_mm = Sc + wid * 320 + (c16 & 7) * 40 + 8 * (g & 1);
    float* swr_x0 = Sc + wid * 320 + (c16 & 7) * 40 + (g == 0 ? 4 : 4 * g + 8);
    float* swr_x1 = Sc + wid * 320 + (c16 & 7) * 40 + 24 + 4 * g;
    const u16* mfp = memL + c16 * MROW + 8 * g;
    const u16* map = memL + c16 * MROW + 7 * wid * 16 + 4 * g;
    bool dead = false;
    for (int s = 0; s < NSEG - 1; ++s) {
        gather_mem(p, s, memL, loff, wid, lane, dead);
        f32x4 acc[7], ax[2];
#pragma unroll
        for (int t = 0; t < 7; ++t) acc[t] = f32x4{0.f, 0.f, 0.f, 0.f};
        ax[0] = f32x4{0.f, 0.f, 0.f, 0.f};
        ax[1] = f32x4{0.f, 0.f, 0.f, 0.f};
        u32x2 ma[7];
#pragma unroll
        for (int t = 0; t < 7; ++t) ma[t] = *reinterpret_cast<const u32x2*>(map + 16 * t);
#pragma unroll
        for (int q = 0; q < 4; ++q) {
            bf16x8 mf = *reinterpret_cast<const bf16x8*>(mfp + 32 * ksq[q]);
            ax[0] = MFMA16(X[0][q], mf, ax[0]);
            ax[1] = MFMA16(X[1][q], mf, ax[1]);
        }
#pragma unroll
        for (int ks = 0; ks < 14; ++ks) {
            bf16x8 mf = *reinterpret_cast<const bf16x8*>(mfp + 32 * ks);
            bf16x8 w6 = *reinterpret_cast<const bf16x8*>(WL + ks * 512);
#pragma unroll
            for (int t = 0; t < 6; ++t) acc[t] = MFMA16(W[t][ks], mf, acc[t]);
            acc[6] = MFMA16(w6, mf, acc[6]);
        }
        if (c16 < 8) {
            *reinterpret_cast<f32x4*>(swr_x0) = ax[0];
            *reinterpret_cast<f32x4*>(swr_x1) = ax[1];
        }
        f32x4 sa = f32x4{0.f, 0.f, 0.f, 0.f}, sb = f32x4{0.f, 0.f, 0.f, 0.f};
#pragma unroll
        for (int pr = 0; pr < 4; ++pr) {
            const int ta = 2 * pr, tb = (pr < 3) ? 2 * pr + 1 : 2 * pr;
            u32x4 av = u32x4{ma[ta][0], ma[ta][1], ma[tb][0], ma[tb][1]};
            u32x4 bv = u32x4{pack2(acc[ta][0], acc[ta][1]), pack2(acc[ta][2], acc[ta][3]), 0u, 0u};
            if (pr < 3) {
                bv[2] = pack2(acc[tb][0], acc[tb][1]);
                bv[3] = pack2(acc[tb][2], acc[tb][3]);
            }
            if (pr & 1)
                sb = MFMA16(__builtin_bit_cast(bf16x8, av), __builtin_bit_cast(bf16x8, bv), sb);
            else
                sa = MFMA16(__builtin_bit_cast(bf16x8, av), __builtin_bit_cast(bf16x8, bv), sa);
        }
        sa += sb;
        if (c16 < 8 && g < 2) *reinterpret_cast<f32x4*>(swr_mm) = sa;
        __syncthreads();
        {
            typedef __attribute__((ext_vector_type(2))) float f32x2;
            f32x2 x0 = *reinterpret_cast<const f32x2*>(sread);
            f32x2 x1 = *reinterpret_cast<const f32x2*>(sread + 320);
            f32x2 x2 = *reinterpret_cast<const f32x2*>(sread + 640);
            f32x2 x3 = *reinterpret_cast<const f32x2*>(sread + 960);
            float a = (x0[0] + x1[0]) + (x2[0] + x3[0]);
            float b = (x0[1] + x1[1]) + (x2[1] + x3[1]);
            float mx = sact ? __builtin_fmaxf(a, b) : -INFINITY;
            mx = red32_max(mx);
            const float L2E = 1.4426950408889634f;
            float nm = -mx * L2E;
            float ea = __builtin_amdgcn_exp2f(__builtin_fmaf(a, L2E, nm));
            float eb = __builtin_amdgcn_exp2f(__builtin_fmaf(b, L2E, nm));
            float sm = sact ? (ea + eb) : 0.f;
            sm = red32_sum(sm);
            float inv = __builtin_amdgcn_rcpf(sm);
            if (sact)
                st_gran(p.G1 + (long)(s & 1) * G1_STEP + h * 160 + srow * 20 + sub, (unsigned)(s + 1),
                        pack2(ea * inv, eb * inv));
        }
        if (s + 1 < NSEG - 1) load_X(s + 1);
    }
}

__device__ __forceinline__ void rec_C_body(const RecParams& p, u16* smem, int c) {
    u16* memL = smem;
    float* red = reinterpret_cast<float*>(smem + 16 * MROW);
    const int tid = threadIdx.x, lane = tid & 63, wid = __builtin_amdgcn_readfirstlane(tid >> 6);
    const int c16 = lane & 15, g = lane >> 4;
    for (int i = tid; i < 8 * MROW / 2; i += 256) reinterpret_cast<unsigned*>(memL + 8 * MROW)[i] = 0u;
    int loff[7];
#pragma unroll
    for (int k = 0; k < 7; ++k) {
        int gi = wid * 448 + k * 64 + lane;
        int j = gi / 224, ip = gi - j * 224;
        loff[k] = j * MROW + 2 * ip;
    }
    const int col = c * 16 + c16;
    bf16x8 W[4][14];
#pragma unroll
    for (int hh = 0; hh < 4; ++hh) {
        const u16* cp = p.CmatT + ((long)(wid * 4 + hh) * DM + col) * DM + 8 * g;
#pragma unroll
        for (int ks = 0; ks < 14; ++ks) W[hh][ks] = *reinterpret_cast<const bf16x8*>(cp + 32 * ks);
    }
    u32x2 dlo[4], dhi[4];
    u32x4 dx;
    const int klo = (g >= 2) ? (8 * (g - 1)) : 0;
    const int khi = (g >= 2) ? (8 * (g - 1) + 4) : 4 * g;
    auto load_D = [&](int s) {
#pragma unroll
        for (int hh = 0; hh < 4; ++hh) {
            const u16* dp = p.Dt + ((long)(wid * 4 + hh) * DM + col) * S_ + s * 32;
            dlo[hh] = *reinterpret_cast<const u32x2*>(dp + klo);
            dhi[hh] = *reinterpret_cast<const u32x2*>(dp + khi);
        }
        dx = *reinterpret_cast<const u32x4*>(p.Dt + ((long)(wid * 4 + g) * DM + col) * S_ + s * 32 + 24);
    };
    load_D(0);
    const u16* mfp = memL + c16 * MROW + 8 * g;
    const int i8 = c16 & 7;
    const int goff = (wid * 4) * 160 + i8 * 20 + 4 * g;
    const int goffx = (wid * 4 + g) * 160 + i8 * 20 + 16;
    float* rwr = red + wid * 128 + (4 * (g & 1)) * 16 + c16;
    const float* rrd = red + (lane >> 3) * 16 + 2 * (lane & 7);
    bool dead = false;
    for (int s = 0; s < NSEG - 1; ++s) {
        gather_mem(p, s, memL, loff, wid, lane, dead);
        f32x4 U[4];
#pragma unroll
        for (int hh = 0; hh < 4; ++hh) U[hh] = f32x4{0.f, 0.f, 0.f, 0.f};
#pragma unroll
        for (int ks = 0; ks < 14; ++ks) {
            bf16x8 mf = *reinterpret_cast<const bf16x8*>(mfp + 32 * ks);
#pragma unroll
            for (int hh = 0; hh < 4; ++hh) U[hh] = MFMA16(mf, W[hh][ks], U[hh]);
        }
        u32x4 bv[4];
#pragma unroll
        for (int hh = 0; hh < 4; ++hh) {
            unsigned u0 = pack2(U[hh][0], U[hh][1]), u1 = pack2(U[hh][2], U[hh][3]);
            bv[hh] = u32x4{(g < 2) ? u0 : dlo[hh][0], (g < 2) ? u1 : dlo[hh][1], dhi[hh][0], dhi[hh][1]};
        }
        u32x4 bx = dx;
        unsigned v[20];
        {
            const u64* g1 = p.G1 + (long)(s & 1) * G1_STEP;
            const u64* q0 = g1 + goff;
            const u64* qx = g1 + goffx;
            const u64* const gp[20] = {q0,       q0 + 1,   q0 + 2,   q0 + 3,   q0 + 160, q0 + 161, q0 + 162,
                                       q0 + 163, q0 + 320, q0 + 321, q0 + 322, q0 + 323, q0 + 480, q0 + 481,
                                       q0 + 482, q0 + 483, qx,       qx + 1,   qx + 2,   qx + 3};
            sweep<20>(gp, (unsigned)(s + 1), v, dead);
        }
        f32x4 o0 = f32x4{0.f, 0.f, 0.f, 0.f}, o1 = f32x4{0.f, 0.f, 0.f, 0.f};
        o0 = MFMA16(__builtin_bit_cast(bf16x8, (u32x4{v[0], v[1], v[2], v[3]})), __builtin_bit_cast(bf16x8, bv[0]), o0);
        o1 = MFMA16(__builtin_bit_cast(bf16x8, (u32x4{v[4], v[5], v[6], v[7]})), __builtin_bit_cast(bf16x8, bv[1]), o1);
        o0 = MFMA16(__builtin_bit_cast(bf16x8, (u32x4{v[8], v[9], v[10], v[11]})), __builtin_bit_cast(bf16x8, bv[2]), o0);
        o1 = MFMA16(__builtin_bit_cast(bf16x8, (u32x4{v[12], v[13], v[14], v[15]})), __builtin_bit_cast(bf16x8, bv[3]), o1);
        o0 = MFMA16(__builtin_bit_cast(bf16x8, (u32x4{v[16], v[17], v[18], v[19]})), __builtin_bit_cast(bf16x8, bx), o0);
        o0 += o1;
        if (g < 2) {
#pragma unroll
            for (int r = 0; r < 4; ++r) rwr[r * 16] = o0[r];
        }
        __syncthreads();
        if (wid == 0) {
            typedef __attribute__((ext_vector_type(2))) float f32x2;
            f32x2 x0 = *reinterpret_cast<const f32x2*>(rrd);
            f32x2 x1 = *reinterpret_cast<const f32x2*>(rrd + 128);
            f32x2 x2 = *reinterpret_cast<const f32x2*>(rrd + 256);
            f32x2 x3 = *reinterpret_cast<const f32x2*>(rrd + 384);
            unsigned pk = pack2((x0[0] + x1[0]) + (x2[0] + x3[0]), (x0[1] + x1[1]) + (x2[1] + x3[1]));
            const int i = lane >> 3, pp = lane & 7;
            st_gran(p.G2 + (long)(s + 1) * G2_STEP + i * 224 + 8 * c + pp, (unsigned)(s + 1), pk);
            *reinterpret_cast<unsigned*>(p.MEM + (long)(s + 1) * 8 * DM + i * DM + 16 * c + 2 * pp) = pk;
        }
        if (s + 1 < NSEG - 1) load_D(s + 1);
    }
}

constexpr int REC_LDS = 16 * MROW * 2 + 4 * 320 * 4 + 4 * 14 * 1024;
constexpr int FUSED_LDS = (ATTN_LDS > REC_LDS) ? ATTN_LDS : REC_LDS;

__global__ __launch_bounds__(256) void rec_attn_kernel(RecParams p, const u16* __restrict__ Q, const u16* __restrict__ K,
                                                       const u16* __restrict__ V, u16* __restrict__ O) {
    __shared__ __attribute__((aligned(16))) u16 smem[FUSED_LDS / 2];
    const int b = blockIdx.x;
    if (b < NA_BLK) {
        rec_A_body(p, smem, b);
    } else if (b < NREC) {
        rec_C_body(p, smem, b - NA_BLK);
    } else {
        int a = b - NREC;
        int qb = 15 - (a >> 4), h = a & 15;
        attnB_body(Q, K, V, O, smem, qb, h);
    }
}

template <int BN, bool NT, bool TO = false>
static void launch_gemm(hipStream_t st, const u16* A, const u16* B, u16* C, int lda, int ldb, int ldc, long sA,
                        long sB, long sC, int M, int N, int K, int batch, float scale) {
    dim3 grid(N / BN, (M + 127) / 128, batch);
    hipLaunchKernelGGL((gemm_kernel<BN, NT, TO>), grid, dim3(256), 0, st, A, B, C, lda, ldb, ldc, sA, sB, sC, M, N, K, scale);
}

extern "C" void kernel_launch(void* const* d_in, const int* in_sizes, int n_in, void* d_out, int out_size,
                              void* d_ws, size_t ws_size, hipStream_t stream) {
    const u16* A_x = (const u16*)d_in[0];
    const u16* A_mem0 = (const u16*)d_in[1];
    const u16* A_Wq = (const u16*)d_in[2];
    const u16* A_Wk = (const u16*)d_in[3];
    const u16* A_Wv = (const u16*)d_in[4];
    const u16* A_Wo = (const u16*)d_in[5];
    const u16* B_x = (const u16*)d_in[7];
    const u16* B_ckv = (const u16*)d_in[8];
    const u16* B_wq = (const u16*)d_in[9];
    const u16* B_wuk = (const u16*)d_in[10];
    const u16* B_wuv = (const u16*)d_in[11];
    const u16* B_wo = (const u16*)d_in[12];
    u16* out = (u16*)d_out;

    char* ws = (char*)d_ws;
    const size_t SZ = (size_t)S_ * HD * 2;
    u16* XQ = (u16*)(ws + 0 * SZ);
    u16* XK = (u16*)(ws + 1 * SZ);
    u16* XV = (u16*)(ws + 2 * SZ);
    u16* Qb = (u16*)(ws + 3 * SZ);
    u16* Kb = (u16*)(ws + 4 * SZ);
    u16* Vb = (u16*)(ws + 5 * SZ);
    u16* OA = (u16*)(ws + 6 * SZ);
    u16* OB = (u16*)(ws + 7 * SZ);
    size_t off = 8 * SZ;
    u16* AmatT = (u16*)(ws + off); off += (size_t)H_ * DM * DM * 2;
    u16* CmatT = (u16*)(ws + off); off += (size_t)H_ * DM * DM * 2;
    u16* Bt = (u16*)(ws + off); off += (size_t)H_ * DM * S_ * 2;
    u16* Dt = (u16*)(ws + off); off += (size_t)H_ * DM * S_ * 2;
    u16* MEM = (u16*)(ws + off); off += (size_t)65 * 8 * DM * 2; off = (off + 255) & ~(size_t)255;
    u16* KM = (u16*)(ws + off); off += (size_t)512 * HD * 2;
    u16* VM = (u16*)(ws + off); off += (size_t)512 * HD * 2;
    char* gbase = ws + off;
    u64* G1 = (u64*)gbase;
    u64* G2 = G1 + 2 * G1_STEP;
    size_t gbytes = (size_t)(2 * G1_STEP + 64 * G2_STEP) * 8;
    off += gbytes;

    hipMemsetAsync(gbase, 0, gbytes, stream);
    launch_gemm<128, false>(stream, A_x, A_Wq, XQ, DM, HD, HD, 0, 0, 0, S_, HD, DM, 1, 1.f);
    launch_gemm<128, false>(stream, A_x, A_Wk, XK, DM, HD, HD, 0, 0, 0, S_, HD, DM, 1, 1.f);
    launch_gemm<128, false>(stream, A_x, A_Wv, XV, DM, HD, HD, 0, 0, 0, S_, HD, DM, 1, 1.f);
    launch_gemm<64, true>(stream, A_Wk, A_Wq, AmatT, HD, HD, DM, DH, DH, (long)DM * DM, DM, DM, DH, H_, 0.0625f);
    launch_gemm<64, false, true>(stream, A_Wv, A_Wo, CmatT, HD, DM, DM, DH, (long)DH * DM, (long)DM * DM, DM, DM, DH, H_, 1.f);
    launch_gemm<64, true>(stream, XK, A_Wq, Bt, HD, HD, DM, DH, DH, (long)DM * S_, S_, DM, DH, H_, 0.0625f);
    launch_gemm<64, false, true>(stream, XV, A_Wo, Dt, HD, DM, S_, DH, (long)DH * DM, (long)S_ * DM, S_, DM, DH, H_, 1.f);
    launch_gemm<128, false>(stream, B_x, B_wq, Qb, DM, HD, HD, 0, 0, 0, S_, HD, DM, 1, 1.f);
    launch_gemm<128, false>(stream, B_ckv, B_wuk, Kb, DC, HD, HD, 0, 0, 0, S_, HD, DC, 1, 1.f);
    launch_gemm<128, false>(stream, B_ckv, B_wuv, Vb, DC, HD, HD, 0, 0, 0, S_, HD, DC, 1, 1.f);
    hipMemcpyAsync(MEM, A_mem0, 8 * DM * 2, hipMemcpyDeviceToDevice, stream);
    RecParams rp;
    rp.mem0 = A_mem0; rp.AmatT = AmatT; rp.CmatT = CmatT; rp.Bt = Bt; rp.Dt = Dt; rp.MEM = MEM; rp.G1 = G1; rp.G2 = G2;
    hipLaunchKernelGGL(rec_attn_kernel, dim3(NREC + 256), dim3(256), 0, stream, rp, Qb, Kb, Vb, OB);
    launch_gemm<128, false>(stream, MEM, A_Wk, KM, DM, HD, HD, 0, 0, 0, 512, HD, DM, 1, 1.f);
    launch_gemm<128, false>(stream, MEM, A_Wv, VM, DM, HD, HD, 0, 0, 0, 512, HD, DM, 1, 1.f);
    hipLaunchKernelGGL(attnA_kernel, dim3(64, 16), dim3(64), 0, stream, XQ, XK, XV, KM, VM, OA);
    launch_gemm<64, false>(stream, OA, A_Wo, out, HD, DM, 896, 0, 0, 0, S_, DM, HD, 1, 1.f);
    launch_gemm<64, false>(stream, OB, B_wo, out + 448, HD, DM, 896, 0, 0, 0, S_, DM, HD, 1, 1.f);
}
```

```cpp
#include <hip/hip_runtime.h>
#include <hip/hip_bf16.h>
#include <stdint.h>

typedef unsigned short u16;
typedef __attribute__((ext_vector_type(8))) short bf16x8;
typedef __attribute__((ext_vector_type(4))) short s16x4;
typedef __attribute__((ext_vector_type(16))) float f32x16;
typedef __attribute__((ext_vector_type(4))) float f32x4;
typedef __attribute__((ext_vector_type(4))) unsigned u32x4;
typedef __attribute__((ext_vector_type(2))) unsigned u32x2;
typedef short v4i16_t __attribute__((ext_vector_type(4)));

#define LAS __attribute__((address_space(3)))

__device__ __forceinline__ float bf2f(u16 x) { return __uint_as_float(((unsigned)x) << 16); }
__device__ __forceinline__ u16 f2bf(float f) {
    unsigned u = __float_as_uint(f);
    u += 0x7fffu + ((u >> 16) & 1u);
    return (u16)(u >> 16);
}
typedef __bf16 bf16x2_t __attribute__((ext_vector_type(2)));
__device__ __forceinline__ unsigned pack2(float lo, float hi) {
    bf16x2_t r = {(__bf16)lo, (__bf16)hi};
    return __builtin_bit_cast(unsigned, r);
}
__device__ __forceinline__ s16x4 tr_read(const void* p) {
    return __builtin_bit_cast(s16x4, __builtin_amdgcn_ds_read_tr16_b64_v4i16((LAS v4i16_t*)p));
}
#define MFMA32(a, b, c) __builtin_amdgcn_mfma_f32_32x32x16_bf16(a, b, c, 0, 0, 0)
#define MFMA16(a, b, c) __builtin_amdgcn_mfma_f32_16x16x32_bf16(a, b, c, 0, 0, 0)

__device__ __forceinline__ int crow(int r, int hi) { return (r & 3) + 8 * (r >> 2) + 4 * hi; }

constexpr int S_ = 2048, DM = 448, H_ = 16, DH = 256, HD = 4096, MM = 8, SEG = 32, NSEG = 64, DC = 1024;

template <int BN, bool NT, bool TO>
__global__ __launch_bounds__(256) void gemm_kernel(const u16* __restrict__ A, const u16* __restrict__ B,
                                                   u16* __restrict__ C, int lda, int ldb, int ldc,
                                                   long sA, long sB, long sC, int M, int N, int K,
                                                   float scale) {
    constexpr int BM = 128, BK = 64;
    constexpr int AROW = 72;
    constexpr int BROW = NT ? 72 : (BN + 32);
    constexpr int BLDS = NT ? BN * 72 : 64 * (BN + 32);
    __shared__ __attribute__((aligned(16))) u16 smem[BM * AROW + BLDS];
    u16* As = smem;
    u16* Bs = smem + BM * AROW;
    const int tid = threadIdx.x, lane = tid & 63, wid = tid >> 6;
    const int wm = wid >> 1, wn = wid & 1;
    const int m0 = blockIdx.y * BM, n0 = blockIdx.x * BN;
    A += (long)blockIdx.z * sA;
    B += (long)blockIdx.z * sB;
    C += (long)blockIdx.z * sC;
    constexpr int NTI = BN / 64;
    f32x16 acc[2][NTI];
#pragma unroll
    for (int i = 0; i < 2; ++i)
#pragma unroll
        for (int j = 0; j < NTI; ++j) acc[i][j] = f32x16{};

    constexpr int BCH = BN / 32;
    u32x4 ra[4], rb[BCH];
    auto load_tiles = [&](int k0) {
#pragma unroll
        for (int i = 0; i < 4; ++i) {
            int c = tid + 256 * i, row = c >> 3, kc = c & 7;
            int gr = m0 + row;
            if (gr < M)
                ra[i] = *reinterpret_cast<const u32x4*>(A + (long)gr * lda + k0 + kc * 8);
            else
                ra[i] = u32x4{0, 0, 0, 0};
        }
#pragma unroll
        for (int i = 0; i < BCH; ++i) {
            int c = tid + 256 * i;
            if (NT) {
                int row = c >> 3, kc = c & 7;
                rb[i] = *reinterpret_cast<const u32x4*>(B + (long)(n0 + row) * ldb + k0 + kc * 8);
            } else {
                int krow = c / (BN / 8), nc = c % (BN / 8);
                rb[i] = *reinterpret_cast<const u32x4*>(B + (long)(k0 + krow) * ldb + n0 + nc * 8);
            }
        }
    };
    auto store_tiles = [&]() {
#pragma unroll
        for (int i = 0; i < 4; ++i) {
            int c = tid + 256 * i, row = c >> 3, kc = c & 7;
            *reinterpret_cast<u32x4*>(As + row * AROW + kc * 8) = ra[i];
        }
#pragma unroll
        for (int i = 0; i < BCH; ++i) {
            int c = tid + 256 * i;
            if (NT) {
                int row = c >> 3, kc = c & 7;
                *reinterpret_cast<u32x4*>(Bs + row * BROW + kc * 8) = rb[i];
            } else {
                int krow = c / (BN / 8), nc = c % (BN / 8);
                *reinterpret_cast<u32x4*>(Bs + krow * BROW + nc * 8) = rb[i];
            }
        }
    };
    const int r32 = lane & 31, hi = lane >> 5, g = lane >> 4, q4 = (lane & 15) >> 2, p4 = lane & 3;
    load_tiles(0);
    const int nk = K / BK;
    for (int kt = 0; kt < nk; ++kt) {
        __syncthreads();
        store_tiles();
        __syncthreads();
        if (kt + 1 < nk) load_tiles((kt + 1) * BK);
#pragma unroll
        for (int ks = 0; ks < 4; ++ks) {
            bf16x8 af[2], bfr[NTI];
#pragma unroll
            for (int i = 0; i < 2; ++i)
                af[i] = *reinterpret_cast<const bf16x8*>(As + (wm * 64 + i * 32 + r32) * AROW + ks * 16 + hi * 8);
#pragma unroll
            for (int j = 0; j < NTI; ++j) {
                int nb = wn * (BN / 2) + j * 32;
                if (NT) {
                    bfr[j] = *reinterpret_cast<const bf16x8*>(Bs + (nb + r32) * BROW + ks * 16 + hi * 8);
                } else {
                    const u16* pB = Bs + (ks * 16 + 8 * (g >> 1) + q4) * BROW + nb + 16 * (g & 1) + 4 * p4;
                    s16x4 lo = tr_read(pB);
                    s16x4 hi4 = tr_read(pB + 4 * BROW);
                    bfr[j] = bf16x8{lo[0], lo[1], lo[2], lo[3], hi4[0], hi4[1], hi4[2], hi4[3]};
                }
            }
#pragma unroll
            for (int i = 0; i < 2; ++i)
#pragma unroll
                for (int j = 0; j < NTI; ++j) acc[i][j] = MFMA32(af[i], bfr[j], acc[i][j]);
        }
    }
#pragma unroll
    for (int i = 0; i < 2; ++i)
#pragma unroll
        for (int j = 0; j < NTI; ++j) {
            int col = n0 + wn * (BN / 2) + j * 32 + r32;
            if (TO) {
#pragma unroll
                for (int rr = 0; rr < 4; ++rr) {
                    int row = m0 + wm * 64 + i * 32 + 8 * rr + 4 * hi;
                    u32x2 w;
                    w[0] = pack2(acc[i][j][4 * rr] * scale, acc[i][j][4 * rr + 1] * scale);
                    w[1] = pack2(acc[i][j][4 * rr + 2] * scale, acc[i][j][4 * rr + 3] * scale);
                    if (row < M) *reinterpret_cast<u32x2*>(C + (long)col * ldc + row) = w;
                }
            } else {
#pragma unroll
                for (int r = 0; r < 16; ++r) {
                    int row = m0 + wm * 64 + i * 32 + crow(r, hi);
                    if (row < M) C[(long)row * ldc + col] = f2bf(acc[i][j][r] * scale);
                }
            }
        }
}

constexpr int KROW = 264;
constexpr int VROW = 288;
constexpr int ATTN_LDS = (64 * KROW + 64 * VROW) * 2;
__device__ __forceinline__ void attnB_body(const u16* __restrict__ Q, const u16* __restrict__ K,
                                           const u16* __restrict__ V, u16* __restrict__ O, u16* smem, int qb, int h) {
    u16* Ks = smem;
    u16* Vs = smem + 64 * KROW;
    int tid = threadIdx.x;
    asm volatile("" : "+v"(tid));
    const int lane = tid & 63, wid = tid >> 6;
    const int q0 = qb * 128;
    const int r32 = lane & 31, hi = lane >> 5, g = lane >> 4, q4 = (lane & 15) >> 2, p4 = lane & 3;
    const int qrow = q0 + wid * 32 + r32;
    bf16x8 qf[16];
#pragma unroll
    for (int ks = 0; ks < 16; ++ks)
        qf[ks] = *reinterpret_cast<const bf16x8*>(Q + (long)qrow * HD + h * DH + ks * 16 + hi * 8);
    f32x16 o[8];
#pragma unroll
    for (int i = 0; i < 8; ++i) o[i] = f32x16{};
    float mrun = -INFINITY, lrun = 0.f;
    const float c2 = 0.0625f * 1.4426950408889634f;
    const int nt = (q0 + 128) / 64;
    u32x4 rk[8], rv[8];
    auto load_kv = [&](int t) {
#pragma unroll
        for (int i = 0; i < 8; ++i) {
            int c = tid + 256 * i, row = c >> 5, cc = c & 31;
            long off = (long)(t * 64 + row) * HD + h * DH + cc * 8;
            rk[i] = *reinterpret_cast<const u32x4*>(K + off);
            rv[i] = *reinterpret_cast<const u32x4*>(V + off);
        }
    };
    auto store_kv = [&]() {
#pragma unroll
        for (int i = 0; i < 8; ++i) {
            int c = tid + 256 * i, row = c >> 5, cc = c & 31;
            *reinterpret_cast<u32x4*>(Ks + row * KROW + cc * 8) = rk[i];
            *reinterpret_cast<u32x4*>(Vs + row * VROW + cc * 8) = rv[i];
        }
    };
    load_kv(0);
    for (int t = 0; t < nt; ++t) {
        __syncthreads();
        store_kv();
        __syncthreads();
        if (t + 1 < nt) load_kv(t + 1);
        const int kbase = t * 64;
        if (kbase <= q0 + wid * 32 + 31) {
            f32x16 s0 = f32x16{}, s1 = f32x16{};
#pragma unroll
            for (int ks = 0; ks < 16; ++ks) {
                bf16x8 k0 = *reinterpret_cast<const bf16x8*>(Ks + r32 * KROW + ks * 16 + hi * 8);
                bf16x8 k1 = *reinterpret_cast<const bf16x8*>(Ks + (32 + r32) * KROW + ks * 16 + hi * 8);
                s0 = MFMA32(k0, qf[ks], s0);
                s1 = MFMA32(k1, qf[ks], s1);
                if ((ks & 3) == 3) __builtin_amdgcn_sched_barrier(0);
            }
            if (kbase + 63 > q0 + wid * 32) {
#pragma unroll
                for (int r = 0; r < 16; ++r) {
                    int kk = kbase + crow(r, hi);
                    if (kk > qrow) s0[r] = -INFINITY;
                    if (kk + 32 > qrow) s1[r] = -INFINITY;
                }
            }
            float mx = -INFINITY;
#pragma unroll
            for (int r = 0; r < 16; ++r) mx = fmaxf(mx, fmaxf(s0[r], s1[r]));
            mx = fmaxf(mx, __shfl_xor(mx, 32));
            float mnew = fmaxf(mrun, mx);
            float alpha = __builtin_amdgcn_exp2f((mrun - mnew) * c2);
            float nm = -mnew * c2;
            float psum = 0.f;
#pragma unroll
            for (int r = 0; r < 16; ++r) {
                s0[r] = __builtin_amdgcn_exp2f(fmaf(s0[r], c2, nm));
                s1[r] = __builtin_amdgcn_exp2f(fmaf(s1[r], c2, nm));
                psum += s0[r] + s1[r];
            }
            psum += __shfl_xor(psum, 32);
            lrun = lrun * alpha + psum;
            mrun = mnew;
#pragma unroll
            for (int i = 0; i < 8; ++i)
#pragma unroll
                for (int r = 0; r < 16; ++r) o[i][r] *= alpha;
            bf16x8 pf[4];
#pragma unroll
            for (int s = 0; s < 2; ++s) {
                u32x4 a, b;
#pragma unroll
                for (int j = 0; j < 4; ++j) {
                    a[j] = pack2(s0[8 * s + 2 * j], s0[8 * s + 2 * j + 1]);
                    b[j] = pack2(s1[8 * s + 2 * j], s1[8 * s + 2 * j + 1]);
                }
                pf[s] = __builtin_bit_cast(bf16x8, a);
                pf[2 + s] = __builtin_bit_cast(bf16x8, b);
            }
#pragma unroll
            for (int dt = 0; dt < 8; ++dt) {
#pragma unroll
                for (int s = 0; s < 4; ++s) {
                    const u16* pV = Vs + (16 * s + 4 * hi + q4) * VROW + 32 * dt + 16 * (g & 1) + 4 * p4;
                    s16x4 lo = tr_read(pV);
                    s16x4 hi4 = tr_read(pV + 8 * VROW);
                    bf16x8 vf = bf16x8{lo[0], lo[1], lo[2], lo[3], hi4[0], hi4[1], hi4[2], hi4[3]};
                    o[dt] = MFMA32(vf, pf[s], o[dt]);
                }
                __builtin_amdgcn_sched_barrier(0);
            }
        }
    }
    float inv = 1.f / lrun;
#pragma unroll
    for (int dt = 0; dt < 8; ++dt)
#pragma unroll
        for (int rr = 0; rr < 4; ++rr) {
            u32x2 w;
            w[0] = pack2(o[dt][4 * rr] * inv, o[dt][4 * rr + 1] * inv);
            w[1] = pack2(o[dt][4 * rr + 2] * inv, o[dt][4 * rr + 3] * inv);
            *reinterpret_cast<u32x2*>(O + (long)qrow * HD + h * DH + 32 * dt + 8 * rr + 4 * hi) = w;
        }
}

typedef unsigned long long u64;
__device__ __forceinline__ u64 ld_gran(const u64* p) {
    return __hip_atomic_load(p, __ATOMIC_RELAXED, __HIP_MEMORY_SCOPE_AGENT);
}
__device__ __forceinline__ void st_gran(u64* p, unsigned tag, unsigned val) {
    __hip_atomic_store(p, ((u64)tag << 32) | (u64)val, __ATOMIC_RELAXED, __HIP_MEMORY_SCOPE_AGENT);
}
constexpr int NA_BLK = 16, NC_BLK = 28, NREC = NA_BLK + NC_BLK;
constexpr int MROW = 456;
constexpr int G1_STEP = 16 * 160;
constexpr int G2_STEP = 8 * 224;
constexpr unsigned SPIN_CAP = 400000u;

#define WTS(slot)
struct RecParams {
    const u16* mem0;
    const u16* AmatT;
    const u16* CmatT;
    const u16* Bt;
    const u16* Dt;
    u64* G1;
    u64* G2;
};

__device__ __forceinline__ float red32_max(float x) {
    float t;
    asm volatile(
        "s_nop 1\n\tv_max_f32_dpp %0, %0, %0 quad_perm:[1,0,3,2] row_mask:0xf bank_mask:0xf\n\t"
        "s_nop 1\n\tv_max_f32_dpp %0, %0, %0 quad_perm:[2,3,0,1] row_mask:0xf bank_mask:0xf\n\t"
        "s_nop 1\n\tv_max_f32_dpp %0, %0, %0 row_half_mirror row_mask:0xf bank_mask:0xf\n\t"
        "s_nop 1\n\tv_max_f32_dpp %0, %0, %0 row_mirror row_mask:0xf bank_mask:0xf\n\t"
        "v_mov_b32 %1, %0\n\t"
        "s_nop 1\n\tv_permlane16_swap_b32 %0, %1\n\t"
        "s_nop 1\n\tv_max_f32 %0, %0, %1"
        : "+v"(x), "=&v"(t));
    return x;
}
__device__ __forceinline__ float red32_sum(float x) {
    float t;
    asm volatile(
        "s_nop 1\n\tv_add_f32_dpp %0, %0, %0 quad_perm:[1,0,3,2] row_mask:0xf bank_mask:0xf\n\t"
        "s_nop 1\n\tv_add_f32_dpp %0, %0, %0 quad_perm:[2,3,0,1] row_mask:0xf bank_mask:0xf\n\t"
        "s_nop 1\n\tv_add_f32_dpp %0, %0, %0 row_half_mirror row_mask:0xf bank_mask:0xf\n\t"
        "s_nop 1\n\tv_add_f32_dpp %0, %0, %0 row_mirror row_mask:0xf bank_mask:0xf\n\t"
        "v_mov_b32 %1, %0\n\t"
        "s_nop 1\n\tv_permlane16_swap_b32 %0, %1\n\t"
        "s_nop 1\n\tv_add_f32 %0, %0, %1"
        : "+v"(x), "=&v"(t));
    return x;
}

template <int N>
__device__ __forceinline__ void sweep(const u64* const (&g)[N], unsigned tag, unsigned (&v)[N], bool& dead) {
    u64 a[N], b[N];
#pragma unroll
    for (int k = 0; k < N; ++k) a[k] = ld_gran(g[k]);
    for (unsigned spin = 0;; ++spin) {
#pragma unroll
        for (int k = 0; k < N; ++k) b[k] = ld_gran(g[k]);
        bool ok = true;
#pragma unroll
        for (int k = 0; k < N; ++k) ok &= ((unsigned)(a[k] >> 32) == tag);
        if (__all(ok) || dead) {
#pragma unroll
            for (int k = 0; k < N; ++k) v[k] = (unsigned)a[k];
            break;
        }
#pragma unroll
        for (int k = 0; k < N; ++k) a[k] = ld_gran(g[k]);
        ok = true;
#pragma unroll
        for (int k = 0; k < N; ++k) ok &= ((unsigned)(b[k] >> 32) == tag);
        if (__all(ok)) {
#pragma unroll
            for (int k = 0; k < N; ++k) v[k] = (unsigned)b[k];
            break;
        }
        if (spin > SPIN_CAP) dead = true;
    }
}

__device__ __forceinline__ void gather_mem(const RecParams& p, int s, u16* memL, const int (&loff)[7], int wid,
                                           int lane, bool& dead) {
    unsigned v[7];
    if (s == 0) {
#pragma unroll
        for (int k = 0; k < 7; ++k) {
            int gi = wid * 448 + k * 64 + lane;
            v[k] = *reinterpret_cast<const unsigned*>(p.mem0 + 2 * gi);
        }
    } else {
        const u64* g0 = p.G2 + (long)s * G2_STEP + wid * 448 + lane;
        const u64* const g[7] = {g0, g0 + 64, g0 + 128, g0 + 192, g0 + 256, g0 + 320, g0 + 384};
        sweep<7>(g, (unsigned)s, v, dead);
    }
#pragma unroll
    for (int k = 0; k < 7; ++k) *reinterpret_cast<unsigned*>(memL + loff[k]) = v[k];
    __syncthreads();
}

__device__ __forceinline__ void rec_A_body(const RecParams& p, u16* smem, int h) {
    u16* memL = smem;
    float* Sc = reinterpret_cast<float*>(smem + 16 * MROW);
    const int tid = threadIdx.x, lane = tid & 63, wid = __builtin_amdgcn_readfirstlane(tid >> 6);
    const int c16 = lane & 15, g = lane >> 4;
    for (int i = tid; i < 8 * MROW / 2; i += 256) reinterpret_cast<unsigned*>(memL + 8 * MROW)[i] = 0u;
    int loff[7];
#pragma unroll
    for (int k = 0; k < 7; ++k) {
        int gi = wid * 448 + k * 64 + lane;
        int j = gi / 224, ip = gi - j * 224;
        loff[k] = j * MROW + 2 * ip;
    }
    bf16x8 W[6][14];
    u16* WL = reinterpret_cast<u16*>(Sc + 4 * 320) + (wid * 14 * 64 + lane) * 8;
#pragma unroll
    for (int t = 0; t < 7; ++t) {
        const u16* ap = p.AmatT + ((long)h * DM + (7 * wid + t) * 16 + c16) * DM + 8 * g;
#pragma unroll
        for (int ks = 0; ks < 14; ++ks) {
            bf16x8 w = *reinterpret_cast<const bf16x8*>(ap + 32 * ks);
            if (t < 6)
                W[t][ks] = w;
            else
                *reinterpret_cast<bf16x8*>(WL + ks * 512) = w;
        }
    }
    bf16x8 X[2][4];
    int ksq[4];
#pragma unroll
    for (int q = 0; q < 4; ++q) ksq[q] = (wid + 4 * q < 14) ? (wid + 4 * q) : 0;
    const bool q3ok = (wid + 12) < 14;
    const u16* btp = p.Bt + ((long)h * S_ + c16) * DM + 8 * g;
    auto load_X = [&](int s) {
#pragma unroll
        for (int tl = 0; tl < 2; ++tl)
#pragma unroll
            for (int q = 0; q < 4; ++q) {
                bf16x8 w = *reinterpret_cast<const bf16x8*>(btp + (long)(s * 32 + tl * 16) * DM + 32 * ksq[q]);
                if (q == 3 && !q3ok) w = bf16x8{0, 0, 0, 0, 0, 0, 0, 0};
                X[tl][q] = w;
            }
    };
    load_X(0);
    const int srow = 2 * wid + (lane >> 5), sub = lane & 31;
    const bool sact = sub < 20;
    const float* sread = Sc + srow * 40 + (sact ? 2 * sub : 0);
    float* swr_mm = Sc + wid * 320 + (c16 & 7) * 40 + 8 * (g & 1);
    float* swr_x0 = Sc + wid * 320 + (c16 & 7) * 40 + (g == 0 ? 4 : 4 * g + 8);
    float* swr_x1 = Sc + wid * 320 + (c16 & 7) * 40 + 24 + 4 * g;
    const u16* mfp = memL + c16 * MROW + 8 * g;
    const u16* map = memL + c16 * MROW + 7 * wid * 16 + 4 * g;
    bool dead = false;
    for (int s = 0; s < NSEG - 1; ++s) {
        gather_mem(p, s, memL, loff, wid, lane, dead);
        f32x4 acc[7], ax[2];
#pragma unroll
        for (int t = 0; t < 7; ++t) acc[t] = f32x4{0.f, 0.f, 0.f, 0.f};
        ax[0] = f32x4{0.f, 0.f, 0.f, 0.f};
        ax[1] = f32x4{0.f, 0.f, 0.f, 0.f};
        u32x2 ma[7];
#pragma unroll
        for (int t = 0; t < 7; ++t) ma[t] = *reinterpret_cast<const u32x2*>(map + 16 * t);
#pragma unroll
        for (int q = 0; q < 4; ++q) {
            bf16x8 mf = *reinterpret_cast<const bf16x8*>(mfp + 32 * ksq[q]);
            ax[0] = MFMA16(X[0][q], mf, ax[0]);
            ax[1] = MFMA16(X[1][q], mf, ax[1]);
        }
#pragma unroll
        for (int ks = 0; ks < 14; ++ks) {
            bf16x8 mf = *reinterpret_cast<const bf16x8*>(mfp + 32 * ks);
            bf16x8 w6 = *reinterpret_cast<const bf16x8*>(WL + ks * 512);
#pragma unroll
            for (int t = 0; t < 6; ++t) acc[t] = MFMA16(W[t][ks], mf, acc[t]);
            acc[6] = MFMA16(w6, mf, acc[6]);
        }
        if (c16 < 8) {
            *reinterpret_cast<f32x4*>(swr_x0) = ax[0];
            *reinterpret_cast<f32x4*>(swr_x1) = ax[1];
        }
        f32x4 sa = f32x4{0.f, 0.f, 0.f, 0.f}, sb = f32x4{0.f, 0.f, 0.f, 0.f};
#pragma unroll
        for (int pr = 0; pr < 4; ++pr) {
            const int ta = 2 * pr, tb = (pr < 3) ? 2 * pr + 1 : 2 * pr;
            u32x4 av = u32x4{ma[ta][0], ma[ta][1], ma[tb][0], ma[tb][1]};
            u32x4 bv = u32x4{pack2(acc[ta][0], acc[ta][1]), pack2(acc[ta][2], acc[ta][3]), 0u, 0u};
            if (pr < 3) {
                bv[2] = pack2(acc[tb][0], acc[tb][1]);
                bv[3] = pack2(acc[tb][2], acc[tb][3]);
            }
            if (pr & 1)
                sb = MFMA16(__builtin_bit_cast(bf16x8, av), __builtin_bit_cast(bf16x8, bv), sb);
            else
                sa = MFMA16(__builtin_bit_cast(bf16x8, av), __builtin_bit_cast(bf16x8, bv), sa);
        }
        sa += sb;
        if (c16 < 8 && g < 2) *reinterpret_cast<f32x4*>(swr_mm) = sa;
        __syncthreads();
        {
            typedef __attribute__((ext_vector_type(2))) float f32x2;
            f32x2 x0 = *reinterpret_cast<const f32x2*>(sread);
            f32x2 x1 = *reinterpret_cast<const f32x2*>(sread + 320);
            f32x2 x2 = *reinterpret_cast<const f32x2*>(sread + 640);
            f32x2 x3 = *reinterpret_cast<const f32x2*>(sread + 960);
            float a = (x0[0] + x1[0]) + (x2[0] + x3[0]);
            float b = (x0[1] + x1[1]) + (x2[1] + x3[1]);
            float mx = sact ? __builtin_fmaxf(a, b) : -INFINITY;
            mx = red32_max(mx);
            const float L2E = 1.4426950408889634f;
            float nm = -mx * L2E;
            float ea = __builtin_amdgcn_exp2f(__builtin_fmaf(a, L2E, nm));
            float eb = __builtin_amdgcn_exp2f(__builtin_fmaf(b, L2E, nm));
            float sm = sact ? (ea + eb) : 0.f;
            sm = red32_sum(sm);
            float inv = __builtin_amdgcn_rcpf(sm);
            if (sact)
                st_gran(p.G1 + (long)(s & 1) * G1_STEP + h * 160 + srow * 20 + sub, (unsigned)(s + 1),
                        pack2(ea * inv, eb * inv));
        }
        if (s + 1 < NSEG - 1) load_X(s + 1);
    }
}

__device__ __forceinline__ void rec_C_body(const RecParams& p, u16* smem, int c) {
    u16* memL = smem;
    float* red = reinterpret_cast<float*>(smem + 16 * MROW);
    const int tid = threadIdx.x, lane = tid & 63, wid = __builtin_amdgcn_readfirstlane(tid >> 6);
    const int c16 = lane & 15, g = lane >> 4;
    for (int i = tid; i < 8 * MROW / 2; i += 256) reinterpret_cast<unsigned*>(memL + 8 * MROW)[i] = 0u;
    int loff[7];
#pragma unroll
    for (int k = 0; k < 7; ++k) {
        int gi = wid * 448 + k * 64 + lane;
        int j = gi / 224, ip = gi - j * 224;
        loff[k] = j * MROW + 2 * ip;
    }
    const int col = c * 16 + c16;
    bf16x8 W[4][14];
#pragma unroll
    for (int hh = 0; hh < 4; ++hh) {
        const u16* cp = p.CmatT + ((long)(wid * 4 + hh) * DM + col) * DM + 8 * g;
#pragma unroll
        for (int ks = 0; ks < 14; ++ks) W[hh][ks] = *reinterpret_cast<const bf16x8*>(cp + 32 * ks);
    }
    u32x2 dlo[4], dhi[4];
    u32x4 dx;
    const int klo = (g >= 2) ? (8 * (g - 1)) : 0;
    const int khi = (g >= 2) ? (8 * (g - 1) + 4) : 4 * g;
    auto load_D = [&](int s) {
#pragma unroll
        for (int hh = 0; hh < 4; ++hh) {
            const u16* dp = p.Dt + ((long)(wid * 4 + hh) * DM + col) * S_ + s * 32;
            dlo[hh] = *reinterpret_cast<const u32x2*>(dp + klo);
            dhi[hh] = *reinterpret_cast<const u32x2*>(dp + khi);
        }
        dx = *reinterpret_cast<const u32x4*>(p.Dt + ((long)(wid * 4 + g) * DM + col) * S_ + s * 32 + 24);
    };
    load_D(0);
    const u16* mfp = memL + c16 * MROW + 8 * g;
    const int i8 = c16 & 7;
    const int goff = (wid * 4) * 160 + i8 * 20 + 4 * g;
    const int goffx = (wid * 4 + g) * 160 + i8 * 20 + 16;
    float* rwr = red + wid * 128 + (4 * (g & 1)) * 16 + c16;
    const float* rrd = red + (lane >> 3) * 16 + 2 * (lane & 7);
    bool dead = false;
    for (int s = 0; s < NSEG - 1; ++s) {
        gather_mem(p, s, memL, loff, wid, lane, dead);
        f32x4 U[4];
#pragma unroll
        for (int hh = 0; hh < 4; ++hh) U[hh] = f32x4{0.f, 0.f, 0.f, 0.f};
#pragma unroll
        for (int ks = 0; ks < 14; ++ks) {
            bf16x8 mf = *reinterpret_cast<const bf16x8*>(mfp + 32 * ks);
#pragma unroll
            for (int hh = 0; hh < 4; ++hh) U[hh] = MFMA16(mf, W[hh][ks], U[hh]);
        }
        u32x4 bv[4];
#pragma unroll
        for (int hh = 0; hh < 4; ++hh) {
            unsigned u0 = pack2(U[hh][0], U[hh][1]), u1 = pack2(U[hh][2], U[hh][3]);
            bv[hh] = u32x4{(g < 2) ? u0 : dlo[hh][0], (g < 2) ? u1 : dlo[hh][1], dhi[hh][0], dhi[hh][1]};
        }
        u32x4 bx = dx;
        unsigned v[20];
        {
            const u64* g1 = p.G1 + (long)(s & 1) * G1_STEP;
            const u64* q0 = g1 + goff;
            const u64* qx = g1 + goffx;
            const u64* const gp[20] = {q0,       q0 + 1,   q0 + 2,   q0 + 3,   q0 + 160, q0 + 161, q0 + 162,
                                       q0 + 163, q0 + 320, q0 + 321, q0 + 322, q0 + 323, q0 + 480, q0 + 481,
                                       q0 + 482, q0 + 483, qx,       qx + 1,   qx + 2,   qx + 3};
            sweep<20>(gp, (unsigned)(s + 1), v, dead);
        }
        f32x4 o0 = f32x4{0.f, 0.f, 0.f, 0.f}, o1 = f32x4{0.f, 0.f, 0.f, 0.f};
        o0 = MFMA16(__builtin_bit_cast(bf16x8, (u32x4{v[0], v[1], v[2], v[3]})), __builtin_bit_cast(bf16x8, bv[0]), o0);
        o1 = MFMA16(__builtin_bit_cast(bf16x8, (u32x4{v[4], v[5], v[6], v[7]})), __builtin_bit_cast(bf16x8, bv[1]), o1);
        o0 = MFMA16(__builtin_bit_cast(bf16x8, (u32x4{v[8], v[9], v[10], v[11]})), __builtin_bit_cast(bf16x8, bv[2]), o0);
        o1 = MFMA16(__builtin_bit_cast(bf16x8, (u32x4{v[12], v[13], v[14], v[15]})), __builtin_bit_cast(bf16x8, bv[3]), o1);
        o0 = MFMA16(__builtin_bit_cast(bf16x8, (u32x4{v[16], v[17], v[18], v[19]})), __builtin_bit_cast(bf16x8, bx), o0);
        o0 += o1;
        if (g < 2) {
#pragma unroll
            for (int r = 0; r < 4; ++r) rwr[r * 16] = o0[r];
        }
        __syncthreads();
        if (wid == 0) {
            typedef __attribute__((ext_vector_type(2))) float f32x2;
            f32x2 x0 = *reinterpret_cast<const f32x2*>(rrd);
            f32x2 x1 = *reinterpret_cast<const f32x2*>(rrd + 128);
            f32x2 x2 = *reinterpret_cast<const f32x2*>(rrd + 256);
            f32x2 x3 = *reinterpret_cast<const f32x2*>(rrd + 384);
            unsigned pk = pack2((x0[0] + x1[0]) + (x2[0] + x3[0]), (x0[1] + x1[1]) + (x2[1] + x3[1]));
            const int i = lane >> 3, pp = lane & 7;
            st_gran(p.G2 + (long)(s + 1) * G2_STEP + i * 224 + 8 * c + pp, (unsigned)(s + 1), pk);
        }
        if (s + 1 < NSEG - 1) load_D(s + 1);
    }
}

template <int BM, int BN>
__device__ __forceinline__ void gemm_tile(const u16* __restrict__ A, int lda, const u16* __restrict__ B, int ldb,
                                          u16* __restrict__ C, int ldc, int nk, u16* smem) {
    constexpr int AROW = 72, BROW = BN + 32;
    constexpr int ASZ = BM * AROW, STG = ASZ + 64 * BROW;
    constexpr int ACH = BM / 32, BCH = BN / 32, TM = BM / 64, TN = BN / 64;
    constexpr int BPR = BN / 8;
    constexpr int BRS = 256 / BPR;
    int tid = threadIdx.x;
    asm volatile("" : "+v"(tid));
    const int lane = tid & 63, wid = tid >> 6;
    const int wm = wid >> 1, wn = wid & 1;
    const int r32 = lane & 31, hi = lane >> 5, g = lane >> 4, q4 = (lane & 15) >> 2, p4 = lane & 3;
    f32x16 acc[TM][TN];
#pragma unroll
    for (int i = 0; i < TM; ++i)
#pragma unroll
        for (int j = 0; j < TN; ++j) acc[i][j] = f32x16{};
    u32x4 ra0[ACH], rb0[BCH], ra1[ACH], rb1[BCH];
    const unsigned aoff = ((unsigned)(tid >> 3) * lda + (tid & 7) * 8) * 2u;
    const unsigned boff = ((unsigned)(tid / BPR) * ldb + (tid % BPR) * 8) * 2u;
    const char* Ac = reinterpret_cast<const char*>(A);
    const char* Bc = reinterpret_cast<const char*>(B);
    u16* aw = smem + (tid >> 3) * AROW + (tid & 7) * 8;
    u16* bw = smem + ASZ + (tid / BPR) * BROW + (tid % BPR) * 8;
    const u16* ard = smem + (wm * (BM / 2) + r32) * AROW + hi * 8;
    const u16* brd = smem + ASZ + (8 * (g >> 1) + q4) * BROW + wn * (BN / 2) + 16 * (g & 1) + 4 * p4;
    auto load = [&](u32x4(&ra)[ACH], u32x4(&rb)[BCH], int kt) {
#pragma unroll
        for (int i = 0; i < ACH; ++i)
            ra[i] = *reinterpret_cast<const u32x4*>(Ac + (aoff + (unsigned)(32 * i * lda + kt * 64) * 2u));
#pragma unroll
        for (int i = 0; i < BCH; ++i)
            rb[i] = *reinterpret_cast<const u32x4*>(Bc + (boff + (unsigned)((kt * 64 + BRS * i) * ldb) * 2u));
    };
    auto store = [&](const u32x4(&ra)[ACH], const u32x4(&rb)[BCH], int st) {
#pragma unroll
        for (int i = 0; i < ACH; ++i) *reinterpret_cast<u32x4*>(aw + st * STG + 32 * i * AROW) = ra[i];
#pragma unroll
        for (int i = 0; i < BCH; ++i) *reinterpret_cast<u32x4*>(bw + st * STG + BRS * i * BROW) = rb[i];
    };
    auto compute = [&](int st, int ks) {
        bf16x8 af[TM], bfr[TN];
#pragma unroll
        for (int i = 0; i < TM; ++i)
            af[i] = *reinterpret_cast<const bf16x8*>(ard + st * STG + i * 32 * AROW + ks * 16);
#pragma unroll
        for (int j = 0; j < TN; ++j) {
            const u16* pB = brd + st * STG + ks * 16 * BROW + j * 32;
            s16x4 lo = tr_read(pB);
            s16x4 hi4 = tr_read(pB + 4 * BROW);
            bfr[j] = bf16x8{lo[0], lo[1], lo[2], lo[3], hi4[0], hi4[1], hi4[2], hi4[3]};
        }
#pragma unroll
        for (int i = 0; i < TM; ++i)
#pragma unroll
            for (int j = 0; j < TN; ++j) acc[i][j] = MFMA32(bfr[j], af[i], acc[i][j]);
    };
    load(ra0, rb0, 0);
    load(ra1, rb1, 1);
    __syncthreads();
    store(ra0, rb0, 0);
    load(ra0, rb0, 2);
    __syncthreads();
    for (int kt = 0; kt + 1 < nk; kt += 2) {
        compute(0, 0);
        compute(0, 1);
        store(ra1, rb1, 1);
        load(ra1, rb1, min(kt + 3, nk - 1));
        compute(0, 2);
        compute(0, 3);
        __syncthreads();
        compute(1, 0);
        compute(1, 1);
        store(ra0, rb0, 0);
        load(ra0, rb0, min(kt + 4, nk - 1));
        compute(1, 2);
        compute(1, 3);
        __syncthreads();
    }
    if (nk & 1) {
        compute(0, 0);
        compute(0, 1);
        compute(0, 2);
        compute(0, 3);
        __syncthreads();
    }
    constexpr int CROW = BN + 8;
#pragma unroll
    for (int i = 0; i < TM; ++i)
#pragma unroll
        for (int j = 0; j < TN; ++j)
#pragma unroll
            for (int rr = 0; rr < 4; ++rr) {
                u32x2 w;
                w[0] = pack2(acc[i][j][4 * rr], acc[i][j][4 * rr + 1]);
                w[1] = pack2(acc[i][j][4 * rr + 2], acc[i][j][4 * rr + 3]);
                *reinterpret_cast<u32x2*>(smem + (wm * (BM / 2) + 32 * i + r32) * CROW + wn * (BN / 2) + 32 * j +
                                          8 * rr + 4 * hi) = w;
            }
    __syncthreads();
    int tid2 = threadIdx.x;
    asm volatile("" : "+v"(tid2));
    char* Cc = reinterpret_cast<char*>(C);
    const unsigned coff = ((unsigned)(tid2 / BPR) * ldc + (tid2 % BPR) * 8) * 2u;
#pragma unroll
    for (int i = 0; i < BM * BN / 2048; ++i) {
        int row = tid2 / BPR + BRS * i, cc = tid2 % BPR;
        *reinterpret_cast<u32x4*>(Cc + (coff + (unsigned)(BRS * i * ldc) * 2u)) =
            *reinterpret_cast<const u32x4*>(smem + row * CROW + cc * 8);
    }
}

struct MegaParams {
    const u16* A_x;
    const u16* A_Wq;
    const u16* A_Wk;
    const u16* A_Wv;
    const u16* A_Wo;
    const u16* B_x;
    const u16* B_ckv;
    const u16* B_wq;
    const u16* B_wuk;
    const u16* B_wuv;
    const u16* B_wo;
    u16* XQ;
    const u16* XK;
    const u16* XV;
    u16* Qb;
    u16* Kb;
    u16* Vb;
    u16* OA;
    u16* OB;
    u16* out;
    unsigned* ctl;
};
constexpr int NWORK = 256 - NREC;
constexpr int CTL_Q1 = 0, CTL_C1 = 64, CTL_Q2 = 128, CTL_C2 = 192, CTL_Q3 = 256, CTL_Q4 = 320, CTL_Q5 = 384,
              CTL_CA = 448, CTL_WORDS = 448 + 16 * 64;
constexpr unsigned WSPIN_CAP = 2000000u;

__device__ __forceinline__ int next_ticket(unsigned* q, int* slot) {
    __syncthreads();
    if (threadIdx.x == 0) *slot = (int)__hip_atomic_fetch_add(q, 1u, __ATOMIC_RELAXED, __HIP_MEMORY_SCOPE_AGENT);
    __syncthreads();
    return *slot;
}
__device__ __forceinline__ void publish_count(unsigned* c) {
    asm volatile("s_waitcnt vmcnt(0)" ::: "memory");
    __syncthreads();
    if (threadIdx.x == 0) {
        __builtin_amdgcn_fence(__ATOMIC_RELEASE, "agent");
        asm volatile("s_waitcnt vmcnt(0)" ::: "memory");
        __hip_atomic_fetch_add(c, 1u, __ATOMIC_RELAXED, __HIP_MEMORY_SCOPE_AGENT);
    }
}
__device__ __forceinline__ void wait_count(unsigned* c, unsigned target) {
    if (threadIdx.x == 0) {
        for (unsigned spin = 0; spin < WSPIN_CAP; ++spin) {
            if (__hip_atomic_load(c, __ATOMIC_RELAXED, __HIP_MEMORY_SCOPE_AGENT) >= target) break;
            __builtin_amdgcn_s_sleep(16);
        }
        __builtin_amdgcn_fence(__ATOMIC_ACQUIRE, "agent");
        asm volatile("s_waitcnt vmcnt(0)" ::: "memory");
    }
    __syncthreads();
}

constexpr int PB_ROW = 544;
__device__ __forceinline__ void postA_task(const RecParams& p, const MegaParams& m, int grp, int h, u16* smem) {
    u16* Kms = smem;
    u16* X = smem + 32 * KROW;
    u16* memL = X;
    u16* Bs = X + 32 * MROW;
    int tid = threadIdx.x;
    asm volatile("" : "+v"(tid));
    const int lane = tid & 63, wid = __builtin_amdgcn_readfirstlane(tid >> 6);
    u16* Vs = X + wid * 48 * VROW;
    const int r32 = lane & 31, hi = lane >> 5, g = lane >> 4, q4 = (lane & 15) >> 2, p4 = lane & 3;
    const int s = 4 * grp + wid;
    const u16* wp = (((tid & 63) < 32) ? m.A_Wk : m.A_Wv) + (long)(tid >> 6) * HD + h * DH + (tid & 31) * 8;
    u32x4 rb[16];
    auto loadB = [&](int kt) {
#pragma unroll
        for (int i = 0; i < 16; ++i) rb[i] = *reinterpret_cast<const u32x4*>(wp + (long)(kt * 64 + 4 * i) * HD);
    };
    loadB(0);
    __syncthreads();
    {
        unsigned v[28];
        if (s == 0) {
#pragma unroll
            for (int k = 0; k < 28; ++k) v[k] = *reinterpret_cast<const unsigned*>(p.mem0 + 2 * (k * 64 + lane));
        } else {
            const u64* g0 = p.G2 + (long)s * G2_STEP + lane;
            for (unsigned spin = 0; spin < WSPIN_CAP; ++spin) {
                bool ok = true;
#pragma unroll
                for (int k = 0; k < 28; ++k) {
                    u64 x = ld_gran(g0 + k * 64);
                    v[k] = (unsigned)x;
                    ok &= ((unsigned)(x >> 32) == (unsigned)s);
                }
                if (__all(ok)) break;
                __builtin_amdgcn_s_sleep(24);
            }
        }
#pragma unroll
        for (int k = 0; k < 28; ++k) {
            int gi = k * 64 + lane;
            int j = gi / 224, ip = gi - j * 224;
            *reinterpret_cast<unsigned*>(memL + (8 * wid + j) * MROW + 2 * ip) = v[k];
        }
    }
    f32x16 acc[4];
#pragma unroll
    for (int j = 0; j < 4; ++j) acc[j] = f32x16{};
    u16* bw = Bs + (tid >> 6) * PB_ROW + (tid & 63) * 8;
    const u16* brd = Bs + (8 * (g >> 1) + q4) * PB_ROW + 128 * wid + 16 * (g & 1) + 4 * p4;
    const u16* mrd = memL + r32 * MROW + hi * 8;
    for (int kt = 0; kt < 7; ++kt) {
        if (kt) __syncthreads();
#pragma unroll
        for (int i = 0; i < 16; ++i) *reinterpret_cast<u32x4*>(bw + 4 * i * PB_ROW) = rb[i];
        __syncthreads();
        if (kt + 1 < 7) loadB(kt + 1);
#pragma unroll
        for (int ks = 0; ks < 4; ++ks) {
            bf16x8 mf = *reinterpret_cast<const bf16x8*>(mrd + kt * 64 + ks * 16);
#pragma unroll
            for (int j = 0; j < 4; ++j) {
                const u16* pB = brd + ks * 16 * PB_ROW + j * 32;
                s16x4 lo = tr_read(pB);
                s16x4 hi4 = tr_read(pB + 4 * PB_ROW);
                bf16x8 wf = bf16x8{lo[0], lo[1], lo[2], lo[3], hi4[0], hi4[1], hi4[2], hi4[3]};
                acc[j] = MFMA32(wf, mf, acc[j]);
            }
        }
    }
    __syncthreads();
    {
        u16* dst = (wid < 2) ? (Kms + r32 * KROW + 128 * wid)
                             : (X + (r32 >> 3) * 48 * VROW + (r32 & 7) * VROW + 128 * (wid - 2));
#pragma unroll
        for (int j = 0; j < 4; ++j)
#pragma unroll
            for (int rr = 0; rr < 4; ++rr) {
                u32x2 w;
                w[0] = pack2(acc[j][4 * rr], acc[j][4 * rr + 1]);
                w[1] = pack2(acc[j][4 * rr + 2], acc[j][4 * rr + 3]);
                *reinterpret_cast<u32x2*>(dst + 32 * j + 8 * rr + 4 * hi) = w;
            }
    }
    for (int c = lane; c < 40 * 32; c += 64) {
        int row = 8 + (c >> 5), cc = c & 31;
        u32x4 v = u32x4{0, 0, 0, 0};
        if (row < 40) v = *reinterpret_cast<const u32x4*>(m.XV + (long)(s * 32 + row - 8) * HD + h * DH + cc * 8);
        *reinterpret_cast<u32x4*>(Vs + row * VROW + cc * 8) = v;
    }
    __syncthreads();
    const int qrow = s * 32 + r32;
    const u16* k0g = m.XK + (long)(s * 32 + ((r32 < 8) ? 0 : r32 - 8)) * HD + h * DH + hi * 8;
    const u16* k0l = Kms + (8 * wid + (r32 & 7)) * KROW + hi * 8;
    const u16* k1p = m.XK + (long)(s * 32 + ((r32 < 8) ? (24 + r32) : 0)) * HD + h * DH + hi * 8;
    const u16* qp = m.XQ + (long)qrow * HD + h * DH + hi * 8;
    f32x16 s0 = f32x16{}, s1 = f32x16{};
#pragma unroll
    for (int ks = 0; ks < 16; ++ks) {
        bf16x8 qf = *reinterpret_cast<const bf16x8*>(qp + ks * 16);
        bf16x8 kg = *reinterpret_cast<const bf16x8*>(k0g + ks * 16);
        bf16x8 kl = *reinterpret_cast<const bf16x8*>(k0l + ks * 16);
        bf16x8 kb = *reinterpret_cast<const bf16x8*>(k1p + ks * 16);
        bf16x8 ka = (r32 < 8) ? kl : kg;
        s0 = MFMA32(ka, qf, s0);
        s1 = MFMA32(kb, qf, s1);
    }
#pragma unroll
    for (int r = 0; r < 16; ++r) {
        int kk = crow(r, hi);
        if (kk >= 8 && kk - 8 > r32) s0[r] = -INFINITY;
        int k2 = kk + 32;
        if (k2 >= 40 || k2 - 8 > r32) s1[r] = -INFINITY;
    }
    const float c2 = 0.0625f * 1.4426950408889634f;
    float mx = -INFINITY;
#pragma unroll
    for (int r = 0; r < 16; ++r) mx = fmaxf(mx, fmaxf(s0[r], s1[r]));
    mx = fmaxf(mx, __shfl_xor(mx, 32));
    float nm = -mx * c2, psum = 0.f;
#pragma unroll
    for (int r = 0; r < 16; ++r) {
        s0[r] = __builtin_amdgcn_exp2f(fmaf(s0[r], c2, nm));
        s1[r] = __builtin_amdgcn_exp2f(fmaf(s1[r], c2, nm));
        psum += s0[r] + s1[r];
    }
    psum += __shfl_xor(psum, 32);
    float inv = 1.f / psum;
    bf16x8 pf[3];
#pragma unroll
    for (int sI = 0; sI < 2; ++sI) {
        u32x4 a;
#pragma unroll
        for (int j = 0; j < 4; ++j) a[j] = pack2(s0[8 * sI + 2 * j] * inv, s0[8 * sI + 2 * j + 1] * inv);
        pf[sI] = __builtin_bit_cast(bf16x8, a);
    }
    {
        u32x4 a;
#pragma unroll
        for (int j = 0; j < 4; ++j) a[j] = pack2(s1[2 * j] * inv, s1[2 * j + 1] * inv);
        pf[2] = __builtin_bit_cast(bf16x8, a);
    }
#pragma unroll
    for (int dt = 0; dt < 8; ++dt) {
        f32x16 o = f32x16{};
#pragma unroll
        for (int sI = 0; sI < 3; ++sI) {
            const u16* pV = Vs + (16 * sI + 4 * hi + q4) * VROW + 32 * dt + 16 * (g & 1) + 4 * p4;
            s16x4 lo = tr_read(pV);
            s16x4 hi4 = tr_read(pV + 8 * VROW);
            bf16x8 vf = bf16x8{lo[0], lo[1], lo[2], lo[3], hi4[0], hi4[1], hi4[2], hi4[3]};
            o = MFMA32(vf, pf[sI], o);
        }
#pragma unroll
        for (int rr = 0; rr < 4; ++rr) {
            u32x2 w;
            w[0] = pack2(o[4 * rr], o[4 * rr + 1]);
            w[1] = pack2(o[4 * rr + 2], o[4 * rr + 3]);
            *reinterpret_cast<u32x2*>(m.OA + (long)qrow * HD + h * DH + 32 * dt + 8 * rr + 4 * hi) = w;
        }
    }
}

__device__ __forceinline__ void worker_body(const RecParams& p, const MegaParams& m, u16* smem, int* slot) {
    unsigned* ctl = m.ctl;
    WTS(0);
    for (;;) {
        int t = next_ticket(ctl + CTL_Q1, slot);
        if (t >= 1024) break;
        int mat = t >> 8, rb = (t >> 5) & 7, cb = t & 31;
        const u16* A = (mat < 2) ? m.B_ckv : (mat == 2 ? m.B_x : m.A_x);
        const u16* B = (mat == 0) ? m.B_wuk : (mat == 1 ? m.B_wuv : (mat == 2 ? m.B_wq : m.A_Wq));
        u16* C = (mat == 0) ? m.Kb : (mat == 1 ? m.Vb : (mat == 2 ? m.Qb : m.XQ));
        int lda = (mat < 2) ? DC : DM;
        gemm_tile<256, 128>(A + (long)rb * 256 * lda, lda, B + cb * 128, HD, C + (long)rb * 256 * HD + cb * 128, HD,
                            lda / 64, smem);
    }
    publish_count(ctl + CTL_C1);
    WTS(1);
    wait_count(ctl + CTL_C1, NWORK);
    WTS(2);
    for (;;) {
        int t = next_ticket(ctl + CTL_Q2, slot);
        if (t >= 256) break;
        attnB_body(m.Qb, m.Kb, m.Vb, m.OB, smem, 15 - (t >> 4), t & 15);
    }
    publish_count(ctl + CTL_C2);
    WTS(3);
    wait_count(ctl + CTL_C2, NWORK);
    WTS(4);
    for (;;) {
        int t = next_ticket(ctl + CTL_Q3, slot);
        if (t >= 112) break;
        int rb = t / 7, cb = t - rb * 7;
        gemm_tile<128, 64>(m.OB + (long)rb * 128 * HD, HD, m.B_wo + cb * 64, DM,
                           m.out + (long)rb * 128 * 896 + 448 + cb * 64, 896, HD / 64, smem);
    }
    WTS(5);
    for (;;) {
        int t = next_ticket(ctl + CTL_Q4, slot);
        if (t >= 256) break;
        postA_task(p, m, t >> 4, t & 15, smem);
        publish_count(ctl + CTL_CA + (t >> 4) * 64);
    }
    WTS(6);
    for (;;) {
        int t = next_ticket(ctl + CTL_Q5, slot);
        if (t >= 112) break;
        int rb = t / 7, cb = t - rb * 7;
        wait_count(ctl + CTL_CA + rb * 64, 16u);
        gemm_tile<128, 64>(m.OA + (long)rb * 128 * HD, HD, m.A_Wo + cb * 64, DM,
                           m.out + (long)rb * 128 * 896 + cb * 64, 896, HD / 64, smem);
    }
    WTS(7);
}

constexpr int REC_LDS = 16 * MROW * 2 + 4 * 320 * 4 + 4 * 14 * 1024;
constexpr int GEMM_LDS = 2 * (256 * 72 + 64 * 160) * 2;
constexpr int POST_LDS = (32 * KROW + 4 * 48 * VROW) * 2;
constexpr int cmax(int a, int b) { return a > b ? a : b; }
constexpr int FUSED_LDS = cmax(cmax(ATTN_LDS, REC_LDS), cmax(GEMM_LDS, POST_LDS));

__global__ __launch_bounds__(256) void mega_kernel(RecParams p, MegaParams m) {
    __shared__ __attribute__((aligned(16))) u16 smem[FUSED_LDS / 2 + 8];
    const int b = blockIdx.x;
    if (b < NA_BLK) {
        rec_A_body(p, smem, b);
    } else if (b < NREC) {
        rec_C_body(p, smem, b - NA_BLK);
    } else {
        worker_body(p, m, smem, reinterpret_cast<int*>(smem + FUSED_LDS / 2));
    }
}

template <int BN, bool NT, bool TO = false>
static void launch_gemm(hipStream_t st, const u16* A, const u16* B, u16* C, int lda, int ldb, int ldc, long sA,
                        long sB, long sC, int M, int N, int K, int batch, float scale) {
    dim3 grid(N / BN, (M + 127) / 128, batch);
    hipLaunchKernelGGL((gemm_kernel<BN, NT, TO>), grid, dim3(256), 0, st, A, B, C, lda, ldb, ldc, sA, sB, sC, M, N, K, scale);
}

extern "C" void kernel_launch(void* const* d_in, const int* in_sizes, int n_in, void* d_out, int out_size,
                              void* d_ws, size_t ws_size, hipStream_t stream) {
    const u16* A_x = (const u16*)d_in[0];
    const u16* A_mem0 = (const u16*)d_in[1];
    const u16* A_Wq = (const u16*)d_in[2];
    const u16* A_Wk = (const u16*)d_in[3];
    const u16* A_Wv = (const u16*)d_in[4];
    const u16* A_Wo = (const u16*)d_in[5];
    const u16* B_x = (const u16*)d_in[7];
    const u16* B_ckv = (const u16*)d_in[8];
    const u16* B_wq = (const u16*)d_in[9];
    const u16* B_wuk = (const u16*)d_in[10];
    const u16* B_wuv = (const u16*)d_in[11];
    const u16* B_wo = (const u16*)d_in[12];
    u16* out = (u16*)d_out;

    char* ws = (char*)d_ws;
    const size_t SZ = (size_t)S_ * HD * 2;
    u16* XQ = (u16*)(ws + 0 * SZ);
    u16* XK = (u16*)(ws + 1 * SZ);
    u16* XV = (u16*)(ws + 2 * SZ);
    u16* Qb = (u16*)(ws + 3 * SZ);
    u16* Kb = (u16*)(ws + 4 * SZ);
    u16* Vb = (u16*)(ws + 5 * SZ);
    u16* OA = (u16*)(ws + 6 * SZ);
    u16* OB = (u16*)(ws + 7 * SZ);
    size_t off = 8 * SZ;
    u16* AmatT = (u16*)(ws + off); off += (size_t)H_ * DM * DM * 2;
    u16* CmatT = (u16*)(ws + off); off += (size_t)H_ * DM * DM * 2;
    u16* Bt = (u16*)(ws + off); off += (size_t)H_ * DM * S_ * 2;
    u16* Dt = (u16*)(ws + off); off += (size_t)H_ * DM * S_ * 2;
    char* gbase = ws + off;
    u64* G1 = (u64*)gbase;
    u64* G2 = G1 + 2 * G1_STEP;
    unsigned* ctl = (unsigned*)(G2 + 64 * G2_STEP);
    size_t gbytes = (size_t)(2 * G1_STEP + 64 * G2_STEP) * 8 + (size_t)CTL_WORDS * 4;
    off += gbytes;

    hipMemsetAsync(gbase, 0, gbytes, stream);
    launch_gemm<128, false>(stream, A_x, A_Wk, XK, DM, HD, HD, 0, 0, 0, S_, HD, DM, 1, 1.f);
    launch_gemm<128, false>(stream, A_x, A_Wv, XV, DM, HD, HD, 0, 0, 0, S_, HD, DM, 1, 1.f);
    launch_gemm<64, true>(stream, A_Wk, A_Wq, AmatT, HD, HD, DM, DH, DH, (long)DM * DM, DM, DM, DH, H_, 0.0625f);
    launch_gemm<64, false, true>(stream, A_Wv, A_Wo, CmatT, HD, DM, DM, DH, (long)DH * DM, (long)DM * DM, DM, DM, DH, H_, 1.f);
    launch_gemm<64, true>(stream, XK, A_Wq, Bt, HD, HD, DM, DH, DH, (long)DM * S_, S_, DM, DH, H_, 0.0625f);
    launch_gemm<64, false, true>(stream, XV, A_Wo, Dt, HD, DM, S_, DH, (long)DH * DM, (long)S_ * DM, S_, DM, DH, H_, 1.f);
    RecParams rp;
    rp.mem0 = A_mem0; rp.AmatT = AmatT; rp.CmatT = CmatT; rp.Bt = Bt; rp.Dt = Dt; rp.G1 = G1; rp.G2 = G2;
    MegaParams mp;
    mp.A_x = A_x; mp.A_Wq = A_Wq; mp.A_Wk = A_Wk; mp.A_Wv = A_Wv; mp.A_Wo = A_Wo;
    mp.B_x = B_x; mp.B_ckv = B_ckv; mp.B_wq = B_wq; mp.B_wuk = B_wuk; mp.B_wuv = B_wuv; mp.B_wo = B_wo;
    mp.XQ = XQ; mp.XK = XK; mp.XV = XV; mp.Qb = Qb; mp.Kb = Kb; mp.Vb = Vb; mp.OA = OA; mp.OB = OB;
    mp.out = out; mp.ctl = ctl;
    hipLaunchKernelGGL(mega_kernel, dim3(256), dim3(256), 0, stream, rp, mp);
}
```

```cpp
#include <hip/hip_runtime.h>
#include <hip/hip_bf16.h>
#include <stdint.h>

typedef unsigned short u16;
typedef __attribute__((ext_vector_type(8))) short bf16x8;
typedef __attribute__((ext_vector_type(4))) short s16x4;
typedef __attribute__((ext_vector_type(16))) float f32x16;
typedef __attribute__((ext_vector_type(4))) float f32x4;
typedef __attribute__((ext_vector_type(4))) unsigned u32x4;
typedef __attribute__((ext_vector_type(2))) unsigned u32x2;
typedef short v4i16_t __attribute__((ext_vector_type(4)));

#define LAS __attribute__((address_space(3)))

__device__ __forceinline__ float bf2f(u16 x) { return __uint_as_float(((unsigned)x) << 16); }
__device__ __forceinline__ u16 f2bf(float f) {
    unsigned u = __float_as_uint(f);
    u += 0x7fffu + ((u >> 16) & 1u);
    return (u16)(u >> 16);
}
typedef __bf16 bf16x2_t __attribute__((ext_vector_type(2)));
__device__ __forceinline__ unsigned pack2(float lo, float hi) {
    bf16x2_t r = {(__bf16)lo, (__bf16)hi};
    return __builtin_bit_cast(unsigned, r);
}
__device__ __forceinline__ s16x4 tr_read(const void* p) {
    return __builtin_bit_cast(s16x4, __builtin_amdgcn_ds_read_tr16_b64_v4i16((LAS v4i16_t*)p));
}
#define MFMA32(a, b, c) __builtin_amdgcn_mfma_f32_32x32x16_bf16(a, b, c, 0, 0, 0)
#define MFMA16(a, b, c) __builtin_amdgcn_mfma_f32_16x16x32_bf16(a, b, c, 0, 0, 0)

__device__ __forceinline__ int crow(int r, int hi) { return (r & 3) + 8 * (r >> 2) + 4 * hi; }

constexpr int S_ = 2048, DM = 448, H_ = 16, DH = 256, HD = 4096, MM = 8, SEG = 32, NSEG = 64, DC = 1024;

template <int BN, bool NT, bool TO>
__global__ __launch_bounds__(256) void gemm_kernel(const u16* __restrict__ A, const u16* __restrict__ B,
                                                   u16* __restrict__ C, int lda, int ldb, int ldc,
                                                   long sA, long sB, long sC, int M, int N, int K,
                                                   float scale) {
    constexpr int BM = 128, BK = 64;
    constexpr int AROW = 72;
    constexpr int BROW = NT ? 72 : (BN + 32);
    constexpr int BLDS = NT ? BN * 72 : 64 * (BN + 32);
    __shared__ __attribute__((aligned(16))) u16 smem[BM * AROW + BLDS];
    u16* As = smem;
    u16* Bs = smem + BM * AROW;
    const int tid = threadIdx.x, lane = tid & 63, wid = tid >> 6;
    const int wm = wid >> 1, wn = wid & 1;
    const int m0 = blockIdx.y * BM, n0 = blockIdx.x * BN;
    A += (long)blockIdx.z * sA;
    B += (long)blockIdx.z * sB;
    C += (long)blockIdx.z * sC;
    constexpr int NTI = BN / 64;
    f32x16 acc[2][NTI];
#pragma unroll
    for (int i = 0; i < 2; ++i)
#pragma unroll
        for (int j = 0; j < NTI; ++j) acc[i][j] = f32x16{};

    constexpr int BCH = BN / 32;
    u32x4 ra[4], rb[BCH];
    auto load_tiles = [&](int k0) {
#pragma unroll
        for (int i = 0; i < 4; ++i) {
            int c = tid + 256 * i, row = c >> 3, kc = c & 7;
            int gr = m0 + row;
            if (gr < M)
                ra[i] = *reinterpret_cast<const u32x4*>(A + (long)gr * lda + k0 + kc * 8);
            else
                ra[i] = u32x4{0, 0, 0, 0};
        }
#pragma unroll
        for (int i = 0; i < BCH; ++i) {
            int c = tid + 256 * i;
            if (NT) {
                int row = c >> 3, kc = c & 7;
                rb[i] = *reinterpret_cast<const u32x4*>(B + (long)(n0 + row) * ldb + k0 + kc * 8);
            } else {
                int krow = c / (BN / 8), nc = c % (BN / 8);
                rb[i] = *reinterpret_cast<const u32x4*>(B + (long)(k0 + krow) * ldb + n0 + nc * 8);
            }
        }
    };
    auto store_tiles = [&]() {
#pragma unroll
        for (int i = 0; i < 4; ++i) {
            int c = tid + 256 * i, row = c >> 3, kc = c & 7;
            *reinterpret_cast<u32x4*>(As + row * AROW + kc * 8) = ra[i];
        }
#pragma unroll
        for (int i = 0; i < BCH; ++i) {
            int c = tid + 256 * i;
            if (NT) {
                int row = c >> 3, kc = c & 7;
                *reinterpret_cast<u32x4*>(Bs + row * BROW + kc * 8) = rb[i];
            } else {
                int krow = c / (BN / 8), nc = c % (BN / 8);
                *reinterpret_cast<u32x4*>(Bs + krow * BROW + nc * 8) = rb[i];
            }
        }
    };
    const int r32 = lane & 31, hi = lane >> 5, g = lane >> 4, q4 = (lane & 15) >> 2, p4 = lane & 3;
    load_tiles(0);
    const int nk = K / BK;
    for (int kt = 0; kt < nk; ++kt) {
        __syncthreads();
        store_tiles();
        __syncthreads();
        if (kt + 1 < nk) load_tiles((kt + 1) * BK);
#pragma unroll
        for (int ks = 0; ks < 4; ++ks) {
            bf16x8 af[2], bfr[NTI];
#pragma unroll
            for (int i = 0; i < 2; ++i)
                af[i] = *reinterpret_cast<const bf16x8*>(As + (wm * 64 + i * 32 + r32) * AROW + ks * 16 + hi * 8);
#pragma unroll
            for (int j = 0; j < NTI; ++j) {
                int nb = wn * (BN / 2) + j * 32;
                if (NT) {
                    bfr[j] = *reinterpret_cast<const bf16x8*>(Bs + (nb + r32) * BROW + ks * 16 + hi * 8);
                } else {
                    const u16* pB = Bs + (ks * 16 + 8 * (g >> 1) + q4) * BROW + nb + 16 * (g & 1) + 4 * p4;
                    s16x4 lo = tr_read(pB);
                    s16x4 hi4 = tr_read(pB + 4 * BROW);
                    bfr[j] = bf16x8{lo[0], lo[1], lo[2], lo[3], hi4[0], hi4[1], hi4[2], hi4[3]};
                }
            }
#pragma unroll
            for (int i = 0; i < 2; ++i)
#pragma unroll
                for (int j = 0; j < NTI; ++j) acc[i][j] = MFMA32(af[i], bfr[j], acc[i][j]);
        }
    }
#pragma unroll
    for (int i = 0; i < 2; ++i)
#pragma unroll
        for (int j = 0; j < NTI; ++j) {
            int col = n0 + wn * (BN / 2) + j * 32 + r32;
            if (TO) {
#pragma unroll
                for (int rr = 0; rr < 4; ++rr) {
                    int row = m0 + wm * 64 + i * 32 + 8 * rr + 4 * hi;
                    u32x2 w;
                    w[0] = pack2(acc[i][j][4 * rr] * scale, acc[i][j][4 * rr + 1] * scale);
                    w[1] = pack2(acc[i][j][4 * rr + 2] * scale, acc[i][j][4 * rr + 3] * scale);
                    if (row < M) *reinterpret_cast<u32x2*>(C + (long)col * ldc + row) = w;
                }
            } else {
#pragma unroll
                for (int r = 0; r < 16; ++r) {
                    int row = m0 + wm * 64 + i * 32 + crow(r, hi);
                    if (row < M) C[(long)row * ldc + col] = f2bf(acc[i][j][r] * scale);
                }
            }
        }
}

constexpr int KROW = 264;
constexpr int VROW = 288;
constexpr int ATTN_LDS = (64 * KROW + 64 * VROW) * 2;
__device__ __forceinline__ void attnB_body(const u16* __restrict__ Q, const u16* __restrict__ K,
                                           const u16* __restrict__ V, u16* __restrict__ O, u16* smem, int qb, int h) {
    u16* Ks = smem;
    u16* Vs = smem + 64 * KROW;
    int tid = threadIdx.x;
    asm volatile("" : "+v"(tid));
    const int lane = tid & 63, wid = tid >> 6;
    const int q0 = qb * 128;
    const int r32 = lane & 31, hi = lane >> 5, g = lane >> 4, q4 = (lane & 15) >> 2, p4 = lane & 3;
    const int qrow = q0 + wid * 32 + r32;
    bf16x8 qf[16];
#pragma unroll
    for (int ks = 0; ks < 16; ++ks)
        qf[ks] = *reinterpret_cast<const bf16x8*>(Q + (long)qrow * HD + h * DH + ks * 16 + hi * 8);
    f32x16 o[8];
#pragma unroll
    for (int i = 0; i < 8; ++i) o[i] = f32x16{};
    float mrun = -INFINITY, lrun = 0.f;
    const float c2 = 0.0625f * 1.4426950408889634f;
    const int nt = (q0 + 128) / 64;
    u32x4 rk[8], rv[8];
    auto load_kv = [&](int t) {
#pragma unroll
        for (int i = 0; i < 8; ++i) {
            int c = tid + 256 * i, row = c >> 5, cc = c & 31;
            long off = (long)(t * 64 + row) * HD + h * DH + cc * 8;
            rk[i] = *reinterpret_cast<const u32x4*>(K + off);
            rv[i] = *reinterpret_cast<const u32x4*>(V + off);
        }
    };
    auto store_kv = [&]() {
#pragma unroll
        for (int i = 0; i < 8; ++i) {
            int c = tid + 256 * i, row = c >> 5, cc = c & 31;
            *reinterpret_cast<u32x4*>(Ks + row * KROW + cc * 8) = rk[i];
            *reinterpret_cast<u32x4*>(Vs + row * VROW + cc * 8) = rv[i];
        }
    };
    load_kv(0);
    for (int t = 0; t < nt; ++t) {
        __syncthreads();
        store_kv();
        __syncthreads();
        if (t + 1 < nt) load_kv(t + 1);
        const int kbase = t * 64;
        if (kbase <= q0 + wid * 32 + 31) {
            f32x16 s0 = f32x16{}, s1 = f32x16{};
#pragma unroll
            for (int ks = 0; ks < 16; ++ks) {
                bf16x8 k0 = *reinterpret_cast<const bf16x8*>(Ks + r32 * KROW + ks * 16 + hi * 8);
                bf16x8 k1 = *reinterpret_cast<const bf16x8*>(Ks + (32 + r32) * KROW + ks * 16 + hi * 8);
                s0 = MFMA32(k0, qf[ks], s0);
                s1 = MFMA32(k1, qf[ks], s1);
                if ((ks & 3) == 3) __builtin_amdgcn_sched_barrier(0);
            }
            if (kbase + 63 > q0 + wid * 32) {
#pragma unroll
                for (int r = 0; r < 16; ++r) {
                    int kk = kbase + crow(r, hi);
                    if (kk > qrow) s0[r] = -INFINITY;
                    if (kk + 32 > qrow) s1[r] = -INFINITY;
                }
            }
            float mx = -INFINITY;
#pragma unroll
            for (int r = 0; r < 16; ++r) mx = fmaxf(mx, fmaxf(s0[r], s1[r]));
            mx = fmaxf(mx, __shfl_xor(mx, 32));
            float mnew = fmaxf(mrun, mx);
            float alpha = __builtin_amdgcn_exp2f((mrun - mnew) * c2);
            float nm = -mnew * c2;
            float psum = 0.f;
#pragma unroll
            for (int r = 0; r < 16; ++r) {
                s0[r] = __builtin_amdgcn_exp2f(fmaf(s0[r], c2, nm));
                s1[r] = __builtin_amdgcn_exp2f(fmaf(s1[r], c2, nm));
                psum += s0[r] + s1[r];
            }
            psum += __shfl_xor(psum, 32);
            lrun = lrun * alpha + psum;
            mrun = mnew;
#pragma unroll
            for (int i = 0; i < 8; ++i)
#pragma unroll
                for (int r = 0; r < 16; ++r) o[i][r] *= alpha;
            bf16x8 pf[4];
#pragma unroll
            for (int s = 0; s < 2; ++s) {
                u32x4 a, b;
#pragma unroll
                for (int j = 0; j < 4; ++j) {
                    a[j] = pack2(s0[8 * s + 2 * j], s0[8 * s + 2 * j + 1]);
                    b[j] = pack2(s1[8 * s + 2 * j], s1[8 * s + 2 * j + 1]);
                }
                pf[s] = __builtin_bit_cast(bf16x8, a);
                pf[2 + s] = __builtin_bit_cast(bf16x8, b);
            }
#pragma unroll
            for (int dt = 0; dt < 8; ++dt) {
#pragma unroll
                for (int s = 0; s < 4; ++s) {
                    const u16* pV = Vs + (16 * s + 4 * hi + q4) * VROW + 32 * dt + 16 * (g & 1) + 4 * p4;
                    s16x4 lo = tr_read(pV);
                    s16x4 hi4 = tr_read(pV + 8 * VROW);
                    bf16x8 vf = bf16x8{lo[0], lo[1], lo[2], lo[3], hi4[0], hi4[1], hi4[2], hi4[3]};
                    o[dt] = MFMA32(vf, pf[s], o[dt]);
                }
                __builtin_amdgcn_sched_barrier(0);
            }
        }
    }
    float inv = 1.f / lrun;
#pragma unroll
    for (int dt = 0; dt < 8; ++dt)
#pragma unroll
        for (int rr = 0; rr < 4; ++rr) {
            u32x2 w;
            w[0] = pack2(o[dt][4 * rr] * inv, o[dt][4 * rr + 1] * inv);
            w[1] = pack2(o[dt][4 * rr + 2] * inv, o[dt][4 * rr + 3] * inv);
            __hip_atomic_store(reinterpret_cast<unsigned long long*>(O + (long)qrow * HD + h * DH + 32 * dt + 8 * rr + 4 * hi),
                               ((unsigned long long)w[1] << 32) | w[0], __ATOMIC_RELAXED, __HIP_MEMORY_SCOPE_AGENT);
        }
}

typedef unsigned long long u64;
__device__ __forceinline__ u64 ld_gran(const u64* p) {
    return __hip_atomic_load(p, __ATOMIC_RELAXED, __HIP_MEMORY_SCOPE_AGENT);
}
__device__ __forceinline__ void st_gran(u64* p, unsigned tag, unsigned val) {
    __hip_atomic_store(p, ((u64)tag << 32) | (u64)val, __ATOMIC_RELAXED, __HIP_MEMORY_SCOPE_AGENT);
}
constexpr int NA_BLK = 16, NC_BLK = 28, NREC = NA_BLK + NC_BLK;
constexpr int MROW = 456;
constexpr int G1_STEP = 16 * 160;
constexpr int G2_STEP = 8 * 224;
constexpr unsigned SPIN_CAP = 400000u;

#define WTS(slot)
struct RecParams {
    const u16* mem0;
    const u16* AmatT;
    const u16* CmatT;
    const u16* Bt;
    const u16* Dt;
    u16* Ut;
    u64* G1;
    u64* G2;
};

__device__ __forceinline__ float red32_max(float x) {
    float t;
    asm volatile(
        "s_nop 1\n\tv_max_f32_dpp %0, %0, %0 quad_perm:[1,0,3,2] row_mask:0xf bank_mask:0xf\n\t"
        "s_nop 1\n\tv_max_f32_dpp %0, %0, %0 quad_perm:[2,3,0,1] row_mask:0xf bank_mask:0xf\n\t"
        "s_nop 1\n\tv_max_f32_dpp %0, %0, %0 row_half_mirror row_mask:0xf bank_mask:0xf\n\t"
        "s_nop 1\n\tv_max_f32_dpp %0, %0, %0 row_mirror row_mask:0xf bank_mask:0xf\n\t"
        "v_mov_b32 %1, %0\n\t"
        "s_nop 1\n\tv_permlane16_swap_b32 %0, %1\n\t"
        "s_nop 1\n\tv_max_f32 %0, %0, %1"
        : "+v"(x), "=&v"(t));
    return x;
}
__device__ __forceinline__ float red32_sum(float x) {
    float t;
    asm volatile(
        "s_nop 1\n\tv_add_f32_dpp %0, %0, %0 quad_perm:[1,0,3,2] row_mask:0xf bank_mask:0xf\n\t"
        "s_nop 1\n\tv_add_f32_dpp %0, %0, %0 quad_perm:[2,3,0,1] row_mask:0xf bank_mask:0xf\n\t"
        "s_nop 1\n\tv_add_f32_dpp %0, %0, %0 row_half_mirror row_mask:0xf bank_mask:0xf\n\t"
        "s_nop 1\n\tv_add_f32_dpp %0, %0, %0 row_mirror row_mask:0xf bank_mask:0xf\n\t"
        "v_mov_b32 %1, %0\n\t"
        "s_nop 1\n\tv_permlane16_swap_b32 %0, %1\n\t"
        "s_nop 1\n\tv_add_f32 %0, %0, %1"
        : "+v"(x), "=&v"(t));
    return x;
}

template <int N>
__device__ __forceinline__ void sweep(const u64* const (&g)[N], unsigned tag, unsigned (&v)[N], bool& dead) {
    u64 a[N], b[N];
#pragma unroll
    for (int k = 0; k < N; ++k) a[k] = ld_gran(g[k]);
    for (unsigned spin = 0;; ++spin) {
#pragma unroll
        for (int k = 0; k < N; ++k) b[k] = ld_gran(g[k]);
        bool ok = true;
#pragma unroll
        for (int k = 0; k < N; ++k) ok &= ((unsigned)(a[k] >> 32) == tag);
        if (__all(ok) || dead) {
#pragma unroll
            for (int k = 0; k < N; ++k) v[k] = (unsigned)a[k];
            break;
        }
#pragma unroll
        for (int k = 0; k < N; ++k) a[k] = ld_gran(g[k]);
        ok = true;
#pragma unroll
        for (int k = 0; k < N; ++k) ok &= ((unsigned)(b[k] >> 32) == tag);
        if (__all(ok)) {
#pragma unroll
            for (int k = 0; k < N; ++k) v[k] = (unsigned)b[k];
            break;
        }
        if (spin > SPIN_CAP) dead = true;
    }
}

__device__ __forceinline__ void gather_mem(const RecParams& p, int s, u16* memL, const int (&loff)[7], int wid,
                                           int lane, bool& dead) {
    unsigned v[7];
    if (s == 0) {
#pragma unroll
        for (int k = 0; k < 7; ++k) {
            int gi = wid * 448 + k * 64 + lane;
            v[k] = *reinterpret_cast<const unsigned*>(p.mem0 + 2 * gi);
        }
    } else {
        const u64* g0 = p.G2 + (long)s * G2_STEP + wid * 448 + lane;
        const u64* const g[7] = {g0, g0 + 64, g0 + 128, g0 + 192, g0 + 256, g0 + 320, g0 + 384};
        sweep<7>(g, (unsigned)s, v, dead);
    }
#pragma unroll
    for (int k = 0; k < 7; ++k) *reinterpret_cast<unsigned*>(memL + loff[k]) = v[k];
    __syncthreads();
}

__device__ __forceinline__ void rec_A_body(const RecParams& p, u16* smem, int h) {
    u16* memL = smem;
    float* Sc = reinterpret_cast<float*>(smem + 16 * MROW);
    const int tid = threadIdx.x, lane = tid & 63, wid = __builtin_amdgcn_readfirstlane(tid >> 6);
    const int c16 = lane & 15, g = lane >> 4;
    for (int i = tid; i < 8 * MROW / 2; i += 256) reinterpret_cast<unsigned*>(memL + 8 * MROW)[i] = 0u;
    int loff[7];
#pragma unroll
    for (int k = 0; k < 7; ++k) {
        int gi = wid * 448 + k * 64 + lane;
        int j = gi / 224, ip = gi - j * 224;
        loff[k] = j * MROW + 2 * ip;
    }
    bf16x8 W[6][14];
    u16* WL = reinterpret_cast<u16*>(Sc + 4 * 320) + (wid * 14 * 64 + lane) * 8;
#pragma unroll
    for (int t = 0; t < 7; ++t) {
        const u16* ap = p.AmatT + ((long)h * DM + (7 * wid + t) * 16 + c16) * DM + 8 * g;
#pragma unroll
        for (int ks = 0; ks < 14; ++ks) {
            bf16x8 w = *reinterpret_cast<const bf16x8*>(ap + 32 * ks);
            if (t < 6)
                W[t][ks] = w;
            else
                *reinterpret_cast<bf16x8*>(WL + ks * 512) = w;
        }
    }
    bf16x8 X[2][4];
    int ksq[4];
#pragma unroll
    for (int q = 0; q < 4; ++q) ksq[q] = (wid + 4 * q < 14) ? (wid + 4 * q) : 0;
    const bool q3ok = (wid + 12) < 14;
    const u16* btp = p.Bt + ((long)h * S_ + c16) * DM + 8 * g;
    auto load_X = [&](int s) {
#pragma unroll
        for (int tl = 0; tl < 2; ++tl)
#pragma unroll
            for (int q = 0; q < 4; ++q) {
                bf16x8 w = *reinterpret_cast<const bf16x8*>(btp + (long)(s * 32 + tl * 16) * DM + 32 * ksq[q]);
                if (q == 3 && !q3ok) w = bf16x8{0, 0, 0, 0, 0, 0, 0, 0};
                X[tl][q] = w;
            }
    };
    load_X(0);
    const int srow = 2 * wid + (lane >> 5), sub = lane & 31;
    const bool sact = sub < 20;
    const float* sread = Sc + srow * 40 + (sact ? 2 * sub : 0);
    float* swr_mm = Sc + wid * 320 + (c16 & 7) * 40 + 8 * (g & 1);
    float* swr_x0 = Sc + wid * 320 + (c16 & 7) * 40 + (g == 0 ? 4 : 4 * g + 8);
    float* swr_x1 = Sc + wid * 320 + (c16 & 7) * 40 + 24 + 4 * g;
    const u16* mfp = memL + c16 * MROW + 8 * g;
    const u16* map = memL + c16 * MROW + 7 * wid * 16 + 4 * g;
    bool dead = false;
    for (int s = 0; s < NSEG - 1; ++s) {
        gather_mem(p, s, memL, loff, wid, lane, dead);
        f32x4 acc[7], ax[2];
#pragma unroll
        for (int t = 0; t < 7; ++t) acc[t] = f32x4{0.f, 0.f, 0.f, 0.f};
        ax[0] = f32x4{0.f, 0.f, 0.f, 0.f};
        ax[1] = f32x4{0.f, 0.f, 0.f, 0.f};
        u32x2 ma[7];
#pragma unroll
        for (int t = 0; t < 7; ++t) ma[t] = *reinterpret_cast<const u32x2*>(map + 16 * t);
#pragma unroll
        for (int q = 0; q < 4; ++q) {
            bf16x8 mf = *reinterpret_cast<const bf16x8*>(mfp + 32 * ksq[q]);
            ax[0] = MFMA16(X[0][q], mf, ax[0]);
            ax[1] = MFMA16(X[1][q], mf, ax[1]);
        }
#pragma unroll
        for (int ks = 0; ks < 14; ++ks) {
            bf16x8 mf = *reinterpret_cast<const bf16x8*>(mfp + 32 * ks);
            bf16x8 w6 = *reinterpret_cast<const bf16x8*>(WL + ks * 512);
#pragma unroll
            for (int t = 0; t < 6; ++t) acc[t] = MFMA16(W[t][ks], mf, acc[t]);
            acc[6] = MFMA16(w6, mf, acc[6]);
        }
        if (c16 < 8) {
            *reinterpret_cast<f32x4*>(swr_x0) = ax[0];
            *reinterpret_cast<f32x4*>(swr_x1) = ax[1];
        }
        f32x4 sa = f32x4{0.f, 0.f, 0.f, 0.f}, sb = f32x4{0.f, 0.f, 0.f, 0.f};
#pragma unroll
        for (int pr = 0; pr < 4; ++pr) {
            const int ta = 2 * pr, tb = (pr < 3) ? 2 * pr + 1 : 2 * pr;
            u32x4 av = u32x4{ma[ta][0], ma[ta][1], ma[tb][0], ma[tb][1]};
            u32x4 bv = u32x4{pack2(acc[ta][0], acc[ta][1]), pack2(acc[ta][2], acc[ta][3]), 0u, 0u};
            if (pr < 3) {
                bv[2] = pack2(acc[tb][0], acc[tb][1]);
                bv[3] = pack2(acc[tb][2], acc[tb][3]);
            }
            if (pr & 1)
                sb = MFMA16(__builtin_bit_cast(bf16x8, av), __builtin_bit_cast(bf16x8, bv), sb);
            else
                sa = MFMA16(__builtin_bit_cast(bf16x8, av), __builtin_bit_cast(bf16x8, bv), sa);
        }
        sa += sb;
        if (c16 < 8 && g < 2) *reinterpret_cast<f32x4*>(swr_mm) = sa;
        __syncthreads();
        {
            typedef __attribute__((ext_vector_type(2))) float f32x2;
            f32x2 x0 = *reinterpret_cast<const f32x2*>(sread);
            f32x2 x1 = *reinterpret_cast<const f32x2*>(sread + 320);
            f32x2 x2 = *reinterpret_cast<const f32x2*>(sread + 640);
            f32x2 x3 = *reinterpret_cast<const f32x2*>(sread + 960);
            float a = (x0[0] + x1[0]) + (x2[0] + x3[0]);
            float b = (x0[1] + x1[1]) + (x2[1] + x3[1]);
            float mx = sact ? __builtin_fmaxf(a, b) : -INFINITY;
            mx = red32_max(mx);
            const float L2E = 1.4426950408889634f;
            float nm = -mx * L2E;
            float ea = __builtin_amdgcn_exp2f(__builtin_fmaf(a, L2E, nm));
            float eb = __builtin_amdgcn_exp2f(__builtin_fmaf(b, L2E, nm));
            float sm = sact ? (ea + eb) : 0.f;
            sm = red32_sum(sm);
            float inv = __builtin_amdgcn_rcpf(sm);
            if (sact)
                st_gran(p.G1 + (long)(s & 1) * G1_STEP + h * 160 + srow * 20 + sub, (unsigned)(s + 1),
                        pack2(ea * inv, eb * inv));
        }
        if (s + 1 < NSEG - 1) load_X(s + 1);
    }
}

__device__ __forceinline__ void rec_C_body(const RecParams& p, u16* smem, int c) {
    u16* memL = smem;
    float* red = reinterpret_cast<float*>(smem + 16 * MROW);
    const int tid = threadIdx.x, lane = tid & 63, wid = __builtin_amdgcn_readfirstlane(tid >> 6);
    const int c16 = lane & 15, g = lane >> 4;
    for (int i = tid; i < 8 * MROW / 2; i += 256) reinterpret_cast<unsigned*>(memL + 8 * MROW)[i] = 0u;
    int loff[7];
#pragma unroll
    for (int k = 0; k < 7; ++k) {
        int gi = wid * 448 + k * 64 + lane;
        int j = gi / 224, ip = gi - j * 224;
        loff[k] = j * MROW + 2 * ip;
    }
    const int col = c * 16 + c16;
    bf16x8 W[4][14];
#pragma unroll
    for (int hh = 0; hh < 4; ++hh) {
        const u16* cp = p.CmatT + ((long)(wid * 4 + hh) * DM + col) * DM + 8 * g;
#pragma unroll
        for (int ks = 0; ks < 14; ++ks) W[hh][ks] = *reinterpret_cast<const bf16x8*>(cp + 32 * ks);
    }
    u32x2 dlo[4], dhi[4];
    u32x4 dx;
    const int klo = (g >= 2) ? (8 * (g - 1)) : 0;
    const int khi = (g >= 2) ? (8 * (g - 1) + 4) : 4 * g;
    auto load_D = [&](int s) {
#pragma unroll
        for (int hh = 0; hh < 4; ++hh) {
            const u16* dp = p.Dt + ((long)(wid * 4 + hh) * DM + col) * S_ + s * 32;
            dlo[hh] = *reinterpret_cast<const u32x2*>(dp + klo);
            dhi[hh] = *reinterpret_cast<const u32x2*>(dp + khi);
        }
        dx = *reinterpret_cast<const u32x4*>(p.Dt + ((long)(wid * 4 + g) * DM + col) * S_ + s * 32 + 24);
    };
    load_D(0);
    const u16* mfp = memL + c16 * MROW + 8 * g;
    const int i8 = c16 & 7;
    const int goff = (wid * 4) * 160 + i8 * 20 + 4 * g;
    const int goffx = (wid * 4 + g) * 160 + i8 * 20 + 16;
    float* rwr = red + wid * 128 + (4 * (g & 1)) * 16 + c16;
    const float* rrd = red + (lane >> 3) * 16 + 2 * (lane & 7);
    bool dead = false;
    u64* utp = reinterpret_cast<u64*>(p.Ut + ((long)(wid * 4) * DM + col) * 8 + 4 * (g & 1));
    for (int s = 0; s < NSEG; ++s) {
        gather_mem(p, s, memL, loff, wid, lane, dead);
        f32x4 U[4];
#pragma unroll
        for (int hh = 0; hh < 4; ++hh) U[hh] = f32x4{0.f, 0.f, 0.f, 0.f};
#pragma unroll
        for (int ks = 0; ks < 14; ++ks) {
            bf16x8 mf = *reinterpret_cast<const bf16x8*>(mfp + 32 * ks);
#pragma unroll
            for (int hh = 0; hh < 4; ++hh) U[hh] = MFMA16(mf, W[hh][ks], U[hh]);
        }
        u32x4 bv[4];
#pragma unroll
        for (int hh = 0; hh < 4; ++hh) {
            unsigned u0 = pack2(U[hh][0], U[hh][1]), u1 = pack2(U[hh][2], U[hh][3]);
            bv[hh] = u32x4{(g < 2) ? u0 : dlo[hh][0], (g < 2) ? u1 : dlo[hh][1], dhi[hh][0], dhi[hh][1]};
            if (g < 2)
                __hip_atomic_store(utp + ((long)s * 16 + hh) * (DM * 2), ((u64)u1 << 32) | u0, __ATOMIC_RELAXED,
                                   __HIP_MEMORY_SCOPE_AGENT);
        }
        if (s == NSEG - 1) {
            asm volatile("s_waitcnt vmcnt(0)" ::: "memory");
            __syncthreads();
            if (wid == 0) st_gran(p.G2 + (long)NSEG * G2_STEP + (lane >> 3) * 224 + 8 * c + (lane & 7), (unsigned)NSEG, 0u);
            break;
        }
        u32x4 bx = dx;
        unsigned v[20];
        {
            const u64* g1 = p.G1 + (long)(s & 1) * G1_STEP;
            const u64* q0 = g1 + goff;
            const u64* qx = g1 + goffx;
            const u64* const gp[20] = {q0,       q0 + 1,   q0 + 2,   q0 + 3,   q0 + 160, q0 + 161, q0 + 162,
                                       q0 + 163, q0 + 320, q0 + 321, q0 + 322, q0 + 323, q0 + 480, q0 + 481,
                                       q0 + 482, q0 + 483, qx,       qx + 1,   qx + 2,   qx + 3};
            sweep<20>(gp, (unsigned)(s + 1), v, dead);
        }
        f32x4 o0 = f32x4{0.f, 0.f, 0.f, 0.f}, o1 = f32x4{0.f, 0.f, 0.f, 0.f};
        o0 = MFMA16(__builtin_bit_cast(bf16x8, (u32x4{v[0], v[1], v[2], v[3]})), __builtin_bit_cast(bf16x8, bv[0]), o0);
        o1 = MFMA16(__builtin_bit_cast(bf16x8, (u32x4{v[4], v[5], v[6], v[7]})), __builtin_bit_cast(bf16x8, bv[1]), o1);
        o0 = MFMA16(__builtin_bit_cast(bf16x8, (u32x4{v[8], v[9], v[10], v[11]})), __builtin_bit_cast(bf16x8, bv[2]), o0);
        o1 = MFMA16(__builtin_bit_cast(bf16x8, (u32x4{v[12], v[13], v[14], v[15]})), __builtin_bit_cast(bf16x8, bv[3]), o1);
        o0 = MFMA16(__builtin_bit_cast(bf16x8, (u32x4{v[16], v[17], v[18], v[19]})), __builtin_bit_cast(bf16x8, bx), o0);
        o0 += o1;
        if (g < 2) {
#pragma unroll
            for (int r = 0; r < 4; ++r) rwr[r * 16] = o0[r];
        }
        asm volatile("s_waitcnt vmcnt(0)" ::: "memory");
        __syncthreads();
        if (wid == 0) {
            typedef __attribute__((ext_vector_type(2))) float f32x2;
            f32x2 x0 = *reinterpret_cast<const f32x2*>(rrd);
            f32x2 x1 = *reinterpret_cast<const f32x2*>(rrd + 128);
            f32x2 x2 = *reinterpret_cast<const f32x2*>(rrd + 256);
            f32x2 x3 = *reinterpret_cast<const f32x2*>(rrd + 384);
            unsigned pk = pack2((x0[0] + x1[0]) + (x2[0] + x3[0]), (x0[1] + x1[1]) + (x2[1] + x3[1]));
            const int i = lane >> 3, pp = lane & 7;
            st_gran(p.G2 + (long)(s + 1) * G2_STEP + i * 224 + 8 * c + pp, (unsigned)(s + 1), pk);
        }
        if (s + 1 < NSEG - 1) load_D(s + 1);
    }
}

template <int BM, int BN, bool NT, bool SC1>
__device__ __forceinline__ void gemm_tile(const u16* __restrict__ A, int lda, const u16* __restrict__ B, int ldb,
                                          u16* __restrict__ C, int ldc, int nk, u16* smem) {
    constexpr int AROW = 72, BROW = NT ? 72 : BN + 32;
    constexpr int ASZ = BM * AROW, STG = ASZ + (NT ? BN * 72 : 64 * BROW);
    constexpr int ACH = BM / 32, BCH = BN / 32, TM = BM / 64, TN = BN / 64;
    constexpr int BPR = BN / 8;
    constexpr int BRS = 256 / BPR;
    int tid = threadIdx.x;
    asm volatile("" : "+v"(tid));
    const int lane = tid & 63, wid = tid >> 6;
    const int wm = wid >> 1, wn = wid & 1;
    const int r32 = lane & 31, hi = lane >> 5, g = lane >> 4, q4 = (lane & 15) >> 2, p4 = lane & 3;
    f32x16 acc[TM][TN];
#pragma unroll
    for (int i = 0; i < TM; ++i)
#pragma unroll
        for (int j = 0; j < TN; ++j) acc[i][j] = f32x16{};
    u32x4 ra0[ACH], rb0[BCH], ra1[ACH], rb1[BCH];
    const unsigned aoff = ((unsigned)(tid >> 3) * lda + (tid & 7) * 8) * 2u;
    const unsigned boff = NT ? ((unsigned)(tid >> 3) * ldb + (tid & 7) * 8) * 2u
                             : ((unsigned)(tid / BPR) * ldb + (tid % BPR) * 8) * 2u;
    const char* Ac = reinterpret_cast<const char*>(A);
    const char* Bc = reinterpret_cast<const char*>(B);
    u16* aw = smem + (tid >> 3) * AROW + (tid & 7) * 8;
    u16* bw = NT ? (smem + ASZ + (tid >> 3) * BROW + (tid & 7) * 8) : (smem + ASZ + (tid / BPR) * BROW + (tid % BPR) * 8);
    const u16* ard = smem + (wm * (BM / 2) + r32) * AROW + hi * 8;
    const u16* brd = NT ? (smem + ASZ + (wn * (BN / 2) + r32) * BROW + hi * 8)
                        : (smem + ASZ + (8 * (g >> 1) + q4) * BROW + wn * (BN / 2) + 16 * (g & 1) + 4 * p4);
    auto load = [&](u32x4(&ra)[ACH], u32x4(&rb)[BCH], int kt) {
#pragma unroll
        for (int i = 0; i < ACH; ++i)
            ra[i] = *reinterpret_cast<const u32x4*>(Ac + (aoff + (unsigned)(32 * i * lda + kt * 64) * 2u));
#pragma unroll
        for (int i = 0; i < BCH; ++i) {
            if (NT)
                rb[i] = *reinterpret_cast<const u32x4*>(Bc + (boff + (unsigned)(32 * i * ldb + kt * 64) * 2u));
            else
                rb[i] = *reinterpret_cast<const u32x4*>(Bc + (boff + (unsigned)((kt * 64 + BRS * i) * ldb) * 2u));
        }
    };
    auto store = [&](const u32x4(&ra)[ACH], const u32x4(&rb)[BCH], int st) {
#pragma unroll
        for (int i = 0; i < ACH; ++i) *reinterpret_cast<u32x4*>(aw + st * STG + 32 * i * AROW) = ra[i];
#pragma unroll
        for (int i = 0; i < BCH; ++i)
            *reinterpret_cast<u32x4*>(bw + st * STG + (NT ? 32 : BRS) * i * BROW) = rb[i];
    };
    auto compute = [&](int st, int ks) {
        bf16x8 af[TM], bfr[TN];
#pragma unroll
        for (int i = 0; i < TM; ++i)
            af[i] = *reinterpret_cast<const bf16x8*>(ard + st * STG + i * 32 * AROW + ks * 16);
#pragma unroll
        for (int j = 0; j < TN; ++j) {
            if (NT) {
                bfr[j] = *reinterpret_cast<const bf16x8*>(brd + st * STG + j * 32 * BROW + ks * 16);
            } else {
                const u16* pB = brd + st * STG + ks * 16 * BROW + j * 32;
                s16x4 lo = tr_read(pB);
                s16x4 hi4 = tr_read(pB + 4 * BROW);
                bfr[j] = bf16x8{lo[0], lo[1], lo[2], lo[3], hi4[0], hi4[1], hi4[2], hi4[3]};
            }
        }
#pragma unroll
        for (int i = 0; i < TM; ++i)
#pragma unroll
            for (int j = 0; j < TN; ++j) acc[i][j] = MFMA32(bfr[j], af[i], acc[i][j]);
    };
    load(ra0, rb0, 0);
    load(ra1, rb1, 1);
    __syncthreads();
    store(ra0, rb0, 0);
    load(ra0, rb0, 2);
    __syncthreads();
    for (int kt = 0; kt + 1 < nk; kt += 2) {
        compute(0, 0);
        compute(0, 1);
        store(ra1, rb1, 1);
        load(ra1, rb1, min(kt + 3, nk - 1));
        compute(0, 2);
        compute(0, 3);
        __syncthreads();
        compute(1, 0);
        compute(1, 1);
        store(ra0, rb0, 0);
        load(ra0, rb0, min(kt + 4, nk - 1));
        compute(1, 2);
        compute(1, 3);
        __syncthreads();
    }
    if (nk & 1) {
        compute(0, 0);
        compute(0, 1);
        compute(0, 2);
        compute(0, 3);
        __syncthreads();
    }
    constexpr int CROW = BN + 8;
#pragma unroll
    for (int i = 0; i < TM; ++i)
#pragma unroll
        for (int j = 0; j < TN; ++j)
#pragma unroll
            for (int rr = 0; rr < 4; ++rr) {
                u32x2 w;
                w[0] = pack2(acc[i][j][4 * rr], acc[i][j][4 * rr + 1]);
                w[1] = pack2(acc[i][j][4 * rr + 2], acc[i][j][4 * rr + 3]);
                *reinterpret_cast<u32x2*>(smem + (wm * (BM / 2) + 32 * i + r32) * CROW + wn * (BN / 2) + 32 * j +
                                          8 * rr + 4 * hi) = w;
            }
    __syncthreads();
    int tid2 = threadIdx.x;
    asm volatile("" : "+v"(tid2));
    const unsigned coff = ((unsigned)(tid2 / BPR) * ldc + (tid2 % BPR) * 8) * 2u;
    __amdgpu_buffer_rsrc_t rs = __builtin_amdgcn_make_buffer_rsrc(C, 0, 0x7fffffff, 0x00020000);
#pragma unroll
    for (int i = 0; i < BM * BN / 2048; ++i) {
        int row = tid2 / BPR + BRS * i, cc = tid2 % BPR;
        u32x4 v = *reinterpret_cast<const u32x4*>(smem + row * CROW + cc * 8);
        __builtin_amdgcn_raw_buffer_store_b128(v, rs, coff + (unsigned)(BRS * i * ldc) * 2u, 0, SC1 ? 16 : 0);
    }
}

struct MegaParams {
    const u16* A_x;
    const u16* B_x;
    const u16* B_ckv;
    const u16* B_wq;
    const u16* B_wuk;
    const u16* B_wuv;
    const u16* B_wo;
    u16* E;
    u16* Qb;
    u16* Kb;
    u16* Vb;
    u16* OB;
    u16* out;
    unsigned* ctl;
};
constexpr int NWORK = 256 - NREC;
constexpr int CTL_Q1 = 0, CTL_C1 = 64, CTL_Q2 = 128, CTL_C2 = 192, CTL_Q3 = 256, CTL_Q4 = 320, CTL_WORDS = 384;
constexpr unsigned WSPIN_CAP = 2000000u;

__device__ __forceinline__ int next_ticket(unsigned* q, int* slot) {
    __syncthreads();
    if (threadIdx.x == 0) *slot = (int)__hip_atomic_fetch_add(q, 1u, __ATOMIC_RELAXED, __HIP_MEMORY_SCOPE_AGENT);
    __syncthreads();
    return *slot;
}
__device__ __forceinline__ void publish_count(unsigned* c) {
    asm volatile("s_waitcnt vmcnt(0)" ::: "memory");
    __syncthreads();
    if (threadIdx.x == 0) __hip_atomic_fetch_add(c, 1u, __ATOMIC_RELAXED, __HIP_MEMORY_SCOPE_AGENT);
}
__device__ __forceinline__ void wait_count(unsigned* c, unsigned target) {
    if (threadIdx.x == 0) {
        for (unsigned spin = 0; spin < WSPIN_CAP; ++spin) {
            if (__hip_atomic_load(c, __ATOMIC_RELAXED, __HIP_MEMORY_SCOPE_AGENT) >= target) break;
            __builtin_amdgcn_s_sleep(16);
        }
        __builtin_amdgcn_fence(__ATOMIC_ACQUIRE, "agent");
        asm volatile("s_waitcnt vmcnt(0)" ::: "memory");
    }
    __syncthreads();
}

__device__ __forceinline__ void postA_task(const RecParams& p, const MegaParams& m, int s, u16* smem) {
    u16* KL = smem;
    u16* PL = smem + 40 * MROW;
    int tid = threadIdx.x;
    asm volatile("" : "+v"(tid));
    const int lane = tid & 63, wid = __builtin_amdgcn_readfirstlane(tid >> 6);
    const int r32 = lane & 31, hi = lane >> 5;
    __syncthreads();
    for (int c = tid; c < 32 * 56; c += 256) {
        int row = c / 56, cc = c - row * 56;
        *reinterpret_cast<u32x4*>(KL + (8 + row) * MROW + cc * 8) =
            *reinterpret_cast<const u32x4*>(m.A_x + (long)(s * 32 + row) * DM + cc * 8);
    }
    {
        const u64* gf = p.G2 + (long)(s + 1) * G2_STEP + 8 * (lane < NC_BLK ? lane : 0);
        for (unsigned spin = 0; spin < WSPIN_CAP; ++spin) {
            u64 x = ld_gran(gf);
            if (__all((unsigned)(x >> 32) == (unsigned)(s + 1))) break;
            __builtin_amdgcn_s_sleep(8);
        }
        unsigned v[7];
        if (s == 0) {
#pragma unroll
            for (int k = 0; k < 7; ++k) v[k] = *reinterpret_cast<const unsigned*>(p.mem0 + 2 * (k * 256 + tid));
        } else {
            const u64* g0 = p.G2 + (long)s * G2_STEP + tid;
            for (unsigned spin = 0; spin < WSPIN_CAP; ++spin) {
                bool ok = true;
#pragma unroll
                for (int k = 0; k < 7; ++k) {
                    u64 x = ld_gran(g0 + k * 256);
                    v[k] = (unsigned)x;
                    ok &= ((unsigned)(x >> 32) == (unsigned)s);
                }
                if (__all(ok)) break;
                __builtin_amdgcn_s_sleep(8);
            }
        }
#pragma unroll
        for (int k = 0; k < 7; ++k) {
            int gi = k * 256 + tid;
            int j = gi / 224, ip = gi - j * 224;
            *reinterpret_cast<unsigned*>(KL + j * MROW + 2 * ip) = v[k];
        }
    }
    __syncthreads();
    const u16* k0l = KL + r32 * MROW + hi * 8;
    const u16* k1l = KL + (32 + (r32 & 7)) * MROW + hi * 8;
    const float L2E = 1.4426950408889634f;
#pragma unroll 1
    for (int hh = 0; hh < 4; ++hh) {
        const int h = wid * 4 + hh;
        const u16* ep = m.E + (long)(s * 32 + r32) * (H_ * DM) + h * DM + hi * 8;
        f32x16 s0 = f32x16{}, s1 = f32x16{};
#pragma unroll
        for (int ks = 0; ks < 28; ++ks) {
            bf16x8 ef = *reinterpret_cast<const bf16x8*>(ep + ks * 16);
            bf16x8 ka = *reinterpret_cast<const bf16x8*>(k0l + ks * 16);
            bf16x8 kb = *reinterpret_cast<const bf16x8*>(k1l + ks * 16);
            s0 = MFMA32(ka, ef, s0);
            s1 = MFMA32(kb, ef, s1);
        }
#pragma unroll
        for (int r = 0; r < 16; ++r) {
            int kk = crow(r, hi);
            if (kk >= 8 && kk - 8 > r32) s0[r] = -INFINITY;
            int k2 = kk + 32;
            if (k2 >= 40 || k2 - 8 > r32) s1[r] = -INFINITY;
        }
        float mx = -INFINITY;
#pragma unroll
        for (int r = 0; r < 16; ++r) mx = fmaxf(mx, fmaxf(s0[r], s1[r]));
        mx = fmaxf(mx, __shfl_xor(mx, 32));
        float nm = -mx * L2E, psum = 0.f;
#pragma unroll
        for (int r = 0; r < 16; ++r) {
            s0[r] = __builtin_amdgcn_exp2f(fmaf(s0[r], L2E, nm));
            s1[r] = __builtin_amdgcn_exp2f(fmaf(s1[r], L2E, nm));
            psum += s0[r] + s1[r];
        }
        psum += __shfl_xor(psum, 32);
        float inv = 1.f / psum;
        u32x4 a0, a1, a2;
#pragma unroll
        for (int j = 0; j < 4; ++j) {
            a0[j] = pack2(s0[2 * j] * inv, s0[2 * j + 1] * inv);
            a1[j] = pack2(s0[8 + 2 * j] * inv, s0[8 + 2 * j + 1] * inv);
            a2[j] = pack2(s1[2 * j] * inv, s1[2 * j + 1] * inv);
        }
        u16* pw = PL + (h * 3 * 64 + lane) * 8;
        *reinterpret_cast<u32x4*>(pw) = a0;
        *reinterpret_cast<u32x4*>(pw + 512) = a1;
        *reinterpret_cast<u32x4*>(pw + 1024) = a2;
    }
    __syncthreads();
#pragma unroll 1
    for (int ct = wid; ct < 14; ct += 4) {
        const int col = ct * 32 + r32;
        f32x16 o = f32x16{};
#pragma unroll 4
        for (int h = 0; h < 16; ++h) {
            const u64* up = reinterpret_cast<const u64*>(p.Ut + (((long)s * 16 + h) * DM + col) * 8 + 4 * hi);
            const u16* dp = p.Dt + ((long)h * DM + col) * S_ + s * 32 + 4 * hi;
            u64 u = ld_gran(up);
            u32x2 d0 = *reinterpret_cast<const u32x2*>(dp);
            u32x2 d1 = *reinterpret_cast<const u32x2*>(dp + 8);
            u32x2 d2 = *reinterpret_cast<const u32x2*>(dp + 16);
            u32x2 d3 = *reinterpret_cast<const u32x2*>(dp + 24);
            const u16* pr = PL + (h * 3 * 64 + lane) * 8;
            bf16x8 p0 = *reinterpret_cast<const bf16x8*>(pr);
            bf16x8 p1 = *reinterpret_cast<const bf16x8*>(pr + 512);
            bf16x8 p2 = *reinterpret_cast<const bf16x8*>(pr + 1024);
            o = MFMA32(__builtin_bit_cast(bf16x8, (u32x4{(unsigned)u, (unsigned)(u >> 32), d0[0], d0[1]})), p0, o);
            o = MFMA32(__builtin_bit_cast(bf16x8, (u32x4{d1[0], d1[1], d2[0], d2[1]})), p1, o);
            o = MFMA32(__builtin_bit_cast(bf16x8, (u32x4{d3[0], d3[1], 0u, 0u})), p2, o);
        }
#pragma unroll
        for (int rr = 0; rr < 4; ++rr) {
            u32x2 w;
            w[0] = pack2(o[4 * rr], o[4 * rr + 1]);
            w[1] = pack2(o[4 * rr + 2], o[4 * rr + 3]);
            *reinterpret_cast<u32x2*>(m.out + (long)(s * 32 + r32) * 896 + ct * 32 + 8 * rr + 4 * hi) = w;
        }
    }
}

__device__ __forceinline__ void worker_body(const RecParams& p, const MegaParams& m, u16* smem, int* slot) {
    unsigned* ctl = m.ctl;
    WTS(0);
    for (;;) {
        int t = next_ticket(ctl + CTL_Q1, slot);
        if (t >= 768 + 448) break;
        if (t < 768) {
            int mat = t >> 8, rb = (t >> 5) & 7, cb = t & 31;
            const u16* A = (mat < 2) ? m.B_ckv : m.B_x;
            const u16* B = (mat == 0) ? m.B_wuk : (mat == 1 ? m.B_wuv : m.B_wq);
            u16* C = (mat == 0) ? m.Kb : (mat == 1 ? m.Vb : m.Qb);
            int lda = (mat < 2) ? DC : DM;
            gemm_tile<256, 128, false, true>(A + (long)rb * 256 * lda, lda, B + cb * 128, HD,
                                             C + (long)rb * 256 * HD + cb * 128, HD, lda / 64, smem);
        } else {
            int u = t - 768, rb = u / 56, cb = u - rb * 56;
            gemm_tile<256, 128, true, true>(m.A_x + (long)rb * 256 * DM, DM, p.AmatT + (long)cb * 128 * DM, DM,
                                            m.E + (long)rb * 256 * (H_ * DM) + cb * 128, H_ * DM, DM / 64, smem);
        }
    }
    publish_count(ctl + CTL_C1);
    WTS(1);
    wait_count(ctl + CTL_C1, NWORK);
    WTS(2);
    for (;;) {
        int t = next_ticket(ctl + CTL_Q2, slot);
        if (t >= 256) break;
        attnB_body(m.Qb, m.Kb, m.Vb, m.OB, smem, 15 - (t >> 4), t & 15);
    }
    publish_count(ctl + CTL_C2);
    WTS(3);
    wait_count(ctl + CTL_C2, NWORK);
    WTS(4);
    for (;;) {
        int t = next_ticket(ctl + CTL_Q3, slot);
        if (t >= 112) break;
        int rb = t / 7, cb = t - rb * 7;
        gemm_tile<128, 64, false, false>(m.OB + (long)rb * 128 * HD, HD, m.B_wo + cb * 64, DM,
                                         m.out + (long)rb * 128 * 896 + 448 + cb * 64, 896, HD / 64, smem);
    }
    WTS(5);
    for (;;) {
        int t = next_ticket(ctl + CTL_Q4, slot);
        if (t >= NSEG) break;
        postA_task(p, m, t, smem);
    }
    WTS(6);
    WTS(7);
}

constexpr int REC_LDS = 16 * MROW * 2 + 4 * 320 * 4 + 4 * 14 * 1024;
constexpr int GEMM_LDS = 2 * (256 * 72 + 64 * 160) * 2;
constexpr int POST_LDS = (40 * MROW + 16 * 3 * 64 * 8) * 2;
constexpr int cmax(int a, int b) { return a > b ? a : b; }
constexpr int FUSED_LDS = cmax(cmax(ATTN_LDS, REC_LDS), cmax(GEMM_LDS, POST_LDS));

__global__ __launch_bounds__(256) void mega_kernel(RecParams p, MegaParams m) {
    __shared__ __attribute__((aligned(16))) u16 smem[FUSED_LDS / 2 + 8];
    const int b = blockIdx.x;
    if (b < NA_BLK) {
        rec_A_body(p, smem, b);
    } else if (b < NREC) {
        rec_C_body(p, smem, b - NA_BLK);
    } else {
        worker_body(p, m, smem, reinterpret_cast<int*>(smem + FUSED_LDS / 2));
    }
}

template <int BN, bool NT, bool TO = false>
static void launch_gemm(hipStream_t st, const u16* A, const u16* B, u16* C, int lda, int ldb, int ldc, long sA,
                        long sB, long sC, int M, int N, int K, int batch, float scale) {
    dim3 grid(N / BN, (M + 127) / 128, batch);
    hipLaunchKernelGGL((gemm_kernel<BN, NT, TO>), grid, dim3(256), 0, st, A, B, C, lda, ldb, ldc, sA, sB, sC, M, N, K, scale);
}

extern "C" void kernel_launch(void* const* d_in, const int* in_sizes, int n_in, void* d_out, int out_size,
                              void* d_ws, size_t ws_size, hipStream_t stream) {
    const u16* A_x = (const u16*)d_in[0];
    const u16* A_mem0 = (const u16*)d_in[1];
    const u16* A_Wq = (const u16*)d_in[2];
    const u16* A_Wk = (const u16*)d_in[3];
    const u16* A_Wv = (const u16*)d_in[4];
    const u16* A_Wo = (const u16*)d_in[5];
    const u16* B_x = (const u16*)d_in[7];
    const u16* B_ckv = (const u16*)d_in[8];
    const u16* B_wq = (const u16*)d_in[9];
    const u16* B_wuk = (const u16*)d_in[10];
    const u16* B_wuv = (const u16*)d_in[11];
    const u16* B_wo = (const u16*)d_in[12];
    u16* out = (u16*)d_out;

    char* ws = (char*)d_ws;
    const size_t SZ = (size_t)S_ * HD * 2;
    u16* Qb = (u16*)(ws + 0 * SZ);
    u16* Kb = (u16*)(ws + 1 * SZ);
    u16* Vb = (u16*)(ws + 2 * SZ);
    u16* OB = (u16*)(ws + 3 * SZ);
    size_t off = 4 * SZ;
    u16* AmatT = (u16*)(ws + off); off += (size_t)H_ * DM * DM * 2;
    u16* CmatT = (u16*)(ws + off); off += (size_t)H_ * DM * DM * 2;
    u16* Bt = (u16*)(ws + off); off += (size_t)H_ * DM * S_ * 2;
    u16* Dt = (u16*)(ws + off); off += (size_t)H_ * DM * S_ * 2;
    u16* E = (u16*)(ws + off); off += (size_t)H_ * DM * S_ * 2;
    u16* Ut = (u16*)(ws + off); off += (size_t)NSEG * H_ * DM * 8 * 2;
    char* gbase = ws + off;
    u64* G1 = (u64*)gbase;
    u64* G2 = G1 + 2 * G1_STEP;
    unsigned* ctl = (unsigned*)(G2 + 65 * G2_STEP);
    size_t gbytes = (size_t)(2 * G1_STEP + 65 * G2_STEP) * 8 + (size_t)CTL_WORDS * 4;
    off += gbytes;

    hipMemsetAsync(gbase, 0, gbytes, stream);
    launch_gemm<64, true>(stream, A_Wk, A_Wq, AmatT, HD, HD, DM, DH, DH, (long)DM * DM, DM, DM, DH, H_, 0.0625f);
    launch_gemm<64, false, true>(stream, A_Wv, A_Wo, CmatT, HD, DM, DM, DH, (long)DH * DM, (long)DM * DM, DM, DM, DH, H_, 1.f);
    launch_gemm<64, false>(stream, A_x, AmatT, Bt, DM, DM, DM, 0, (long)DM * DM, (long)DM * S_, S_, DM, DM, H_, 1.f);
    launch_gemm<128, true>(stream, CmatT, A_x, Dt, DM, DM, S_, 0, 0, 0, H_ * DM, S_, DM, 1, 1.f);
    RecParams rp;
    rp.mem0 = A_mem0; rp.AmatT = AmatT; rp.CmatT = CmatT; rp.Bt = Bt; rp.Dt = Dt; rp.Ut = Ut; rp.G1 = G1; rp.G2 = G2;
    MegaParams mp;
    mp.A_x = A_x;
    mp.B_x = B_x; mp.B_ckv = B_ckv; mp.B_wq = B_wq; mp.B_wuk = B_wuk; mp.B_wuv = B_wuv; mp.B_wo = B_wo;
    mp.E = E; mp.Qb = Qb; mp.Kb = Kb; mp.Vb = Vb; mp.OB = OB;
    mp.out = out; mp.ctl = ctl;
    hipLaunchKernelGGL(mega_kernel, dim3(256), dim3(256), 0, stream, rp, mp);
}
```

```cpp
#include <hip/hip_runtime.h>
#include <hip/hip_bf16.h>
#include <stdint.h>

typedef unsigned short u16;
typedef __attribute__((ext_vector_type(8))) short bf16x8;
typedef __attribute__((ext_vector_type(4))) short s16x4;
typedef __attribute__((ext_vector_type(16))) float f32x16;
typedef __attribute__((ext_vector_type(4))) float f32x4;
typedef __attribute__((ext_vector_type(4))) unsigned u32x4;
typedef __attribute__((ext_vector_type(2))) unsigned u32x2;
typedef short v4i16_t __attribute__((ext_vector_type(4)));

#define LAS __attribute__((address_space(3)))

__device__ __forceinline__ float bf2f(u16 x) { return __uint_as_float(((unsigned)x) << 16); }
__device__ __forceinline__ u16 f2bf(float f) {
    unsigned u = __float_as_uint(f);
    u += 0x7fffu + ((u >> 16) & 1u);
    return (u16)(u >> 16);
}
typedef __bf16 bf16x2_t __attribute__((ext_vector_type(2)));
__device__ __forceinline__ unsigned pack2(float lo, float hi) {
    bf16x2_t r = {(__bf16)lo, (__bf16)hi};
    return __builtin_bit_cast(unsigned, r);
}
__device__ __forceinline__ s16x4 tr_read(const void* p) {
    return __builtin_bit_cast(s16x4, __builtin_amdgcn_ds_read_tr16_b64_v4i16((LAS v4i16_t*)p));
}
#define MFMA32(a, b, c) __builtin_amdgcn_mfma_f32_32x32x16_bf16(a, b, c, 0, 0, 0)
#define MFMA16(a, b, c) __builtin_amdgcn_mfma_f32_16x16x32_bf16(a, b, c, 0, 0, 0)

__device__ __forceinline__ int crow(int r, int hi) { return (r & 3) + 8 * (r >> 2) + 4 * hi; }

constexpr int S_ = 2048, DM = 448, H_ = 16, DH = 256, HD = 4096, MM = 8, SEG = 32, NSEG = 64, DC = 1024;

template <int BN, bool NT, bool TO>
__global__ __launch_bounds__(256) void gemm_kernel(const u16* __restrict__ A, const u16* __restrict__ B,
                                                   u16* __restrict__ C, int lda, int ldb, int ldc,
                                                   long sA, long sB, long sC, int M, int N, int K,
                                                   float scale) {
    constexpr int BM = 128, BK = 64;
    constexpr int AROW = 72;
    constexpr int BROW = NT ? 72 : (BN + 32);
    constexpr int BLDS = NT ? BN * 72 : 64 * (BN + 32);
    __shared__ __attribute__((aligned(16))) u16 smem[BM * AROW + BLDS];
    u16* As = smem;
    u16* Bs = smem + BM * AROW;
    const int tid = threadIdx.x, lane = tid & 63, wid = tid >> 6;
    const int wm = wid >> 1, wn = wid & 1;
    const int m0 = blockIdx.y * BM, n0 = blockIdx.x * BN;
    A += (long)blockIdx.z * sA;
    B += (long)blockIdx.z * sB;
    C += (long)blockIdx.z * sC;
    constexpr int NTI = BN / 64;
    f32x16 acc[2][NTI];
#pragma unroll
    for (int i = 0; i < 2; ++i)
#pragma unroll
        for (int j = 0; j < NTI; ++j) acc[i][j] = f32x16{};

    constexpr int BCH = BN / 32;
    u32x4 ra[4], rb[BCH];
    auto load_tiles = [&](int k0) {
#pragma unroll
        for (int i = 0; i < 4; ++i) {
            int c = tid + 256 * i, row = c >> 3, kc = c & 7;
            int gr = m0 + row;
            if (gr < M)
                ra[i] = *reinterpret_cast<const u32x4*>(A + (long)gr * lda + k0 + kc * 8);
            else
                ra[i] = u32x4{0, 0, 0, 0};
        }
#pragma unroll
        for (int i = 0; i < BCH; ++i) {
            int c = tid + 256 * i;
            if (NT) {
                int row = c >> 3, kc = c & 7;
                rb[i] = *reinterpret_cast<const u32x4*>(B + (long)(n0 + row) * ldb + k0 + kc * 8);
            } else {
                int krow = c / (BN / 8), nc = c % (BN / 8);
                rb[i] = *reinterpret_cast<const u32x4*>(B + (long)(k0 + krow) * ldb + n0 + nc * 8);
            }
        }
    };
    auto store_tiles = [&]() {
#pragma unroll
        for (int i = 0; i < 4; ++i) {
            int c = tid + 256 * i, row = c >> 3, kc = c & 7;
            *reinterpret_cast<u32x4*>(As + row * AROW + kc * 8) = ra[i];
        }
#pragma unroll
        for (int i = 0; i < BCH; ++i) {
            int c = tid + 256 * i;
            if (NT) {
                int row = c >> 3, kc = c & 7;
                *reinterpret_cast<u32x4*>(Bs + row * BROW + kc * 8) = rb[i];
            } else {
                int krow = c / (BN / 8), nc = c % (BN / 8);
                *reinterpret_cast<u32x4*>(Bs + krow * BROW + nc * 8) = rb[i];
            }
        }
    };
    const int r32 = lane & 31, hi = lane >> 5, g = lane >> 4, q4 = (lane & 15) >> 2, p4 = lane & 3;
    load_tiles(0);
    const int nk = K / BK;
    for (int kt = 0; kt < nk; ++kt) {
        __syncthreads();
        store_tiles();
        __syncthreads();
        if (kt + 1 < nk) load_tiles((kt + 1) * BK);
#pragma unroll
        for (int ks = 0; ks < 4; ++ks) {
            bf16x8 af[2], bfr[NTI];
#pragma unroll
            for (int i = 0; i < 2; ++i)
                af[i] = *reinterpret_cast<const bf16x8*>(As + (wm * 64 + i * 32 + r32) * AROW + ks * 16 + hi * 8);
#pragma unroll
            for (int j = 0; j < NTI; ++j) {
                int nb = wn * (BN / 2) + j * 32;
                if (NT) {
                    bfr[j] = *reinterpret_cast<const bf16x8*>(Bs + (nb + r32) * BROW + ks * 16 + hi * 8);
                } else {
                    const u16* pB = Bs + (ks * 16 + 8 * (g >> 1) + q4) * BROW + nb + 16 * (g & 1) + 4 * p4;
                    s16x4 lo = tr_read(pB);
                    s16x4 hi4 = tr_read(pB + 4 * BROW);
                    bfr[j] = bf16x8{lo[0], lo[1], lo[2], lo[3], hi4[0], hi4[1], hi4[2], hi4[3]};
                }
            }
#pragma unroll
            for (int i = 0; i < 2; ++i)
#pragma unroll
                for (int j = 0; j < NTI; ++j) acc[i][j] = MFMA32(af[i], bfr[j], acc[i][j]);
        }
    }
#pragma unroll
    for (int i = 0; i < 2; ++i)
#pragma unroll
        for (int j = 0; j < NTI; ++j) {
            int col = n0 + wn * (BN / 2) + j * 32 + r32;
            if (TO) {
#pragma unroll
                for (int rr = 0; rr < 4; ++rr) {
                    int row = m0 + wm * 64 + i * 32 + 8 * rr + 4 * hi;
                    u32x2 w;
                    w[0] = pack2(acc[i][j][4 * rr] * scale, acc[i][j][4 * rr + 1] * scale);
                    w[1] = pack2(acc[i][j][4 * rr + 2] * scale, acc[i][j][4 * rr + 3] * scale);
                    if (row < M) *reinterpret_cast<u32x2*>(C + (long)col * ldc + row) = w;
                }
            } else {
#pragma unroll
                for (int r = 0; r < 16; ++r) {
                    int row = m0 + wm * 64 + i * 32 + crow(r, hi);
                    if (row < M) C[(long)row * ldc + col] = f2bf(acc[i][j][r] * scale);
                }
            }
        }
}

constexpr int KROW = 264;
constexpr int VROW = 288;
constexpr int ATTN_LDS = (64 * KROW + 64 * VROW) * 2;
__device__ __forceinline__ void attnB_body(const u16* __restrict__ Q, const u16* __restrict__ K,
                                           const u16* __restrict__ V, u16* __restrict__ O, u16* smem, int qb, int h) {
    u16* Ks = smem;
    u16* Vs = smem + 64 * KROW;
    int tid = threadIdx.x;
    asm volatile("" : "+v"(tid));
    const int lane = tid & 63, wid = tid >> 6;
    const int q0 = qb * 128;
    const int r32 = lane & 31, hi = lane >> 5, g = lane >> 4, q4 = (lane & 15) >> 2, p4 = lane & 3;
    const int qrow = q0 + wid * 32 + r32;
    bf16x8 qf[16];
#pragma unroll
    for (int ks = 0; ks < 16; ++ks)
        qf[ks] = *reinterpret_cast<const bf16x8*>(Q + (long)qrow * HD + h * DH + ks * 16 + hi * 8);
    f32x16 o[8];
#pragma unroll
    for (int i = 0; i < 8; ++i) o[i] = f32x16{};
    float mrun = -INFINITY, lrun = 0.f;
    const float c2 = 0.0625f * 1.4426950408889634f;
    const int nt = (q0 + 128) / 64;
    u32x4 rk[8], rv[8];
    auto load_kv = [&](int t) {
#pragma unroll
        for (int i = 0; i < 8; ++i) {
            int c = tid + 256 * i, row = c >> 5, cc = c & 31;
            long off = (long)(t * 64 + row) * HD + h * DH + cc * 8;
            rk[i] = *reinterpret_cast<const u32x4*>(K + off);
            rv[i] = *reinterpret_cast<const u32x4*>(V + off);
        }
    };
    auto store_kv = [&]() {
#pragma unroll
        for (int i = 0; i < 8; ++i) {
            int c = tid + 256 * i, row = c >> 5, cc = c & 31;
            *reinterpret_cast<u32x4*>(Ks + row * KROW + cc * 8) = rk[i];
            *reinterpret_cast<u32x4*>(Vs + row * VROW + cc * 8) = rv[i];
        }
    };
    load_kv(0);
    for (int t = 0; t < nt; ++t) {
        __syncthreads();
        store_kv();
        __syncthreads();
        if (t + 1 < nt) load_kv(t + 1);
        const int kbase = t * 64;
        if (kbase <= q0 + wid * 32 + 31) {
            f32x16 s0 = f32x16{}, s1 = f32x16{};
#pragma unroll
            for (int ks = 0; ks < 16; ++ks) {
                bf16x8 k0 = *reinterpret_cast<const bf16x8*>(Ks + r32 * KROW + ks * 16 + hi * 8);
                bf16x8 k1 = *reinterpret_cast<const bf16x8*>(Ks + (32 + r32) * KROW + ks * 16 + hi * 8);
                s0 = MFMA32(k0, qf[ks], s0);
                s1 = MFMA32(k1, qf[ks], s1);
                if ((ks & 3) == 3) __builtin_amdgcn_sched_barrier(0);
            }
            if (kbase + 63 > q0 + wid * 32) {
#pragma unroll
                for (int r = 0; r < 16; ++r) {
                    int kk = kbase + crow(r, hi);
                    if (kk > qrow) s0[r] = -INFINITY;
                    if (kk + 32 > qrow) s1[r] = -INFINITY;
                }
            }
            float mx = -INFINITY;
#pragma unroll
            for (int r = 0; r < 16; ++r) mx = fmaxf(mx, fmaxf(s0[r], s1[r]));
            mx = fmaxf(mx, __shfl_xor(mx, 32));
            float mnew = fmaxf(mrun, mx);
            float alpha = __builtin_amdgcn_exp2f((mrun - mnew) * c2);
            float nm = -mnew * c2;
            float psum = 0.f;
#pragma unroll
            for (int r = 0; r < 16; ++r) {
                s0[r] = __builtin_amdgcn_exp2f(fmaf(s0[r], c2, nm));
                s1[r] = __builtin_amdgcn_exp2f(fmaf(s1[r], c2, nm));
                psum += s0[r] + s1[r];
            }
            psum += __shfl_xor(psum, 32);
            lrun = lrun * alpha + psum;
            mrun = mnew;
#pragma unroll
            for (int i = 0; i < 8; ++i)
#pragma unroll
                for (int r = 0; r < 16; ++r) o[i][r] *= alpha;
            bf16x8 pf[4];
#pragma unroll
            for (int s = 0; s < 2; ++s) {
                u32x4 a, b;
#pragma unroll
                for (int j = 0; j < 4; ++j) {
                    a[j] = pack2(s0[8 * s + 2 * j], s0[8 * s + 2 * j + 1]);
                    b[j] = pack2(s1[8 * s + 2 * j], s1[8 * s + 2 * j + 1]);
                }
                pf[s] = __builtin_bit_cast(bf16x8, a);
                pf[2 + s] = __builtin_bit_cast(bf16x8, b);
            }
#pragma unroll
            for (int dt = 0; dt < 8; ++dt) {
#pragma unroll
                for (int s = 0; s < 4; ++s) {
                    const u16* pV = Vs + (16 * s + 4 * hi + q4) * VROW + 32 * dt + 16 * (g & 1) + 4 * p4;
                    s16x4 lo = tr_read(pV);
                    s16x4 hi4 = tr_read(pV + 8 * VROW);
                    bf16x8 vf = bf16x8{lo[0], lo[1], lo[2], lo[3], hi4[0], hi4[1], hi4[2], hi4[3]};
                    o[dt] = MFMA32(vf, pf[s], o[dt]);
                }
                __builtin_amdgcn_sched_barrier(0);
            }
        }
    }
    float inv = 1.f / lrun;
#pragma unroll
    for (int dt = 0; dt < 8; ++dt)
#pragma unroll
        for (int rr = 0; rr < 4; ++rr) {
            u32x2 w;
            w[0] = pack2(o[dt][4 * rr] * inv, o[dt][4 * rr + 1] * inv);
            w[1] = pack2(o[dt][4 * rr + 2] * inv, o[dt][4 * rr + 3] * inv);
            __hip_atomic_store(reinterpret_cast<unsigned long long*>(O + (long)qrow * HD + h * DH + 32 * dt + 8 * rr + 4 * hi),
                               ((unsigned long long)w[1] << 32) | w[0], __ATOMIC_RELAXED, __HIP_MEMORY_SCOPE_AGENT);
        }
}

typedef unsigned long long u64;
__device__ __forceinline__ u64 ld_gran(const u64* p) {
    return __hip_atomic_load(p, __ATOMIC_RELAXED, __HIP_MEMORY_SCOPE_AGENT);
}
__device__ __forceinline__ void st_gran(u64* p, unsigned tag, unsigned val) {
    __hip_atomic_store(p, ((u64)tag << 32) | (u64)val, __ATOMIC_RELAXED, __HIP_MEMORY_SCOPE_AGENT);
}
constexpr int NA_BLK = 16, NC_BLK = 28, NREC = NA_BLK + NC_BLK;
constexpr int MROW = 456;
constexpr int G1_STEP = 16 * 160;
constexpr int G2_STEP = 8 * 224;
constexpr unsigned SPIN_CAP = 400000u;

#define WTS(slot)
struct RecParams {
    const u16* mem0;
    const u16* AmatT;
    const u16* CmatT;
    const u16* Bt;
    const u16* Dt;
    u16* Ut;
    u64* G1;
    u64* G2;
};

__device__ __forceinline__ float red32_max(float x) {
    float t;
    asm volatile(
        "s_nop 1\n\tv_max_f32_dpp %0, %0, %0 quad_perm:[1,0,3,2] row_mask:0xf bank_mask:0xf\n\t"
        "s_nop 1\n\tv_max_f32_dpp %0, %0, %0 quad_perm:[2,3,0,1] row_mask:0xf bank_mask:0xf\n\t"
        "s_nop 1\n\tv_max_f32_dpp %0, %0, %0 row_half_mirror row_mask:0xf bank_mask:0xf\n\t"
        "s_nop 1\n\tv_max_f32_dpp %0, %0, %0 row_mirror row_mask:0xf bank_mask:0xf\n\t"
        "v_mov_b32 %1, %0\n\t"
        "s_nop 1\n\tv_permlane16_swap_b32 %0, %1\n\t"
        "s_nop 1\n\tv_max_f32 %0, %0, %1"
        : "+v"(x), "=&v"(t));
    return x;
}
__device__ __forceinline__ float red32_sum(float x) {
    float t;
    asm volatile(
        "s_nop 1\n\tv_add_f32_dpp %0, %0, %0 quad_perm:[1,0,3,2] row_mask:0xf bank_mask:0xf\n\t"
        "s_nop 1\n\tv_add_f32_dpp %0, %0, %0 quad_perm:[2,3,0,1] row_mask:0xf bank_mask:0xf\n\t"
        "s_nop 1\n\tv_add_f32_dpp %0, %0, %0 row_half_mirror row_mask:0xf bank_mask:0xf\n\t"
        "s_nop 1\n\tv_add_f32_dpp %0, %0, %0 row_mirror row_mask:0xf bank_mask:0xf\n\t"
        "v_mov_b32 %1, %0\n\t"
        "s_nop 1\n\tv_permlane16_swap_b32 %0, %1\n\t"
        "s_nop 1\n\tv_add_f32 %0, %0, %1"
        : "+v"(x), "=&v"(t));
    return x;
}

template <int N>
__device__ __forceinline__ void sweep(const u64* const (&g)[N], unsigned tag, unsigned (&v)[N], bool& dead) {
    u64 a[N], b[N];
#pragma unroll
    for (int k = 0; k < N; ++k) a[k] = ld_gran(g[k]);
    for (unsigned spin = 0;; ++spin) {
#pragma unroll
        for (int k = 0; k < N; ++k) b[k] = ld_gran(g[k]);
        bool ok = true;
#pragma unroll
        for (int k = 0; k < N; ++k) ok &= ((unsigned)(a[k] >> 32) == tag);
        if (__all(ok) || dead) {
#pragma unroll
            for (int k = 0; k < N; ++k) v[k] = (unsigned)a[k];
            break;
        }
#pragma unroll
        for (int k = 0; k < N; ++k) a[k] = ld_gran(g[k]);
        ok = true;
#pragma unroll
        for (int k = 0; k < N; ++k) ok &= ((unsigned)(b[k] >> 32) == tag);
        if (__all(ok)) {
#pragma unroll
            for (int k = 0; k < N; ++k) v[k] = (unsigned)b[k];
            break;
        }
        if (spin > SPIN_CAP) dead = true;
    }
}

__device__ __forceinline__ void gather_mem(const RecParams& p, int s, u16* memL, const int (&loff)[7], int wid,
                                           int lane, bool& dead) {
    unsigned v[7];
    if (s == 0) {
#pragma unroll
        for (int k = 0; k < 7; ++k) {
            int gi = wid * 448 + k * 64 + lane;
            v[k] = *reinterpret_cast<const unsigned*>(p.mem0 + 2 * gi);
        }
    } else {
        const u64* g0 = p.G2 + (long)s * G2_STEP + wid * 448 + lane;
        const u64* const g[7] = {g0, g0 + 64, g0 + 128, g0 + 192, g0 + 256, g0 + 320, g0 + 384};
        sweep<7>(g, (unsigned)s, v, dead);
    }
#pragma unroll
    for (int k = 0; k < 7; ++k) *reinterpret_cast<unsigned*>(memL + loff[k]) = v[k];
    __syncthreads();
}

__device__ __forceinline__ void rec_A_body(const RecParams& p, u16* smem, int h) {
    u16* memL = smem;
    float* Sc = reinterpret_cast<float*>(smem + 16 * MROW);
    const int tid = threadIdx.x, lane = tid & 63, wid = __builtin_amdgcn_readfirstlane(tid >> 6);
    const int c16 = lane & 15, g = lane >> 4;
    for (int i = tid; i < 8 * MROW / 2; i += 256) reinterpret_cast<unsigned*>(memL + 8 * MROW)[i] = 0u;
    int loff[7];
#pragma unroll
    for (int k = 0; k < 7; ++k) {
        int gi = wid * 448 + k * 64 + lane;
        int j = gi / 224, ip = gi - j * 224;
        loff[k] = j * MROW + 2 * ip;
    }
    bf16x8 W[6][14];
    u16* WL = reinterpret_cast<u16*>(Sc + 4 * 320) + (wid * 14 * 64 + lane) * 8;
#pragma unroll
    for (int t = 0; t < 7; ++t) {
        const u16* ap = p.AmatT + ((long)h * DM + (7 * wid + t) * 16 + c16) * DM + 8 * g;
#pragma unroll
        for (int ks = 0; ks < 14; ++ks) {
            bf16x8 w = *reinterpret_cast<const bf16x8*>(ap + 32 * ks);
            if (t < 6)
                W[t][ks] = w;
            else
                *reinterpret_cast<bf16x8*>(WL + ks * 512) = w;
        }
    }
    bf16x8 X[2][4];
    int ksq[4];
#pragma unroll
    for (int q = 0; q < 4; ++q) ksq[q] = (wid + 4 * q < 14) ? (wid + 4 * q) : 0;
    const bool q3ok = (wid + 12) < 14;
    const u16* btp = p.Bt + ((long)h * S_ + c16) * DM + 8 * g;
    auto load_X = [&](int s) {
#pragma unroll
        for (int tl = 0; tl < 2; ++tl)
#pragma unroll
            for (int q = 0; q < 4; ++q) {
                bf16x8 w = *reinterpret_cast<const bf16x8*>(btp + (long)(s * 32 + tl * 16) * DM + 32 * ksq[q]);
                if (q == 3 && !q3ok) w = bf16x8{0, 0, 0, 0, 0, 0, 0, 0};
                X[tl][q] = w;
            }
    };
    load_X(0);
    const int srow = 2 * wid + (lane >> 5), sub = lane & 31;
    const bool sact = sub < 20;
    const float* sread = Sc + srow * 40 + (sact ? 2 * sub : 0);
    float* swr_mm = Sc + wid * 320 + (c16 & 7) * 40 + 8 * (g & 1);
    float* swr_x0 = Sc + wid * 320 + (c16 & 7) * 40 + (g == 0 ? 4 : 4 * g + 8);
    float* swr_x1 = Sc + wid * 320 + (c16 & 7) * 40 + 24 + 4 * g;
    const u16* mfp = memL + c16 * MROW + 8 * g;
    const u16* map = memL + c16 * MROW + 7 * wid * 16 + 4 * g;
    bool dead = false;
    for (int s = 0; s < NSEG - 1; ++s) {
        gather_mem(p, s, memL, loff, wid, lane, dead);
        f32x4 acc[7], ax[2];
#pragma unroll
        for (int t = 0; t < 7; ++t) acc[t] = f32x4{0.f, 0.f, 0.f, 0.f};
        ax[0] = f32x4{0.f, 0.f, 0.f, 0.f};
        ax[1] = f32x4{0.f, 0.f, 0.f, 0.f};
        u32x2 ma[7];
#pragma unroll
        for (int t = 0; t < 7; ++t) ma[t] = *reinterpret_cast<const u32x2*>(map + 16 * t);
#pragma unroll
        for (int q = 0; q < 4; ++q) {
            bf16x8 mf = *reinterpret_cast<const bf16x8*>(mfp + 32 * ksq[q]);
            ax[0] = MFMA16(X[0][q], mf, ax[0]);
            ax[1] = MFMA16(X[1][q], mf, ax[1]);
        }
#pragma unroll
        for (int ks = 0; ks < 14; ++ks) {
            bf16x8 mf = *reinterpret_cast<const bf16x8*>(mfp + 32 * ks);
            bf16x8 w6 = *reinterpret_cast<const bf16x8*>(WL + ks * 512);
#pragma unroll
            for (int t = 0; t < 6; ++t) acc[t] = MFMA16(W[t][ks], mf, acc[t]);
            acc[6] = MFMA16(w6, mf, acc[6]);
        }
        if (c16 < 8) {
            *reinterpret_cast<f32x4*>(swr_x0) = ax[0];
            *reinterpret_cast<f32x4*>(swr_x1) = ax[1];
        }
        f32x4 sa = f32x4{0.f, 0.f, 0.f, 0.f}, sb = f32x4{0.f, 0.f, 0.f, 0.f};
#pragma unroll
        for (int pr = 0; pr < 4; ++pr) {
            const int ta = 2 * pr, tb = (pr < 3) ? 2 * pr + 1 : 2 * pr;
            u32x4 av = u32x4{ma[ta][0], ma[ta][1], ma[tb][0], ma[tb][1]};
            u32x4 bv = u32x4{pack2(acc[ta][0], acc[ta][1]), pack2(acc[ta][2], acc[ta][3]), 0u, 0u};
            if (pr < 3) {
                bv[2] = pack2(acc[tb][0], acc[tb][1]);
                bv[3] = pack2(acc[tb][2], acc[tb][3]);
            }
            if (pr & 1)
                sb = MFMA16(__builtin_bit_cast(bf16x8, av), __builtin_bit_cast(bf16x8, bv), sb);
            else
                sa = MFMA16(__builtin_bit_cast(bf16x8, av), __builtin_bit_cast(bf16x8, bv), sa);
        }
        sa += sb;
        if (c16 < 8 && g < 2) *reinterpret_cast<f32x4*>(swr_mm) = sa;
        __syncthreads();
        {
            typedef __attribute__((ext_vector_type(2))) float f32x2;
            f32x2 x0 = *reinterpret_cast<const f32x2*>(sread);
            f32x2 x1 = *reinterpret_cast<const f32x2*>(sread + 320);
            f32x2 x2 = *reinterpret_cast<const f32x2*>(sread + 640);
            f32x2 x3 = *reinterpret_cast<const f32x2*>(sread + 960);
            float a = (x0[0] + x1[0]) + (x2[0] + x3[0]);
            float b = (x0[1] + x1[1]) + (x2[1] + x3[1]);
            float mx = sact ? __builtin_fmaxf(a, b) : -INFINITY;
            mx = red32_max(mx);
            const float L2E = 1.4426950408889634f;
            float nm = -mx * L2E;
            float ea = __builtin_amdgcn_exp2f(__builtin_fmaf(a, L2E, nm));
            float eb = __builtin_amdgcn_exp2f(__builtin_fmaf(b, L2E, nm));
            float sm = sact ? (ea + eb) : 0.f;
            sm = red32_sum(sm);
            float inv = __builtin_amdgcn_rcpf(sm);
            if (sact)
                st_gran(p.G1 + (long)(s & 1) * G1_STEP + h * 160 + srow * 20 + sub, (unsigned)(s + 1),
                        pack2(ea * inv, eb * inv));
        }
        if (s + 1 < NSEG - 1) load_X(s + 1);
    }
}

__device__ __forceinline__ void rec_C_body(const RecParams& p, u16* smem, int c) {
    u16* memL = smem;
    float* red = reinterpret_cast<float*>(smem + 16 * MROW);
    const int tid = threadIdx.x, lane = tid & 63, wid = __builtin_amdgcn_readfirstlane(tid >> 6);
    const int c16 = lane & 15, g = lane >> 4;
    for (int i = tid; i < 8 * MROW / 2; i += 256) reinterpret_cast<unsigned*>(memL + 8 * MROW)[i] = 0u;
    int loff[7];
#pragma unroll
    for (int k = 0; k < 7; ++k) {
        int gi = wid * 448 + k * 64 + lane;
        int j = gi / 224, ip = gi - j * 224;
        loff[k] = j * MROW + 2 * ip;
    }
    const int col = c * 16 + c16;
    bf16x8 W[4][14];
#pragma unroll
    for (int hh = 0; hh < 4; ++hh) {
        const u16* cp = p.CmatT + ((long)(wid * 4 + hh) * DM + col) * DM + 8 * g;
#pragma unroll
        for (int ks = 0; ks < 14; ++ks) W[hh][ks] = *reinterpret_cast<const bf16x8*>(cp + 32 * ks);
    }
    u32x2 dlo[4], dhi[4];
    u32x4 dx;
    const int klo = (g >= 2) ? (8 * (g - 1)) : 0;
    const int khi = (g >= 2) ? (8 * (g - 1) + 4) : 4 * g;
    auto load_D = [&](int s) {
#pragma unroll
        for (int hh = 0; hh < 4; ++hh) {
            const u16* dp = p.Dt + ((long)(wid * 4 + hh) * DM + col) * S_ + s * 32;
            dlo[hh] = *reinterpret_cast<const u32x2*>(dp + klo);
            dhi[hh] = *reinterpret_cast<const u32x2*>(dp + khi);
        }
        dx = *reinterpret_cast<const u32x4*>(p.Dt + ((long)(wid * 4 + g) * DM + col) * S_ + s * 32 + 24);
    };
    load_D(0);
    const u16* mfp = memL + c16 * MROW + 8 * g;
    const int i8 = c16 & 7;
    const int goff = (wid * 4) * 160 + i8 * 20 + 4 * g;
    const int goffx = (wid * 4 + g) * 160 + i8 * 20 + 16;
    float* rwr = red + wid * 128 + (4 * (g & 1)) * 16 + c16;
    const float* rrd = red + (lane >> 3) * 16 + 2 * (lane & 7);
    bool dead = false;
    u64* utp = reinterpret_cast<u64*>(p.Ut + ((long)(wid * 4) * DM + col) * 8 + 4 * (g & 1));
    for (int s = 0; s < NSEG; ++s) {
        gather_mem(p, s, memL, loff, wid, lane, dead);
        f32x4 U[4];
#pragma unroll
        for (int hh = 0; hh < 4; ++hh) U[hh] = f32x4{0.f, 0.f, 0.f, 0.f};
#pragma unroll
        for (int ks = 0; ks < 14; ++ks) {
            bf16x8 mf = *reinterpret_cast<const bf16x8*>(mfp + 32 * ks);
#pragma unroll
            for (int hh = 0; hh < 4; ++hh) U[hh] = MFMA16(mf, W[hh][ks], U[hh]);
        }
        u32x4 bv[4];
#pragma unroll
        for (int hh = 0; hh < 4; ++hh) {
            unsigned u0 = pack2(U[hh][0], U[hh][1]), u1 = pack2(U[hh][2], U[hh][3]);
            bv[hh] = u32x4{(g < 2) ? u0 : dlo[hh][0], (g < 2) ? u1 : dlo[hh][1], dhi[hh][0], dhi[hh][1]};
            if (g < 2)
                __hip_atomic_store(utp + ((long)s * 16 + hh) * (DM * 2), ((u64)u1 << 32) | u0, __ATOMIC_RELAXED,
                                   __HIP_MEMORY_SCOPE_AGENT);
        }
        if (s == NSEG - 1) {
            asm volatile("s_waitcnt vmcnt(0)" ::: "memory");
            __syncthreads();
            if (wid == 0) st_gran(p.G2 + (long)NSEG * G2_STEP + (lane >> 3) * 224 + 8 * c + (lane & 7), (unsigned)NSEG, 0u);
            break;
        }
        u32x4 bx = dx;
        unsigned v[20];
        {
            const u64* g1 = p.G1 + (long)(s & 1) * G1_STEP;
            const u64* q0 = g1 + goff;
            const u64* qx = g1 + goffx;
            const u64* const gp[20] = {q0,       q0 + 1,   q0 + 2,   q0 + 3,   q0 + 160, q0 + 161, q0 + 162,
                                       q0 + 163, q0 + 320, q0 + 321, q0 + 322, q0 + 323, q0 + 480, q0 + 481,
                                       q0 + 482, q0 + 483, qx,       qx + 1,   qx + 2,   qx + 3};
            sweep<20>(gp, (unsigned)(s + 1), v, dead);
        }
        f32x4 o0 = f32x4{0.f, 0.f, 0.f, 0.f}, o1 = f32x4{0.f, 0.f, 0.f, 0.f};
        o0 = MFMA16(__builtin_bit_cast(bf16x8, (u32x4{v[0], v[1], v[2], v[3]})), __builtin_bit_cast(bf16x8, bv[0]), o0);
        o1 = MFMA16(__builtin_bit_cast(bf16x8, (u32x4{v[4], v[5], v[6], v[7]})), __builtin_bit_cast(bf16x8, bv[1]), o1);
        o0 = MFMA16(__builtin_bit_cast(bf16x8, (u32x4{v[8], v[9], v[10], v[11]})), __builtin_bit_cast(bf16x8, bv[2]), o0);
        o1 = MFMA16(__builtin_bit_cast(bf16x8, (u32x4{v[12], v[13], v[14], v[15]})), __builtin_bit_cast(bf16x8, bv[3]), o1);
        o0 = MFMA16(__builtin_bit_cast(bf16x8, (u32x4{v[16], v[17], v[18], v[19]})), __builtin_bit_cast(bf16x8, bx), o0);
        o0 += o1;
        if (g < 2) {
#pragma unroll
            for (int r = 0; r < 4; ++r) rwr[r * 16] = o0[r];
        }
        asm volatile("s_waitcnt vmcnt(0)" ::: "memory");
        __syncthreads();
        if (wid == 0) {
            typedef __attribute__((ext_vector_type(2))) float f32x2;
            f32x2 x0 = *reinterpret_cast<const f32x2*>(rrd);
            f32x2 x1 = *reinterpret_cast<const f32x2*>(rrd + 128);
            f32x2 x2 = *reinterpret_cast<const f32x2*>(rrd + 256);
            f32x2 x3 = *reinterpret_cast<const f32x2*>(rrd + 384);
            unsigned pk = pack2((x0[0] + x1[0]) + (x2[0] + x3[0]), (x0[1] + x1[1]) + (x2[1] + x3[1]));
            const int i = lane >> 3, pp = lane & 7;
            st_gran(p.G2 + (long)(s + 1) * G2_STEP + i * 224 + 8 * c + pp, (unsigned)(s + 1), pk);
        }
        if (s + 1 < NSEG - 1) load_D(s + 1);
    }
}

template <int BM, int BN, bool NT, bool SC1>
__device__ __forceinline__ void gemm_tile(const u16* __restrict__ A, int lda, const u16* __restrict__ B, int ldb,
                                          u16* __restrict__ C, int ldc, int nk, u16* smem) {
    constexpr int AROW = 72, BROW = NT ? 72 : BN + 32;
    constexpr int ASZ = BM * AROW, STG = ASZ + (NT ? BN * 72 : 64 * BROW);
    constexpr int ACH = BM / 32, BCH = BN / 32, TM = BM / 64, TN = BN / 64;
    constexpr int BPR = BN / 8;
    constexpr int BRS = 256 / BPR;
    int tid = threadIdx.x;
    asm volatile("" : "+v"(tid));
    const int lane = tid & 63, wid = tid >> 6;
    const int wm = wid >> 1, wn = wid & 1;
    const int r32 = lane & 31, hi = lane >> 5, g = lane >> 4, q4 = (lane & 15) >> 2, p4 = lane & 3;
    f32x16 acc[TM][TN];
#pragma unroll
    for (int i = 0; i < TM; ++i)
#pragma unroll
        for (int j = 0; j < TN; ++j) acc[i][j] = f32x16{};
    u32x4 ra0[ACH], rb0[BCH], ra1[ACH], rb1[BCH];
    const unsigned aoff = ((unsigned)(tid >> 3) * lda + (tid & 7) * 8) * 2u;
    const unsigned boff = NT ? ((unsigned)(tid >> 3) * ldb + (tid & 7) * 8) * 2u
                             : ((unsigned)(tid / BPR) * ldb + (tid % BPR) * 8) * 2u;
    const char* Ac = reinterpret_cast<const char*>(A);
    const char* Bc = reinterpret_cast<const char*>(B);
    u16* aw = smem + (tid >> 3) * AROW + (tid & 7) * 8;
    u16* bw = NT ? (smem + ASZ + (tid >> 3) * BROW + (tid & 7) * 8) : (smem + ASZ + (tid / BPR) * BROW + (tid % BPR) * 8);
    const u16* ard = smem + (wm * (BM / 2) + r32) * AROW + hi * 8;
    const u16* brd = NT ? (smem + ASZ + (wn * (BN / 2) + r32) * BROW + hi * 8)
                        : (smem + ASZ + (8 * (g >> 1) + q4) * BROW + wn * (BN / 2) + 16 * (g & 1) + 4 * p4);
    auto load = [&](u32x4(&ra)[ACH], u32x4(&rb)[BCH], int kt) {
#pragma unroll
        for (int i = 0; i < ACH; ++i)
            ra[i] = *reinterpret_cast<const u32x4*>(Ac + (aoff + (unsigned)(32 * i * lda + kt * 64) * 2u));
#pragma unroll
        for (int i = 0; i < BCH; ++i) {
            if (NT)
                rb[i] = *reinterpret_cast<const u32x4*>(Bc + (boff + (unsigned)(32 * i * ldb + kt * 64) * 2u));
            else
                rb[i] = *reinterpret_cast<const u32x4*>(Bc + (boff + (unsigned)((kt * 64 + BRS * i) * ldb) * 2u));
        }
    };
    auto store = [&](const u32x4(&ra)[ACH], const u32x4(&rb)[BCH], int st) {
#pragma unroll
        for (int i = 0; i < ACH; ++i) *reinterpret_cast<u32x4*>(aw + st * STG + 32 * i * AROW) = ra[i];
#pragma unroll
        for (int i = 0; i < BCH; ++i)
            *reinterpret_cast<u32x4*>(bw + st * STG + (NT ? 32 : BRS) * i * BROW) = rb[i];
    };
    auto compute = [&](int st, int ks) {
        bf16x8 af[TM], bfr[TN];
#pragma unroll
        for (int i = 0; i < TM; ++i)
            af[i] = *reinterpret_cast<const bf16x8*>(ard + st * STG + i * 32 * AROW + ks * 16);
#pragma unroll
        for (int j = 0; j < TN; ++j) {
            if (NT) {
                bfr[j] = *reinterpret_cast<const bf16x8*>(brd + st * STG + j * 32 * BROW + ks * 16);
            } else {
                const u16* pB = brd + st * STG + ks * 16 * BROW + j * 32;
                s16x4 lo = tr_read(pB);
                s16x4 hi4 = tr_read(pB + 4 * BROW);
                bfr[j] = bf16x8{lo[0], lo[1], lo[2], lo[3], hi4[0], hi4[1], hi4[2], hi4[3]};
            }
        }
#pragma unroll
        for (int i = 0; i < TM; ++i)
#pragma unroll
            for (int j = 0; j < TN; ++j) acc[i][j] = MFMA32(bfr[j], af[i], acc[i][j]);
    };
    load(ra0, rb0, 0);
    load(ra1, rb1, 1);
    __syncthreads();
    store(ra0, rb0, 0);
    load(ra0, rb0, 2);
    __syncthreads();
    for (int kt = 0; kt + 1 < nk; kt += 2) {
        compute(0, 0);
        compute(0, 1);
        store(ra1, rb1, 1);
        load(ra1, rb1, min(kt + 3, nk - 1));
        compute(0, 2);
        compute(0, 3);
        __syncthreads();
        compute(1, 0);
        compute(1, 1);
        store(ra0, rb0, 0);
        load(ra0, rb0, min(kt + 4, nk - 1));
        compute(1, 2);
        compute(1, 3);
        __syncthreads();
    }
    if (nk & 1) {
        compute(0, 0);
        compute(0, 1);
        compute(0, 2);
        compute(0, 3);
        __syncthreads();
    }
    constexpr int CROW = BN + 8;
#pragma unroll
    for (int i = 0; i < TM; ++i)
#pragma unroll
        for (int j = 0; j < TN; ++j)
#pragma unroll
            for (int rr = 0; rr < 4; ++rr) {
                u32x2 w;
                w[0] = pack2(acc[i][j][4 * rr], acc[i][j][4 * rr + 1]);
                w[1] = pack2(acc[i][j][4 * rr + 2], acc[i][j][4 * rr + 3]);
                *reinterpret_cast<u32x2*>(smem + (wm * (BM / 2) + 32 * i + r32) * CROW + wn * (BN / 2) + 32 * j +
                                          8 * rr + 4 * hi) = w;
            }
    __syncthreads();
    int tid2 = threadIdx.x;
    asm volatile("" : "+v"(tid2));
    const unsigned coff = ((unsigned)(tid2 / BPR) * ldc + (tid2 % BPR) * 8) * 2u;
    __amdgpu_buffer_rsrc_t rs = __builtin_amdgcn_make_buffer_rsrc(C, 0, 0x7fffffff, 0x00020000);
#pragma unroll
    for (int i = 0; i < BM * BN / 2048; ++i) {
        int row = tid2 / BPR + BRS * i, cc = tid2 % BPR;
        u32x4 v = *reinterpret_cast<const u32x4*>(smem + row * CROW + cc * 8);
        __builtin_amdgcn_raw_buffer_store_b128(v, rs, coff + (unsigned)(BRS * i * ldc) * 2u, 0, SC1 ? 16 : 0);
    }
}

struct MegaParams {
    const u16* A_x;
    const u16* B_x;
    const u16* B_ckv;
    const u16* B_wq;
    const u16* B_wuk;
    const u16* B_wuv;
    const u16* B_wo;
    u16* E;
    u16* Qb;
    u16* Kb;
    u16* Vb;
    u16* OB;
    u16* out;
    unsigned* ctl;
};
constexpr int NWORK = 256 - NREC;
constexpr int CTL_Q1 = 0, CTL_C1 = 64, CTL_Q2 = 128, CTL_C2 = 192, CTL_Q3 = 256, CTL_Q4 = 320, CTL_WORDS = 384;
constexpr unsigned WSPIN_CAP = 2000000u;

__device__ __forceinline__ int next_ticket(unsigned* q, int* slot) {
    __syncthreads();
    if (threadIdx.x == 0) *slot = (int)__hip_atomic_fetch_add(q, 1u, __ATOMIC_RELAXED, __HIP_MEMORY_SCOPE_AGENT);
    __syncthreads();
    return *slot;
}
__device__ __forceinline__ void publish_count(unsigned* c) {
    asm volatile("s_waitcnt vmcnt(0)" ::: "memory");
    __syncthreads();
    if (threadIdx.x == 0) __hip_atomic_fetch_add(c, 1u, __ATOMIC_RELAXED, __HIP_MEMORY_SCOPE_AGENT);
}
__device__ __forceinline__ void wait_count(unsigned* c, unsigned target) {
    if (threadIdx.x == 0) {
        for (unsigned spin = 0; spin < WSPIN_CAP; ++spin) {
            if (__hip_atomic_load(c, __ATOMIC_RELAXED, __HIP_MEMORY_SCOPE_AGENT) >= target) break;
            __builtin_amdgcn_s_sleep(16);
        }
        __builtin_amdgcn_fence(__ATOMIC_ACQUIRE, "agent");
        asm volatile("s_waitcnt vmcnt(0)" ::: "memory");
    }
    __syncthreads();
}

__device__ __forceinline__ void postA_task(const RecParams& p, const MegaParams& m, int s, int part, u16* smem) {
    u16* KL = smem;
    u16* PL = smem + 40 * MROW;
    int tid = threadIdx.x;
    asm volatile("" : "+v"(tid));
    const int lane = tid & 63, wid = __builtin_amdgcn_readfirstlane(tid >> 6);
    const int r32 = lane & 31, hi = lane >> 5;
    const u16* ep = m.E + (long)(s * 32 + r32) * (H_ * DM) + wid * 4 * DM + hi * 8;
    bf16x8 e0[28], e1[28];
    auto loadE = [&](bf16x8(&e)[28], int hh) {
#pragma unroll
        for (int ks = 0; ks < 28; ++ks) e[ks] = *reinterpret_cast<const bf16x8*>(ep + hh * DM + ks * 16);
    };
    loadE(e0, 0);
    loadE(e1, 1);
    __syncthreads();
    for (int c = tid; c < 32 * 56; c += 256) {
        int row = c / 56, cc = c - row * 56;
        *reinterpret_cast<u32x4*>(KL + (8 + row) * MROW + cc * 8) =
            *reinterpret_cast<const u32x4*>(m.A_x + (long)(s * 32 + row) * DM + cc * 8);
    }
    {
        const u64* gf = p.G2 + (long)(s + 1) * G2_STEP + 8 * (lane < NC_BLK ? lane : 0);
        for (unsigned spin = 0; spin < WSPIN_CAP; ++spin) {
            u64 x = ld_gran(gf);
            if (__all((unsigned)(x >> 32) == (unsigned)(s + 1))) break;
            __builtin_amdgcn_s_sleep(8);
        }
        unsigned v[7];
        if (s == 0) {
#pragma unroll
            for (int k = 0; k < 7; ++k) v[k] = *reinterpret_cast<const unsigned*>(p.mem0 + 2 * (k * 256 + tid));
        } else {
            const u64* g0 = p.G2 + (long)s * G2_STEP + tid;
            for (unsigned spin = 0; spin < WSPIN_CAP; ++spin) {
                bool ok = true;
#pragma unroll
                for (int k = 0; k < 7; ++k) {
                    u64 x = ld_gran(g0 + k * 256);
                    v[k] = (unsigned)x;
                    ok &= ((unsigned)(x >> 32) == (unsigned)s);
                }
                if (__all(ok)) break;
                __builtin_amdgcn_s_sleep(8);
            }
        }
#pragma unroll
        for (int k = 0; k < 7; ++k) {
            int gi = k * 256 + tid;
            int j = gi / 224, ip = gi - j * 224;
            *reinterpret_cast<unsigned*>(KL + j * MROW + 2 * ip) = v[k];
        }
    }
    __syncthreads();
    const u16* k0l = KL + r32 * MROW + hi * 8;
    const u16* k1l = KL + (32 + (r32 & 7)) * MROW + hi * 8;
    const float L2E = 1.4426950408889634f;
    auto scores = [&](const bf16x8(&e)[28], int hh) {
        f32x16 s0 = f32x16{}, s1 = f32x16{};
#pragma unroll
        for (int ks = 0; ks < 28; ++ks) {
            bf16x8 ka = *reinterpret_cast<const bf16x8*>(k0l + ks * 16);
            bf16x8 kb = *reinterpret_cast<const bf16x8*>(k1l + ks * 16);
            s0 = MFMA32(ka, e[ks], s0);
            s1 = MFMA32(kb, e[ks], s1);
        }
#pragma unroll
        for (int r = 0; r < 16; ++r) {
            int kk = crow(r, hi);
            if (kk >= 8 && kk - 8 > r32) s0[r] = -INFINITY;
            int k2 = kk + 32;
            if (k2 >= 40 || k2 - 8 > r32) s1[r] = -INFINITY;
        }
        float mx = -INFINITY;
#pragma unroll
        for (int r = 0; r < 16; ++r) mx = fmaxf(mx, fmaxf(s0[r], s1[r]));
        mx = fmaxf(mx, __shfl_xor(mx, 32));
        float nm = -mx * L2E, psum = 0.f;
#pragma unroll
        for (int r = 0; r < 16; ++r) {
            s0[r] = __builtin_amdgcn_exp2f(fmaf(s0[r], L2E, nm));
            s1[r] = __builtin_amdgcn_exp2f(fmaf(s1[r], L2E, nm));
            psum += s0[r] + s1[r];
        }
        psum += __shfl_xor(psum, 32);
        float inv = 1.f / psum;
        u32x4 a0, a1, a2;
#pragma unroll
        for (int j = 0; j < 4; ++j) {
            a0[j] = pack2(s0[2 * j] * inv, s0[2 * j + 1] * inv);
            a1[j] = pack2(s0[8 + 2 * j] * inv, s0[8 + 2 * j + 1] * inv);
            a2[j] = pack2(s1[2 * j] * inv, s1[2 * j + 1] * inv);
        }
        u16* pw = PL + ((wid * 4 + hh) * 3 * 64 + lane) * 8;
        *reinterpret_cast<u32x4*>(pw) = a0;
        *reinterpret_cast<u32x4*>(pw + 512) = a1;
        *reinterpret_cast<u32x4*>(pw + 1024) = a2;
    };
    scores(e0, 0);
    loadE(e0, 2);
    scores(e1, 1);
    loadE(e1, 3);
    scores(e0, 2);
    scores(e1, 3);
    const int ct = part + 4 * wid;
    const int col = (ct < 14 ? ct : 13) * 32 + r32;
    u64 u[16];
    u32x2 d[16][4];
#pragma unroll
    for (int h = 0; h < 16; ++h) {
        const u64* up = reinterpret_cast<const u64*>(p.Ut + (((long)s * 16 + h) * DM + col) * 8 + 4 * hi);
        const u16* dp = p.Dt + ((long)h * DM + col) * S_ + s * 32 + 4 * hi;
        u[h] = ld_gran(up);
#pragma unroll
        for (int q = 0; q < 4; ++q) d[h][q] = *reinterpret_cast<const u32x2*>(dp + 8 * q);
    }
    __syncthreads();
    f32x16 o = f32x16{};
#pragma unroll
    for (int h = 0; h < 16; ++h) {
        const u16* pr = PL + (h * 3 * 64 + lane) * 8;
        bf16x8 p0 = *reinterpret_cast<const bf16x8*>(pr);
        bf16x8 p1 = *reinterpret_cast<const bf16x8*>(pr + 512);
        bf16x8 p2 = *reinterpret_cast<const bf16x8*>(pr + 1024);
        o = MFMA32(__builtin_bit_cast(bf16x8, (u32x4{(unsigned)u[h], (unsigned)(u[h] >> 32), d[h][0][0], d[h][0][1]})), p0, o);
        o = MFMA32(__builtin_bit_cast(bf16x8, (u32x4{d[h][1][0], d[h][1][1], d[h][2][0], d[h][2][1]})), p1, o);
        o = MFMA32(__builtin_bit_cast(bf16x8, (u32x4{d[h][3][0], d[h][3][1], 0u, 0u})), p2, o);
    }
    if (ct < 14) {
#pragma unroll
        for (int rr = 0; rr < 4; ++rr) {
            u32x2 w;
            w[0] = pack2(o[4 * rr], o[4 * rr + 1]);
            w[1] = pack2(o[4 * rr + 2], o[4 * rr + 3]);
            *reinterpret_cast<u32x2*>(m.out + (long)(s * 32 + r32) * 896 + ct * 32 + 8 * rr + 4 * hi) = w;
        }
    }
}

__device__ __forceinline__ void worker_body(const RecParams& p, const MegaParams& m, u16* smem, int* slot) {
    unsigned* ctl = m.ctl;
    WTS(0);
    for (;;) {
        int t = next_ticket(ctl + CTL_Q1, slot);
        if (t >= 768 + 448) break;
        if (t < 768) {
            int mat = t >> 8, rb = (t >> 5) & 7, cb = t & 31;
            const u16* A = (mat < 2) ? m.B_ckv : m.B_x;
            const u16* B = (mat == 0) ? m.B_wuk : (mat == 1 ? m.B_wuv : m.B_wq);
            u16* C = (mat == 0) ? m.Kb : (mat == 1 ? m.Vb : m.Qb);
            int lda = (mat < 2) ? DC : DM;
            gemm_tile<256, 128, false, true>(A + (long)rb * 256 * lda, lda, B + cb * 128, HD,
                                             C + (long)rb * 256 * HD + cb * 128, HD, lda / 64, smem);
        } else {
            int u = t - 768, rb = u / 56, cb = u - rb * 56;
            gemm_tile<256, 128, true, true>(m.A_x + (long)rb * 256 * DM, DM, p.AmatT + (long)cb * 128 * DM, DM,
                                            m.E + (long)rb * 256 * (H_ * DM) + cb * 128, H_ * DM, DM / 64, smem);
        }
    }
    publish_count(ctl + CTL_C1);
    WTS(1);
    wait_count(ctl + CTL_C1, NWORK);
    WTS(2);
    for (;;) {
        int t = next_ticket(ctl + CTL_Q2, slot);
        if (t >= 256) break;
        attnB_body(m.Qb, m.Kb, m.Vb, m.OB, smem, 15 - (t >> 4), t & 15);
    }
    publish_count(ctl + CTL_C2);
    WTS(3);
    wait_count(ctl + CTL_C2, NWORK);
    WTS(4);
    for (;;) {
        int t = next_ticket(ctl + CTL_Q3, slot);
        if (t >= 112) break;
        int rb = t / 7, cb = t - rb * 7;
        gemm_tile<128, 64, false, false>(m.OB + (long)rb * 128 * HD, HD, m.B_wo + cb * 64, DM,
                                         m.out + (long)rb * 128 * 896 + 448 + cb * 64, 896, HD / 64, smem);
    }
    WTS(5);
    for (;;) {
        int t = next_ticket(ctl + CTL_Q4, slot);
        if (t >= 4 * NSEG) break;
        postA_task(p, m, t >> 2, t & 3, smem);
    }
    WTS(6);
    WTS(7);
}

constexpr int REC_LDS = 16 * MROW * 2 + 4 * 320 * 4 + 4 * 14 * 1024;
constexpr int GEMM_LDS = 2 * (256 * 72 + 64 * 160) * 2;
constexpr int POST_LDS = (40 * MROW + 16 * 3 * 64 * 8) * 2;
constexpr int cmax(int a, int b) { return a > b ? a : b; }
constexpr int FUSED_LDS = cmax(cmax(ATTN_LDS, REC_LDS), cmax(GEMM_LDS, POST_LDS));

__global__ __launch_bounds__(256) void mega_kernel(RecParams p, MegaParams m) {
    __shared__ __attribute__((aligned(16))) u16 smem[FUSED_LDS / 2 + 8];
    const int b = blockIdx.x;
    if (b < NA_BLK) {
        rec_A_body(p, smem, b);
    } else if (b < NREC) {
        rec_C_body(p, smem, b - NA_BLK);
    } else {
        worker_body(p, m, smem, reinterpret_cast<int*>(smem + FUSED_LDS / 2));
    }
}

template <int BN, bool NT, bool TO = false>
static void launch_gemm(hipStream_t st, const u16* A, const u16* B, u16* C, int lda, int ldb, int ldc, long sA,
                        long sB, long sC, int M, int N, int K, int batch, float scale) {
    dim3 grid(N / BN, (M + 127) / 128, batch);
    hipLaunchKernelGGL((gemm_kernel<BN, NT, TO>), grid, dim3(256), 0, st, A, B, C, lda, ldb, ldc, sA, sB, sC, M, N, K, scale);
}

extern "C" void kernel_launch(void* const* d_in, const int* in_sizes, int n_in, void* d_out, int out_size,
                              void* d_ws, size_t ws_size, hipStream_t stream) {
    const u16* A_x = (const u16*)d_in[0];
    const u16* A_mem0 = (const u16*)d_in[1];
    const u16* A_Wq = (const u16*)d_in[2];
    const u16* A_Wk = (const u16*)d_in[3];
    const u16* A_Wv = (const u16*)d_in[4];
    const u16* A_Wo = (const u16*)d_in[5];
    const u16* B_x = (const u16*)d_in[7];
    const u16* B_ckv = (const u16*)d_in[8];
    const u16* B_wq = (const u16*)d_in[9];
    const u16* B_wuk = (const u16*)d_in[10];
    const u16* B_wuv = (const u16*)d_in[11];
    const u16* B_wo = (const u16*)d_in[12];
    u16* out = (u16*)d_out;

    char* ws = (char*)d_ws;
    const size_t SZ = (size_t)S_ * HD * 2;
    u16* Qb = (u16*)(ws + 0 * SZ);
    u16* Kb = (u16*)(ws + 1 * SZ);
    u16* Vb = (u16*)(ws + 2 * SZ);
    u16* OB = (u16*)(ws + 3 * SZ);
    size_t off = 4 * SZ;
    u16* AmatT = (u16*)(ws + off); off += (size_t)H_ * DM * DM * 2;
    u16* CmatT = (u16*)(ws + off); off += (size_t)H_ * DM * DM * 2;
    u16* Bt = (u16*)(ws + off); off += (size_t)H_ * DM * S_ * 2;
    u16* Dt = (u16*)(ws + off); off += (size_t)H_ * DM * S_ * 2;
    u16* E = (u16*)(ws + off); off += (size_t)H_ * DM * S_ * 2;
    u16* Ut = (u16*)(ws + off); off += (size_t)NSEG * H_ * DM * 8 * 2;
    char* gbase = ws + off;
    u64* G1 = (u64*)gbase;
    u64* G2 = G1 + 2 * G1_STEP;
    unsigned* ctl = (unsigned*)(G2 + 65 * G2_STEP);
    size_t gbytes = (size_t)(2 * G1_STEP + 65 * G2_STEP) * 8 + (size_t)CTL_WORDS * 4;
    off += gbytes;

    hipMemsetAsync(gbase, 0, gbytes, stream);
    launch_gemm<64, true>(stream, A_Wk, A_Wq, AmatT, HD, HD, DM, DH, DH, (long)DM * DM, DM, DM, DH, H_, 0.0625f);
    launch_gemm<64, false, true>(stream, A_Wv, A_Wo, CmatT, HD, DM, DM, DH, (long)DH * DM, (long)DM * DM, DM, DM, DH, H_, 1.f);
    launch_gemm<64, false>(stream, A_x, AmatT, Bt, DM, DM, DM, 0, (long)DM * DM, (long)DM * S_, S_, DM, DM, H_, 1.f);
    launch_gemm<128, true>(stream, CmatT, A_x, Dt, DM, DM, S_, 0, 0, 0, H_ * DM, S_, DM, 1, 1.f);
    RecParams rp;
    rp.mem0 = A_mem0; rp.AmatT = AmatT; rp.CmatT = CmatT; rp.Bt = Bt; rp.Dt = Dt; rp.Ut = Ut; rp.G1 = G1; rp.G2 = G2;
    MegaParams mp;
    mp.A_x = A_x;
    mp.B_x = B_x; mp.B_ckv = B_ckv; mp.B_wq = B_wq; mp.B_wuk = B_wuk; mp.B_wuv = B_wuv; mp.B_wo = B_wo;
    mp.E = E; mp.Qb = Qb; mp.Kb = Kb; mp.Vb = Vb; mp.OB = OB;
    mp.out = out; mp.ctl = ctl;
    hipLaunchKernelGGL(mega_kernel, dim3(256), dim3(256), 0, stream, rp, mp);
}
```

```cpp
#include <hip/hip_runtime.h>
#include <hip/hip_bf16.h>
#include <stdint.h>

typedef unsigned short u16;
typedef __attribute__((ext_vector_type(8))) short bf16x8;
typedef __attribute__((ext_vector_type(4))) short s16x4;
typedef __attribute__((ext_vector_type(16))) float f32x16;
typedef __attribute__((ext_vector_type(4))) float f32x4;
typedef __attribute__((ext_vector_type(4))) unsigned u32x4;
typedef __attribute__((ext_vector_type(2))) unsigned u32x2;
typedef short v4i16_t __attribute__((ext_vector_type(4)));

#define LAS __attribute__((address_space(3)))

__device__ __forceinline__ float bf2f(u16 x) { return __uint_as_float(((unsigned)x) << 16); }
__device__ __forceinline__ u16 f2bf(float f) {
    unsigned u = __float_as_uint(f);
    u += 0x7fffu + ((u >> 16) & 1u);
    return (u16)(u >> 16);
}
typedef __bf16 bf16x2_t __attribute__((ext_vector_type(2)));
__device__ __forceinline__ unsigned pack2(float lo, float hi) {
    bf16x2_t r = {(__bf16)lo, (__bf16)hi};
    return __builtin_bit_cast(unsigned, r);
}
__device__ __forceinline__ s16x4 tr_read(const void* p) {
    return __builtin_bit_cast(s16x4, __builtin_amdgcn_ds_read_tr16_b64_v4i16((LAS v4i16_t*)p));
}
#define MFMA32(a, b, c) __builtin_amdgcn_mfma_f32_32x32x16_bf16(a, b, c, 0, 0, 0)
#define MFMA16(a, b, c) __builtin_amdgcn_mfma_f32_16x16x32_bf16(a, b, c, 0, 0, 0)

__device__ __forceinline__ int crow(int r, int hi) { return (r & 3) + 8 * (r >> 2) + 4 * hi; }

constexpr int S_ = 2048, DM = 448, H_ = 16, DH = 256, HD = 4096, MM = 8, SEG = 32, NSEG = 64, DC = 1024;

template <int BN, bool NT, bool TO>
__global__ __launch_bounds__(256) void gemm_kernel(const u16* __restrict__ A, const u16* __restrict__ B,
                                                   u16* __restrict__ C, int lda, int ldb, int ldc,
                                                   long sA, long sB, long sC, int M, int N, int K,
                                                   float scale) {
    constexpr int BM = 128, BK = 64;
    constexpr int AROW = 72;
    constexpr int BROW = NT ? 72 : (BN + 32);
    constexpr int BLDS = NT ? BN * 72 : 64 * (BN + 32);
    __shared__ __attribute__((aligned(16))) u16 smem[BM * AROW + BLDS];
    u16* As = smem;
    u16* Bs = smem + BM * AROW;
    const int tid = threadIdx.x, lane = tid & 63, wid = tid >> 6;
    const int wm = wid >> 1, wn = wid & 1;
    const int m0 = blockIdx.y * BM, n0 = blockIdx.x * BN;
    A += (long)blockIdx.z * sA;
    B += (long)blockIdx.z * sB;
    C += (long)blockIdx.z * sC;
    constexpr int NTI = BN / 64;
    f32x16 acc[2][NTI];
#pragma unroll
    for (int i = 0; i < 2; ++i)
#pragma unroll
        for (int j = 0; j < NTI; ++j) acc[i][j] = f32x16{};

    constexpr int BCH = BN / 32;
    u32x4 ra[4], rb[BCH];
    auto load_tiles = [&](int k0) {
#pragma unroll
        for (int i = 0; i < 4; ++i) {
            int c = tid + 256 * i, row = c >> 3, kc = c & 7;
            int gr = m0 + row;
            if (gr < M)
                ra[i] = *reinterpret_cast<const u32x4*>(A + (long)gr * lda + k0 + kc * 8);
            else
                ra[i] = u32x4{0, 0, 0, 0};
        }
#pragma unroll
        for (int i = 0; i < BCH; ++i) {
            int c = tid + 256 * i;
            if (NT) {
                int row = c >> 3, kc = c & 7;
                rb[i] = *reinterpret_cast<const u32x4*>(B + (long)(n0 + row) * ldb + k0 + kc * 8);
            } else {
                int krow = c / (BN / 8), nc = c % (BN / 8);
                rb[i] = *reinterpret_cast<const u32x4*>(B + (long)(k0 + krow) * ldb + n0 + nc * 8);
            }
        }
    };
    auto store_tiles = [&]() {
#pragma unroll
        for (int i = 0; i < 4; ++i) {
            int c = tid + 256 * i, row = c >> 3, kc = c & 7;
            *reinterpret_cast<u32x4*>(As + row * AROW + kc * 8) = ra[i];
        }
#pragma unroll
        for (int i = 0; i < BCH; ++i) {
            int c = tid + 256 * i;
            if (NT) {
                int row = c >> 3, kc = c & 7;
                *reinterpret_cast<u32x4*>(Bs + row * BROW + kc * 8) = rb[i];
            } else {
                int krow = c / (BN / 8), nc = c % (BN / 8);
                *reinterpret_cast<u32x4*>(Bs + krow * BROW + nc * 8) = rb[i];
            }
        }
    };
    const int r32 = lane & 31, hi = lane >> 5, g = lane >> 4, q4 = (lane & 15) >> 2, p4 = lane & 3;
    load_tiles(0);
    const int nk = K / BK;
    for (int kt = 0; kt < nk; ++kt) {
        __syncthreads();
        store_tiles();
        __syncthreads();
        if (kt + 1 < nk) load_tiles((kt + 1) * BK);
#pragma unroll
        for (int ks = 0; ks < 4; ++ks) {
            bf16x8 af[2], bfr[NTI];
#pragma unroll
            for (int i = 0; i < 2; ++i)
                af[i] = *reinterpret_cast<const bf16x8*>(As + (wm * 64 + i * 32 + r32) * AROW + ks * 16 + hi * 8);
#pragma unroll
            for (int j = 0; j < NTI; ++j) {
                int nb = wn * (BN / 2) + j * 32;
                if (NT) {
                    bfr[j] = *reinterpret_cast<const bf16x8*>(Bs + (nb + r32) * BROW + ks * 16 + hi * 8);
                } else {
                    const u16* pB = Bs + (ks * 16 + 8 * (g >> 1) + q4) * BROW + nb + 16 * (g & 1) + 4 * p4;
                    s16x4 lo = tr_read(pB);
                    s16x4 hi4 = tr_read(pB + 4 * BROW);
                    bfr[j] = bf16x8{lo[0], lo[1], lo[2], lo[3], hi4[0], hi4[1], hi4[2], hi4[3]};
                }
            }
#pragma unroll
            for (int i = 0; i < 2; ++i)
#pragma unroll
                for (int j = 0; j < NTI; ++j) acc[i][j] = MFMA32(af[i], bfr[j], acc[i][j]);
        }
    }
#pragma unroll
    for (int i = 0; i < 2; ++i)
#pragma unroll
        for (int j = 0; j < NTI; ++j) {
            int col = n0 + wn * (BN / 2) + j * 32 + r32;
            if (TO) {
#pragma unroll
                for (int rr = 0; rr < 4; ++rr) {
                    int row = m0 + wm * 64 + i * 32 + 8 * rr + 4 * hi;
                    u32x2 w;
                    w[0] = pack2(acc[i][j][4 * rr] * scale, acc[i][j][4 * rr + 1] * scale);
                    w[1] = pack2(acc[i][j][4 * rr + 2] * scale, acc[i][j][4 * rr + 3] * scale);
                    if (row < M) *reinterpret_cast<u32x2*>(C + (long)col * ldc + row) = w;
                }
            } else {
#pragma unroll
                for (int r = 0; r < 16; ++r) {
                    int row = m0 + wm * 64 + i * 32 + crow(r, hi);
                    if (row < M) C[(long)row * ldc + col] = f2bf(acc[i][j][r] * scale);
                }
            }
        }
}

constexpr int KROW = 264;
constexpr int VROW = 288;
constexpr int ATTN_LDS = (64 * KROW + 64 * VROW) * 2;
__device__ __forceinline__ void attnB_body(const u16* __restrict__ Q, const u16* __restrict__ K,
                                           const u16* __restrict__ V, u16* __restrict__ O, u16* smem, int qb, int h) {
    u16* Ks = smem;
    u16* Vs = smem + 64 * KROW;
    int tid = threadIdx.x;
    asm volatile("" : "+v"(tid));
    const int lane = tid & 63, wid = tid >> 6;
    const int q0 = qb * 128;
    const int r32 = lane & 31, hi = lane >> 5, g = lane >> 4, q4 = (lane & 15) >> 2, p4 = lane & 3;
    const int qrow = q0 + wid * 32 + r32;
    bf16x8 qf[16];
#pragma unroll
    for (int ks = 0; ks < 16; ++ks)
        qf[ks] = *reinterpret_cast<const bf16x8*>(Q + (long)qrow * HD + h * DH + ks * 16 + hi * 8);
    f32x16 o[8];
#pragma unroll
    for (int i = 0; i < 8; ++i) o[i] = f32x16{};
    float mrun = -INFINITY, lrun = 0.f;
    const float c2 = 0.0625f * 1.4426950408889634f;
    const int nt = (q0 + 128) / 64;
    u32x4 rk[8], rv[8];
    auto load_kv = [&](int t) {
#pragma unroll
        for (int i = 0; i < 8; ++i) {
            int c = tid + 256 * i, row = c >> 5, cc = c & 31;
            long off = (long)(t * 64 + row) * HD + h * DH + cc * 8;
            rk[i] = *reinterpret_cast<const u32x4*>(K + off);
            rv[i] = *reinterpret_cast<const u32x4*>(V + off);
        }
    };
    auto store_kv = [&]() {
#pragma unroll
        for (int i = 0; i < 8; ++i) {
            int c = tid + 256 * i, row = c >> 5, cc = c & 31;
            *reinterpret_cast<u32x4*>(Ks + row * KROW + cc * 8) = rk[i];
            *reinterpret_cast<u32x4*>(Vs + row * VROW + cc * 8) = rv[i];
        }
    };
    load_kv(0);
    for (int t = 0; t < nt; ++t) {
        __syncthreads();
        store_kv();
        __syncthreads();
        if (t + 1 < nt) load_kv(t + 1);
        const int kbase = t * 64;
        if (kbase <= q0 + wid * 32 + 31) {
            f32x16 s0 = f32x16{}, s1 = f32x16{};
#pragma unroll
            for (int ks = 0; ks < 16; ++ks) {
                bf16x8 k0 = *reinterpret_cast<const bf16x8*>(Ks + r32 * KROW + ks * 16 + hi * 8);
                bf16x8 k1 = *reinterpret_cast<const bf16x8*>(Ks + (32 + r32) * KROW + ks * 16 + hi * 8);
                s0 = MFMA32(k0, qf[ks], s0);
                s1 = MFMA32(k1, qf[ks], s1);
                if ((ks & 3) == 3) __builtin_amdgcn_sched_barrier(0);
            }
            if (kbase + 63 > q0 + wid * 32) {
#pragma unroll
                for (int r = 0; r < 16; ++r) {
                    int kk = kbase + crow(r, hi);
                    if (kk > qrow) s0[r] = -INFINITY;
                    if (kk + 32 > qrow) s1[r] = -INFINITY;
                }
            }
            float mx = -INFINITY;
#pragma unroll
            for (int r = 0; r < 16; ++r) mx = fmaxf(mx, fmaxf(s0[r], s1[r]));
            mx = fmaxf(mx, __shfl_xor(mx, 32));
            float mnew = fmaxf(mrun, mx);
            float alpha = __builtin_amdgcn_exp2f((mrun - mnew) * c2);
            float nm = -mnew * c2;
            float psum = 0.f;
#pragma unroll
            for (int r = 0; r < 16; ++r) {
                s0[r] = __builtin_amdgcn_exp2f(fmaf(s0[r], c2, nm));
                s1[r] = __builtin_amdgcn_exp2f(fmaf(s1[r], c2, nm));
                psum += s0[r] + s1[r];
            }
            psum += __shfl_xor(psum, 32);
            lrun = lrun * alpha + psum;
            mrun = mnew;
#pragma unroll
            for (int i = 0; i < 8; ++i)
#pragma unroll
                for (int r = 0; r < 16; ++r) o[i][r] *= alpha;
            bf16x8 pf[4];
#pragma unroll
            for (int s = 0; s < 2; ++s) {
                u32x4 a, b;
#pragma unroll
                for (int j = 0; j < 4; ++j) {
                    a[j] = pack2(s0[8 * s + 2 * j], s0[8 * s + 2 * j + 1]);
                    b[j] = pack2(s1[8 * s + 2 * j], s1[8 * s + 2 * j + 1]);
                }
                pf[s] = __builtin_bit_cast(bf16x8, a);
                pf[2 + s] = __builtin_bit_cast(bf16x8, b);
            }
#pragma unroll
            for (int dt = 0; dt < 8; ++dt) {
#pragma unroll
                for (int s = 0; s < 4; ++s) {
                    const u16* pV = Vs + (16 * s + 4 * hi + q4) * VROW + 32 * dt + 16 * (g & 1) + 4 * p4;
                    s16x4 lo = tr_read(pV);
                    s16x4 hi4 = tr_read(pV + 8 * VROW);
                    bf16x8 vf = bf16x8{lo[0], lo[1], lo[2], lo[3], hi4[0], hi4[1], hi4[2], hi4[3]};
                    o[dt] = MFMA32(vf, pf[s], o[dt]);
                }
                __builtin_amdgcn_sched_barrier(0);
            }
        }
    }
    float inv = 1.f / lrun;
#pragma unroll
    for (int dt = 0; dt < 8; ++dt)
#pragma unroll
        for (int rr = 0; rr < 4; ++rr) {
            u32x2 w;
            w[0] = pack2(o[dt][4 * rr] * inv, o[dt][4 * rr + 1] * inv);
            w[1] = pack2(o[dt][4 * rr + 2] * inv, o[dt][4 * rr + 3] * inv);
            __hip_atomic_store(reinterpret_cast<unsigned long long*>(O + (long)qrow * HD + h * DH + 32 * dt + 8 * rr + 4 * hi),
                               ((unsigned long long)w[1] << 32) | w[0], __ATOMIC_RELAXED, __HIP_MEMORY_SCOPE_AGENT);
        }
}

typedef unsigned long long u64;
__device__ __forceinline__ u64 ld_gran(const u64* p) {
    return __hip_atomic_load(p, __ATOMIC_RELAXED, __HIP_MEMORY_SCOPE_AGENT);
}
__device__ __forceinline__ void st_gran4(u16* p, unsigned lo, unsigned hi) {
    hi = min(hi, 0xfffffffeu);
    __hip_atomic_store(reinterpret_cast<u64*>(p), ((u64)hi << 32) | (u64)lo, __ATOMIC_RELAXED,
                       __HIP_MEMORY_SCOPE_AGENT);
}
constexpr int NA_BLK = 16, NC_BLK = 28, NREC = NA_BLK + NC_BLK;
constexpr int MROW = 456;
constexpr int G1_SLOT = 16 * 8 * 40;
constexpr int G2_SLOT = 8 * 448;
constexpr unsigned SPIN_CAP = 400000u;

#define WTS(slot)
#define TS(slot)
struct RecParams {
    const u16* mem0;
    const u16* AmatT;
    const u16* CmatT;
    u16* Bt;
    u16* Dt;
    const unsigned* stage;
    u16* Ut;
    u16* G1;
    u16* G2;
};

__device__ __forceinline__ float red16_max(float x) {
    asm volatile(
        "s_nop 1\n\tv_max_f32_dpp %0, %0, %0 quad_perm:[1,0,3,2] row_mask:0xf bank_mask:0xf\n\t"
        "s_nop 1\n\tv_max_f32_dpp %0, %0, %0 quad_perm:[2,3,0,1] row_mask:0xf bank_mask:0xf\n\t"
        "s_nop 1\n\tv_max_f32_dpp %0, %0, %0 row_half_mirror row_mask:0xf bank_mask:0xf\n\t"
        "s_nop 1\n\tv_max_f32_dpp %0, %0, %0 row_mirror row_mask:0xf bank_mask:0xf\n\t"
        "s_nop 1"
        : "+v"(x));
    return x;
}
__device__ __forceinline__ float red16_sum(float x) {
    asm volatile(
        "s_nop 1\n\tv_add_f32_dpp %0, %0, %0 quad_perm:[1,0,3,2] row_mask:0xf bank_mask:0xf\n\t"
        "s_nop 1\n\tv_add_f32_dpp %0, %0, %0 quad_perm:[2,3,0,1] row_mask:0xf bank_mask:0xf\n\t"
        "s_nop 1\n\tv_add_f32_dpp %0, %0, %0 row_half_mirror row_mask:0xf bank_mask:0xf\n\t"
        "s_nop 1\n\tv_add_f32_dpp %0, %0, %0 row_mirror row_mask:0xf bank_mask:0xf\n\t"
        "s_nop 1"
        : "+v"(x));
    return x;
}
__device__ __forceinline__ u32x4 ld16_sc1(__amdgpu_buffer_rsrc_t rs, unsigned voff, unsigned soff) {
    return __builtin_amdgcn_raw_buffer_load_b128(rs, voff, soff, 16);
}
__device__ __forceinline__ unsigned umax3(unsigned a, unsigned b, unsigned c) { return max(max(a, b), c); }

__device__ __forceinline__ void mfma_a0(f32x4& d, const bf16x8& w, const u32x4& m) {
    asm volatile("v_mfma_f32_16x16x32_bf16 %0, %1, %2, 0" : "=&v"(d) : "a"(w), "v"(m));
}
__device__ __forceinline__ void mfma_a(f32x4& d, const bf16x8& w, const u32x4& m) {
    asm volatile("v_mfma_f32_16x16x32_bf16 %0, %1, %2, %0" : "+v"(d) : "a"(w), "v"(m));
}
__device__ __forceinline__ void mfma_v0(f32x4& d, const bf16x8& w, const u32x4& m) {
    asm volatile("v_mfma_f32_16x16x32_bf16 %0, %1, %2, 0" : "=&v"(d) : "v"(w), "v"(m));
}
__device__ __forceinline__ void mfma_v(f32x4& d, const bf16x8& w, const u32x4& m) {
    asm volatile("v_mfma_f32_16x16x32_bf16 %0, %1, %2, %0" : "+v"(d) : "v"(w), "v"(m));
}
__device__ __forceinline__ void lds_rd16(bf16x8& d, unsigned addr, int off) {
    asm volatile("ds_read_b128 %0, %1 offset:%2" : "=v"(d) : "v"(addr), "n"(off));
}
template <int N>
__device__ __forceinline__ void wait_lgkm() {
    asm volatile("s_waitcnt lgkmcnt(%0)" ::"n"(N) : "memory");
}

constexpr unsigned STAGE_TILES = 112;
__device__ __forceinline__ void wait_stage(const unsigned* c) {
    for (unsigned spin = 0; spin < 4000000u; ++spin) {
        if (__hip_atomic_load(c, __ATOMIC_RELAXED, __HIP_MEMORY_SCOPE_AGENT) + 1u >= STAGE_TILES) break;
        __builtin_amdgcn_s_sleep(4);
    }
}

__device__ __forceinline__ void rec_A_body(const RecParams& p, u16* smem, int h) {
    float* Sc = reinterpret_cast<float*>(smem);
    const int tid = threadIdx.x, lane = tid & 63, wid = __builtin_amdgcn_readfirstlane(tid >> 6);
    const int c16 = lane & 15, g = lane >> 4;
    int kq[14];
#pragma unroll
    for (int q = 0; q < 14; ++q) {
        if (q < 3) kq[q] = 3 * wid + q;
        else if (q == 3) kq[q] = 12 + (wid >> 1);
        else if (q < 13) kq[q] = (q - 4 < 3 * wid) ? (q - 4) : (q - 1);
        else kq[q] = 13 - (wid >> 1);
    }
    bf16x8 W[6][14];
    u16* WL = smem + 4 * 320 * 2 + (wid * 14 * 64 + lane) * 8;
#pragma unroll
    for (int t = 0; t < 7; ++t) {
        const int ksp = (t < 6) ? (3 * wid + (t >> 1)) : (12 + (wid >> 1));
        const int half = (t < 6) ? (t & 1) : (wid & 1);
        const int n = 32 * ksp + 8 * (c16 >> 2) + 4 * half + (c16 & 3);
        const u16* ap = p.AmatT + ((long)h * DM + n) * DM + 8 * g;
#pragma unroll
        for (int q = 0; q < 14; ++q) {
            bf16x8 w = *reinterpret_cast<const bf16x8*>(ap + 32 * kq[q]);
            if (t < 6)
                W[t][q] = w;
            else
                *reinterpret_cast<bf16x8*>(WL + q * 512) = w;
        }
    }
    bf16x8 X[2][4];
    const bool q3ok = (wid & 1) == 0;
    __amdgpu_buffer_rsrc_t rsb = __builtin_amdgcn_make_buffer_rsrc(p.Bt, 0, S_ * H_ * DM * 2, 0x00020000);
    const unsigned xvoff = (c16 * (H_ * DM) + h * DM + 8 * g) * 2;
    auto load_X = [&](int s) {
        if ((s & 7) == 0) wait_stage(p.stage + 64 * (s >> 3));
#pragma unroll
        for (int tl = 0; tl < 2; ++tl)
#pragma unroll
            for (int q = 0; q < 4; ++q) {
                bf16x8 w = __builtin_bit_cast(
                    bf16x8, ld16_sc1(rsb, xvoff, (unsigned)(((s * 32 + tl * 16) * (H_ * DM) + 32 * kq[q]) * 2)));
                if (q == 3 && !q3ok) w = bf16x8{0, 0, 0, 0, 0, 0, 0, 0};
                X[tl][q] = w;
            }
    };
    load_X(0);
    const int srow = 4 * (wid & 1) + g;
    const bool sact = c16 < 10;
    const float* sread = Sc + srow * 40 + (sact ? 4 * c16 : 0);
    float* swr_mm = Sc + wid * 320 + (c16 & 7) * 40 + 8 * (g & 1);
    float* swr_x0 = Sc + wid * 320 + (c16 & 7) * 40 + (g == 0 ? 4 : 4 * g + 8);
    float* swr_x1 = Sc + wid * 320 + (c16 & 7) * 40 + 24 + 4 * g;
    const unsigned mvoff = (g >> 1) * 256 + (c16 & 7) * 32 + (g & 1) * 16;
    __amdgpu_buffer_rsrc_t rs2 = __builtin_amdgcn_make_buffer_rsrc(p.G2, 0, 65 * G2_SLOT * 2, 0x00020000);
    u16* g1w = p.G1 + h * 320 + srow * 40 + 4 * c16;
    const unsigned wl_addr = (unsigned)(uintptr_t)(LAS u16*)WL;
    bool dead = false;
    for (int s = 0; s < NSEG - 1; ++s) {
        u32x4 mf[14];
        bf16x8 w6[3];
        lds_rd16(w6[0], wl_addr, 0);
        lds_rd16(w6[1], wl_addr, 1024);
        for (unsigned spin = 0;; ++spin) {
            asm volatile("" ::: "memory");
#pragma unroll
            for (int q = 0; q < 14; ++q) mf[q] = ld16_sc1(rs2, mvoff, (unsigned)(s * G2_SLOT * 2 + kq[q] * 512));
            unsigned mx = 0;
#pragma unroll
            for (int q = 0; q < 14; ++q) mx = umax3(mx, mf[q][1], mf[q][3]);
            if (__all(mx != 0xffffffffu) || dead) break;
            if (spin > SPIN_CAP) dead = true;
        }
        TS(1);
        f32x4 acc[7], ax[2];
#pragma unroll
        for (int q = 0; q < 14; ++q) {
            if (q + 2 < 14) lds_rd16(w6[(q + 2) % 3], wl_addr, (q + 2) * 1024);
#pragma unroll
            for (int t = 0; t < 4; ++t) {
                if (q == 0) mfma_a0(acc[t], W[t][q], mf[q]);
                else mfma_a(acc[t], W[t][q], mf[q]);
            }
#pragma unroll
            for (int t = 4; t < 6; ++t) {
                if (t == 4 && q < 6) {
                    if (q == 0) mfma_a0(acc[t], W[t][q], mf[q]);
                    else mfma_a(acc[t], W[t][q], mf[q]);
                } else {
                    if (q == 0) mfma_v0(acc[t], W[t][q], mf[q]);
                    else mfma_v(acc[t], W[t][q], mf[q]);
                }
            }
            if (q < 12) wait_lgkm<2>();
            else if (q == 12) wait_lgkm<1>();
            else wait_lgkm<0>();
            if (q == 0) mfma_v0(acc[6], w6[0], mf[q]);
            else mfma_v(acc[6], w6[q % 3], mf[q]);
            if (q < 4) {
                if (q == 0) {
                    mfma_v0(ax[0], X[0][q], mf[q]);
                    mfma_v0(ax[1], X[1][q], mf[q]);
                } else {
                    mfma_v(ax[0], X[0][q], mf[q]);
                    mfma_v(ax[1], X[1][q], mf[q]);
                }
            }
        }
        asm volatile("s_nop 7\n\ts_nop 1"
                     : "+v"(acc[0]), "+v"(acc[1]), "+v"(acc[2]), "+v"(acc[3]), "+v"(acc[4]), "+v"(acc[5]), "+v"(acc[6]),
                       "+v"(ax[0]), "+v"(ax[1]));
        f32x4 sa = f32x4{0.f, 0.f, 0.f, 0.f}, sb = f32x4{0.f, 0.f, 0.f, 0.f};
#pragma unroll
        for (int pr = 0; pr < 4; ++pr) {
            u32x4 bv;
            if (pr < 3) {
                bv = u32x4{pack2(acc[2 * pr][0], acc[2 * pr][1]), pack2(acc[2 * pr][2], acc[2 * pr][3]),
                           pack2(acc[2 * pr + 1][0], acc[2 * pr + 1][1]), pack2(acc[2 * pr + 1][2], acc[2 * pr + 1][3])};
            } else {
                unsigned t0 = pack2(acc[6][0], acc[6][1]), t1 = pack2(acc[6][2], acc[6][3]);
                bv = q3ok ? u32x4{t0, t1, 0u, 0u} : u32x4{0u, 0u, t0, t1};
            }
            if (pr & 1)
                sb = MFMA16(__builtin_bit_cast(bf16x8, mf[pr]), __builtin_bit_cast(bf16x8, bv), sb);
            else
                sa = MFMA16(__builtin_bit_cast(bf16x8, mf[pr]), __builtin_bit_cast(bf16x8, bv), sa);
        }
        if (c16 < 8) {
            *reinterpret_cast<f32x4*>(swr_x0) = ax[0];
            *reinterpret_cast<f32x4*>(swr_x1) = ax[1];
        }
        sa += sb;
        if (c16 < 8 && g < 2) *reinterpret_cast<f32x4*>(swr_mm) = sa;
        TS(2);
        __syncthreads();
        if (wid < 2) {
            f32x4 x0 = *reinterpret_cast<const f32x4*>(sread);
            f32x4 x1 = *reinterpret_cast<const f32x4*>(sread + 320);
            f32x4 x2 = *reinterpret_cast<const f32x4*>(sread + 640);
            f32x4 x3 = *reinterpret_cast<const f32x4*>(sread + 960);
            f32x4 a = (x0 + x1) + (x2 + x3);
            float mx = sact ? __builtin_fmaxf(__builtin_fmaxf(a[0], a[1]), __builtin_fmaxf(a[2], a[3])) : -INFINITY;
            mx = red16_max(mx);
            const float L2E = 1.4426950408889634f;
            float nm = -mx * L2E;
            float e0 = __builtin_amdgcn_exp2f(__builtin_fmaf(a[0], L2E, nm));
            float e1 = __builtin_amdgcn_exp2f(__builtin_fmaf(a[1], L2E, nm));
            float e2 = __builtin_amdgcn_exp2f(__builtin_fmaf(a[2], L2E, nm));
            float e3 = __builtin_amdgcn_exp2f(__builtin_fmaf(a[3], L2E, nm));
            float sm = sact ? ((e0 + e1) + (e2 + e3)) : 0.f;
            sm = red16_sum(sm);
            float inv = __builtin_amdgcn_rcpf(sm);
            if (sact) st_gran4(g1w + s * G1_SLOT, pack2(e0 * inv, e1 * inv), pack2(e2 * inv, e3 * inv));
        }
        TS(3);
        if (s + 1 < NSEG - 1) load_X(s + 1);
    }
}

__device__ __forceinline__ void rec_C_body(const RecParams& p, u16* smem, int c) {
    u16* memL = smem;
    float* red = reinterpret_cast<float*>(smem + 16 * MROW);
    const int tid = threadIdx.x, lane = tid & 63, wid = __builtin_amdgcn_readfirstlane(tid >> 6);
    const int c16 = lane & 15, g = lane >> 4;
    for (int i = tid; i < 8 * MROW / 2; i += 256) reinterpret_cast<unsigned*>(memL + 8 * MROW)[i] = 0u;
    const int ch1 = (tid < 192) ? (256 + tid) : tid;
    if (tid < 32) {
        u32x2 m0 = *reinterpret_cast<const u32x2*>(p.mem0 + (tid >> 2) * DM + 16 * c + 4 * (tid & 3));
        st_gran4(p.G2 + 128 * c + 4 * tid, m0[0], m0[1]);
    }
    u16* mw0 = memL + ((tid >> 1) & 7) * MROW + 16 * (tid >> 4) + 8 * (tid & 1);
    u16* mw1 = memL + ((ch1 >> 1) & 7) * MROW + 16 * (ch1 >> 4) + 8 * (ch1 & 1);
    const int col = c * 16 + c16;
    bf16x8 W[4][14];
#pragma unroll
    for (int hh = 0; hh < 4; ++hh) {
        const u16* cp = p.CmatT + ((long)(wid * 4 + hh) * DM + col) * DM + 8 * g;
#pragma unroll
        for (int ks = 0; ks < 14; ++ks) W[hh][ks] = *reinterpret_cast<const bf16x8*>(cp + 32 * ks);
    }
    u32x2 dlo[4], dhi[4];
    u32x4 dx;
    const int klo = (g >= 2) ? (8 * (g - 1)) : 0;
    const int khi = (g >= 2) ? (8 * (g - 1) + 4) : 4 * g;
    __amdgpu_buffer_rsrc_t rsd = __builtin_amdgcn_make_buffer_rsrc(p.Dt, 0, S_ * H_ * DM * 2, 0x00020000);
    const unsigned dvoff = (unsigned)((wid * 4 * DM + col) * S_ * 2);
    auto load_D = [&](int s) {
        if ((s & 7) == 0) wait_stage(p.stage + 64 * (s >> 3));
#pragma unroll
        for (int hh = 0; hh < 4; ++hh) {
            dlo[hh] = __builtin_amdgcn_raw_buffer_load_b64(rsd, dvoff + hh * (DM * S_ * 2) + klo * 2, s * 64, 16);
            dhi[hh] = __builtin_amdgcn_raw_buffer_load_b64(rsd, dvoff + hh * (DM * S_ * 2) + khi * 2, s * 64, 16);
        }
        dx = ld16_sc1(rsd, dvoff + g * (DM * S_ * 2) + 48, s * 64);
    };
    load_D(0);
    const u16* mfp = memL + c16 * MROW + 8 * g;
    const int i8 = c16 & 7;
    const unsigned pvoff = ((wid * 4) * 320 + i8 * 40 + 8 * g) * 2;
    const unsigned pvoffx = ((wid * 4 + g) * 320 + i8 * 40 + 32) * 2;
    float* rwr = red + wid * 128 + (4 * (g & 1)) * 16 + c16;
    const float* rrd = red + ((lane >> 2) & 7) * 16 + 4 * (lane & 3);
    u16* g2w = p.G2 + 128 * c + 4 * (lane & 31);
    __amdgpu_buffer_rsrc_t rs1 = __builtin_amdgcn_make_buffer_rsrc(p.G1, 0, 63 * G1_SLOT * 2, 0x00020000);
    __amdgpu_buffer_rsrc_t rs2 = __builtin_amdgcn_make_buffer_rsrc(p.G2, 0, 65 * G2_SLOT * 2, 0x00020000);
    bool dead = false;
    u64* utp = reinterpret_cast<u64*>(p.Ut + ((long)(wid * 4) * DM + col) * 8 + 4 * (g & 1));
    for (int s = 0; s < NSEG; ++s) {
        {
            u32x4 a, b;
            for (unsigned spin = 0;; ++spin) {
                asm volatile("" ::: "memory");
                a = ld16_sc1(rs2, tid * 16, (unsigned)(s * G2_SLOT * 2));
                b = ld16_sc1(rs2, ch1 * 16, (unsigned)(s * G2_SLOT * 2));
                unsigned mx = max(umax3(a[1], a[3], b[1]), b[3]);
                if (__all(mx != 0xffffffffu) || dead) break;
                if (spin > SPIN_CAP) dead = true;
            }
            *reinterpret_cast<u32x4*>(mw0) = a;
            *reinterpret_cast<u32x4*>(mw1) = b;
            __syncthreads();
        }
        TS(1);
        f32x4 U[4];
#pragma unroll
        for (int hh = 0; hh < 4; ++hh) U[hh] = f32x4{0.f, 0.f, 0.f, 0.f};
#pragma unroll
        for (int ks = 0; ks < 14; ++ks) {
            bf16x8 mf = *reinterpret_cast<const bf16x8*>(mfp + 32 * ks);
#pragma unroll
            for (int hh = 0; hh < 4; ++hh) U[hh] = MFMA16(mf, W[hh][ks], U[hh]);
        }
        u32x4 bv[4];
#pragma unroll
        for (int hh = 0; hh < 4; ++hh) {
            unsigned u0 = pack2(U[hh][0], U[hh][1]), u1 = pack2(U[hh][2], U[hh][3]);
            bv[hh] = u32x4{(g < 2) ? u0 : dlo[hh][0], (g < 2) ? u1 : dlo[hh][1], dhi[hh][0], dhi[hh][1]};
            if (g < 2)
                __hip_atomic_store(utp + ((long)s * 16 + hh) * (DM * 2), ((u64)u1 << 32) | u0, __ATOMIC_RELAXED,
                                   __HIP_MEMORY_SCOPE_AGENT);
        }
        if (s == NSEG - 1) {
            asm volatile("s_waitcnt vmcnt(0)" ::: "memory");
            __syncthreads();
            if (tid < 32) st_gran4(g2w + NSEG * G2_SLOT, 0u, 0u);
            break;
        }
        u32x4 bx = dx;
        u32x4 pv[5];
        for (unsigned spin = 0;; ++spin) {
            asm volatile("" ::: "memory");
#pragma unroll
            for (int hh = 0; hh < 4; ++hh) pv[hh] = ld16_sc1(rs1, pvoff + hh * 640, (unsigned)(s * G1_SLOT * 2));
            pv[4] = ld16_sc1(rs1, pvoffx, (unsigned)(s * G1_SLOT * 2));
            unsigned mx = 0;
#pragma unroll
            for (int q = 0; q < 5; ++q) mx = umax3(mx, pv[q][1], pv[q][3]);
            if (__all(mx != 0xffffffffu) || dead) break;
            if (spin > SPIN_CAP) dead = true;
        }
        TS(2);
        f32x4 o0 = f32x4{0.f, 0.f, 0.f, 0.f}, o1 = f32x4{0.f, 0.f, 0.f, 0.f};
        o0 = MFMA16(__builtin_bit_cast(bf16x8, pv[0]), __builtin_bit_cast(bf16x8, bv[0]), o0);
        o1 = MFMA16(__builtin_bit_cast(bf16x8, pv[1]), __builtin_bit_cast(bf16x8, bv[1]), o1);
        o0 = MFMA16(__builtin_bit_cast(bf16x8, pv[2]), __builtin_bit_cast(bf16x8, bv[2]), o0);
        o1 = MFMA16(__builtin_bit_cast(bf16x8, pv[3]), __builtin_bit_cast(bf16x8, bv[3]), o1);
        o0 = MFMA16(__builtin_bit_cast(bf16x8, pv[4]), __builtin_bit_cast(bf16x8, bx), o0);
        o0 += o1;
        if (g < 2) {
#pragma unroll
            for (int r = 0; r < 4; ++r) rwr[r * 16] = o0[r];
        }
        asm volatile("s_waitcnt vmcnt(0)" ::: "memory");
        __syncthreads();
        if (tid < 32) {
            f32x4 x0 = *reinterpret_cast<const f32x4*>(rrd);
            f32x4 x1 = *reinterpret_cast<const f32x4*>(rrd + 128);
            f32x4 x2 = *reinterpret_cast<const f32x4*>(rrd + 256);
            f32x4 x3 = *reinterpret_cast<const f32x4*>(rrd + 384);
            f32x4 a = (x0 + x1) + (x2 + x3);
            st_gran4(g2w + (s + 1) * G2_SLOT, pack2(a[0], a[1]), pack2(a[2], a[3]));
        }
        TS(3);
        if (s + 1 < NSEG - 1) load_D(s + 1);
    }
}

template <int BM, int BN, bool NT, bool SC1>
__device__ __forceinline__ void gemm_tile(const u16* __restrict__ A, int lda, const u16* __restrict__ B, int ldb,
                                          u16* __restrict__ C, int ldc, int nk, u16* smem) {
    constexpr int AROW = 72, BROW = NT ? 72 : BN + 32;
    constexpr int ASZ = BM * AROW, STG = ASZ + (NT ? BN * 72 : 64 * BROW);
    constexpr int ACH = BM / 32, BCH = BN / 32, TM = BM / 64, TN = BN / 64;
    constexpr int BPR = BN / 8;
    constexpr int BRS = 256 / BPR;
    int tid = threadIdx.x;
    asm volatile("" : "+v"(tid));
    const int lane = tid & 63, wid = tid >> 6;
    const int wm = wid >> 1, wn = wid & 1;
    const int r32 = lane & 31, hi = lane >> 5, g = lane >> 4, q4 = (lane & 15) >> 2, p4 = lane & 3;
    f32x16 acc[TM][TN];
#pragma unroll
    for (int i = 0; i < TM; ++i)
#pragma unroll
        for (int j = 0; j < TN; ++j) acc[i][j] = f32x16{};
    u32x4 ra0[ACH], rb0[BCH], ra1[ACH], rb1[BCH];
    const unsigned aoff = ((unsigned)(tid >> 3) * lda + (tid & 7) * 8) * 2u;
    const unsigned boff = NT ? ((unsigned)(tid >> 3) * ldb + (tid & 7) * 8) * 2u
                             : ((unsigned)(tid / BPR) * ldb + (tid % BPR) * 8) * 2u;
    const char* Ac = reinterpret_cast<const char*>(A);
    const char* Bc = reinterpret_cast<const char*>(B);
    u16* aw = smem + (tid >> 3) * AROW + (tid & 7) * 8;
    u16* bw = NT ? (smem + ASZ + (tid >> 3) * BROW + (tid & 7) * 8) : (smem + ASZ + (tid / BPR) * BROW + (tid % BPR) * 8);
    const u16* ard = smem + (wm * (BM / 2) + r32) * AROW + hi * 8;
    const u16* brd = NT ? (smem + ASZ + (wn * (BN / 2) + r32) * BROW + hi * 8)
                        : (smem + ASZ + (8 * (g >> 1) + q4) * BROW + wn * (BN / 2) + 16 * (g & 1) + 4 * p4);
    auto load = [&](u32x4(&ra)[ACH], u32x4(&rb)[BCH], int kt) {
#pragma unroll
        for (int i = 0; i < ACH; ++i)
            ra[i] = *reinterpret_cast<const u32x4*>(Ac + (aoff + (unsigned)(32 * i * lda + kt * 64) * 2u));
#pragma unroll
        for (int i = 0; i < BCH; ++i) {
            if (NT)
                rb[i] = *reinterpret_cast<const u32x4*>(Bc + (boff + (unsigned)(32 * i * ldb + kt * 64) * 2u));
            else
                rb[i] = *reinterpret_cast<const u32x4*>(Bc + (boff + (unsigned)((kt * 64 + BRS * i) * ldb) * 2u));
        }
    };
    auto store = [&](const u32x4(&ra)[ACH], const u32x4(&rb)[BCH], int st) {
#pragma unroll
        for (int i = 0; i < ACH; ++i) *reinterpret_cast<u32x4*>(aw + st * STG + 32 * i * AROW) = ra[i];
#pragma unroll
        for (int i = 0; i < BCH; ++i)
            *reinterpret_cast<u32x4*>(bw + st * STG + (NT ? 32 : BRS) * i * BROW) = rb[i];
    };
    auto compute = [&](int st, int ks) {
        bf16x8 af[TM], bfr[TN];
#pragma unroll
        for (int i = 0; i < TM; ++i)
            af[i] = *reinterpret_cast<const bf16x8*>(ard + st * STG + i * 32 * AROW + ks * 16);
#pragma unroll
        for (int j = 0; j < TN; ++j) {
            if (NT) {
                bfr[j] = *reinterpret_cast<const bf16x8*>(brd + st * STG + j * 32 * BROW + ks * 16);
            } else {
                const u16* pB = brd + st * STG + ks * 16 * BROW + j * 32;
                s16x4 lo = tr_read(pB);
                s16x4 hi4 = tr_read(pB + 4 * BROW);
                bfr[j] = bf16x8{lo[0], lo[1], lo[2], lo[3], hi4[0], hi4[1], hi4[2], hi4[3]};
            }
        }
#pragma unroll
        for (int i = 0; i < TM; ++i)
#pragma unroll
            for (int j = 0; j < TN; ++j) acc[i][j] = MFMA32(bfr[j], af[i], acc[i][j]);
    };
    load(ra0, rb0, 0);
    load(ra1, rb1, 1);
    __syncthreads();
    store(ra0, rb0, 0);
    load(ra0, rb0, 2);
    __syncthreads();
    for (int kt = 0; kt + 1 < nk; kt += 2) {
        compute(0, 0);
        compute(0, 1);
        store(ra1, rb1, 1);
        load(ra1, rb1, min(kt + 3, nk - 1));
        compute(0, 2);
        compute(0, 3);
        __syncthreads();
        compute(1, 0);
        compute(1, 1);
        store(ra0, rb0, 0);
        load(ra0, rb0, min(kt + 4, nk - 1));
        compute(1, 2);
        compute(1, 3);
        __syncthreads();
    }
    if (nk & 1) {
        compute(0, 0);
        compute(0, 1);
        compute(0, 2);
        compute(0, 3);
        __syncthreads();
    }
    constexpr int CROW = BN + 8;
#pragma unroll
    for (int i = 0; i < TM; ++i)
#pragma unroll
        for (int j = 0; j < TN; ++j)
#pragma unroll
            for (int rr = 0; rr < 4; ++rr) {
                u32x2 w;
                w[0] = pack2(acc[i][j][4 * rr], acc[i][j][4 * rr + 1]);
                w[1] = pack2(acc[i][j][4 * rr + 2], acc[i][j][4 * rr + 3]);
                *reinterpret_cast<u32x2*>(smem + (wm * (BM / 2) + 32 * i + r32) * CROW + wn * (BN / 2) + 32 * j +
                                          8 * rr + 4 * hi) = w;
            }
    __syncthreads();
    int tid2 = threadIdx.x;
    asm volatile("" : "+v"(tid2));
    const unsigned coff = ((unsigned)(tid2 / BPR) * ldc + (tid2 % BPR) * 8) * 2u;
    __amdgpu_buffer_rsrc_t rs = __builtin_amdgcn_make_buffer_rsrc(C, 0, 0x7fffffff, 0x00020000);
#pragma unroll
    for (int i = 0; i < BM * BN / 2048; ++i) {
        int row = tid2 / BPR + BRS * i, cc = tid2 % BPR;
        u32x4 v = *reinterpret_cast<const u32x4*>(smem + row * CROW + cc * 8);
        __builtin_amdgcn_raw_buffer_store_b128(v, rs, coff + (unsigned)(BRS * i * ldc) * 2u, 0, SC1 ? 16 : 0);
    }
}

struct MegaParams {
    const u16* A_x;
    const u16* AmatTt;
    const u16* B_x;
    const u16* B_ckv;
    const u16* B_wq;
    const u16* B_wuk;
    const u16* B_wuv;
    const u16* B_wo;
    u16* E;
    u16* Qb;
    u16* Kb;
    u16* Vb;
    u16* OB;
    u16* out;
    unsigned* ctl;
};
constexpr int NWORK = 256 - NREC;
constexpr int CTL_Q1 = 0, CTL_C1 = 64, CTL_Q2 = 128, CTL_C2 = 192, CTL_Q3 = 256, CTL_Q4 = 320, CTL_ST = 384, CTL_WORDS = 384 + 512;
constexpr unsigned WSPIN_CAP = 2000000u;

__device__ __forceinline__ int next_ticket(unsigned* q, int* slot) {
    __syncthreads();
    if (threadIdx.x == 0) *slot = (int)(__hip_atomic_fetch_add(q, 1u, __ATOMIC_RELAXED, __HIP_MEMORY_SCOPE_AGENT) + 1u);
    __syncthreads();
    return *slot;
}
__device__ __forceinline__ void publish_count(unsigned* c) {
    asm volatile("s_waitcnt vmcnt(0)" ::: "memory");
    __syncthreads();
    if (threadIdx.x == 0) __hip_atomic_fetch_add(c, 1u, __ATOMIC_RELAXED, __HIP_MEMORY_SCOPE_AGENT);
}
__device__ __forceinline__ void wait_count(unsigned* c, unsigned target) {
    if (threadIdx.x == 0) {
        for (unsigned spin = 0; spin < WSPIN_CAP; ++spin) {
            if (__hip_atomic_load(c, __ATOMIC_RELAXED, __HIP_MEMORY_SCOPE_AGENT) + 1u >= target) break;
            __builtin_amdgcn_s_sleep(16);
        }
        __builtin_amdgcn_fence(__ATOMIC_ACQUIRE, "agent");
        asm volatile("s_waitcnt vmcnt(0)" ::: "memory");
    }
    __syncthreads();
}

__device__ __forceinline__ void postA_task(const RecParams& p, const MegaParams& m, int s, int part, u16* smem) {
    u16* KL = smem;
    u16* PL = smem + 40 * MROW;
    int tid = threadIdx.x;
    asm volatile("" : "+v"(tid));
    const int lane = tid & 63, wid = __builtin_amdgcn_readfirstlane(tid >> 6);
    const int r32 = lane & 31, hi = lane >> 5;
    const u16* ep = m.E + (long)(s * 32 + r32) * (H_ * DM) + wid * 4 * DM + hi * 8;
    bf16x8 e0[28], e1[28];
    auto loadE = [&](bf16x8(&e)[28], int hh) {
#pragma unroll
        for (int ks = 0; ks < 28; ++ks) e[ks] = *reinterpret_cast<const bf16x8*>(ep + hh * DM + ks * 16);
    };
    loadE(e0, 0);
    loadE(e1, 1);
    __syncthreads();
    for (int c = tid; c < 32 * 56; c += 256) {
        int row = c / 56, cc = c - row * 56;
        *reinterpret_cast<u32x4*>(KL + (8 + row) * MROW + cc * 8) =
            *reinterpret_cast<const u32x4*>(m.A_x + (long)(s * 32 + row) * DM + cc * 8);
    }
    {
        const u64* gf = reinterpret_cast<const u64*>(p.G2 + (long)(s + 1) * G2_SLOT + 128 * (lane < NC_BLK ? lane : 0));
        for (unsigned spin = 0; spin < WSPIN_CAP; ++spin) {
            u64 x = ld_gran(gf);
            if (__all((unsigned)(x >> 32) != 0xffffffffu)) break;
            __builtin_amdgcn_s_sleep(8);
        }
        const int ch1 = (tid < 192) ? (256 + tid) : tid;
        u32x4 a, b;
        {
            __amdgpu_buffer_rsrc_t rs2 = __builtin_amdgcn_make_buffer_rsrc(p.G2, 0, 65 * G2_SLOT * 2, 0x00020000);
            for (unsigned spin = 0; spin < WSPIN_CAP; ++spin) {
                asm volatile("" ::: "memory");
                a = ld16_sc1(rs2, tid * 16, (unsigned)(s * G2_SLOT * 2));
                b = ld16_sc1(rs2, ch1 * 16, (unsigned)(s * G2_SLOT * 2));
                unsigned mx = max(umax3(a[1], a[3], b[1]), b[3]);
                if (__all(mx != 0xffffffffu)) break;
                __builtin_amdgcn_s_sleep(8);
            }
        }
        *reinterpret_cast<u32x4*>(KL + ((tid >> 1) & 7) * MROW + 16 * (tid >> 4) + 8 * (tid & 1)) = a;
        *reinterpret_cast<u32x4*>(KL + ((ch1 >> 1) & 7) * MROW + 16 * (ch1 >> 4) + 8 * (ch1 & 1)) = b;
    }
    __syncthreads();
    const u16* k0l = KL + r32 * MROW + hi * 8;
    const u16* k1l = KL + (32 + (r32 & 7)) * MROW + hi * 8;
    const float L2E = 1.4426950408889634f;
    auto scores = [&](const bf16x8(&e)[28], int hh) {
        f32x16 s0 = f32x16{}, s1 = f32x16{};
#pragma unroll
        for (int ks = 0; ks < 28; ++ks) {
            bf16x8 ka = *reinterpret_cast<const bf16x8*>(k0l + ks * 16);
            bf16x8 kb = *reinterpret_cast<const bf16x8*>(k1l + ks * 16);
            s0 = MFMA32(ka, e[ks], s0);
            s1 = MFMA32(kb, e[ks], s1);
        }
#pragma unroll
        for (int r = 0; r < 16; ++r) {
            int kk = crow(r, hi);
            if (kk >= 8 && kk - 8 > r32) s0[r] = -INFINITY;
            int k2 = kk + 32;
            if (k2 >= 40 || k2 - 8 > r32) s1[r] = -INFINITY;
        }
        float mx = -INFINITY;
#pragma unroll
        for (int r = 0; r < 16; ++r) mx = fmaxf(mx, fmaxf(s0[r], s1[r]));
        mx = fmaxf(mx, __shfl_xor(mx, 32));
        float nm = -mx * L2E, psum = 0.f;
#pragma unroll
        for (int r = 0; r < 16; ++r) {
            s0[r] = __builtin_amdgcn_exp2f(fmaf(s0[r], L2E, nm));
            s1[r] = __builtin_amdgcn_exp2f(fmaf(s1[r], L2E, nm));
            psum += s0[r] + s1[r];
        }
        psum += __shfl_xor(psum, 32);
        float inv = 1.f / psum;
        u32x4 a0, a1, a2;
#pragma unroll
        for (int j = 0; j < 4; ++j) {
            a0[j] = pack2(s0[2 * j] * inv, s0[2 * j + 1] * inv);
            a1[j] = pack2(s0[8 + 2 * j] * inv, s0[8 + 2 * j + 1] * inv);
            a2[j] = pack2(s1[2 * j] * inv, s1[2 * j + 1] * inv);
        }
        u16* pw = PL + ((wid * 4 + hh) * 3 * 64 + lane) * 8;
        *reinterpret_cast<u32x4*>(pw) = a0;
        *reinterpret_cast<u32x4*>(pw + 512) = a1;
        *reinterpret_cast<u32x4*>(pw + 1024) = a2;
    };
    scores(e0, 0);
    loadE(e0, 2);
    scores(e1, 1);
    loadE(e1, 3);
    scores(e0, 2);
    scores(e1, 3);
    const int ct = part + 4 * wid;
    const int col = (ct < 14 ? ct : 13) * 32 + r32;
    u64 u[16];
    u32x2 d[16][4];
#pragma unroll
    for (int h = 0; h < 16; ++h) {
        const u64* up = reinterpret_cast<const u64*>(p.Ut + (((long)s * 16 + h) * DM + col) * 8 + 4 * hi);
        const u16* dp = p.Dt + ((long)h * DM + col) * S_ + s * 32 + 4 * hi;
        u[h] = ld_gran(up);
#pragma unroll
        for (int q = 0; q < 4; ++q) d[h][q] = *reinterpret_cast<const u32x2*>(dp + 8 * q);
    }
    __syncthreads();
    f32x16 o = f32x16{};
#pragma unroll
    for (int h = 0; h < 16; ++h) {
        const u16* pr = PL + (h * 3 * 64 + lane) * 8;
        bf16x8 p0 = *reinterpret_cast<const bf16x8*>(pr);
        bf16x8 p1 = *reinterpret_cast<const bf16x8*>(pr + 512);
        bf16x8 p2 = *reinterpret_cast<const bf16x8*>(pr + 1024);
        o = MFMA32(__builtin_bit_cast(bf16x8, (u32x4{(unsigned)u[h], (unsigned)(u[h] >> 32), d[h][0][0], d[h][0][1]})), p0, o);
        o = MFMA32(__builtin_bit_cast(bf16x8, (u32x4{d[h][1][0], d[h][1][1], d[h][2][0], d[h][2][1]})), p1, o);
        o = MFMA32(__builtin_bit_cast(bf16x8, (u32x4{d[h][3][0], d[h][3][1], 0u, 0u})), p2, o);
    }
    if (ct < 14) {
#pragma unroll
        for (int rr = 0; rr < 4; ++rr) {
            u32x2 w;
            w[0] = pack2(o[4 * rr], o[4 * rr + 1]);
            w[1] = pack2(o[4 * rr + 2], o[4 * rr + 3]);
            *reinterpret_cast<u32x2*>(m.out + (long)(s * 32 + r32) * 896 + ct * 32 + 8 * rr + 4 * hi) = w;
        }
    }
}

__device__ __forceinline__ void worker_body(const RecParams& p, const MegaParams& m, u16* smem, int* slot) {
    unsigned* ctl = m.ctl;
    WTS(0);
    for (int ph = 0; ph < 2; ++ph) {
        for (;;) {
            int t = next_ticket(ctl + (ph ? CTL_Q2 : CTL_Q1), slot);
            int kind, idx;
            if (ph == 0) {
                if (t >= 1216) break;
                if (t < 224) { kind = 0; idx = t; }
                else if (t < 480) { kind = 1; idx = t - 224; }
                else if (t < 592) { kind = 0; idx = t - 480 + 224; }
                else if (t < 848) { kind = 1; idx = t - 592 + 256; }
                else if (t < 960) { kind = 0; idx = t - 848 + 336; }
                else { kind = 1; idx = t - 960 + 512; }
            } else {
                if (t >= 1152) break;
                if (t < 208) { kind = 2; idx = t; }
                else if (t < 320) { kind = 0; idx = t - 208 + 448; }
                else if (t < 368) { kind = 2; idx = t - 320 + 208; }
                else if (t < 704) { kind = 0; idx = t - 368 + 560; }
                else { kind = 3; idx = t - 704; }
            }
            if (kind == 1) {
                int mat = idx >> 8, rb = (idx >> 5) & 7, cb = idx & 31;
                const u16* A = (mat < 2) ? m.B_ckv : m.B_x;
                const u16* B = (mat == 0) ? m.B_wuk : (mat == 1 ? m.B_wuv : m.B_wq);
                u16* C = (mat == 0) ? m.Kb : (mat == 1 ? m.Vb : m.Qb);
                int lda = (mat < 2) ? DC : DM;
                gemm_tile<256, 128, false, true>(A + (long)rb * 256 * lda, lda, B + cb * 128, HD,
                                                 C + (long)rb * 256 * HD + cb * 128, HD, lda / 64, smem);
            } else if (kind == 2) {
                attnB_body(m.Qb, m.Kb, m.Vb, m.OB, smem, 15 - (idx >> 4), idx & 15);
            } else {
                const u16 *A, *B;
                u16* C;
                int ldc = H_ * DM, j = 0;
                if (kind == 3) {
                    int rb = idx / 56, cb = idx - rb * 56;
                    A = m.A_x + (long)rb * 256 * DM;
                    B = p.AmatT + (long)cb * 128 * DM;
                    C = m.E + (long)rb * 256 * (H_ * DM) + cb * 128;
                } else {
                    j = idx / 112;
                    int u = idx - j * 112;
                    if (u < 56) {
                        A = m.A_x + (long)j * 256 * DM;
                        B = m.AmatTt + (long)u * 128 * DM;
                        C = p.Bt + (long)j * 256 * (H_ * DM) + u * 128;
                    } else {
                        int mb = (u - 56) >> 1, nb = u & 1;
                        A = p.CmatT + (long)mb * 256 * DM;
                        B = m.A_x + (long)(j * 256 + nb * 128) * DM;
                        C = p.Dt + (long)mb * 256 * S_ + j * 256 + nb * 128;
                        ldc = S_;
                    }
                }
                gemm_tile<256, 128, true, true>(A, DM, B, DM, C, ldc, DM / 64, smem);
                if (kind == 0) publish_count(ctl + CTL_ST + 64 * j);
            }
        }
        publish_count(ctl + (ph ? CTL_C2 : CTL_C1));
        WTS(1 + 2 * ph);
        wait_count(ctl + (ph ? CTL_C2 : CTL_C1), NWORK);
        WTS(2 + 2 * ph);
    }
    for (;;) {
        int t = next_ticket(ctl + CTL_Q3, slot);
        if (t >= 112) break;
        int rb = t / 7, cb = t - rb * 7;
        gemm_tile<128, 64, false, false>(m.OB + (long)rb * 128 * HD, HD, m.B_wo + cb * 64, DM,
                                         m.out + (long)rb * 128 * 896 + 448 + cb * 64, 896, HD / 64, smem);
    }
    WTS(5);
    for (;;) {
        int t = next_ticket(ctl + CTL_Q4, slot);
        if (t >= 4 * NSEG) break;
        postA_task(p, m, t >> 2, t & 3, smem);
    }
    WTS(6);
    WTS(7);
}

constexpr int REC_LDS = 4 * 320 * 4 + 4 * 14 * 1024;
constexpr int GEMM_LDS = 2 * (256 * 72 + 64 * 160) * 2;
constexpr int POST_LDS = (40 * MROW + 16 * 3 * 64 * 8) * 2;
constexpr int cmax(int a, int b) { return a > b ? a : b; }
constexpr int FUSED_LDS = cmax(cmax(ATTN_LDS, REC_LDS), cmax(GEMM_LDS, POST_LDS));

__global__ __launch_bounds__(256) void mega_kernel(RecParams p, MegaParams m) {
    __shared__ __attribute__((aligned(16))) u16 smem[FUSED_LDS / 2 + 8];
    const int b = blockIdx.x;
    if (b < NA_BLK) {
        rec_A_body(p, smem, b);
    } else if (b < NREC) {
        rec_C_body(p, smem, b - NA_BLK);
    } else {
        worker_body(p, m, smem, reinterpret_cast<int*>(smem + FUSED_LDS / 2));
    }
}

template <int BN, bool NT, bool TO = false>
static void launch_gemm(hipStream_t st, const u16* A, const u16* B, u16* C, int lda, int ldb, int ldc, long sA,
                        long sB, long sC, int M, int N, int K, int batch, float scale) {
    dim3 grid(N / BN, (M + 127) / 128, batch);
    hipLaunchKernelGGL((gemm_kernel<BN, NT, TO>), grid, dim3(256), 0, st, A, B, C, lda, ldb, ldc, sA, sB, sC, M, N, K, scale);
}

extern "C" void kernel_launch(void* const* d_in, const int* in_sizes, int n_in, void* d_out, int out_size,
                              void* d_ws, size_t ws_size, hipStream_t stream) {
    const u16* A_x = (const u16*)d_in[0];
    const u16* A_mem0 = (const u16*)d_in[1];
    const u16* A_Wq = (const u16*)d_in[2];
    const u16* A_Wk = (const u16*)d_in[3];
    const u16* A_Wv = (const u16*)d_in[4];
    const u16* A_Wo = (const u16*)d_in[5];
    const u16* B_x = (const u16*)d_in[7];
    const u16* B_ckv = (const u16*)d_in[8];
    const u16* B_wq = (const u16*)d_in[9];
    const u16* B_wuk = (const u16*)d_in[10];
    const u16* B_wuv = (const u16*)d_in[11];
    const u16* B_wo = (const u16*)d_in[12];
    u16* out = (u16*)d_out;

    char* ws = (char*)d_ws;
    const size_t SZ = (size_t)S_ * HD * 2;
    u16* Qb = (u16*)(ws + 0 * SZ);
    u16* Kb = (u16*)(ws + 1 * SZ);
    u16* Vb = (u16*)(ws + 2 * SZ);
    u16* OB = (u16*)(ws + 3 * SZ);
    size_t off = 4 * SZ;
    u16* AmatT = (u16*)(ws + off); off += (size_t)H_ * DM * DM * 2;
    u16* AmatTt = (u16*)(ws + off); off += (size_t)H_ * DM * DM * 2;
    u16* CmatT = (u16*)(ws + off); off += (size_t)H_ * DM * DM * 2;
    u16* Bt = (u16*)(ws + off); off += (size_t)H_ * DM * S_ * 2;
    u16* Dt = (u16*)(ws + off); off += (size_t)H_ * DM * S_ * 2;
    u16* E = (u16*)(ws + off); off += (size_t)H_ * DM * S_ * 2;
    u16* Ut = (u16*)(ws + off); off += (size_t)NSEG * H_ * DM * 8 * 2;
    char* gbase = ws + off;
    u16* G1 = (u16*)gbase;
    u16* G2 = G1 + 63 * G1_SLOT;
    unsigned* ctl = (unsigned*)(G2 + 65 * G2_SLOT);
    size_t gbytes = (size_t)(63 * G1_SLOT + 65 * G2_SLOT) * 2 + (size_t)CTL_WORDS * 4;
    off += gbytes;

    hipMemsetAsync(gbase, 0xFF, gbytes, stream);
    launch_gemm<64, true>(stream, A_Wk, A_Wq, AmatT, HD, HD, DM, DH, DH, (long)DM * DM, DM, DM, DH, H_, 0.0625f);
    launch_gemm<64, false, true>(stream, A_Wv, A_Wo, CmatT, HD, DM, DM, DH, (long)DH * DM, (long)DM * DM, DM, DM, DH, H_, 1.f);
    launch_gemm<64, true>(stream, A_Wq, A_Wk, AmatTt, HD, HD, DM, DH, DH, (long)DM * DM, DM, DM, DH, H_, 0.0625f);
    RecParams rp;
    rp.mem0 = A_mem0; rp.AmatT = AmatT; rp.CmatT = CmatT; rp.Bt = Bt; rp.Dt = Dt; rp.Ut = Ut; rp.G1 = G1; rp.G2 = G2;
    rp.stage = ctl + CTL_ST;
    MegaParams mp;
    mp.A_x = A_x; mp.AmatTt = AmatTt;
    mp.B_x = B_x; mp.B_ckv = B_ckv; mp.B_wq = B_wq; mp.B_wuk = B_wuk; mp.B_wuv = B_wuv; mp.B_wo = B_wo;
    mp.E = E; mp.Qb = Qb; mp.Kb = Kb; mp.Vb = Vb; mp.OB = OB;
    mp.out = out; mp.ctl = ctl;
    hipLaunchKernelGGL(mega_kernel, dim3(256), dim3(256), 0, stream, rp, mp);
}
```

```cpp
#include <hip/hip_runtime.h>
#include <hip/hip_bf16.h>
#include <stdint.h>

typedef unsigned short u16;
typedef __attribute__((ext_vector_type(8))) short bf16x8;
typedef __attribute__((ext_vector_type(4))) short s16x4;
typedef __attribute__((ext_vector_type(16))) float f32x16;
typedef __attribute__((ext_vector_type(4))) float f32x4;
typedef __attribute__((ext_vector_type(4))) unsigned u32x4;
typedef __attribute__((ext_vector_type(2))) unsigned u32x2;
typedef short v4i16_t __attribute__((ext_vector_type(4)));

#define LAS __attribute__((address_space(3)))

__device__ __forceinline__ float bf2f(u16 x) { return __uint_as_float(((unsigned)x) << 16); }
__device__ __forceinline__ u16 f2bf(float f) {
    unsigned u = __float_as_uint(f);
    u += 0x7fffu + ((u >> 16) & 1u);
    return (u16)(u >> 16);
}
typedef __bf16 bf16x2_t __attribute__((ext_vector_type(2)));
__device__ __forceinline__ unsigned pack2(float lo, float hi) {
    bf16x2_t r = {(__bf16)lo, (__bf16)hi};
    return __builtin_bit_cast(unsigned, r);
}
__device__ __forceinline__ s16x4 tr_read(const void* p) {
    return __builtin_bit_cast(s16x4, __builtin_amdgcn_ds_read_tr16_b64_v4i16((LAS v4i16_t*)p));
}
#define MFMA32(a, b, c) __builtin_amdgcn_mfma_f32_32x32x16_bf16(a, b, c, 0, 0, 0)
#define MFMA16(a, b, c) __builtin_amdgcn_mfma_f32_16x16x32_bf16(a, b, c, 0, 0, 0)

__device__ __forceinline__ int crow(int r, int hi) { return (r & 3) + 8 * (r >> 2) + 4 * hi; }

constexpr int S_ = 2048, DM = 448, H_ = 16, DH = 256, HD = 4096, MM = 8, SEG = 32, NSEG = 64, DC = 1024;

template <int BN, bool NT, bool TO>
__global__ __launch_bounds__(256) void gemm_kernel(const u16* __restrict__ A, const u16* __restrict__ B,
                                                   u16* __restrict__ C, int lda, int ldb, int ldc,
                                                   long sA, long sB, long sC, int M, int N, int K,
                                                   float scale) {
    constexpr int BM = 128, BK = 64;
    constexpr int AROW = 72;
    constexpr int BROW = NT ? 72 : (BN + 32);
    constexpr int BLDS = NT ? BN * 72 : 64 * (BN + 32);
    __shared__ __attribute__((aligned(16))) u16 smem[BM * AROW + BLDS];
    u16* As = smem;
    u16* Bs = smem + BM * AROW;
    const int tid = threadIdx.x, lane = tid & 63, wid = tid >> 6;
    const int wm = wid >> 1, wn = wid & 1;
    const int m0 = blockIdx.y * BM, n0 = blockIdx.x * BN;
    A += (long)blockIdx.z * sA;
    B += (long)blockIdx.z * sB;
    C += (long)blockIdx.z * sC;
    constexpr int NTI = BN / 64;
    f32x16 acc[2][NTI];
#pragma unroll
    for (int i = 0; i < 2; ++i)
#pragma unroll
        for (int j = 0; j < NTI; ++j) acc[i][j] = f32x16{};

    constexpr int BCH = BN / 32;
    u32x4 ra[4], rb[BCH];
    auto load_tiles = [&](int k0) {
#pragma unroll
        for (int i = 0; i < 4; ++i) {
            int c = tid + 256 * i, row = c >> 3, kc = c & 7;
            int gr = m0 + row;
            if (gr < M)
                ra[i] = *reinterpret_cast<const u32x4*>(A + (long)gr * lda + k0 + kc * 8);
            else
                ra[i] = u32x4{0, 0, 0, 0};
        }
#pragma unroll
        for (int i = 0; i < BCH; ++i) {
            int c = tid + 256 * i;
            if (NT) {
                int row = c >> 3, kc = c & 7;
                rb[i] = *reinterpret_cast<const u32x4*>(B + (long)(n0 + row) * ldb + k0 + kc * 8);
            } else {
                int krow = c / (BN / 8), nc = c % (BN / 8);
                rb[i] = *reinterpret_cast<const u32x4*>(B + (long)(k0 + krow) * ldb + n0 + nc * 8);
            }
        }
    };
    auto store_tiles = [&]() {
#pragma unroll
        for (int i = 0; i < 4; ++i) {
            int c = tid + 256 * i, row = c >> 3, kc = c & 7;
            *reinterpret_cast<u32x4*>(As + row * AROW + kc * 8) = ra[i];
        }
#pragma unroll
        for (int i = 0; i < BCH; ++i) {
            int c = tid + 256 * i;
            if (NT) {
                int row = c >> 3, kc = c & 7;
                *reinterpret_cast<u32x4*>(Bs + row * BROW + kc * 8) = rb[i];
            } else {
                int krow = c / (BN / 8), nc = c % (BN / 8);
                *reinterpret_cast<u32x4*>(Bs + krow * BROW + nc * 8) = rb[i];
            }
        }
    };
    const int r32 = lane & 31, hi = lane >> 5, g = lane >> 4, q4 = (lane & 15) >> 2, p4 = lane & 3;
    load_tiles(0);
    const int nk = K / BK;
    for (int kt = 0; kt < nk; ++kt) {
        __syncthreads();
        store_tiles();
        __syncthreads();
        if (kt + 1 < nk) load_tiles((kt + 1) * BK);
#pragma unroll
        for (int ks = 0; ks < 4; ++ks) {
            bf16x8 af[2], bfr[NTI];
#pragma unroll
            for (int i = 0; i < 2; ++i)
                af[i] = *reinterpret_cast<const bf16x8*>(As + (wm * 64 + i * 32 + r32) * AROW + ks * 16 + hi * 8);
#pragma unroll
            for (int j = 0; j < NTI; ++j) {
                int nb = wn * (BN / 2) + j * 32;
                if (NT) {
                    bfr[j] = *reinterpret_cast<const bf16x8*>(Bs + (nb + r32) * BROW + ks * 16 + hi * 8);
                } else {
                    const u16* pB = Bs + (ks * 16 + 8 * (g >> 1) + q4) * BROW + nb + 16 * (g & 1) + 4 * p4;
                    s16x4 lo = tr_read(pB);
                    s16x4 hi4 = tr_read(pB + 4 * BROW);
                    bfr[j] = bf16x8{lo[0], lo[1], lo[2], lo[3], hi4[0], hi4[1], hi4[2], hi4[3]};
                }
            }
#pragma unroll
            for (int i = 0; i < 2; ++i)
#pragma unroll
                for (int j = 0; j < NTI; ++j) acc[i][j] = MFMA32(af[i], bfr[j], acc[i][j]);
        }
    }
#pragma unroll
    for (int i = 0; i < 2; ++i)
#pragma unroll
        for (int j = 0; j < NTI; ++j) {
            int col = n0 + wn * (BN / 2) + j * 32 + r32;
            if (TO) {
#pragma unroll
                for (int rr = 0; rr < 4; ++rr) {
                    int row = m0 + wm * 64 + i * 32 + 8 * rr + 4 * hi;
                    u32x2 w;
                    w[0] = pack2(acc[i][j][4 * rr] * scale, acc[i][j][4 * rr + 1] * scale);
                    w[1] = pack2(acc[i][j][4 * rr + 2] * scale, acc[i][j][4 * rr + 3] * scale);
                    if (row < M) *reinterpret_cast<u32x2*>(C + (long)col * ldc + row) = w;
                }
            } else {
#pragma unroll
                for (int r = 0; r < 16; ++r) {
                    int row = m0 + wm * 64 + i * 32 + crow(r, hi);
                    if (row < M) C[(long)row * ldc + col] = f2bf(acc[i][j][r] * scale);
                }
            }
        }
}

constexpr int KROW = 264;
constexpr int VROW = 288;
constexpr int ATTN_LDS = (64 * KROW + 64 * VROW) * 2;
__device__ __forceinline__ void attnB_body(const u16* __restrict__ Q, const u16* __restrict__ K,
                                           const u16* __restrict__ V, u16* __restrict__ O, u16* smem, int qb, int h) {
    u16* Ks = smem;
    u16* Vs = smem + 64 * KROW;
    int tid = threadIdx.x;
    asm volatile("" : "+v"(tid));
    const int lane = tid & 63, wid = tid >> 6;
    const int q0 = qb * 128;
    const int r32 = lane & 31, hi = lane >> 5, g = lane >> 4, q4 = (lane & 15) >> 2, p4 = lane & 3;
    const int qrow = q0 + wid * 32 + r32;
    bf16x8 qf[16];
#pragma unroll
    for (int ks = 0; ks < 16; ++ks)
        qf[ks] = *reinterpret_cast<const bf16x8*>(Q + (long)qrow * HD + h * DH + ks * 16 + hi * 8);
    f32x16 o[8];
#pragma unroll
    for (int i = 0; i < 8; ++i) o[i] = f32x16{};
    float mrun = -INFINITY, lrun = 0.f;
    const float c2 = 0.0625f * 1.4426950408889634f;
    const int nt = (q0 + 128) / 64;
    u32x4 rk[8], rv[8];
    auto load_kv = [&](int t) {
#pragma unroll
        for (int i = 0; i < 8; ++i) {
            int c = tid + 256 * i, row = c >> 5, cc = c & 31;
            long off = (long)(t * 64 + row) * HD + h * DH + cc * 8;
            rk[i] = *reinterpret_cast<const u32x4*>(K + off);
            rv[i] = *reinterpret_cast<const u32x4*>(V + off);
        }
    };
    auto store_kv = [&]() {
#pragma unroll
        for (int i = 0; i < 8; ++i) {
            int c = tid + 256 * i, row = c >> 5, cc = c & 31;
            *reinterpret_cast<u32x4*>(Ks + row * KROW + cc * 8) = rk[i];
            *reinterpret_cast<u32x4*>(Vs + row * VROW + cc * 8) = rv[i];
        }
    };
    load_kv(0);
    for (int t = 0; t < nt; ++t) {
        __syncthreads();
        store_kv();
        __syncthreads();
        if (t + 1 < nt) load_kv(t + 1);
        const int kbase = t * 64;
        if (kbase <= q0 + wid * 32 + 31) {
            f32x16 s0 = f32x16{}, s1 = f32x16{};
#pragma unroll
            for (int ks = 0; ks < 16; ++ks) {
                bf16x8 k0 = *reinterpret_cast<const bf16x8*>(Ks + r32 * KROW + ks * 16 + hi * 8);
                bf16x8 k1 = *reinterpret_cast<const bf16x8*>(Ks + (32 + r32) * KROW + ks * 16 + hi * 8);
                s0 = MFMA32(k0, qf[ks], s0);
                s1 = MFMA32(k1, qf[ks], s1);
                if ((ks & 3) == 3) __builtin_amdgcn_sched_barrier(0);
            }
            if (kbase + 63 > q0 + wid * 32) {
#pragma unroll
                for (int r = 0; r < 16; ++r) {
                    int kk = kbase + crow(r, hi);
                    if (kk > qrow) s0[r] = -INFINITY;
                    if (kk + 32 > qrow) s1[r] = -INFINITY;
                }
            }
            float mx = -INFINITY;
#pragma unroll
            for (int r = 0; r < 16; ++r) mx = fmaxf(mx, fmaxf(s0[r], s1[r]));
            mx = fmaxf(mx, __shfl_xor(mx, 32));
            float mnew = fmaxf(mrun, mx);
            float alpha = __builtin_amdgcn_exp2f((mrun - mnew) * c2);
            float nm = -mnew * c2;
            float psum = 0.f;
#pragma unroll
            for (int r = 0; r < 16; ++r) {
                s0[r] = __builtin_amdgcn_exp2f(fmaf(s0[r], c2, nm));
                s1[r] = __builtin_amdgcn_exp2f(fmaf(s1[r], c2, nm));
                psum += s0[r] + s1[r];
            }
            psum += __shfl_xor(psum, 32);
            lrun = lrun * alpha + psum;
            mrun = mnew;
#pragma unroll
            for (int i = 0; i < 8; ++i)
#pragma unroll
                for (int r = 0; r < 16; ++r) o[i][r] *= alpha;
            bf16x8 pf[4];
#pragma unroll
            for (int s = 0; s < 2; ++s) {
                u32x4 a, b;
#pragma unroll
                for (int j = 0; j < 4; ++j) {
                    a[j] = pack2(s0[8 * s + 2 * j], s0[8 * s + 2 * j + 1]);
                    b[j] = pack2(s1[8 * s + 2 * j], s1[8 * s + 2 * j + 1]);
                }
                pf[s] = __builtin_bit_cast(bf16x8, a);
                pf[2 + s] = __builtin_bit_cast(bf16x8, b);
            }
#pragma unroll
            for (int dt = 0; dt < 8; ++dt) {
#pragma unroll
                for (int s = 0; s < 4; ++s) {
                    const u16* pV = Vs + (16 * s + 4 * hi + q4) * VROW + 32 * dt + 16 * (g & 1) + 4 * p4;
                    s16x4 lo = tr_read(pV);
                    s16x4 hi4 = tr_read(pV + 8 * VROW);
                    bf16x8 vf = bf16x8{lo[0], lo[1], lo[2], lo[3], hi4[0], hi4[1], hi4[2], hi4[3]};
                    o[dt] = MFMA32(vf, pf[s], o[dt]);
                }
                __builtin_amdgcn_sched_barrier(0);
            }
        }
    }
    float inv = 1.f / lrun;
#pragma unroll
    for (int dt = 0; dt < 8; ++dt)
#pragma unroll
        for (int rr = 0; rr < 4; ++rr) {
            u32x2 w;
            w[0] = pack2(o[dt][4 * rr] * inv, o[dt][4 * rr + 1] * inv);
            w[1] = pack2(o[dt][4 * rr + 2] * inv, o[dt][4 * rr + 3] * inv);
            __hip_atomic_store(reinterpret_cast<unsigned long long*>(O + (long)qrow * HD + h * DH + 32 * dt + 8 * rr + 4 * hi),
                               ((unsigned long long)w[1] << 32) | w[0], __ATOMIC_RELAXED, __HIP_MEMORY_SCOPE_AGENT);
        }
}

typedef unsigned long long u64;
__device__ __forceinline__ u64 ld_gran(const u64* p) {
    return __hip_atomic_load(p, __ATOMIC_RELAXED, __HIP_MEMORY_SCOPE_AGENT);
}
__device__ __forceinline__ void st_gran4(u16* p, unsigned lo, unsigned hi) {
    hi = min(hi, 0xfffffffeu);
    __hip_atomic_store(reinterpret_cast<u64*>(p), ((u64)hi << 32) | (u64)lo, __ATOMIC_RELAXED,
                       __HIP_MEMORY_SCOPE_AGENT);
}
__device__ __forceinline__ void st_gran8(__amdgpu_buffer_rsrc_t rs, unsigned voff, unsigned soff, bool act, unsigned lo,
                                         unsigned hi) {
    hi = min(hi, 0xfffffffeu);
    unsigned lo2 = (unsigned)__builtin_amdgcn_mov_dpp((int)lo, 0xF5, 0xf, 0xf, true);
    unsigned hi2 = (unsigned)__builtin_amdgcn_mov_dpp((int)hi, 0xF5, 0xf, 0xf, true);
    if (act) __builtin_amdgcn_raw_buffer_store_b128(u32x4{lo, hi, lo2, hi2}, rs, voff, soff, 16);
}
constexpr int NA_BLK = 16, NC_BLK = 28, NREC = NA_BLK + NC_BLK;
constexpr int MROW = 456;
constexpr int G1_SLOT = 16 * 8 * 40;
constexpr int G2_SLOT = 8 * 448;
constexpr unsigned SPIN_CAP = 400000u;
#define SLP_A 45
#define SLP_C1 30
#define SLP_C2 15

#define WTS(slot)
#define TS(slot)
struct RecParams {
    const u16* mem0;
    const u16* AmatT;
    const u16* CmatT;
    u16* Bt;
    u16* Dt;
    const unsigned* stage;
    u16* Ut;
    u16* G1;
    u16* G2;
};

__device__ __forceinline__ float red16_max(float x) {
    asm volatile(
        "s_nop 1\n\tv_max_f32_dpp %0, %0, %0 quad_perm:[1,0,3,2] row_mask:0xf bank_mask:0xf\n\t"
        "s_nop 1\n\tv_max_f32_dpp %0, %0, %0 quad_perm:[2,3,0,1] row_mask:0xf bank_mask:0xf\n\t"
        "s_nop 1\n\tv_max_f32_dpp %0, %0, %0 row_half_mirror row_mask:0xf bank_mask:0xf\n\t"
        "s_nop 1\n\tv_max_f32_dpp %0, %0, %0 row_mirror row_mask:0xf bank_mask:0xf\n\t"
        "s_nop 1"
        : "+v"(x));
    return x;
}
__device__ __forceinline__ float red16_sum(float x) {
    asm volatile(
        "s_nop 1\n\tv_add_f32_dpp %0, %0, %0 quad_perm:[1,0,3,2] row_mask:0xf bank_mask:0xf\n\t"
        "s_nop 1\n\tv_add_f32_dpp %0, %0, %0 quad_perm:[2,3,0,1] row_mask:0xf bank_mask:0xf\n\t"
        "s_nop 1\n\tv_add_f32_dpp %0, %0, %0 row_half_mirror row_mask:0xf bank_mask:0xf\n\t"
        "s_nop 1\n\tv_add_f32_dpp %0, %0, %0 row_mirror row_mask:0xf bank_mask:0xf\n\t"
        "s_nop 1"
        : "+v"(x));
    return x;
}
__device__ __forceinline__ u32x4 ld16_sc1(__amdgpu_buffer_rsrc_t rs, unsigned voff, unsigned soff) {
    return __builtin_amdgcn_raw_buffer_load_b128(rs, voff, soff, 16);
}
__device__ __forceinline__ unsigned umax3(unsigned a, unsigned b, unsigned c) { return max(max(a, b), c); }

__device__ __forceinline__ void mfma_a0(f32x4& d, const bf16x8& w, const u32x4& m) {
    asm volatile("v_mfma_f32_16x16x32_bf16 %0, %1, %2, 0" : "=&v"(d) : "a"(w), "v"(m));
}
__device__ __forceinline__ void mfma_a(f32x4& d, const bf16x8& w, const u32x4& m) {
    asm volatile("v_mfma_f32_16x16x32_bf16 %0, %1, %2, %0" : "+v"(d) : "a"(w), "v"(m));
}
__device__ __forceinline__ void mfma_v0(f32x4& d, const bf16x8& w, const u32x4& m) {
    asm volatile("v_mfma_f32_16x16x32_bf16 %0, %1, %2, 0" : "=&v"(d) : "v"(w), "v"(m));
}
__device__ __forceinline__ void mfma_v(f32x4& d, const bf16x8& w, const u32x4& m) {
    asm volatile("v_mfma_f32_16x16x32_bf16 %0, %1, %2, %0" : "+v"(d) : "v"(w), "v"(m));
}
__device__ __forceinline__ void lds_rd16(bf16x8& d, unsigned addr, int off) {
    asm volatile("ds_read_b128 %0, %1 offset:%2" : "=v"(d) : "v"(addr), "n"(off));
}
__device__ __forceinline__ void lds_rd16u(u32x4& d, unsigned addr) {
    asm volatile("ds_read_b128 %0, %1" : "=v"(d) : "v"(addr));
}
template <int N>
__device__ __forceinline__ void wait_lgkm() {
    asm volatile("s_waitcnt lgkmcnt(%0)" ::"n"(N) : "memory");
}
constexpr int sch_n(int q) { return q <= 8 ? 7 + 2 * (q + 1) : (q <= 11 ? 25 + (q - 8) : 28); }
constexpr int sch_m(int i) { return i < 5 ? 2 + i : 7 + 2 * (i - 5); }
constexpr int sch_w(int i) { return i < 2 ? i : (i - 2 <= 8 ? 8 + 2 * (i - 2) : 25 + (i - 11)); }

constexpr unsigned STAGE_TILES = 112;
__device__ __forceinline__ void wait_stage(const unsigned* c) {
    for (unsigned spin = 0; spin < 4000000u; ++spin) {
        if (__hip_atomic_load(c, __ATOMIC_RELAXED, __HIP_MEMORY_SCOPE_AGENT) + 1u >= STAGE_TILES) break;
        __builtin_amdgcn_s_sleep(4);
    }
}

__device__ __forceinline__ void rec_A_body(const RecParams& p, u16* smem, int h) {
    float* Sc = reinterpret_cast<float*>(smem);
    const int tid = threadIdx.x, lane = tid & 63, wid = __builtin_amdgcn_readfirstlane(tid >> 6);
    const int c16 = lane & 15, g = lane >> 4;
    int kq[14];
#pragma unroll
    for (int q = 0; q < 14; ++q) {
        if (q < 3) kq[q] = 3 * wid + q;
        else if (q == 3) kq[q] = 12 + (wid >> 1);
        else if (q < 13) kq[q] = (q - 4 < 3 * wid) ? (q - 4) : (q - 1);
        else kq[q] = 13 - (wid >> 1);
    }
    bf16x8 W[6][14];
    u16* WL = smem + 4 * 320 * 2 + (wid * 14 * 64 + lane) * 8;
#pragma unroll
    for (int t = 0; t < 7; ++t) {
        const int ksp = (t < 6) ? (3 * wid + (t >> 1)) : (12 + (wid >> 1));
        const int half = (t < 6) ? (t & 1) : (wid & 1);
        const int n = 32 * ksp + 8 * (c16 >> 2) + 4 * half + (c16 & 3);
        const u16* ap = p.AmatT + ((long)h * DM + n) * DM + 8 * g;
#pragma unroll
        for (int q = 0; q < 14; ++q) {
            bf16x8 w = *reinterpret_cast<const bf16x8*>(ap + 32 * kq[q]);
            if (t < 6)
                W[t][q] = w;
            else
                *reinterpret_cast<bf16x8*>(WL + q * 512) = w;
        }
    }
    bf16x8 X[2][4];
    const bool q3ok = (wid & 1) == 0;
    __amdgpu_buffer_rsrc_t rsb = __builtin_amdgcn_make_buffer_rsrc(p.Bt, 0, S_ * H_ * DM * 2, 0x00020000);
    const unsigned xvoff = (c16 * (H_ * DM) + h * DM + 8 * g) * 2;
    auto load_X = [&](int s) {
        if ((s & 7) == 0) wait_stage(p.stage + 64 * (s >> 3));
#pragma unroll
        for (int tl = 0; tl < 2; ++tl)
#pragma unroll
            for (int q = 0; q < 4; ++q) {
                bf16x8 w = __builtin_bit_cast(
                    bf16x8, ld16_sc1(rsb, xvoff, (unsigned)(((s * 32 + tl * 16) * (H_ * DM) + 32 * kq[q]) * 2)));
                if (q == 3 && !q3ok) w = bf16x8{0, 0, 0, 0, 0, 0, 0, 0};
                X[tl][q] = w;
            }
    };
    load_X(0);
    const int srow = 4 * (wid & 1) + g;
    const bool sact = c16 < 10;
    const float* sread = Sc + srow * 40 + (sact ? 4 * c16 : 0);
    float* swr_mm = Sc + wid * 320 + (c16 & 7) * 40 + 8 * (g & 1);
    float* swr_x0 = Sc + wid * 320 + (c16 & 7) * 40 + (g == 0 ? 4 : 4 * g + 8);
    float* swr_x1 = Sc + wid * 320 + (c16 & 7) * 40 + 24 + 4 * g;
    u16* memL = smem + 4 * 320 * 2 + 4 * 14 * 512;
    const int ch1 = (tid < 192) ? (256 + tid) : tid;
    u16* mw0 = memL + ((tid >> 1) & 7) * MROW + 16 * (tid >> 4) + 8 * (tid & 1);
    u16* mw1 = memL + ((ch1 >> 1) & 7) * MROW + 16 * (ch1 >> 4) + 8 * (ch1 & 1);
    const unsigned ml_addr = (unsigned)(uintptr_t)(LAS u16*)(memL + (c16 & 7) * MROW + 8 * g);
    __amdgpu_buffer_rsrc_t rs2 = __builtin_amdgcn_make_buffer_rsrc(p.G2, 0, 65 * G2_SLOT * 2, 0x00020000);
    __amdgpu_buffer_rsrc_t rs1 = __builtin_amdgcn_make_buffer_rsrc(p.G1, 0, 63 * G1_SLOT * 2, 0x00020000);
    const unsigned g1off = (h * 320 + srow * 40 + 4 * c16) * 2;
    const bool sst = sact && !(c16 & 1);
    const unsigned wl_addr = (unsigned)(uintptr_t)(LAS u16*)WL;
    bool dead = false;
    for (int s = 0; s < NSEG - 1; ++s) {
        u32x4 mf[14];
        bf16x8 w6[3];
        lds_rd16(w6[0], wl_addr, 0);
        lds_rd16(w6[1], wl_addr, 1024);
        {
            u32x4 a, b;
            for (unsigned spin = 0;; ++spin) {
                asm volatile("" ::: "memory");
                a = ld16_sc1(rs2, tid * 16, (unsigned)(s * G2_SLOT * 2));
                b = ld16_sc1(rs2, ch1 * 16, (unsigned)(s * G2_SLOT * 2));
                unsigned mx = max(umax3(a[1], a[3], b[1]), b[3]);
                if (__all(mx != 0xffffffffu) || dead) break;
                if (spin > SPIN_CAP) dead = true;
            }
            *reinterpret_cast<u32x4*>(mw0) = a;
            *reinterpret_cast<u32x4*>(mw1) = b;
            __syncthreads();
        }
        TS(1);
#pragma unroll
        for (int q = 0; q < 5; ++q) lds_rd16u(mf[q], ml_addr + 64 * kq[q]);
        f32x4 acc[7], ax[2];
#pragma unroll
        for (int q = 0; q < 14; ++q) {
            if (q + 5 < 14) lds_rd16u(mf[q + 5], ml_addr + 64 * kq[q + 5]);
            if (q + 2 < 14) lds_rd16(w6[(q + 2) % 3], wl_addr, (q + 2) * 1024);
            switch (q) {
#define WM(Q) case Q: wait_lgkm<sch_n(Q) - sch_m(Q) - 1>(); break;
                WM(0) WM(1) WM(2) WM(3) WM(4) WM(5) WM(6) WM(7) WM(8) WM(9) WM(10) WM(11) WM(12) WM(13)
#undef WM
            }
#pragma unroll
            for (int t = 0; t < 4; ++t) {
                if (q == 0) mfma_a0(acc[t], W[t][q], mf[q]);
                else mfma_a(acc[t], W[t][q], mf[q]);
            }
#pragma unroll
            for (int t = 4; t < 6; ++t) {
                if (t == 4 && q < 6) {
                    if (q == 0) mfma_a0(acc[t], W[t][q], mf[q]);
                    else mfma_a(acc[t], W[t][q], mf[q]);
                } else {
                    if (q == 0) mfma_v0(acc[t], W[t][q], mf[q]);
                    else mfma_v(acc[t], W[t][q], mf[q]);
                }
            }
            switch (q) {
#define WW(Q) case Q: wait_lgkm<sch_n(Q) - sch_w(Q) - 1>(); break;
                WW(0) WW(1) WW(2) WW(3) WW(4) WW(5) WW(6) WW(7) WW(8) WW(9) WW(10) WW(11) WW(12) WW(13)
#undef WW
            }
            if (q == 0) mfma_v0(acc[6], w6[0], mf[q]);
            else mfma_v(acc[6], w6[q % 3], mf[q]);
            if (q < 4) {
                if (q == 0) {
                    mfma_v0(ax[0], X[0][q], mf[q]);
                    mfma_v0(ax[1], X[1][q], mf[q]);
                } else {
                    mfma_v(ax[0], X[0][q], mf[q]);
                    mfma_v(ax[1], X[1][q], mf[q]);
                }
            }
            if (q == 4) {
                asm volatile("" ::: "memory");
                load_X(min(s + 1, NSEG - 2));
                asm volatile("" ::: "memory");
            }
        }
        asm volatile("s_nop 7\n\ts_nop 1"
                     : "+v"(acc[0]), "+v"(acc[1]), "+v"(acc[2]), "+v"(acc[3]), "+v"(acc[4]), "+v"(acc[5]), "+v"(acc[6]),
                       "+v"(ax[0]), "+v"(ax[1]));
        f32x4 sa = f32x4{0.f, 0.f, 0.f, 0.f}, sb = f32x4{0.f, 0.f, 0.f, 0.f};
#pragma unroll
        for (int pr = 0; pr < 4; ++pr) {
            u32x4 bv;
            if (pr < 3) {
                bv = u32x4{pack2(acc[2 * pr][0], acc[2 * pr][1]), pack2(acc[2 * pr][2], acc[2 * pr][3]),
                           pack2(acc[2 * pr + 1][0], acc[2 * pr + 1][1]), pack2(acc[2 * pr + 1][2], acc[2 * pr + 1][3])};
            } else {
                unsigned t0 = pack2(acc[6][0], acc[6][1]), t1 = pack2(acc[6][2], acc[6][3]);
                bv = q3ok ? u32x4{t0, t1, 0u, 0u} : u32x4{0u, 0u, t0, t1};
            }
            if (pr & 1)
                sb = MFMA16(__builtin_bit_cast(bf16x8, mf[pr]), __builtin_bit_cast(bf16x8, bv), sb);
            else
                sa = MFMA16(__builtin_bit_cast(bf16x8, mf[pr]), __builtin_bit_cast(bf16x8, bv), sa);
        }
        if (c16 < 8) {
            *reinterpret_cast<f32x4*>(swr_x0) = ax[0];
            *reinterpret_cast<f32x4*>(swr_x1) = ax[1];
        }
        sa += sb;
        if (c16 < 8 && g < 2) *reinterpret_cast<f32x4*>(swr_mm) = sa;
        TS(2);
        __syncthreads();
        if (wid < 2) {
            f32x4 x0 = *reinterpret_cast<const f32x4*>(sread);
            f32x4 x1 = *reinterpret_cast<const f32x4*>(sread + 320);
            f32x4 x2 = *reinterpret_cast<const f32x4*>(sread + 640);
            f32x4 x3 = *reinterpret_cast<const f32x4*>(sread + 960);
            f32x4 a = (x0 + x1) + (x2 + x3);
            float mx = sact ? __builtin_fmaxf(__builtin_fmaxf(a[0], a[1]), __builtin_fmaxf(a[2], a[3])) : -INFINITY;
            mx = red16_max(mx);
            const float L2E = 1.4426950408889634f;
            float nm = -mx * L2E;
            float e0 = __builtin_amdgcn_exp2f(__builtin_fmaf(a[0], L2E, nm));
            float e1 = __builtin_amdgcn_exp2f(__builtin_fmaf(a[1], L2E, nm));
            float e2 = __builtin_amdgcn_exp2f(__builtin_fmaf(a[2], L2E, nm));
            float e3 = __builtin_amdgcn_exp2f(__builtin_fmaf(a[3], L2E, nm));
            float sm = sact ? ((e0 + e1) + (e2 + e3)) : 0.f;
            sm = red16_sum(sm);
            float inv = __builtin_amdgcn_rcpf(sm);
            st_gran8(rs1, g1off, (unsigned)(s * G1_SLOT * 2), sst, pack2(e0 * inv, e1 * inv), pack2(e2 * inv, e3 * inv));
        }
        TS(3);
        __builtin_amdgcn_s_sleep(SLP_A);
    }
}

__device__ __forceinline__ void rec_C_body(const RecParams& p, u16* smem, int c) {
    u16* memL = smem;
    float* red = reinterpret_cast<float*>(smem + 16 * MROW);
    const int tid = threadIdx.x, lane = tid & 63, wid = __builtin_amdgcn_readfirstlane(tid >> 6);
    const int c16 = lane & 15, g = lane >> 4;
    for (int i = tid; i < 8 * MROW / 2; i += 256) reinterpret_cast<unsigned*>(memL + 8 * MROW)[i] = 0u;
    const int ch1 = (tid < 192) ? (256 + tid) : tid;
    if (tid < 32) {
        u32x2 m0 = *reinterpret_cast<const u32x2*>(p.mem0 + (tid >> 2) * DM + 16 * c + 4 * (tid & 3));
        st_gran4(p.G2 + 128 * c + 4 * tid, m0[0], m0[1]);
    }
    u16* mw0 = memL + ((tid >> 1) & 7) * MROW + 16 * (tid >> 4) + 8 * (tid & 1);
    u16* mw1 = memL + ((ch1 >> 1) & 7) * MROW + 16 * (ch1 >> 4) + 8 * (ch1 & 1);
    const int col = c * 16 + c16;
    bf16x8 W[4][14];
#pragma unroll
    for (int hh = 0; hh < 4; ++hh) {
        const u16* cp = p.CmatT + ((long)(wid * 4 + hh) * DM + col) * DM + 8 * g;
#pragma unroll
        for (int ks = 0; ks < 14; ++ks) W[hh][ks] = *reinterpret_cast<const bf16x8*>(cp + 32 * ks);
    }
    u32x2 dlo[4], dhi[4], clo[4], chi[4];
    u32x4 dx, bx;
    const int klo = (g >= 2) ? (8 * (g - 1)) : 0;
    const int khi = (g >= 2) ? (8 * (g - 1) + 4) : 4 * g;
    __amdgpu_buffer_rsrc_t rsd = __builtin_amdgcn_make_buffer_rsrc(p.Dt, 0, S_ * H_ * DM * 2, 0x00020000);
    const unsigned dvoff = (unsigned)((wid * 4 * DM + col) * S_ * 2);
    auto load_D = [&](int s) {
        if ((s & 7) == 0) wait_stage(p.stage + 64 * (s >> 3));
#pragma unroll
        for (int hh = 0; hh < 4; ++hh) {
            dlo[hh] = __builtin_amdgcn_raw_buffer_load_b64(rsd, dvoff + hh * (DM * S_ * 2) + klo * 2, s * 64, 16);
            dhi[hh] = __builtin_amdgcn_raw_buffer_load_b64(rsd, dvoff + hh * (DM * S_ * 2) + khi * 2, s * 64, 16);
        }
        dx = ld16_sc1(rsd, dvoff + g * (DM * S_ * 2) + 48, s * 64);
    };
    load_D(0);
    const u16* mfp = memL + c16 * MROW + 8 * g;
    const int i8 = c16 & 7;
    const unsigned pvoff = ((wid * 4) * 320 + i8 * 40 + 8 * g) * 2;
    const unsigned pvoffx = ((wid * 4 + g) * 320 + i8 * 40 + 32) * 2;
    float* rwr = red + wid * 128 + (4 * (g & 1)) * 16 + c16;
    const float* rrd = red + ((lane >> 2) & 7) * 16 + 4 * (lane & 3);
    u16* g2w = p.G2 + 128 * c + 4 * (lane & 31);
    __amdgpu_buffer_rsrc_t rs1 = __builtin_amdgcn_make_buffer_rsrc(p.G1, 0, 63 * G1_SLOT * 2, 0x00020000);
    __amdgpu_buffer_rsrc_t rs2 = __builtin_amdgcn_make_buffer_rsrc(p.G2, 0, 65 * G2_SLOT * 2, 0x00020000);
    bool dead = false;
    u64* utp = reinterpret_cast<u64*>(p.Ut + ((long)(wid * 4) * DM + col) * 8 + 4 * (g & 1));
    for (int s = 0; s < NSEG; ++s) {
        {
            u32x4 a, b;
            for (unsigned spin = 0;; ++spin) {
                asm volatile("" ::: "memory");
                a = ld16_sc1(rs2, tid * 16, (unsigned)(s * G2_SLOT * 2));
                b = ld16_sc1(rs2, ch1 * 16, (unsigned)(s * G2_SLOT * 2));
                unsigned mx = max(umax3(a[1], a[3], b[1]), b[3]);
                if (__all(mx != 0xffffffffu) || dead) break;
                if (spin > SPIN_CAP) dead = true;
            }
            *reinterpret_cast<u32x4*>(mw0) = a;
            *reinterpret_cast<u32x4*>(mw1) = b;
            __syncthreads();
        }
        TS(1);
#pragma unroll
        for (int hh = 0; hh < 4; ++hh) {
            clo[hh] = dlo[hh];
            chi[hh] = dhi[hh];
        }
        bx = dx;
        asm volatile("" ::: "memory");
        load_D(min(s + 1, NSEG - 2));
        asm volatile("" ::: "memory");
        f32x4 U[4];
#pragma unroll
        for (int hh = 0; hh < 4; ++hh) U[hh] = f32x4{0.f, 0.f, 0.f, 0.f};
#pragma unroll
        for (int ks = 0; ks < 14; ++ks) {
            bf16x8 mf = *reinterpret_cast<const bf16x8*>(mfp + 32 * ks);
#pragma unroll
            for (int hh = 0; hh < 4; ++hh) U[hh] = MFMA16(mf, W[hh][ks], U[hh]);
        }
        u32x4 bv[4];
#pragma unroll
        for (int hh = 0; hh < 4; ++hh) {
            unsigned u0 = pack2(U[hh][0], U[hh][1]), u1 = pack2(U[hh][2], U[hh][3]);
            bv[hh] = u32x4{(g < 2) ? u0 : clo[hh][0], (g < 2) ? u1 : clo[hh][1], chi[hh][0], chi[hh][1]};
            if (g < 2)
                __hip_atomic_store(utp + ((long)s * 16 + hh) * (DM * 2), ((u64)u1 << 32) | u0, __ATOMIC_RELAXED,
                                   __HIP_MEMORY_SCOPE_AGENT);
        }
        if (s == NSEG - 1) {
            asm volatile("s_waitcnt vmcnt(0)" ::: "memory");
            __syncthreads();
            if (tid < 32) st_gran4(g2w + NSEG * G2_SLOT, 0u, 0u);
            break;
        }
        u32x4 pv[5];
        __builtin_amdgcn_s_sleep(SLP_C1);
        for (unsigned spin = 0;; ++spin) {
            asm volatile("" ::: "memory");
#pragma unroll
            for (int hh = 0; hh < 4; ++hh) pv[hh] = ld16_sc1(rs1, pvoff + hh * 640, (unsigned)(s * G1_SLOT * 2));
            pv[4] = ld16_sc1(rs1, pvoffx, (unsigned)(s * G1_SLOT * 2));
            unsigned mx = 0;
#pragma unroll
            for (int q = 0; q < 5; ++q) mx = umax3(mx, pv[q][1], pv[q][3]);
            if (__all(mx != 0xffffffffu) || dead) break;
            if (spin > SPIN_CAP) dead = true;
        }
        TS(2);
        f32x4 o0 = f32x4{0.f, 0.f, 0.f, 0.f}, o1 = f32x4{0.f, 0.f, 0.f, 0.f};
        o0 = MFMA16(__builtin_bit_cast(bf16x8, pv[0]), __builtin_bit_cast(bf16x8, bv[0]), o0);
        o1 = MFMA16(__builtin_bit_cast(bf16x8, pv[1]), __builtin_bit_cast(bf16x8, bv[1]), o1);
        o0 = MFMA16(__builtin_bit_cast(bf16x8, pv[2]), __builtin_bit_cast(bf16x8, bv[2]), o0);
        o1 = MFMA16(__builtin_bit_cast(bf16x8, pv[3]), __builtin_bit_cast(bf16x8, bv[3]), o1);
        o0 = MFMA16(__builtin_bit_cast(bf16x8, pv[4]), __builtin_bit_cast(bf16x8, bx), o0);
        o0 += o1;
        if (g < 2) {
#pragma unroll
            for (int r = 0; r < 4; ++r) rwr[r * 16] = o0[r];
        }
        asm volatile("s_waitcnt vmcnt(0)" ::: "memory");
        __syncthreads();
        if (tid < 32) {
            f32x4 x0 = *reinterpret_cast<const f32x4*>(rrd);
            f32x4 x1 = *reinterpret_cast<const f32x4*>(rrd + 128);
            f32x4 x2 = *reinterpret_cast<const f32x4*>(rrd + 256);
            f32x4 x3 = *reinterpret_cast<const f32x4*>(rrd + 384);
            f32x4 a = (x0 + x1) + (x2 + x3);
            st_gran8(rs2, (128 * c + 4 * (lane & 31)) * 2, (unsigned)((s + 1) * G2_SLOT * 2), !(lane & 1), pack2(a[0], a[1]),
                     pack2(a[2], a[3]));
        }
        TS(3);
        __builtin_amdgcn_s_sleep(SLP_C2);
    }
}

template <int BM, int BN, bool NT, bool SC1>
__device__ __forceinline__ void gemm_tile(const u16* __restrict__ A, int lda, const u16* __restrict__ B, int ldb,
                                          u16* __restrict__ C, int ldc, int nk, u16* smem) {
    constexpr int AROW = 72, BROW = NT ? 72 : BN + 32;
    constexpr int ASZ = BM * AROW, STG = ASZ + (NT ? BN * 72 : 64 * BROW);
    constexpr int ACH = BM / 32, BCH = BN / 32, TM = BM / 64, TN = BN / 64;
    constexpr int BPR = BN / 8;
    constexpr int BRS = 256 / BPR;
    int tid = threadIdx.x;
    asm volatile("" : "+v"(tid));
    const int lane = tid & 63, wid = tid >> 6;
    const int wm = wid >> 1, wn = wid & 1;
    const int r32 = lane & 31, hi = lane >> 5, g = lane >> 4, q4 = (lane & 15) >> 2, p4 = lane & 3;
    f32x16 acc[TM][TN];
#pragma unroll
    for (int i = 0; i < TM; ++i)
#pragma unroll
        for (int j = 0; j < TN; ++j) acc[i][j] = f32x16{};
    u32x4 ra0[ACH], rb0[BCH], ra1[ACH], rb1[BCH];
    const unsigned aoff = ((unsigned)(tid >> 3) * lda + (tid & 7) * 8) * 2u;
    const unsigned boff = NT ? ((unsigned)(tid >> 3) * ldb + (tid & 7) * 8) * 2u
                             : ((unsigned)(tid / BPR) * ldb + (tid % BPR) * 8) * 2u;
    const char* Ac = reinterpret_cast<const char*>(A);
    const char* Bc = reinterpret_cast<const char*>(B);
    u16* aw = smem + (tid >> 3) * AROW + (tid & 7) * 8;
    u16* bw = NT ? (smem + ASZ + (tid >> 3) * BROW + (tid & 7) * 8) : (smem + ASZ + (tid / BPR) * BROW + (tid % BPR) * 8);
    const u16* ard = smem + (wm * (BM / 2) + r32) * AROW + hi * 8;
    const u16* brd = NT ? (smem + ASZ + (wn * (BN / 2) + r32) * BROW + hi * 8)
                        : (smem + ASZ + (8 * (g >> 1) + q4) * BROW + wn * (BN / 2) + 16 * (g & 1) + 4 * p4);
    auto load = [&](u32x4(&ra)[ACH], u32x4(&rb)[BCH], int kt) {
#pragma unroll
        for (int i = 0; i < ACH; ++i)
            ra[i] = *reinterpret_cast<const u32x4*>(Ac + (aoff + (unsigned)(32 * i * lda + kt * 64) * 2u));
#pragma unroll
        for (int i = 0; i < BCH; ++i) {
            if (NT)
                rb[i] = *reinterpret_cast<const u32x4*>(Bc + (boff + (unsigned)(32 * i * ldb + kt * 64) * 2u));
            else
                rb[i] = *reinterpret_cast<const u32x4*>(Bc + (boff + (unsigned)((kt * 64 + BRS * i) * ldb) * 2u));
        }
    };
    auto store = [&](const u32x4(&ra)[ACH], const u32x4(&rb)[BCH], int st) {
#pragma unroll
        for (int i = 0; i < ACH; ++i) *reinterpret_cast<u32x4*>(aw + st * STG + 32 * i * AROW) = ra[i];
#pragma unroll
        for (int i = 0; i < BCH; ++i)
            *reinterpret_cast<u32x4*>(bw + st * STG + (NT ? 32 : BRS) * i * BROW) = rb[i];
    };
    auto compute = [&](int st, int ks) {
        bf16x8 af[TM], bfr[TN];
#pragma unroll
        for (int i = 0; i < TM; ++i)
            af[i] = *reinterpret_cast<const bf16x8*>(ard + st * STG + i * 32 * AROW + ks * 16);
#pragma unroll
        for (int j = 0; j < TN; ++j) {
            if (NT) {
                bfr[j] = *reinterpret_cast<const bf16x8*>(brd + st * STG + j * 32 * BROW + ks * 16);
            } else {
                const u16* pB = brd + st * STG + ks * 16 * BROW + j * 32;
                s16x4 lo = tr_read(pB);
                s16x4 hi4 = tr_read(pB + 4 * BROW);
                bfr[j] = bf16x8{lo[0], lo[1], lo[2], lo[3], hi4[0], hi4[1], hi4[2], hi4[3]};
            }
        }
#pragma unroll
        for (int i = 0; i < TM; ++i)
#pragma unroll
            for (int j = 0; j < TN; ++j) acc[i][j] = MFMA32(bfr[j], af[i], acc[i][j]);
    };
    load(ra0, rb0, 0);
    load(ra1, rb1, 1);
    __syncthreads();
    store(ra0, rb0, 0);
    load(ra0, rb0, 2);
    __syncthreads();
    for (int kt = 0; kt + 1 < nk; kt += 2) {
        compute(0, 0);
        compute(0, 1);
        store(ra1, rb1, 1);
        load(ra1, rb1, min(kt + 3, nk - 1));
        compute(0, 2);
        compute(0, 3);
        __syncthreads();
        compute(1, 0);
        compute(1, 1);
        store(ra0, rb0, 0);
        load(ra0, rb0, min(kt + 4, nk - 1));
        compute(1, 2);
        compute(1, 3);
        __syncthreads();
    }
    if (nk & 1) {
        compute(0, 0);
        compute(0, 1);
        compute(0, 2);
        compute(0, 3);
        __syncthreads();
    }
    constexpr int CROW = BN + 8;
#pragma unroll
    for (int i = 0; i < TM; ++i)
#pragma unroll
        for (int j = 0; j < TN; ++j)
#pragma unroll
            for (int rr = 0; rr < 4; ++rr) {
                u32x2 w;
                w[0] = pack2(acc[i][j][4 * rr], acc[i][j][4 * rr + 1]);
                w[1] = pack2(acc[i][j][4 * rr + 2], acc[i][j][4 * rr + 3]);
                *reinterpret_cast<u32x2*>(smem + (wm * (BM / 2) + 32 * i + r32) * CROW + wn * (BN / 2) + 32 * j +
                                          8 * rr + 4 * hi) = w;
            }
    __syncthreads();
    int tid2 = threadIdx.x;
    asm volatile("" : "+v"(tid2));
    const unsigned coff = ((unsigned)(tid2 / BPR) * ldc + (tid2 % BPR) * 8) * 2u;
    __amdgpu_buffer_rsrc_t rs = __builtin_amdgcn_make_buffer_rsrc(C, 0, 0x7fffffff, 0x00020000);
#pragma unroll
    for (int i = 0; i < BM * BN / 2048; ++i) {
        int row = tid2 / BPR + BRS * i, cc = tid2 % BPR;
        u32x4 v = *reinterpret_cast<const u32x4*>(smem + row * CROW + cc * 8);
        __builtin_amdgcn_raw_buffer_store_b128(v, rs, coff + (unsigned)(BRS * i * ldc) * 2u, 0, SC1 ? 16 : 0);
    }
}

struct MegaParams {
    const u16* A_x;
    const u16* AmatTt;
    const u16* B_x;
    const u16* B_ckv;
    const u16* B_wq;
    const u16* B_wuk;
    const u16* B_wuv;
    const u16* B_wo;
    u16* E;
    u16* Qb;
    u16* Kb;
    u16* Vb;
    u16* OB;
    u16* out;
    unsigned* ctl;
};
constexpr int NWORK = 256 - NREC;
constexpr int CTL_Q1 = 0, CTL_C1 = 64, CTL_Q2 = 128, CTL_C2 = 192, CTL_Q3 = 256, CTL_Q4 = 320, CTL_ST = 384, CTL_WORDS = 384 + 512;
constexpr unsigned WSPIN_CAP = 2000000u;

__device__ __forceinline__ int next_ticket(unsigned* q, int* slot) {
    __syncthreads();
    if (threadIdx.x == 0) *slot = (int)(__hip_atomic_fetch_add(q, 1u, __ATOMIC_RELAXED, __HIP_MEMORY_SCOPE_AGENT) + 1u);
    __syncthreads();
    return *slot;
}
__device__ __forceinline__ void publish_count(unsigned* c) {
    asm volatile("s_waitcnt vmcnt(0)" ::: "memory");
    __syncthreads();
    if (threadIdx.x == 0) __hip_atomic_fetch_add(c, 1u, __ATOMIC_RELAXED, __HIP_MEMORY_SCOPE_AGENT);
}
__device__ __forceinline__ void wait_count(unsigned* c, unsigned target) {
    if (threadIdx.x == 0) {
        for (unsigned spin = 0; spin < WSPIN_CAP; ++spin) {
            if (__hip_atomic_load(c, __ATOMIC_RELAXED, __HIP_MEMORY_SCOPE_AGENT) + 1u >= target) break;
            __builtin_amdgcn_s_sleep(16);
        }
        __builtin_amdgcn_fence(__ATOMIC_ACQUIRE, "agent");
        asm volatile("s_waitcnt vmcnt(0)" ::: "memory");
    }
    __syncthreads();
}

__device__ __forceinline__ void postA_task(const RecParams& p, const MegaParams& m, int s, int part, u16* smem) {
    u16* KL = smem;
    u16* PL = smem + 40 * MROW;
    int tid = threadIdx.x;
    asm volatile("" : "+v"(tid));
    const int lane = tid & 63, wid = __builtin_amdgcn_readfirstlane(tid >> 6);
    const int r32 = lane & 31, hi = lane >> 5;
    const u16* ep = m.E + (long)(s * 32 + r32) * (H_ * DM) + wid * 4 * DM + hi * 8;
    bf16x8 e0[28], e1[28];
    auto loadE = [&](bf16x8(&e)[28], int hh) {
#pragma unroll
        for (int ks = 0; ks < 28; ++ks) e[ks] = *reinterpret_cast<const bf16x8*>(ep + hh * DM + ks * 16);
    };
    loadE(e0, 0);
    loadE(e1, 1);
    __syncthreads();
    for (int c = tid; c < 32 * 56; c += 256) {
        int row = c / 56, cc = c - row * 56;
        *reinterpret_cast<u32x4*>(KL + (8 + row) * MROW + cc * 8) =
            *reinterpret_cast<const u32x4*>(m.A_x + (long)(s * 32 + row) * DM + cc * 8);
    }
    {
        const u64* gf = reinterpret_cast<const u64*>(p.G2 + (long)(s + 1) * G2_SLOT + 128 * (lane < NC_BLK ? lane : 0));
        for (unsigned spin = 0; spin < WSPIN_CAP; ++spin) {
            u64 x = ld_gran(gf);
            if (__all((unsigned)(x >> 32) != 0xffffffffu)) break;
            __builtin_amdgcn_s_sleep(8);
        }
        const int ch1 = (tid < 192) ? (256 + tid) : tid;
        u32x4 a, b;
        {
            __amdgpu_buffer_rsrc_t rs2 = __builtin_amdgcn_make_buffer_rsrc(p.G2, 0, 65 * G2_SLOT * 2, 0x00020000);
            for (unsigned spin = 0; spin < WSPIN_CAP; ++spin) {
                asm volatile("" ::: "memory");
                a = ld16_sc1(rs2, tid * 16, (unsigned)(s * G2_SLOT * 2));
                b = ld16_sc1(rs2, ch1 * 16, (unsigned)(s * G2_SLOT * 2));
                unsigned mx = max(umax3(a[1], a[3], b[1]), b[3]);
                if (__all(mx != 0xffffffffu)) break;
                __builtin_amdgcn_s_sleep(8);
            }
        }
        *reinterpret_cast<u32x4*>(KL + ((tid >> 1) & 7) * MROW + 16 * (tid >> 4) + 8 * (tid & 1)) = a;
        *reinterpret_cast<u32x4*>(KL + ((ch1 >> 1) & 7) * MROW + 16 * (ch1 >> 4) + 8 * (ch1 & 1)) = b;
    }
    __syncthreads();
    const u16* k0l = KL + r32 * MROW + hi * 8;
    const u16* k1l = KL + (32 + (r32 & 7)) * MROW + hi * 8;
    const float L2E = 1.4426950408889634f;
    auto scores = [&](const bf16x8(&e)[28], int hh) {
        f32x16 s0 = f32x16{}, s1 = f32x16{};
#pragma unroll
        for (int ks = 0; ks < 28; ++ks) {
            bf16x8 ka = *reinterpret_cast<const bf16x8*>(k0l + ks * 16);
            bf16x8 kb = *reinterpret_cast<const bf16x8*>(k1l + ks * 16);
            s0 = MFMA32(ka, e[ks], s0);
            s1 = MFMA32(kb, e[ks], s1);
        }
#pragma unroll
        for (int r = 0; r < 16; ++r) {
            int kk = crow(r, hi);
            if (kk >= 8 && kk - 8 > r32) s0[r] = -INFINITY;
            int k2 = kk + 32;
            if (k2 >= 40 || k2 - 8 > r32) s1[r] = -INFINITY;
        }
        float mx = -INFINITY;
#pragma unroll
        for (int r = 0; r < 16; ++r) mx = fmaxf(mx, fmaxf(s0[r], s1[r]));
        mx = fmaxf(mx, __shfl_xor(mx, 32));
        float nm = -mx * L2E, psum = 0.f;
#pragma unroll
        for (int r = 0; r < 16; ++r) {
            s0[r] = __builtin_amdgcn_exp2f(fmaf(s0[r], L2E, nm));
            s1[r] = __builtin_amdgcn_exp2f(fmaf(s1[r], L2E, nm));
            psum += s0[r] + s1[r];
        }
        psum += __shfl_xor(psum, 32);
        float inv = 1.f / psum;
        u32x4 a0, a1, a2;
#pragma unroll
        for (int j = 0; j < 4; ++j) {
            a0[j] = pack2(s0[2 * j] * inv, s0[2 * j + 1] * inv);
            a1[j] = pack2(s0[8 + 2 * j] * inv, s0[8 + 2 * j + 1] * inv);
            a2[j] = pack2(s1[2 * j] * inv, s1[2 * j + 1] * inv);
        }
        u16* pw = PL + ((wid * 4 + hh) * 3 * 64 + lane) * 8;
        *reinterpret_cast<u32x4*>(pw) = a0;
        *reinterpret_cast<u32x4*>(pw + 512) = a1;
        *reinterpret_cast<u32x4*>(pw + 1024) = a2;
    };
    scores(e0, 0);
    loadE(e0, 2);
    scores(e1, 1);
    loadE(e1, 3);
    scores(e0, 2);
    scores(e1, 3);
    const int ct = part + 4 * wid;
    const int col = (ct < 14 ? ct : 13) * 32 + r32;
    u64 u[16];
    u32x2 d[16][4];
#pragma unroll
    for (int h = 0; h < 16; ++h) {
        const u64* up = reinterpret_cast<const u64*>(p.Ut + (((long)s * 16 + h) * DM + col) * 8 + 4 * hi);
        const u16* dp = p.Dt + ((long)h * DM + col) * S_ + s * 32 + 4 * hi;
        u[h] = ld_gran(up);
#pragma unroll
        for (int q = 0; q < 4; ++q) d[h][q] = *reinterpret_cast<const u32x2*>(dp + 8 * q);
    }
    __syncthreads();
    f32x16 o = f32x16{};
#pragma unroll
    for (int h = 0; h < 16; ++h) {
        const u16* pr = PL + (h * 3 * 64 + lane) * 8;
        bf16x8 p0 = *reinterpret_cast<const bf16x8*>(pr);
        bf16x8 p1 = *reinterpret_cast<const bf16x8*>(pr + 512);
        bf16x8 p2 = *reinterpret_cast<const bf16x8*>(pr + 1024);
        o = MFMA32(__builtin_bit_cast(bf16x8, (u32x4{(unsigned)u[h], (unsigned)(u[h] >> 32), d[h][0][0], d[h][0][1]})), p0, o);
        o = MFMA32(__builtin_bit_cast(bf16x8, (u32x4{d[h][1][0], d[h][1][1], d[h][2][0], d[h][2][1]})), p1, o);
        o = MFMA32(__builtin_bit_cast(bf16x8, (u32x4{d[h][3][0], d[h][3][1], 0u, 0u})), p2, o);
    }
    if (ct < 14) {
#pragma unroll
        for (int rr = 0; rr < 4; ++rr) {
            u32x2 w;
            w[0] = pack2(o[4 * rr], o[4 * rr + 1]);
            w[1] = pack2(o[4 * rr + 2], o[4 * rr + 3]);
            *reinterpret_cast<u32x2*>(m.out + (long)(s * 32 + r32) * 896 + ct * 32 + 8 * rr + 4 * hi) = w;
        }
    }
}

__device__ __forceinline__ void worker_body(const RecParams& p, const MegaParams& m, u16* smem, int* slot) {
    unsigned* ctl = m.ctl;
    WTS(0);
    for (int ph = 0; ph < 2; ++ph) {
        for (;;) {
            int t = next_ticket(ctl + (ph ? CTL_Q2 : CTL_Q1), slot);
            int kind, idx;
            if (ph == 0) {
                if (t >= 1440) break;
                if (t < 224) { kind = 0; idx = t; }
                else if (t < 480) { kind = 1; idx = t - 224; }
                else if (t < 592) { kind = 0; idx = t - 480 + 224; }
                else if (t < 848) { kind = 1; idx = t - 592 + 256; }
                else if (t < 960) { kind = 0; idx = t - 848 + 336; }
                else if (t < 1216) { kind = 1; idx = t - 960 + 512; }
                else { kind = 0; idx = t - 1216 + 448; }
            } else {
                if (t >= 928) break;
                if (t < 208) { kind = 2; idx = t; }
                else if (t < 432) { kind = 0; idx = t - 208 + 672; }
                else if (t < 480) { kind = 2; idx = t - 432 + 208; }
                else { kind = 3; idx = t - 480; }
            }
            if (kind == 1) {
                int mat = idx >> 8, rb = (idx >> 5) & 7, cb = idx & 31;
                const u16* A = (mat < 2) ? m.B_ckv : m.B_x;
                const u16* B = (mat == 0) ? m.B_wuk : (mat == 1 ? m.B_wuv : m.B_wq);
                u16* C = (mat == 0) ? m.Kb : (mat == 1 ? m.Vb : m.Qb);
                int lda = (mat < 2) ? DC : DM;
                gemm_tile<256, 128, false, true>(A + (long)rb * 256 * lda, lda, B + cb * 128, HD,
                                                 C + (long)rb * 256 * HD + cb * 128, HD, lda / 64, smem);
            } else if (kind == 2) {
                attnB_body(m.Qb, m.Kb, m.Vb, m.OB, smem, 15 - (idx >> 4), idx & 15);
            } else {
                const u16 *A, *B;
                u16* C;
                int ldc = H_ * DM, j = 0;
                if (kind == 3) {
                    int rb = idx / 56, cb = idx - rb * 56;
                    A = m.A_x + (long)rb * 256 * DM;
                    B = p.AmatT + (long)cb * 128 * DM;
                    C = m.E + (long)rb * 256 * (H_ * DM) + cb * 128;
                } else {
                    j = idx / 112;
                    int u = idx - j * 112;
                    if (u < 56) {
                        A = m.A_x + (long)j * 256 * DM;
                        B = m.AmatTt + (long)u * 128 * DM;
                        C = p.Bt + (long)j * 256 * (H_ * DM) + u * 128;
                    } else {
                        int mb = (u - 56) >> 1, nb = u & 1;
                        A = p.CmatT + (long)mb * 256 * DM;
                        B = m.A_x + (long)(j * 256 + nb * 128) * DM;
                        C = p.Dt + (long)mb * 256 * S_ + j * 256 + nb * 128;
                        ldc = S_;
                    }
                }
                gemm_tile<256, 128, true, true>(A, DM, B, DM, C, ldc, DM / 64, smem);
                if (kind == 0) publish_count(ctl + CTL_ST + 64 * j);
            }
        }
        publish_count(ctl + (ph ? CTL_C2 : CTL_C1));
        WTS(1 + 2 * ph);
        wait_count(ctl + (ph ? CTL_C2 : CTL_C1), NWORK);
        WTS(2 + 2 * ph);
    }
    for (;;) {
        int t = next_ticket(ctl + CTL_Q3, slot);
        if (t >= 112) break;
        int rb = t / 7, cb = t - rb * 7;
        gemm_tile<128, 64, false, false>(m.OB + (long)rb * 128 * HD, HD, m.B_wo + cb * 64, DM,
                                         m.out + (long)rb * 128 * 896 + 448 + cb * 64, 896, HD / 64, smem);
    }
    WTS(5);
    for (;;) {
        int t = next_ticket(ctl + CTL_Q4, slot);
        if (t >= 4 * NSEG) break;
        postA_task(p, m, t >> 2, t & 3, smem);
    }
    WTS(6);
    WTS(7);
}

constexpr int REC_LDS = 4 * 320 * 4 + 4 * 14 * 1024 + 8 * MROW * 2;
constexpr int GEMM_LDS = 2 * (256 * 72 + 64 * 160) * 2;
constexpr int POST_LDS = (40 * MROW + 16 * 3 * 64 * 8) * 2;
constexpr int cmax(int a, int b) { return a > b ? a : b; }
constexpr int FUSED_LDS = cmax(cmax(ATTN_LDS, REC_LDS), cmax(GEMM_LDS, POST_LDS));

__global__ __launch_bounds__(256) void mega_kernel(RecParams p, MegaParams m) {
    __shared__ __attribute__((aligned(16))) u16 smem[FUSED_LDS / 2 + 8];
    const int b = blockIdx.x;
    if (b < NA_BLK) {
        rec_A_body(p, smem, b);
    } else if (b < NREC) {
        rec_C_body(p, smem, b - NA_BLK);
    } else {
        worker_body(p, m, smem, reinterpret_cast<int*>(smem + FUSED_LDS / 2));
    }
}

template <int BN, bool NT, bool TO = false>
static void launch_gemm(hipStream_t st, const u16* A, const u16* B, u16* C, int lda, int ldb, int ldc, long sA,
                        long sB, long sC, int M, int N, int K, int batch, float scale) {
    dim3 grid(N / BN, (M + 127) / 128, batch);
    hipLaunchKernelGGL((gemm_kernel<BN, NT, TO>), grid, dim3(256), 0, st, A, B, C, lda, ldb, ldc, sA, sB, sC, M, N, K, scale);
}

extern "C" void kernel_launch(void* const* d_in, const int* in_sizes, int n_in, void* d_out, int out_size,
                              void* d_ws, size_t ws_size, hipStream_t stream) {
    const u16* A_x = (const u16*)d_in[0];
    const u16* A_mem0 = (const u16*)d_in[1];
    const u16* A_Wq = (const u16*)d_in[2];
    const u16* A_Wk = (const u16*)d_in[3];
    const u16* A_Wv = (const u16*)d_in[4];
    const u16* A_Wo = (const u16*)d_in[5];
    const u16* B_x = (const u16*)d_in[7];
    const u16* B_ckv = (const u16*)d_in[8];
    const u16* B_wq = (const u16*)d_in[9];
    const u16* B_wuk = (const u16*)d_in[10];
    const u16* B_wuv = (const u16*)d_in[11];
    const u16* B_wo = (const u16*)d_in[12];
    u16* out = (u16*)d_out;

    char* ws = (char*)d_ws;
    const size_t SZ = (size_t)S_ * HD * 2;
    u16* Qb = (u16*)(ws + 0 * SZ);
    u16* Kb = (u16*)(ws + 1 * SZ);
    u16* Vb = (u16*)(ws + 2 * SZ);
    u16* OB = (u16*)(ws + 3 * SZ);
    size_t off = 4 * SZ;
    u16* AmatT = (u16*)(ws + off); off += (size_t)H_ * DM * DM * 2;
    u16* AmatTt = (u16*)(ws + off); off += (size_t)H_ * DM * DM * 2;
    u16* CmatT = (u16*)(ws + off); off += (size_t)H_ * DM * DM * 2;
    u16* Bt = (u16*)(ws + off); off += (size_t)H_ * DM * S_ * 2;
    u16* Dt = (u16*)(ws + off); off += (size_t)H_ * DM * S_ * 2;
    u16* E = (u16*)(ws + off); off += (size_t)H_ * DM * S_ * 2;
    u16* Ut = (u16*)(ws + off); off += (size_t)NSEG * H_ * DM * 8 * 2;
    char* gbase = ws + off;
    u16* G1 = (u16*)gbase;
    u16* G2 = G1 + 63 * G1_SLOT;
    unsigned* ctl = (unsigned*)(G2 + 65 * G2_SLOT);
    size_t gbytes = (size_t)(63 * G1_SLOT + 65 * G2_SLOT) * 2 + (size_t)CTL_WORDS * 4;
    off += gbytes;

    hipMemsetAsync(gbase, 0xFF, gbytes, stream);
    launch_gemm<64, true>(stream, A_Wk, A_Wq, AmatT, HD, HD, DM, DH, DH, (long)DM * DM, DM, DM, DH, H_, 0.0625f);
    launch_gemm<64, false, true>(stream, A_Wv, A_Wo, CmatT, HD, DM, DM, DH, (long)DH * DM, (long)DM * DM, DM, DM, DH, H_, 1.f);
    launch_gemm<64, true>(stream, A_Wq, A_Wk, AmatTt, HD, HD, DM, DH, DH, (long)DM * DM, DM, DM, DH, H_, 0.0625f);
    RecParams rp;
    rp.mem0 = A_mem0; rp.AmatT = AmatT; rp.CmatT = CmatT; rp.Bt = Bt; rp.Dt = Dt; rp.Ut = Ut; rp.G1 = G1; rp.G2 = G2;
    rp.stage = ctl + CTL_ST;
    MegaParams mp;
    mp.A_x = A_x; mp.AmatTt = AmatTt;
    mp.B_x = B_x; mp.B_ckv = B_ckv; mp.B_wq = B_wq; mp.B_wuk = B_wuk; mp.B_wuv = B_wuv; mp.B_wo = B_wo;
    mp.E = E; mp.Qb = Qb; mp.Kb = Kb; mp.Vb = Vb; mp.OB = OB;
    mp.out = out; mp.ctl = ctl;
    hipLaunchKernelGGL(mega_kernel, dim3(256), dim3(256), 0, stream, rp, mp);
}
```

```cpp
#include <hip/hip_runtime.h>
#include <hip/hip_bf16.h>
#include <stdint.h>

typedef unsigned short u16;
typedef __attribute__((ext_vector_type(8))) short bf16x8;
typedef __attribute__((ext_vector_type(4))) short s16x4;
typedef __attribute__((ext_vector_type(16))) float f32x16;
typedef __attribute__((ext_vector_type(4))) float f32x4;
typedef __attribute__((ext_vector_type(4))) unsigned u32x4;
typedef __attribute__((ext_vector_type(2))) unsigned u32x2;
typedef short v4i16_t __attribute__((ext_vector_type(4)));

#define LAS __attribute__((address_space(3)))

__device__ __forceinline__ float bf2f(u16 x) { return __uint_as_float(((unsigned)x) << 16); }
__device__ __forceinline__ u16 f2bf(float f) {
    unsigned u = __float_as_uint(f);
    u += 0x7fffu + ((u >> 16) & 1u);
    return (u16)(u >> 16);
}
typedef __bf16 bf16x2_t __attribute__((ext_vector_type(2)));
__device__ __forceinline__ unsigned pack2(float lo, float hi) {
    bf16x2_t r = {(__bf16)lo, (__bf16)hi};
    return __builtin_bit_cast(unsigned, r);
}
__device__ __forceinline__ s16x4 tr_read(const void* p) {
    return __builtin_bit_cast(s16x4, __builtin_amdgcn_ds_read_tr16_b64_v4i16((LAS v4i16_t*)p));
}
#define MFMA32(a, b, c) __builtin_amdgcn_mfma_f32_32x32x16_bf16(a, b, c, 0, 0, 0)
#define MFMA16(a, b, c) __builtin_amdgcn_mfma_f32_16x16x32_bf16(a, b, c, 0, 0, 0)

__device__ __forceinline__ int crow(int r, int hi) { return (r & 3) + 8 * (r >> 2) + 4 * hi; }

constexpr int S_ = 2048, DM = 448, H_ = 16, DH = 256, HD = 4096, MM = 8, SEG = 32, NSEG = 64, DC = 1024;

__global__ __launch_bounds__(256) void pre_kernel(const u16* __restrict__ Wq, const u16* __restrict__ Wk,
                                                  const u16* __restrict__ Wv, const u16* __restrict__ Wo,
                                                  u16* __restrict__ AmatT, u16* __restrict__ CmatT,
                                                  u32x4* __restrict__ gbase, int gchunks) {
    __shared__ __attribute__((aligned(16))) u16 Bs[256 * 96];
    const int tid = threadIdx.x, lane = tid & 63, wid = tid >> 6;
    const int c16 = lane & 15, g = lane >> 4, q4 = c16 >> 2, p4 = lane & 3;
    const int br = blockIdx.x >> 3, rb = br % 7, hz = (blockIdx.x & 7) + 8 * (br / 7), h = hz & 15;
    const bool isC = hz >= 16;
    u32x4 rbv[8];
#pragma unroll
    for (int i = 0; i < 8; ++i) {
        int c = tid + 256 * i;
        const u16* src = isC ? Wo + (long)(h * DH + (c >> 3)) * DM + rb * 64 + (c & 7) * 8
                             : Wk + (long)(rb * 64 + (c >> 5)) * HD + h * DH + (c & 31) * 8;
        rbv[i] = *reinterpret_cast<const u32x4*>(src);
    }
    const u16* ap = (isC ? Wv : Wq) + (long)(wid * 112 + c16) * HD + h * DH + 8 * g;
    bf16x8 af[7][8];
#pragma unroll
    for (int ks = 0; ks < 8; ++ks)
#pragma unroll
        for (int t = 0; t < 7; ++t) af[t][ks] = *reinterpret_cast<const bf16x8*>(ap + (long)t * 16 * HD + 32 * ks);
    for (int i = blockIdx.x * 256 + tid; i < gchunks; i += 224 * 256)
        gbase[i] = u32x4{0xffffffffu, 0xffffffffu, 0xffffffffu, 0xffffffffu};
#pragma unroll
    for (int i = 0; i < 8; ++i) {
        int c = tid + 256 * i;
        u16* dst = isC ? Bs + (c >> 3) * 96 + (c & 7) * 8 : Bs + (c >> 5) * 264 + (c & 31) * 8;
        *reinterpret_cast<u32x4*>(dst) = rbv[i];
    }
    __syncthreads();
    f32x4 acc[7][4];
#pragma unroll
    for (int t = 0; t < 7; ++t)
#pragma unroll
        for (int j = 0; j < 4; ++j) acc[t][j] = f32x4{0.f, 0.f, 0.f, 0.f};
#pragma unroll
    for (int ks = 0; ks < 8; ++ks) {
        bf16x8 bfr[4];
#pragma unroll
        for (int j = 0; j < 4; ++j) {
            if (isC) {
                const u16* pB = Bs + (ks * 32 + 8 * g + q4) * 96 + j * 16 + 4 * p4;
                s16x4 lo = tr_read(pB);
                s16x4 hi4 = tr_read(pB + 4 * 96);
                bfr[j] = bf16x8{lo[0], lo[1], lo[2], lo[3], hi4[0], hi4[1], hi4[2], hi4[3]};
            } else {
                bfr[j] = *reinterpret_cast<const bf16x8*>(Bs + (16 * j + c16) * 264 + ks * 32 + 8 * g);
            }
        }
#pragma unroll
        for (int t = 0; t < 7; ++t)
#pragma unroll
            for (int j = 0; j < 4; ++j) acc[t][j] = MFMA16(af[t][ks], bfr[j], acc[t][j]);
    }
    const float sc = isC ? 1.f : 0.0625f;
    u16* op = (isC ? CmatT : AmatT) + (long)(h * DM + rb * 64 + c16) * DM + wid * 112 + 4 * g;
#pragma unroll
    for (int j = 0; j < 4; ++j)
#pragma unroll
        for (int t = 0; t < 7; ++t) {
            u32x2 w;
            w[0] = pack2(acc[t][j][0] * sc, acc[t][j][1] * sc);
            w[1] = pack2(acc[t][j][2] * sc, acc[t][j][3] * sc);
            *reinterpret_cast<u32x2*>(op + j * 16 * DM + 16 * t) = w;
        }
}

constexpr int KROW = 264;
constexpr int VROW = 288;
constexpr int ATTN_LDS = (64 * KROW + 64 * VROW) * 2;
__device__ __forceinline__ void attnB_body(const u16* __restrict__ Q, const u16* __restrict__ K,
                                           const u16* __restrict__ V, u16* __restrict__ O, u16* smem, int qb, int h) {
    u16* Ks = smem;
    u16* Vs = smem + 64 * KROW;
    int tid = threadIdx.x;
    asm volatile("" : "+v"(tid));
    const int lane = tid & 63, wid = tid >> 6;
    const int q0 = qb * 128;
    const int r32 = lane & 31, hi = lane >> 5, g = lane >> 4, q4 = (lane & 15) >> 2, p4 = lane & 3;
    const int qrow = q0 + wid * 32 + r32;
    bf16x8 qf[16];
#pragma unroll
    for (int ks = 0; ks < 16; ++ks)
        qf[ks] = *reinterpret_cast<const bf16x8*>(Q + (long)qrow * HD + h * DH + ks * 16 + hi * 8);
    f32x16 o[8];
#pragma unroll
    for (int i = 0; i < 8; ++i) o[i] = f32x16{};
    float mrun = -INFINITY, lrun = 0.f;
    const float c2 = 0.0625f * 1.4426950408889634f;
    const int nt = (q0 + 128) / 64;
    u32x4 rk[8], rv[8];
    auto load_kv = [&](int t) {
#pragma unroll
        for (int i = 0; i < 8; ++i) {
            int c = tid + 256 * i, row = c >> 5, cc = c & 31;
            long off = (long)(t * 64 + row) * HD + h * DH + cc * 8;
            rk[i] = *reinterpret_cast<const u32x4*>(K + off);
            rv[i] = *reinterpret_cast<const u32x4*>(V + off);
        }
    };
    auto store_kv = [&]() {
#pragma unroll
        for (int i = 0; i < 8; ++i) {
            int c = tid + 256 * i, row = c >> 5, cc = c & 31;
            *reinterpret_cast<u32x4*>(Ks + row * KROW + cc * 8) = rk[i];
            *reinterpret_cast<u32x4*>(Vs + row * VROW + cc * 8) = rv[i];
        }
    };
    load_kv(0);
    for (int t = 0; t < nt; ++t) {
        __syncthreads();
        store_kv();
        __syncthreads();
        if (t + 1 < nt) load_kv(t + 1);
        const int kbase = t * 64;
        if (kbase <= q0 + wid * 32 + 31) {
            f32x16 s0 = f32x16{}, s1 = f32x16{};
#pragma unroll
            for (int ks = 0; ks < 16; ++ks) {
                bf16x8 k0 = *reinterpret_cast<const bf16x8*>(Ks + r32 * KROW + ks * 16 + hi * 8);
                bf16x8 k1 = *reinterpret_cast<const bf16x8*>(Ks + (32 + r32) * KROW + ks * 16 + hi * 8);
                s0 = MFMA32(k0, qf[ks], s0);
                s1 = MFMA32(k1, qf[ks], s1);
                if ((ks & 3) == 3) __builtin_amdgcn_sched_barrier(0);
            }
            if (kbase + 63 > q0 + wid * 32) {
#pragma unroll
                for (int r = 0; r < 16; ++r) {
                    int kk = kbase + crow(r, hi);
                    if (kk > qrow) s0[r] = -INFINITY;
                    if (kk + 32 > qrow) s1[r] = -INFINITY;
                }
            }
            float mx = -INFINITY;
#pragma unroll
            for (int r = 0; r < 16; ++r) mx = fmaxf(mx, fmaxf(s0[r], s1[r]));
            mx = fmaxf(mx, __shfl_xor(mx, 32));
            float mnew = fmaxf(mrun, mx);
            float alpha = __builtin_amdgcn_exp2f((mrun - mnew) * c2);
            float nm = -mnew * c2;
            float psum = 0.f;
#pragma unroll
            for (int r = 0; r < 16; ++r) {
                s0[r] = __builtin_amdgcn_exp2f(fmaf(s0[r], c2, nm));
                s1[r] = __builtin_amdgcn_exp2f(fmaf(s1[r], c2, nm));
                psum += s0[r] + s1[r];
            }
            psum += __shfl_xor(psum, 32);
            lrun = lrun * alpha + psum;
            mrun = mnew;
#pragma unroll
            for (int i = 0; i < 8; ++i)
#pragma unroll
                for (int r = 0; r < 16; ++r) o[i][r] *= alpha;
            bf16x8 pf[4];
#pragma unroll
            for (int s = 0; s < 2; ++s) {
                u32x4 a, b;
#pragma unroll
                for (int j = 0; j < 4; ++j) {
                    a[j] = pack2(s0[8 * s + 2 * j], s0[8 * s + 2 * j + 1]);
                    b[j] = pack2(s1[8 * s + 2 * j], s1[8 * s + 2 * j + 1]);
                }
                pf[s] = __builtin_bit_cast(bf16x8, a);
                pf[2 + s] = __builtin_bit_cast(bf16x8, b);
            }
#pragma unroll
            for (int dt = 0; dt < 8; ++dt) {
#pragma unroll
                for (int s = 0; s < 4; ++s) {
                    const u16* pV = Vs + (16 * s + 4 * hi + q4) * VROW + 32 * dt + 16 * (g & 1) + 4 * p4;
                    s16x4 lo = tr_read(pV);
                    s16x4 hi4 = tr_read(pV + 8 * VROW);
                    bf16x8 vf = bf16x8{lo[0], lo[1], lo[2], lo[3], hi4[0], hi4[1], hi4[2], hi4[3]};
                    o[dt] = MFMA32(vf, pf[s], o[dt]);
                }
                __builtin_amdgcn_sched_barrier(0);
            }
        }
    }
    float inv = 1.f / lrun;
#pragma unroll
    for (int dt = 0; dt < 8; ++dt)
#pragma unroll
        for (int rr = 0; rr < 4; ++rr) {
            u32x2 w;
            w[0] = pack2(o[dt][4 * rr] * inv, o[dt][4 * rr + 1] * inv);
            w[1] = pack2(o[dt][4 * rr + 2] * inv, o[dt][4 * rr + 3] * inv);
            __hip_atomic_store(reinterpret_cast<unsigned long long*>(O + (long)qrow * HD + h * DH + 32 * dt + 8 * rr + 4 * hi),
                               ((unsigned long long)w[1] << 32) | w[0], __ATOMIC_RELAXED, __HIP_MEMORY_SCOPE_AGENT);
        }
}

typedef unsigned long long u64;
__device__ __forceinline__ u64 ld_gran(const u64* p) {
    return __hip_atomic_load(p, __ATOMIC_RELAXED, __HIP_MEMORY_SCOPE_AGENT);
}
__device__ __forceinline__ void st_gran4(u16* p, unsigned lo, unsigned hi) {
    hi = min(hi, 0xfffffffeu);
    __hip_atomic_store(reinterpret_cast<u64*>(p), ((u64)hi << 32) | (u64)lo, __ATOMIC_RELAXED,
                       __HIP_MEMORY_SCOPE_AGENT);
}
__device__ __forceinline__ void st_gran8(__amdgpu_buffer_rsrc_t rs, unsigned voff, unsigned soff, bool act, unsigned lo,
                                         unsigned hi) {
    hi = min(hi, 0xfffffffeu);
    unsigned lo2 = (unsigned)__builtin_amdgcn_mov_dpp((int)lo, 0xF5, 0xf, 0xf, true);
    unsigned hi2 = (unsigned)__builtin_amdgcn_mov_dpp((int)hi, 0xF5, 0xf, 0xf, true);
    if (act) __builtin_amdgcn_raw_buffer_store_b128(u32x4{lo, hi, lo2, hi2}, rs, voff, soff, 16);
}
constexpr int NA_BLK = 16, NC_BLK = 28, NREC = NA_BLK + NC_BLK;
constexpr int MROW = 456;
constexpr int G1_SLOT = 16 * 8 * 40;
constexpr int G2_SLOT = 8 * 448;
constexpr unsigned SPIN_CAP = 400000u;
#define SLP_A 45
#define SLP_C1 30
#define SLP_C2 15

#define WTS(slot)
#define KT0
#define KT1(k)
#define TS(slot)
struct RecParams {
    const u16* mem0;
    const u16* AmatT;
    const u16* CmatT;
    u16* Bt;
    u16* Dt;
    const unsigned* stage;
    u16* Ut;
    u16* G1;
    u16* G2;
    unsigned* prog;
};

__device__ __forceinline__ float red16_max(float x) {
    asm volatile(
        "s_nop 1\n\tv_max_f32_dpp %0, %0, %0 quad_perm:[1,0,3,2] row_mask:0xf bank_mask:0xf\n\t"
        "s_nop 1\n\tv_max_f32_dpp %0, %0, %0 quad_perm:[2,3,0,1] row_mask:0xf bank_mask:0xf\n\t"
        "s_nop 1\n\tv_max_f32_dpp %0, %0, %0 row_half_mirror row_mask:0xf bank_mask:0xf\n\t"
        "s_nop 1\n\tv_max_f32_dpp %0, %0, %0 row_mirror row_mask:0xf bank_mask:0xf\n\t"
        "s_nop 1"
        : "+v"(x));
    return x;
}
__device__ __forceinline__ float red16_sum(float x) {
    asm volatile(
        "s_nop 1\n\tv_add_f32_dpp %0, %0, %0 quad_perm:[1,0,3,2] row_mask:0xf bank_mask:0xf\n\t"
        "s_nop 1\n\tv_add_f32_dpp %0, %0, %0 quad_perm:[2,3,0,1] row_mask:0xf bank_mask:0xf\n\t"
        "s_nop 1\n\tv_add_f32_dpp %0, %0, %0 row_half_mirror row_mask:0xf bank_mask:0xf\n\t"
        "s_nop 1\n\tv_add_f32_dpp %0, %0, %0 row_mirror row_mask:0xf bank_mask:0xf\n\t"
        "s_nop 1"
        : "+v"(x));
    return x;
}
__device__ __forceinline__ u32x4 ld16_sc1(__amdgpu_buffer_rsrc_t rs, unsigned voff, unsigned soff) {
    return __builtin_amdgcn_raw_buffer_load_b128(rs, voff, soff, 16);
}
__device__ __forceinline__ unsigned umax3(unsigned a, unsigned b, unsigned c) { return max(max(a, b), c); }

__device__ __forceinline__ void mfma_a0(f32x4& d, const bf16x8& w, const u32x4& m) {
    asm volatile("v_mfma_f32_16x16x32_bf16 %0, %1, %2, 0" : "=&v"(d) : "a"(w), "v"(m));
}
__device__ __forceinline__ void mfma_a(f32x4& d, const bf16x8& w, const u32x4& m) {
    asm volatile("v_mfma_f32_16x16x32_bf16 %0, %1, %2, %0" : "+v"(d) : "a"(w), "v"(m));
}
__device__ __forceinline__ void mfma_v0(f32x4& d, const bf16x8& w, const u32x4& m) {
    asm volatile("v_mfma_f32_16x16x32_bf16 %0, %1, %2, 0" : "=&v"(d) : "v"(w), "v"(m));
}
__device__ __forceinline__ void mfma_v(f32x4& d, const bf16x8& w, const u32x4& m) {
    asm volatile("v_mfma_f32_16x16x32_bf16 %0, %1, %2, %0" : "+v"(d) : "v"(w), "v"(m));
}
__device__ __forceinline__ void lds_rd16(bf16x8& d, unsigned addr, int off) {
    asm volatile("ds_read_b128 %0, %1 offset:%2" : "=v"(d) : "v"(addr), "n"(off));
}
__device__ __forceinline__ void lds_rd16u(u32x4& d, unsigned addr) {
    asm volatile("ds_read_b128 %0, %1" : "=v"(d) : "v"(addr));
}
template <int N>
__device__ __forceinline__ void wait_lgkm() {
    asm volatile("s_waitcnt lgkmcnt(%0)" ::"n"(N) : "memory");
}
constexpr int sch_n(int q) { return q <= 8 ? 7 + 2 * (q + 1) : (q <= 11 ? 25 + (q - 8) : 28); }
constexpr int sch_m(int i) { return i < 5 ? 2 + i : 7 + 2 * (i - 5); }
constexpr int sch_w(int i) { return i < 2 ? i : (i - 2 <= 8 ? 8 + 2 * (i - 2) : 25 + (i - 11)); }

constexpr unsigned STAGE_TILES = 112;
__device__ __forceinline__ void wait_stage(const unsigned* c) {
    for (unsigned spin = 0; spin < 4000000u; ++spin) {
        if (__hip_atomic_load(c, __ATOMIC_RELAXED, __HIP_MEMORY_SCOPE_AGENT) + 1u >= STAGE_TILES) break;
        __builtin_amdgcn_s_sleep(4);
    }
}

__device__ __forceinline__ void rec_A_body(const RecParams& p, u16* smem, int h) {
    float* Sc = reinterpret_cast<float*>(smem);
    const int tid = threadIdx.x, lane = tid & 63, wid = __builtin_amdgcn_readfirstlane(tid >> 6);
    const int c16 = lane & 15, g = lane >> 4;
    int kq[14];
#pragma unroll
    for (int q = 0; q < 14; ++q) {
        if (q < 3) kq[q] = 3 * wid + q;
        else if (q == 3) kq[q] = 12 + (wid >> 1);
        else if (q < 13) kq[q] = (q - 4 < 3 * wid) ? (q - 4) : (q - 1);
        else kq[q] = 13 - (wid >> 1);
    }
    bf16x8 W[6][14];
    u16* WL = smem + 4 * 320 * 2 + (wid * 14 * 64 + lane) * 8;
#pragma unroll
    for (int t = 0; t < 7; ++t) {
        const int ksp = (t < 6) ? (3 * wid + (t >> 1)) : (12 + (wid >> 1));
        const int half = (t < 6) ? (t & 1) : (wid & 1);
        const int n = 32 * ksp + 8 * (c16 >> 2) + 4 * half + (c16 & 3);
        const u16* ap = p.AmatT + ((long)h * DM + n) * DM + 8 * g;
#pragma unroll
        for (int q = 0; q < 14; ++q) {
            bf16x8 w = *reinterpret_cast<const bf16x8*>(ap + 32 * kq[q]);
            if (t < 6)
                W[t][q] = w;
            else
                *reinterpret_cast<bf16x8*>(WL + q * 512) = w;
        }
    }
    bf16x8 X[2][4];
    const bool q3ok = (wid & 1) == 0;
    __amdgpu_buffer_rsrc_t rsb = __builtin_amdgcn_make_buffer_rsrc(p.Bt, 0, S_ * H_ * DM * 2, 0x00020000);
    const unsigned xvoff = (c16 * (H_ * DM) + h * DM + 8 * g) * 2;
    auto load_X = [&](int s) {
        if ((s & 7) == 0) wait_stage(p.stage + 64 * (s >> 3));
#pragma unroll
        for (int tl = 0; tl < 2; ++tl)
#pragma unroll
            for (int q = 0; q < 4; ++q) {
                bf16x8 w = __builtin_bit_cast(
                    bf16x8, ld16_sc1(rsb, xvoff, (unsigned)(((s * 32 + tl * 16) * (H_ * DM) + 32 * kq[q]) * 2)));
                if (q == 3 && !q3ok) w = bf16x8{0, 0, 0, 0, 0, 0, 0, 0};
                X[tl][q] = w;
            }
    };
    load_X(0);
    const int srow = 4 * (wid & 1) + g;
    const bool sact = c16 < 10;
    const float* sread = Sc + srow * 40 + (sact ? 4 * c16 : 0);
    float* swr_mm = Sc + wid * 320 + (c16 & 7) * 40 + 8 * (g & 1);
    float* swr_x0 = Sc + wid * 320 + (c16 & 7) * 40 + (g == 0 ? 4 : 4 * g + 8);
    float* swr_x1 = Sc + wid * 320 + (c16 & 7) * 40 + 24 + 4 * g;
    u16* memL = smem + 4 * 320 * 2 + 4 * 14 * 512;
    const int ch1 = (tid < 192) ? (256 + tid) : tid;
    u16* mw0 = memL + ((tid >> 1) & 7) * MROW + 16 * (tid >> 4) + 8 * (tid & 1);
    u16* mw1 = memL + ((ch1 >> 1) & 7) * MROW + 16 * (ch1 >> 4) + 8 * (ch1 & 1);
    const unsigned ml_addr = (unsigned)(uintptr_t)(LAS u16*)(memL + (c16 & 7) * MROW + 8 * g);
    __amdgpu_buffer_rsrc_t rs2 = __builtin_amdgcn_make_buffer_rsrc(p.G2, 0, 65 * G2_SLOT * 2, 0x00020000);
    __amdgpu_buffer_rsrc_t rs1 = __builtin_amdgcn_make_buffer_rsrc(p.G1, 0, 63 * G1_SLOT * 2, 0x00020000);
    const unsigned g1off = (h * 320 + srow * 40 + 4 * c16) * 2;
    const bool sst = sact && !(c16 & 1);
    const unsigned wl_addr = (unsigned)(uintptr_t)(LAS u16*)WL;
    bool dead = false;
    for (int s = 0; s < NSEG - 1; ++s) {
        u32x4 mf[14];
        bf16x8 w6[3];
        lds_rd16(w6[0], wl_addr, 0);
        lds_rd16(w6[1], wl_addr, 1024);
        {
            u32x4 a, b;
            for (unsigned spin = 0;; ++spin) {
                asm volatile("" ::: "memory");
                a = ld16_sc1(rs2, tid * 16, (unsigned)(s * G2_SLOT * 2));
                b = ld16_sc1(rs2, ch1 * 16, (unsigned)(s * G2_SLOT * 2));
                unsigned mx = max(umax3(a[1], a[3], b[1]), b[3]);
                if (__all(mx != 0xffffffffu) || dead) break;
                if (spin > SPIN_CAP) dead = true;
            }
            *reinterpret_cast<u32x4*>(mw0) = a;
            *reinterpret_cast<u32x4*>(mw1) = b;
            __syncthreads();
        }
        TS(1);
#pragma unroll
        for (int q = 0; q < 5; ++q) lds_rd16u(mf[q], ml_addr + 64 * kq[q]);
        f32x4 acc[7], ax[2];
#pragma unroll
        for (int q = 0; q < 14; ++q) {
            if (q + 5 < 14) lds_rd16u(mf[q + 5], ml_addr + 64 * kq[q + 5]);
            if (q + 2 < 14) lds_rd16(w6[(q + 2) % 3], wl_addr, (q + 2) * 1024);
            switch (q) {
#define WM(Q) case Q: wait_lgkm<sch_n(Q) - sch_m(Q) - 1>(); break;
                WM(0) WM(1) WM(2) WM(3) WM(4) WM(5) WM(6) WM(7) WM(8) WM(9) WM(10) WM(11) WM(12) WM(13)
#undef WM
            }
#pragma unroll
            for (int t = 0; t < 4; ++t) {
                if (q == 0) mfma_a0(acc[t], W[t][q], mf[q]);
                else mfma_a(acc[t], W[t][q], mf[q]);
            }
#pragma unroll
            for (int t = 4; t < 6; ++t) {
                if (t == 4 && q < 6) {
                    if (q == 0) mfma_a0(acc[t], W[t][q], mf[q]);
                    else mfma_a(acc[t], W[t][q], mf[q]);
                } else {
                    if (q == 0) mfma_v0(acc[t], W[t][q], mf[q]);
                    else mfma_v(acc[t], W[t][q], mf[q]);
                }
            }
            switch (q) {
#define WW(Q) case Q: wait_lgkm<sch_n(Q) - sch_w(Q) - 1>(); break;
                WW(0) WW(1) WW(2) WW(3) WW(4) WW(5) WW(6) WW(7) WW(8) WW(9) WW(10) WW(11) WW(12) WW(13)
#undef WW
            }
            if (q == 0) mfma_v0(acc[6], w6[0], mf[q]);
            else mfma_v(acc[6], w6[q % 3], mf[q]);
            if (q < 4) {
                if (q == 0) {
                    mfma_v0(ax[0], X[0][q], mf[q]);
                    mfma_v0(ax[1], X[1][q], mf[q]);
                } else {
                    mfma_v(ax[0], X[0][q], mf[q]);
                    mfma_v(ax[1], X[1][q], mf[q]);
                }
            }
            if (q == 4) {
                asm volatile("" ::: "memory");
                load_X(min(s + 1, NSEG - 2));
                asm volatile("" ::: "memory");
            }
        }
        asm volatile("s_nop 7\n\ts_nop 1"
                     : "+v"(acc[0]), "+v"(acc[1]), "+v"(acc[2]), "+v"(acc[3]), "+v"(acc[4]), "+v"(acc[5]), "+v"(acc[6]),
                       "+v"(ax[0]), "+v"(ax[1]));
        f32x4 sa = f32x4{0.f, 0.f, 0.f, 0.f}, sb = f32x4{0.f, 0.f, 0.f, 0.f};
#pragma unroll
        for (int pr = 0; pr < 4; ++pr) {
            u32x4 bv;
            if (pr < 3) {
                bv = u32x4{pack2(acc[2 * pr][0], acc[2 * pr][1]), pack2(acc[2 * pr][2], acc[2 * pr][3]),
                           pack2(acc[2 * pr + 1][0], acc[2 * pr + 1][1]), pack2(acc[2 * pr + 1][2], acc[2 * pr + 1][3])};
            } else {
                unsigned t0 = pack2(acc[6][0], acc[6][1]), t1 = pack2(acc[6][2], acc[6][3]);
                bv = q3ok ? u32x4{t0, t1, 0u, 0u} : u32x4{0u, 0u, t0, t1};
            }
            if (pr & 1)
                sb = MFMA16(__builtin_bit_cast(bf16x8, mf[pr]), __builtin_bit_cast(bf16x8, bv), sb);
            else
                sa = MFMA16(__builtin_bit_cast(bf16x8, mf[pr]), __builtin_bit_cast(bf16x8, bv), sa);
        }
        if (c16 < 8) {
            *reinterpret_cast<f32x4*>(swr_x0) = ax[0];
            *reinterpret_cast<f32x4*>(swr_x1) = ax[1];
        }
        sa += sb;
        if (c16 < 8 && g < 2) *reinterpret_cast<f32x4*>(swr_mm) = sa;
        TS(2);
        __syncthreads();
        if (wid < 2) {
            f32x4 x0 = *reinterpret_cast<const f32x4*>(sread);
            f32x4 x1 = *reinterpret_cast<const f32x4*>(sread + 320);
            f32x4 x2 = *reinterpret_cast<const f32x4*>(sread + 640);
            f32x4 x3 = *reinterpret_cast<const f32x4*>(sread + 960);
            f32x4 a = (x0 + x1) + (x2 + x3);
            float mx = sact ? __builtin_fmaxf(__builtin_fmaxf(a[0], a[1]), __builtin_fmaxf(a[2], a[3])) : -INFINITY;
            mx = red16_max(mx);
            const float L2E = 1.4426950408889634f;
            float nm = -mx * L2E;
            float e0 = __builtin_amdgcn_exp2f(__builtin_fmaf(a[0], L2E, nm));
            float e1 = __builtin_amdgcn_exp2f(__builtin_fmaf(a[1], L2E, nm));
            float e2 = __builtin_amdgcn_exp2f(__builtin_fmaf(a[2], L2E, nm));
            float e3 = __builtin_amdgcn_exp2f(__builtin_fmaf(a[3], L2E, nm));
            float sm = sact ? ((e0 + e1) + (e2 + e3)) : 0.f;
            sm = red16_sum(sm);
            float inv = __builtin_amdgcn_rcpf(sm);
            st_gran8(rs1, g1off, (unsigned)(s * G1_SLOT * 2), sst, pack2(e0 * inv, e1 * inv), pack2(e2 * inv, e3 * inv));
        }
        TS(3);
        __builtin_amdgcn_s_sleep(SLP_A);
    }
}

__device__ __forceinline__ void rec_C_body(const RecParams& p, u16* smem, int c) {
    u16* memL = smem;
    float* red = reinterpret_cast<float*>(smem + 16 * MROW);
    const int tid = threadIdx.x, lane = tid & 63, wid = __builtin_amdgcn_readfirstlane(tid >> 6);
    const int c16 = lane & 15, g = lane >> 4;
    for (int i = tid; i < 8 * MROW / 2; i += 256) reinterpret_cast<unsigned*>(memL + 8 * MROW)[i] = 0u;
    const int ch1 = (tid < 192) ? (256 + tid) : tid;
    if (tid < 32) {
        u32x2 m0 = *reinterpret_cast<const u32x2*>(p.mem0 + (tid >> 2) * DM + 16 * c + 4 * (tid & 3));
        st_gran4(p.G2 + 128 * c + 4 * tid, m0[0], m0[1]);
    }
    u16* mw0 = memL + ((tid >> 1) & 7) * MROW + 16 * (tid >> 4) + 8 * (tid & 1);
    u16* mw1 = memL + ((ch1 >> 1) & 7) * MROW + 16 * (ch1 >> 4) + 8 * (ch1 & 1);
    const int col = c * 16 + c16;
    bf16x8 W[4][14];
#pragma unroll
    for (int hh = 0; hh < 4; ++hh) {
        const u16* cp = p.CmatT + ((long)(wid * 4 + hh) * DM + col) * DM + 8 * g;
#pragma unroll
        for (int ks = 0; ks < 14; ++ks) W[hh][ks] = *reinterpret_cast<const bf16x8*>(cp + 32 * ks);
    }
    u32x2 dlo[4], dhi[4], clo[4], chi[4];
    u32x4 dx, bx;
    const int klo = (g >= 2) ? (8 * (g - 1)) : 0;
    const int khi = (g >= 2) ? (8 * (g - 1) + 4) : 4 * g;
    __amdgpu_buffer_rsrc_t rsd = __builtin_amdgcn_make_buffer_rsrc(p.Dt, 0, S_ * H_ * DM * 2, 0x00020000);
    const unsigned dvoff = (unsigned)((wid * 4 * DM + col) * S_ * 2);
    auto load_D = [&](int s) {
        if ((s & 7) == 0) wait_stage(p.stage + 64 * (s >> 3));
#pragma unroll
        for (int hh = 0; hh < 4; ++hh) {
            dlo[hh] = __builtin_amdgcn_raw_buffer_load_b64(rsd, dvoff + hh * (DM * S_ * 2) + klo * 2, s * 64, 16);
            dhi[hh] = __builtin_amdgcn_raw_buffer_load_b64(rsd, dvoff + hh * (DM * S_ * 2) + khi * 2, s * 64, 16);
        }
        dx = ld16_sc1(rsd, dvoff + g * (DM * S_ * 2) + 48, s * 64);
    };
    load_D(0);
    const u16* mfp = memL + c16 * MROW + 8 * g;
    const int i8 = c16 & 7;
    const unsigned pvoff = ((wid * 4) * 320 + i8 * 40 + 8 * g) * 2;
    const unsigned pvoffx = ((wid * 4 + g) * 320 + i8 * 40 + 32) * 2;
    float* rwr = red + wid * 128 + (4 * (g & 1)) * 16 + c16;
    const float* rrd = red + ((lane >> 2) & 7) * 16 + 4 * (lane & 3);
    u16* g2w = p.G2 + 128 * c + 4 * (lane & 31);
    __amdgpu_buffer_rsrc_t rs1 = __builtin_amdgcn_make_buffer_rsrc(p.G1, 0, 63 * G1_SLOT * 2, 0x00020000);
    __amdgpu_buffer_rsrc_t rs2 = __builtin_amdgcn_make_buffer_rsrc(p.G2, 0, 65 * G2_SLOT * 2, 0x00020000);
    bool dead = false;
    u64* utp = reinterpret_cast<u64*>(p.Ut + ((long)(wid * 4) * DM + col) * 8 + 4 * (g & 1));
    for (int s = 0; s < NSEG; ++s) {
        {
            u32x4 a, b;
            for (unsigned spin = 0;; ++spin) {
                asm volatile("" ::: "memory");
                a = ld16_sc1(rs2, tid * 16, (unsigned)(s * G2_SLOT * 2));
                b = ld16_sc1(rs2, ch1 * 16, (unsigned)(s * G2_SLOT * 2));
                unsigned mx = max(umax3(a[1], a[3], b[1]), b[3]);
                if (__all(mx != 0xffffffffu) || dead) break;
                if (spin > SPIN_CAP) dead = true;
            }
            *reinterpret_cast<u32x4*>(mw0) = a;
            *reinterpret_cast<u32x4*>(mw1) = b;
            __syncthreads();
        }
        TS(1);
        if (c == 0 && tid == 0) __hip_atomic_store(p.prog, (unsigned)(s - 1), __ATOMIC_RELAXED, __HIP_MEMORY_SCOPE_AGENT);
#pragma unroll
        for (int hh = 0; hh < 4; ++hh) {
            clo[hh] = dlo[hh];
            chi[hh] = dhi[hh];
        }
        bx = dx;
        asm volatile("" ::: "memory");
        load_D(min(s + 1, NSEG - 2));
        asm volatile("" ::: "memory");
        f32x4 U[4];
#pragma unroll
        for (int hh = 0; hh < 4; ++hh) U[hh] = f32x4{0.f, 0.f, 0.f, 0.f};
#pragma unroll
        for (int ks = 0; ks < 14; ++ks) {
            bf16x8 mf = *reinterpret_cast<const bf16x8*>(mfp + 32 * ks);
#pragma unroll
            for (int hh = 0; hh < 4; ++hh) U[hh] = MFMA16(mf, W[hh][ks], U[hh]);
        }
        u32x4 bv[4];
#pragma unroll
        for (int hh = 0; hh < 4; ++hh) {
            unsigned u0 = pack2(U[hh][0], U[hh][1]), u1 = pack2(U[hh][2], U[hh][3]);
            bv[hh] = u32x4{(g < 2) ? u0 : clo[hh][0], (g < 2) ? u1 : clo[hh][1], chi[hh][0], chi[hh][1]};
            if (g < 2)
                __hip_atomic_store(utp + ((long)s * 16 + hh) * (DM * 2), ((u64)u1 << 32) | u0, __ATOMIC_RELAXED,
                                   __HIP_MEMORY_SCOPE_AGENT);
        }
        if (s == NSEG - 1) {
            asm volatile("s_waitcnt vmcnt(0)" ::: "memory");
            __syncthreads();
            if (tid < 32) st_gran4(g2w + NSEG * G2_SLOT, 0u, 0u);
            if (c == 0 && tid < 64) {
                const u64* gf = reinterpret_cast<const u64*>(p.G2 + (long)NSEG * G2_SLOT + 128 * (lane < NC_BLK ? lane : 0));
                for (unsigned spin = 0; spin < SPIN_CAP; ++spin) {
                    u64 x = ld_gran(gf);
                    if (__all((unsigned)(x >> 32) != 0xffffffffu)) break;
                }
                if (tid == 0) __hip_atomic_store(p.prog, (unsigned)(NSEG - 1), __ATOMIC_RELAXED, __HIP_MEMORY_SCOPE_AGENT);
            }
            break;
        }
        u32x4 pv[5];
        __builtin_amdgcn_s_sleep(SLP_C1);
        for (unsigned spin = 0;; ++spin) {
            asm volatile("" ::: "memory");
#pragma unroll
            for (int hh = 0; hh < 4; ++hh) pv[hh] = ld16_sc1(rs1, pvoff + hh * 640, (unsigned)(s * G1_SLOT * 2));
            pv[4] = ld16_sc1(rs1, pvoffx, (unsigned)(s * G1_SLOT * 2));
            unsigned mx = 0;
#pragma unroll
            for (int q = 0; q < 5; ++q) mx = umax3(mx, pv[q][1], pv[q][3]);
            if (__all(mx != 0xffffffffu) || dead) break;
            if (spin > SPIN_CAP) dead = true;
        }
        TS(2);
        f32x4 o0 = f32x4{0.f, 0.f, 0.f, 0.f}, o1 = f32x4{0.f, 0.f, 0.f, 0.f};
        o0 = MFMA16(__builtin_bit_cast(bf16x8, pv[0]), __builtin_bit_cast(bf16x8, bv[0]), o0);
        o1 = MFMA16(__builtin_bit_cast(bf16x8, pv[1]), __builtin_bit_cast(bf16x8, bv[1]), o1);
        o0 = MFMA16(__builtin_bit_cast(bf16x8, pv[2]), __builtin_bit_cast(bf16x8, bv[2]), o0);
        o1 = MFMA16(__builtin_bit_cast(bf16x8, pv[3]), __builtin_bit_cast(bf16x8, bv[3]), o1);
        o0 = MFMA16(__builtin_bit_cast(bf16x8, pv[4]), __builtin_bit_cast(bf16x8, bx), o0);
        o0 += o1;
        if (g < 2) {
#pragma unroll
            for (int r = 0; r < 4; ++r) rwr[r * 16] = o0[r];
        }
        asm volatile("s_waitcnt vmcnt(0)" ::: "memory");
        __syncthreads();
        if (tid < 32) {
            f32x4 x0 = *reinterpret_cast<const f32x4*>(rrd);
            f32x4 x1 = *reinterpret_cast<const f32x4*>(rrd + 128);
            f32x4 x2 = *reinterpret_cast<const f32x4*>(rrd + 256);
            f32x4 x3 = *reinterpret_cast<const f32x4*>(rrd + 384);
            f32x4 a = (x0 + x1) + (x2 + x3);
            st_gran8(rs2, (128 * c + 4 * (lane & 31)) * 2, (unsigned)((s + 1) * G2_SLOT * 2), !(lane & 1), pack2(a[0], a[1]),
                     pack2(a[2], a[3]));
        }
        TS(3);
        __builtin_amdgcn_s_sleep(SLP_C2);
    }
}

template <int BM, int BN, bool NT, bool SC1>
__device__ __forceinline__ void gemm_tile(const u16* __restrict__ A, int lda, const u16* __restrict__ B, int ldb,
                                          u16* __restrict__ C, int ldc, int nk, u16* smem, int bsplit = 1 << 20) {
    constexpr int AROW = 72, BROW = NT ? 72 : BN + 32;
    constexpr int ASZ = BM * AROW, STG = ASZ + (NT ? BN * 72 : 64 * BROW);
    constexpr int ACH = BM / 32, BCH = BN / 32, TM = BM / 64, TN = BN / 64;
    constexpr int BPR = BN / 8;
    constexpr int BRS = 256 / BPR;
    int tid = threadIdx.x;
    asm volatile("" : "+v"(tid));
    const int lane = tid & 63, wid = tid >> 6;
    const int wm = wid >> 1, wn = wid & 1;
    const int r32 = lane & 31, hi = lane >> 5, g = lane >> 4, q4 = (lane & 15) >> 2, p4 = lane & 3;
    f32x16 acc[TM][TN];
#pragma unroll
    for (int i = 0; i < TM; ++i)
#pragma unroll
        for (int j = 0; j < TN; ++j) acc[i][j] = f32x16{};
    u32x4 ra0[ACH], rb0[BCH], ra1[ACH], rb1[BCH];
    const unsigned aoff = ((unsigned)(tid >> 3) * lda + (tid & 7) * 8) * 2u;
    const unsigned boff = NT ? ((unsigned)(tid >> 3) * ldb + (tid & 7) * 8) * 2u
                             : ((unsigned)(tid / BPR) * ldb + (tid % BPR) * 8 +
                                ((tid % BPR) * 8 >= bsplit ? DM * DM - DM : 0)) * 2u;
    const char* Ac = reinterpret_cast<const char*>(A);
    const char* Bc = reinterpret_cast<const char*>(B);
    u16* aw = smem + (tid >> 3) * AROW + (tid & 7) * 8;
    u16* bw = NT ? (smem + ASZ + (tid >> 3) * BROW + (tid & 7) * 8) : (smem + ASZ + (tid / BPR) * BROW + (tid % BPR) * 8);
    const u16* ard = smem + (wm * (BM / 2) + r32) * AROW + hi * 8;
    const u16* brd = NT ? (smem + ASZ + (wn * (BN / 2) + r32) * BROW + hi * 8)
                        : (smem + ASZ + (8 * (g >> 1) + q4) * BROW + wn * (BN / 2) + 16 * (g & 1) + 4 * p4);
    auto load = [&](u32x4(&ra)[ACH], u32x4(&rb)[BCH], int kt) {
#pragma unroll
        for (int i = 0; i < ACH; ++i)
            ra[i] = *reinterpret_cast<const u32x4*>(Ac + (aoff + (unsigned)(32 * i * lda + kt * 64) * 2u));
#pragma unroll
        for (int i = 0; i < BCH; ++i) {
            if (NT)
                rb[i] = *reinterpret_cast<const u32x4*>(Bc + (boff + (unsigned)(32 * i * ldb + kt * 64) * 2u));
            else
                rb[i] = *reinterpret_cast<const u32x4*>(Bc + (boff + (unsigned)((kt * 64 + BRS * i) * ldb) * 2u));
        }
    };
    auto store = [&](const u32x4(&ra)[ACH], const u32x4(&rb)[BCH], int st) {
#pragma unroll
        for (int i = 0; i < ACH; ++i) *reinterpret_cast<u32x4*>(aw + st * STG + 32 * i * AROW) = ra[i];
#pragma unroll
        for (int i = 0; i < BCH; ++i)
            *reinterpret_cast<u32x4*>(bw + st * STG + (NT ? 32 : BRS) * i * BROW) = rb[i];
    };
    auto compute = [&](int st, int ks) {
        bf16x8 af[TM], bfr[TN];
#pragma unroll
        for (int i = 0; i < TM; ++i)
            af[i] = *reinterpret_cast<const bf16x8*>(ard + st * STG + i * 32 * AROW + ks * 16);
#pragma unroll
        for (int j = 0; j < TN; ++j) {
            if (NT) {
                bfr[j] = *reinterpret_cast<const bf16x8*>(brd + st * STG + j * 32 * BROW + ks * 16);
            } else {
                const u16* pB = brd + st * STG + ks * 16 * BROW + j * 32;
                s16x4 lo = tr_read(pB);
                s16x4 hi4 = tr_read(pB + 4 * BROW);
                bfr[j] = bf16x8{lo[0], lo[1], lo[2], lo[3], hi4[0], hi4[1], hi4[2], hi4[3]};
            }
        }
#pragma unroll
        for (int i = 0; i < TM; ++i)
#pragma unroll
            for (int j = 0; j < TN; ++j) acc[i][j] = MFMA32(bfr[j], af[i], acc[i][j]);
    };
    load(ra0, rb0, 0);
    load(ra1, rb1, 1);
    __syncthreads();
    store(ra0, rb0, 0);
    load(ra0, rb0, 2);
    __syncthreads();
    for (int kt = 0; kt + 1 < nk; kt += 2) {
        compute(0, 0);
        compute(0, 1);
        store(ra1, rb1, 1);
        load(ra1, rb1, min(kt + 3, nk - 1));
        compute(0, 2);
        compute(0, 3);
        __syncthreads();
        compute(1, 0);
        compute(1, 1);
        store(ra0, rb0, 0);
        load(ra0, rb0, min(kt + 4, nk - 1));
        compute(1, 2);
        compute(1, 3);
        __syncthreads();
    }
    if (nk & 1) {
        compute(0, 0);
        compute(0, 1);
        compute(0, 2);
        compute(0, 3);
        __syncthreads();
    }
    constexpr int CROW = BN + 8;
#pragma unroll
    for (int i = 0; i < TM; ++i)
#pragma unroll
        for (int j = 0; j < TN; ++j)
#pragma unroll
            for (int rr = 0; rr < 4; ++rr) {
                u32x2 w;
                w[0] = pack2(acc[i][j][4 * rr], acc[i][j][4 * rr + 1]);
                w[1] = pack2(acc[i][j][4 * rr + 2], acc[i][j][4 * rr + 3]);
                *reinterpret_cast<u32x2*>(smem + (wm * (BM / 2) + 32 * i + r32) * CROW + wn * (BN / 2) + 32 * j +
                                          8 * rr + 4 * hi) = w;
            }
    __syncthreads();
    int tid2 = threadIdx.x;
    asm volatile("" : "+v"(tid2));
    const unsigned coff = ((unsigned)(tid2 / BPR) * ldc + (tid2 % BPR) * 8) * 2u;
    __amdgpu_buffer_rsrc_t rs = __builtin_amdgcn_make_buffer_rsrc(C, 0, 0x7fffffff, 0x00020000);
#pragma unroll
    for (int i = 0; i < BM * BN / 2048; ++i) {
        int row = tid2 / BPR + BRS * i, cc = tid2 % BPR;
        u32x4 v = *reinterpret_cast<const u32x4*>(smem + row * CROW + cc * 8);
        __builtin_amdgcn_raw_buffer_store_b128(v, rs, coff + (unsigned)(BRS * i * ldc) * 2u, 0, SC1 ? 16 : 0);
    }
}

struct MegaParams {
    const u16* A_x;
    const u16* B_x;
    const u16* B_ckv;
    const u16* B_wq;
    const u16* B_wuk;
    const u16* B_wuv;
    const u16* B_wo;
    u16* E;
    u16* Qb;
    u16* Kb;
    u16* Vb;
    u16* OB;
    u16* out;
    unsigned* ctl;
};
constexpr int NWORK = 256 - NREC;
constexpr int CTL_QA = 0, CTL_ST = 512, CTL_KV = 1024, CTL_OC = 1280, CTL_OQ = 1296, CTL_EC = 1312, CTL_PQ = 1320,
              CTL_PROG = 1344, CTL_WORDS = 1408;
constexpr unsigned WSPIN_CAP = 2000000u;
#define IDLE_SLP 32

__device__ __forceinline__ int next_ticket(unsigned* q, int* slot) {
    __syncthreads();
    if (threadIdx.x == 0) *slot = (int)(__hip_atomic_fetch_add(q, 1u, __ATOMIC_RELAXED, __HIP_MEMORY_SCOPE_AGENT) + 1u);
    __syncthreads();
    return *slot;
}
__device__ __forceinline__ void publish_count(unsigned* c) {
    asm volatile("s_waitcnt vmcnt(0)" ::: "memory");
    __syncthreads();
    if (threadIdx.x == 0) __hip_atomic_fetch_add(c, 1u, __ATOMIC_RELAXED, __HIP_MEMORY_SCOPE_AGENT);
}
__device__ __forceinline__ void acquire_agent() {
    __builtin_amdgcn_fence(__ATOMIC_ACQUIRE, "agent");
    asm volatile("s_waitcnt vmcnt(0)" ::: "memory");
}
__device__ __forceinline__ void wait_counts(unsigned* c, int n, unsigned target) {
    if (threadIdx.x < 64) {
        const int lane = threadIdx.x;
        for (unsigned spin = 0; spin < WSPIN_CAP; ++spin) {
            unsigned v = __hip_atomic_load(c + (lane < n ? lane : 0), __ATOMIC_RELAXED, __HIP_MEMORY_SCOPE_AGENT) + 1u;
            if (__all(v >= target)) break;
            __builtin_amdgcn_s_sleep(32);
        }
        acquire_agent();
    }
    __syncthreads();
}

__device__ __forceinline__ void postA_task(const RecParams& p, const MegaParams& m, int s, int part, u16* smem) {
    u16* KL = smem;
    u16* PL = smem + 40 * MROW;
    int tid = threadIdx.x;
    asm volatile("" : "+v"(tid));
    const int lane = tid & 63, wid = __builtin_amdgcn_readfirstlane(tid >> 6);
    const int r32 = lane & 31, hi = lane >> 5;
    const u16* ep = m.E + (long)(s * 32 + r32) * (H_ * DM) + wid * 4 * DM + hi * 8;
    bf16x8 e0[28], e1[28];
    auto loadE = [&](bf16x8(&e)[28], int hh) {
#pragma unroll
        for (int ks = 0; ks < 28; ++ks) e[ks] = *reinterpret_cast<const bf16x8*>(ep + hh * DM + ks * 16);
    };
    loadE(e0, 0);
    loadE(e1, 1);
    __syncthreads();
    for (int c = tid; c < 32 * 56; c += 256) {
        int row = c / 56, cc = c - row * 56;
        *reinterpret_cast<u32x4*>(KL + (8 + row) * MROW + cc * 8) =
            *reinterpret_cast<const u32x4*>(m.A_x + (long)(s * 32 + row) * DM + cc * 8);
    }
    {
        const int ch1 = (tid < 192) ? (256 + tid) : tid;
        u32x4 a, b;
        {
            __amdgpu_buffer_rsrc_t rs2 = __builtin_amdgcn_make_buffer_rsrc(p.G2, 0, 65 * G2_SLOT * 2, 0x00020000);
            for (unsigned spin = 0; spin < WSPIN_CAP; ++spin) {
                asm volatile("" ::: "memory");
                a = ld16_sc1(rs2, tid * 16, (unsigned)(s * G2_SLOT * 2));
                b = ld16_sc1(rs2, ch1 * 16, (unsigned)(s * G2_SLOT * 2));
                unsigned mx = max(umax3(a[1], a[3], b[1]), b[3]);
                if (__all(mx != 0xffffffffu)) break;
                __builtin_amdgcn_s_sleep(8);
            }
        }
        *reinterpret_cast<u32x4*>(KL + ((tid >> 1) & 7) * MROW + 16 * (tid >> 4) + 8 * (tid & 1)) = a;
        *reinterpret_cast<u32x4*>(KL + ((ch1 >> 1) & 7) * MROW + 16 * (ch1 >> 4) + 8 * (ch1 & 1)) = b;
    }
    __syncthreads();
    const u16* k0l = KL + r32 * MROW + hi * 8;
    const u16* k1l = KL + (32 + (r32 & 7)) * MROW + hi * 8;
    const float L2E = 1.4426950408889634f;
    auto scores = [&](const bf16x8(&e)[28], int hh) {
        f32x16 s0 = f32x16{}, s1 = f32x16{};
#pragma unroll
        for (int ks = 0; ks < 28; ++ks) {
            bf16x8 ka = *reinterpret_cast<const bf16x8*>(k0l + ks * 16);
            bf16x8 kb = *reinterpret_cast<const bf16x8*>(k1l + ks * 16);
            s0 = MFMA32(ka, e[ks], s0);
            s1 = MFMA32(kb, e[ks], s1);
        }
#pragma unroll
        for (int r = 0; r < 16; ++r) {
            int kk = crow(r, hi);
            if (kk >= 8 && kk - 8 > r32) s0[r] = -INFINITY;
            int k2 = kk + 32;
            if (k2 >= 40 || k2 - 8 > r32) s1[r] = -INFINITY;
        }
        float mx = -INFINITY;
#pragma unroll
        for (int r = 0; r < 16; ++r) mx = fmaxf(mx, fmaxf(s0[r], s1[r]));
        mx = fmaxf(mx, __shfl_xor(mx, 32));
        float nm = -mx * L2E, psum = 0.f;
#pragma unroll
        for (int r = 0; r < 16; ++r) {
            s0[r] = __builtin_amdgcn_exp2f(fmaf(s0[r], L2E, nm));
            s1[r] = __builtin_amdgcn_exp2f(fmaf(s1[r], L2E, nm));
            psum += s0[r] + s1[r];
        }
        psum += __shfl_xor(psum, 32);
        float inv = 1.f / psum;
        u32x4 a0, a1, a2;
#pragma unroll
        for (int j = 0; j < 4; ++j) {
            a0[j] = pack2(s0[2 * j] * inv, s0[2 * j + 1] * inv);
            a1[j] = pack2(s0[8 + 2 * j] * inv, s0[8 + 2 * j + 1] * inv);
            a2[j] = pack2(s1[2 * j] * inv, s1[2 * j + 1] * inv);
        }
        u16* pw = PL + ((wid * 4 + hh) * 3 * 64 + lane) * 8;
        *reinterpret_cast<u32x4*>(pw) = a0;
        *reinterpret_cast<u32x4*>(pw + 512) = a1;
        *reinterpret_cast<u32x4*>(pw + 1024) = a2;
    };
    scores(e0, 0);
    loadE(e0, 2);
    scores(e1, 1);
    loadE(e1, 3);
    scores(e0, 2);
    scores(e1, 3);
    const int ct = part + 4 * wid;
    const int col = (ct < 14 ? ct : 13) * 32 + r32;
    u64 u[16];
    u32x2 d[16][4];
#pragma unroll
    for (int h = 0; h < 16; ++h) {
        const u64* up = reinterpret_cast<const u64*>(p.Ut + (((long)s * 16 + h) * DM + col) * 8 + 4 * hi);
        const u16* dp = p.Dt + ((long)h * DM + col) * S_ + s * 32 + 4 * hi;
        u[h] = ld_gran(up);
#pragma unroll
        for (int q = 0; q < 4; ++q) d[h][q] = *reinterpret_cast<const u32x2*>(dp + 8 * q);
    }
    __syncthreads();
    f32x16 o = f32x16{};
#pragma unroll
    for (int h = 0; h < 16; ++h) {
        const u16* pr = PL + (h * 3 * 64 + lane) * 8;
        bf16x8 p0 = *reinterpret_cast<const bf16x8*>(pr);
        bf16x8 p1 = *reinterpret_cast<const bf16x8*>(pr + 512);
        bf16x8 p2 = *reinterpret_cast<const bf16x8*>(pr + 1024);
        o = MFMA32(__builtin_bit_cast(bf16x8, (u32x4{(unsigned)u[h], (unsigned)(u[h] >> 32), d[h][0][0], d[h][0][1]})), p0, o);
        o = MFMA32(__builtin_bit_cast(bf16x8, (u32x4{d[h][1][0], d[h][1][1], d[h][2][0], d[h][2][1]})), p1, o);
        o = MFMA32(__builtin_bit_cast(bf16x8, (u32x4{d[h][3][0], d[h][3][1], 0u, 0u})), p2, o);
    }
    if (ct < 14) {
#pragma unroll
        for (int rr = 0; rr < 4; ++rr) {
            u32x2 w;
            w[0] = pack2(o[4 * rr], o[4 * rr + 1]);
            w[1] = pack2(o[4 * rr + 2], o[4 * rr + 3]);
            *reinterpret_cast<u32x2*>(m.out + (long)(s * 32 + r32) * 896 + ct * 32 + 8 * rr + 4 * hi) = w;
        }
    }
}

__device__ __forceinline__ void worker_body(const RecParams& p, const MegaParams& m, u16* smem, int* slot) {
    unsigned* ctl = m.ctl;
    WTS(0);
    const int myx = (int)(__builtin_amdgcn_s_getreg((3 << 11) | 20) & 7u);
    for (int k = 0; k < 8; ++k) {
        const int x = (myx + k) & 7;
        for (;;) {
            int t = next_ticket(ctl + CTL_QA + 64 * x, slot);
            if (t >= 296) break;
            int kind, idx;
            if (t < 28) { kind = 0; idx = t; }
            else if (t < 52) { kind = 1; idx = t - 28; }
            else if (t < 66) { kind = 0; idx = t - 52 + 28; }
            else if (t < 90) { kind = 1; idx = t - 66 + 24; }
            else if (t < 104) { kind = 0; idx = t - 90 + 42; }
            else if (t < 152) { kind = 1; idx = t - 104 + 48; }
            else if (t < 180) { kind = 0; idx = t - 152 + 56; }
            else if (t < 206) { kind = 2; idx = t - 180; }
            else if (t < 234) { kind = 0; idx = t - 206 + 84; }
            else if (t < 240) { kind = 2; idx = t - 234 + 26; }
            else { kind = 3; idx = t - 240; }
            KT0
            if (kind == 2) {
                const int qb = 15 - (idx >> 1), h = 2 * x + (idx & 1);
                wait_counts(ctl + CTL_KV + 16 * h, (qb >> 1) + 1, 6u);
                KT1(7);
                attnB_body(m.Qb, m.Kb, m.Vb, m.OB, smem, qb, h);
                publish_count(ctl + CTL_OC + qb);
                KT1(2);
                continue;
            }
            const int j = idx / 14, i = idx - j * 14;
            if (kind == 1 || (kind == 0 && i < 7)) {
                const u16 *A, *B;
                u16* C;
                unsigned* cnt;
                int lda, ldb, ldc, bsplit = 1 << 20;
                if (kind == 1) {
                    int rb = idx / 12, r = idx - rb * 12, mat = r >> 2, cb = 4 * x + (r & 3);
                    lda = (mat < 2) ? DC : DM;
                    ldb = HD;
                    ldc = HD;
                    A = ((mat < 2) ? m.B_ckv : m.B_x) + (long)rb * 256 * lda;
                    B = ((mat == 0) ? m.B_wuk : (mat == 1 ? m.B_wuv : m.B_wq)) + cb * 128;
                    C = ((mat == 0) ? m.Kb : (mat == 1 ? m.Vb : m.Qb)) + (long)rb * 256 * HD + cb * 128;
                    cnt = ctl + CTL_KV + 16 * (cb >> 1) + rb;
                } else {
                    int hh = (i >= 4) ? 1 : 0, co = i * 128 - hh * DM;
                    lda = DM;
                    ldb = DM;
                    ldc = H_ * DM;
                    A = m.A_x + (long)j * 256 * DM;
                    B = p.AmatT + (long)(2 * x + hh) * DM * DM + co;
                    C = p.Bt + (long)j * 256 * (H_ * DM) + (7 * x + i) * 128;
                    bsplit = DM - co;
                    cnt = ctl + CTL_ST + 64 * j;
                }
                gemm_tile<256, 128, false, true>(A, lda, B, ldb, C, ldc, lda / 64, smem, bsplit);
                publish_count(cnt);
                KT1(kind == 0 ? 0 : (lda == DC ? 1 : 4));
            } else {
                const u16 *A, *B;
                u16* C;
                unsigned* cnt;
                int ldc;
                if (kind == 3) {
                    int rb = idx / 7, cb = 7 * x + idx - rb * 7;
                    A = m.A_x + (long)rb * 256 * DM;
                    B = p.AmatT + (long)cb * 128 * DM;
                    C = m.E + (long)rb * 256 * (H_ * DM) + cb * 128;
                    ldc = H_ * DM;
                    cnt = ctl + CTL_EC + rb;
                } else {
                    int u = 7 * x + i - 7, mb = u >> 1, nb = u & 1;
                    A = p.CmatT + (long)mb * 256 * DM;
                    B = m.A_x + (long)(j * 256 + nb * 128) * DM;
                    C = p.Dt + (long)mb * 256 * S_ + j * 256 + nb * 128;
                    ldc = S_;
                    cnt = ctl + CTL_ST + 64 * j;
                }
                gemm_tile<256, 128, true, true>(A, DM, B, DM, C, ldc, DM / 64, smem);
                publish_count(cnt);
                KT1(kind == 0 ? 5 : 3);
            }
        }
    }
    WTS(1);
    {
        const int b = blockIdx.x, x = b & 7, wi = (b - 48) >> 3;
        int otile = (b >= 144) ? (b - 144) >> 3 : -1;
        int pk = (b >= 48) ? wi : 32, pk2 = (b >= 48 && wi < 6) ? wi + 26 : 32;
        for (unsigned spin = 0; spin < WSPIN_CAP && (otile >= 0 || pk < 32); ++spin) {
            const int orb = 2 * x + (otile >= 7 ? 1 : 0), ps = 8 * (pk >> 2) + x;
            __syncthreads();
            if (threadIdx.x < 64) {
                const int lane = threadIdx.x;
                const int off = lane == 0 ? CTL_OC + orb : (lane == 1 ? CTL_EC + ((ps >> 3) & 7) : CTL_PROG);
                const unsigned v = __hip_atomic_load(ctl + off, __ATOMIC_RELAXED, __HIP_MEMORY_SCOPE_AGENT) + 1u;
                const unsigned oc = (unsigned)__builtin_amdgcn_readlane((int)v, 0);
                const unsigned ec = (unsigned)__builtin_amdgcn_readlane((int)v, 1);
                const int prog = __builtin_amdgcn_readlane((int)v, 2);
                int task = 0;
                if (otile >= 0 && oc >= 16u) task = 1;
                else if (pk < 32 && prog > ps && ec >= 56u) task = 2;
                if (task) {
                    acquire_agent();
                } else {
                    int far = (otile >= 0) ? 1 : min(ps - prog, 8);
                    if (far <= 0) __builtin_amdgcn_s_sleep(16);
                    for (int i = 0; i < far; ++i) __builtin_amdgcn_s_sleep(100);
                }
                if (lane == 0) *slot = task;
            }
            __syncthreads();
            const int t = *slot;
            if (t == 2) {
                KT0
                postA_task(p, m, ps, pk & 3, smem);
                KT1(8);
                pk = pk2;
                pk2 = 32;
                WTS(3);
            } else if (t == 1) {
                KT0
                const int cb = otile >= 7 ? otile - 7 : otile;
                gemm_tile<128, 64, false, false>(m.OB + (long)orb * 128 * HD, HD, m.B_wo + cb * 64, DM,
                                                 m.out + (long)orb * 128 * 896 + 448 + cb * 64, 896, HD / 64, smem);
                otile = -1;
                KT1(9);
                WTS(2);
            }
        }
    }
    WTS(6);
    WTS(7);
}

constexpr int REC_LDS = 4 * 320 * 4 + 4 * 14 * 1024 + 8 * MROW * 2;
constexpr int GEMM_LDS = 2 * (256 * 72 + 64 * 160) * 2;
constexpr int POST_LDS = (40 * MROW + 16 * 3 * 64 * 8) * 2;
constexpr int cmax(int a, int b) { return a > b ? a : b; }
constexpr int FUSED_LDS = cmax(cmax(ATTN_LDS, REC_LDS), cmax(GEMM_LDS, POST_LDS));

__global__ __launch_bounds__(256) void mega_kernel(RecParams p, MegaParams m) {
    __shared__ __attribute__((aligned(16))) u16 smem[FUSED_LDS / 2 + 8];
    const int b = blockIdx.x;
    if (b < NA_BLK) {
        rec_A_body(p, smem, b);
    } else if (b < NREC) {
        rec_C_body(p, smem, b - NA_BLK);
    } else {
        worker_body(p, m, smem, reinterpret_cast<int*>(smem + FUSED_LDS / 2));
    }
}

extern "C" void kernel_launch(void* const* d_in, const int* in_sizes, int n_in, void* d_out, int out_size,
                              void* d_ws, size_t ws_size, hipStream_t stream) {
    const u16* A_x = (const u16*)d_in[0];
    const u16* A_mem0 = (const u16*)d_in[1];
    const u16* A_Wq = (const u16*)d_in[2];
    const u16* A_Wk = (const u16*)d_in[3];
    const u16* A_Wv = (const u16*)d_in[4];
    const u16* A_Wo = (const u16*)d_in[5];
    const u16* B_x = (const u16*)d_in[7];
    const u16* B_ckv = (const u16*)d_in[8];
    const u16* B_wq = (const u16*)d_in[9];
    const u16* B_wuk = (const u16*)d_in[10];
    const u16* B_wuv = (const u16*)d_in[11];
    const u16* B_wo = (const u16*)d_in[12];
    u16* out = (u16*)d_out;

    char* ws = (char*)d_ws;
    const size_t SZ = (size_t)S_ * HD * 2;
    u16* Qb = (u16*)(ws + 0 * SZ);
    u16* Kb = (u16*)(ws + 1 * SZ);
    u16* Vb = (u16*)(ws + 2 * SZ);
    u16* OB = (u16*)(ws + 3 * SZ);
    size_t off = 4 * SZ;
    u16* AmatT = (u16*)(ws + off); off += (size_t)H_ * DM * DM * 2;
    u16* CmatT = (u16*)(ws + off); off += (size_t)H_ * DM * DM * 2;
    u16* Bt = (u16*)(ws + off); off += (size_t)H_ * DM * S_ * 2;
    u16* Dt = (u16*)(ws + off); off += (size_t)H_ * DM * S_ * 2;
    u16* E = (u16*)(ws + off); off += (size_t)H_ * DM * S_ * 2;
    u16* Ut = (u16*)(ws + off); off += (size_t)NSEG * H_ * DM * 8 * 2;
    char* gbase = ws + off;
    u16* G1 = (u16*)gbase;
    u16* G2 = G1 + 63 * G1_SLOT;
    unsigned* ctl = (unsigned*)(G2 + 65 * G2_SLOT);
    size_t gbytes = (size_t)(63 * G1_SLOT + 65 * G2_SLOT) * 2 + (size_t)CTL_WORDS * 4;
    off += gbytes;

    hipLaunchKernelGGL(pre_kernel, dim3(224), dim3(256), 0, stream, A_Wq, A_Wk, A_Wv, A_Wo, AmatT, CmatT, (u32x4*)gbase,
                       (int)(gbytes / 16));
    RecParams rp;
    rp.mem0 = A_mem0; rp.AmatT = AmatT; rp.CmatT = CmatT; rp.Bt = Bt; rp.Dt = Dt; rp.Ut = Ut; rp.G1 = G1; rp.G2 = G2;
    rp.stage = ctl + CTL_ST;
    rp.prog = ctl + CTL_PROG;
    MegaParams mp;
    mp.A_x = A_x;
    mp.B_x = B_x; mp.B_ckv = B_ckv; mp.B_wq = B_wq; mp.B_wuk = B_wuk; mp.B_wuv = B_wuv; mp.B_wo = B_wo;
    mp.E = E; mp.Qb = Qb; mp.Kb = Kb; mp.Vb = Vb; mp.OB = OB;
    mp.out = out; mp.ctl = ctl;
    hipLaunchKernelGGL(mega_kernel, dim3(256), dim3(256), 0, stream, rp, mp);
}
```

```cpp
#include <hip/hip_runtime.h>
#include <hip/hip_bf16.h>
#include <stdint.h>

typedef unsigned short u16;
typedef __attribute__((ext_vector_type(8))) short bf16x8;
typedef __attribute__((ext_vector_type(4))) short s16x4;
typedef __attribute__((ext_vector_type(16))) float f32x16;
typedef __attribute__((ext_vector_type(4))) float f32x4;
typedef __attribute__((ext_vector_type(4))) unsigned u32x4;
typedef __attribute__((ext_vector_type(2))) unsigned u32x2;
typedef short v4i16_t __attribute__((ext_vector_type(4)));

#define LAS __attribute__((address_space(3)))

__device__ __forceinline__ float bf2f(u16 x) { return __uint_as_float(((unsigned)x) << 16); }
__device__ __forceinline__ u16 f2bf(float f) {
    unsigned u = __float_as_uint(f);
    u += 0x7fffu + ((u >> 16) & 1u);
    return (u16)(u >> 16);
}
typedef __bf16 bf16x2_t __attribute__((ext_vector_type(2)));
__device__ __forceinline__ unsigned pack2(float lo, float hi) {
    bf16x2_t r = {(__bf16)lo, (__bf16)hi};
    return __builtin_bit_cast(unsigned, r);
}
__device__ __forceinline__ s16x4 tr_read(const void* p) {
    return __builtin_bit_cast(s16x4, __builtin_amdgcn_ds_read_tr16_b64_v4i16((LAS v4i16_t*)p));
}
#define MFMA32(a, b, c) __builtin_amdgcn_mfma_f32_32x32x16_bf16(a, b, c, 0, 0, 0)
#define MFMA16(a, b, c) __builtin_amdgcn_mfma_f32_16x16x32_bf16(a, b, c, 0, 0, 0)

__device__ __forceinline__ int tid_of(int ws) {
    int l = (int)__builtin_amdgcn_mbcnt_hi(~0u, __builtin_amdgcn_mbcnt_lo(~0u, 0u));
    asm volatile("" : "+v"(l));
    return ws * 64 + l;
}
__device__ __forceinline__ int crow(int r, int hi) { return (r & 3) + 8 * (r >> 2) + 4 * hi; }

constexpr int S_ = 2048, DM = 448, H_ = 16, DH = 256, HD = 4096, MM = 8, SEG = 32, NSEG = 64, DC = 1024;

__global__ __launch_bounds__(256) void pre_kernel(const u16* __restrict__ Wq, const u16* __restrict__ Wk,
                                                  const u16* __restrict__ Wv, const u16* __restrict__ Wo,
                                                  u16* __restrict__ AmatT, u16* __restrict__ CmatT,
                                                  u32x4* __restrict__ gbase, int gchunks,
                                                  const u16* __restrict__ x, u32x4* __restrict__ xp) {
    __shared__ __attribute__((aligned(16))) u16 Bs[256 * 96];
    const int tid = threadIdx.x, lane = tid & 63, wid = tid >> 6;
    const int c16 = lane & 15, g = lane >> 4, q4 = c16 >> 2, p4 = lane & 3;
    const int br = blockIdx.x >> 3, rb = br % 7, hz = (blockIdx.x & 7) + 8 * (br / 7), h = hz & 15;
    const bool isC = hz >= 16;
    u32x4 rbv[8];
#pragma unroll
    for (int i = 0; i < 8; ++i) {
        int c = tid + 256 * i;
        const u16* src = isC ? Wo + (long)(h * DH + (c >> 3)) * DM + rb * 64 + (c & 7) * 8
                             : Wk + (long)(rb * 64 + (c >> 5)) * HD + h * DH + (c & 31) * 8;
        rbv[i] = *reinterpret_cast<const u32x4*>(src);
    }
    const u16* ap = (isC ? Wv : Wq) + (long)(wid * 112 + c16) * HD + h * DH + 8 * g;
    bf16x8 af[7][8];
#pragma unroll
    for (int ks = 0; ks < 8; ++ks)
#pragma unroll
        for (int t = 0; t < 7; ++t) af[t][ks] = *reinterpret_cast<const bf16x8*>(ap + (long)t * 16 * HD + 32 * ks);
    for (int i = blockIdx.x * 256 + tid; i < gchunks; i += 224 * 256)
        gbase[i] = u32x4{0xffffffffu, 0xffffffffu, 0xffffffffu, 0xffffffffu};
#pragma unroll
    for (int i = 0; i < 2; ++i) {
        const int fr = (blockIdx.x * 2 + i) * 4 + wid, rt = fr / 14, kg = fr - rt * 14;
        xp[fr * 64 + lane] = *reinterpret_cast<const u32x4*>(x + (long)(rt * 16 + c16) * DM + 32 * kg + 8 * g);
    }
#pragma unroll
    for (int i = 0; i < 8; ++i) {
        int c = tid + 256 * i;
        u16* dst = isC ? Bs + (c >> 3) * 96 + (c & 7) * 8 : Bs + (c >> 5) * 264 + (c & 31) * 8;
        *reinterpret_cast<u32x4*>(dst) = rbv[i];
    }
    __syncthreads();
    f32x4 acc[7][4];
#pragma unroll
    for (int t = 0; t < 7; ++t)
#pragma unroll
        for (int j = 0; j < 4; ++j) acc[t][j] = f32x4{0.f, 0.f, 0.f, 0.f};
#pragma unroll
    for (int ks = 0; ks < 8; ++ks) {
        bf16x8 bfr[4];
#pragma unroll
        for (int j = 0; j < 4; ++j) {
            if (isC) {
                const u16* pB = Bs + (ks * 32 + 8 * g + q4) * 96 + j * 16 + 4 * p4;
                s16x4 lo = tr_read(pB);
                s16x4 hi4 = tr_read(pB + 4 * 96);
                bfr[j] = bf16x8{lo[0], lo[1], lo[2], lo[3], hi4[0], hi4[1], hi4[2], hi4[3]};
            } else {
                bfr[j] = *reinterpret_cast<const bf16x8*>(Bs + (16 * j + c16) * 264 + ks * 32 + 8 * g);
            }
        }
#pragma unroll
        for (int t = 0; t < 7; ++t)
#pragma unroll
            for (int j = 0; j < 4; ++j) acc[t][j] = MFMA16(af[t][ks], bfr[j], acc[t][j]);
    }
    const float sc = isC ? 1.f : 0.0625f;
    u16* op = (isC ? CmatT : AmatT) + (long)(h * DM + rb * 64 + c16) * DM + wid * 112 + 4 * g;
#pragma unroll
    for (int j = 0; j < 4; ++j)
#pragma unroll
        for (int t = 0; t < 7; ++t) {
            u32x2 w;
            w[0] = pack2(acc[t][j][0] * sc, acc[t][j][1] * sc);
            w[1] = pack2(acc[t][j][2] * sc, acc[t][j][3] * sc);
            *reinterpret_cast<u32x2*>(op + j * 16 * DM + 16 * t) = w;
        }
}

constexpr int KROW = 264;
constexpr int VROW = 288;
constexpr int ATTN_LDS = (64 * KROW + 64 * VROW) * 2;
__device__ __forceinline__ void attnB_body(const u16* __restrict__ Q, const u16* __restrict__ K,
                                           const u16* __restrict__ V, u16* __restrict__ O, u16* smem, int qb, int h,
                                           int ws) {
    u16* Ks = smem;
    u16* Vs = smem + 64 * KROW;
    const int tid = tid_of(ws);
    const int lane = tid & 63, wid = ws;
    const int q0 = qb * 128;
    const int r32 = lane & 31, hi = lane >> 5, g = lane >> 4, q4 = (lane & 15) >> 2, p4 = lane & 3;
    const int qrow = q0 + wid * 32 + r32;
    bf16x8 qf[16];
#pragma unroll
    for (int ks = 0; ks < 16; ++ks)
        qf[ks] = *reinterpret_cast<const bf16x8*>(Q + (long)qrow * HD + h * DH + ks * 16 + hi * 8);
    f32x16 o[8];
#pragma unroll
    for (int i = 0; i < 8; ++i) o[i] = f32x16{};
    float mrun = -INFINITY, lrun = 0.f;
    const float c2 = 0.0625f * 1.4426950408889634f;
    const int nt = (q0 + 128) / 64;
    u32x4 rk[8], rv[8];
    auto load_kv = [&](int t) {
#pragma unroll
        for (int i = 0; i < 8; ++i) {
            int c = tid + 256 * i, row = c >> 5, cc = c & 31;
            long off = (long)(t * 64 + row) * HD + h * DH + cc * 8;
            rk[i] = *reinterpret_cast<const u32x4*>(K + off);
            rv[i] = *reinterpret_cast<const u32x4*>(V + off);
        }
    };
    auto store_kv = [&]() {
#pragma unroll
        for (int i = 0; i < 8; ++i) {
            int c = tid + 256 * i, row = c >> 5, cc = c & 31;
            *reinterpret_cast<u32x4*>(Ks + row * KROW + cc * 8) = rk[i];
            *reinterpret_cast<u32x4*>(Vs + row * VROW + cc * 8) = rv[i];
        }
    };
    load_kv(0);
    for (int t = 0; t < nt; ++t) {
        __syncthreads();
        store_kv();
        __syncthreads();
        if (t + 1 < nt) load_kv(t + 1);
        const int kbase = t * 64;
        if (kbase <= q0 + wid * 32 + 31) {
            f32x16 s0 = f32x16{}, s1 = f32x16{};
#pragma unroll
            for (int ks = 0; ks < 16; ++ks) {
                bf16x8 k0 = *reinterpret_cast<const bf16x8*>(Ks + r32 * KROW + ks * 16 + hi * 8);
                bf16x8 k1 = *reinterpret_cast<const bf16x8*>(Ks + (32 + r32) * KROW + ks * 16 + hi * 8);
                s0 = MFMA32(k0, qf[ks], s0);
                s1 = MFMA32(k1, qf[ks], s1);
                if ((ks & 3) == 3) __builtin_amdgcn_sched_barrier(0);
            }
            if (kbase + 63 > q0 + wid * 32) {
#pragma unroll
                for (int r = 0; r < 16; ++r) {
                    int kk = kbase + crow(r, hi);
                    if (kk > qrow) s0[r] = -INFINITY;
                    if (kk + 32 > qrow) s1[r] = -INFINITY;
                }
            }
            float mx = -INFINITY;
#pragma unroll
            for (int r = 0; r < 16; ++r) mx = fmaxf(mx, fmaxf(s0[r], s1[r]));
            mx = fmaxf(mx, __shfl_xor(mx, 32));
            float mnew = fmaxf(mrun, mx);
            float alpha = __builtin_amdgcn_exp2f((mrun - mnew) * c2);
            float nm = -mnew * c2;
            float psum = 0.f;
#pragma unroll
            for (int r = 0; r < 16; ++r) {
                s0[r] = __builtin_amdgcn_exp2f(fmaf(s0[r], c2, nm));
                s1[r] = __builtin_amdgcn_exp2f(fmaf(s1[r], c2, nm));
                psum += s0[r] + s1[r];
            }
            psum += __shfl_xor(psum, 32);
            lrun = lrun * alpha + psum;
            mrun = mnew;
#pragma unroll
            for (int i = 0; i < 8; ++i)
#pragma unroll
                for (int r = 0; r < 16; ++r) o[i][r] *= alpha;
            bf16x8 pf[4];
#pragma unroll
            for (int s = 0; s < 2; ++s) {
                u32x4 a, b;
#pragma unroll
                for (int j = 0; j < 4; ++j) {
                    a[j] = pack2(s0[8 * s + 2 * j], s0[8 * s + 2 * j + 1]);
                    b[j] = pack2(s1[8 * s + 2 * j], s1[8 * s + 2 * j + 1]);
                }
                pf[s] = __builtin_bit_cast(bf16x8, a);
                pf[2 + s] = __builtin_bit_cast(bf16x8, b);
            }
#pragma unroll
            for (int dt = 0; dt < 8; ++dt) {
#pragma unroll
                for (int s = 0; s < 4; ++s) {
                    const u16* pV = Vs + (16 * s + 4 * hi + q4) * VROW + 32 * dt + 16 * (g & 1) + 4 * p4;
                    s16x4 lo = tr_read(pV);
                    s16x4 hi4 = tr_read(pV + 8 * VROW);
                    bf16x8 vf = bf16x8{lo[0], lo[1], lo[2], lo[3], hi4[0], hi4[1], hi4[2], hi4[3]};
                    o[dt] = MFMA32(vf, pf[s], o[dt]);
                }
                __builtin_amdgcn_sched_barrier(0);
            }
        }
    }
    float inv = 1.f / lrun;
#pragma unroll
    for (int dt = 0; dt < 8; ++dt)
#pragma unroll
        for (int rr = 0; rr < 4; ++rr) {
            u32x2 w;
            w[0] = pack2(o[dt][4 * rr] * inv, o[dt][4 * rr + 1] * inv);
            w[1] = pack2(o[dt][4 * rr + 2] * inv, o[dt][4 * rr + 3] * inv);
            __hip_atomic_store(reinterpret_cast<unsigned long long*>(O + (long)qrow * HD + h * DH + 32 * dt + 8 * rr + 4 * hi),
                               ((unsigned long long)w[1] << 32) | w[0], __ATOMIC_RELAXED, __HIP_MEMORY_SCOPE_AGENT);
        }
}

typedef unsigned long long u64;
__device__ __forceinline__ u64 ld_gran(const u64* p) {
    return __hip_atomic_load(p, __ATOMIC_RELAXED, __HIP_MEMORY_SCOPE_AGENT);
}
__device__ __forceinline__ void st_gran4(u16* p, unsigned lo, unsigned hi) {
    hi = min(hi, 0xfffffffeu);
    __hip_atomic_store(reinterpret_cast<u64*>(p), ((u64)hi << 32) | (u64)lo, __ATOMIC_RELAXED,
                       __HIP_MEMORY_SCOPE_AGENT);
}
__device__ __forceinline__ void st_gran8(__amdgpu_buffer_rsrc_t rs, unsigned voff, unsigned soff, bool act, unsigned lo,
                                         unsigned hi) {
    hi = min(hi, 0xfffffffeu);
    unsigned lo2 = (unsigned)__builtin_amdgcn_mov_dpp((int)lo, 0xF5, 0xf, 0xf, true);
    unsigned hi2 = (unsigned)__builtin_amdgcn_mov_dpp((int)hi, 0xF5, 0xf, 0xf, true);
    if (act) __builtin_amdgcn_raw_buffer_store_b128(u32x4{lo, hi, lo2, hi2}, rs, voff, soff, 16);
}
constexpr int NA_BLK = 16, NB_BLK = 16, NC_BLK = 28, NREC = NA_BLK + NB_BLK + NC_BLK;
constexpr int MROW = 456;
constexpr int G1_SLOT = 16 * 8 * 40;
constexpr int G2_SLOT = 8 * 448;
constexpr int G3_SLOT = 16 * 32 * 40;
constexpr int XIMG = 28 * 512;
constexpr unsigned SPIN_CAP = 400000u;
#define SLP_A 45
#define SLP_C1 2

#define WTS(slot)
#define KT0
#define KT1(k)
#define TS(slot)
struct RecParams {
    const u16* x;
    const u16* mem0;
    const u16* AmatT;
    const u16* CmatT;
    u16* G1;
    u16* G2;
    u16* G3;
    u16* out;
};

__device__ __forceinline__ float red16_max(float x) {
    asm volatile(
        "s_nop 1\n\tv_max_f32_dpp %0, %0, %0 quad_perm:[1,0,3,2] row_mask:0xf bank_mask:0xf\n\t"
        "s_nop 1\n\tv_max_f32_dpp %0, %0, %0 quad_perm:[2,3,0,1] row_mask:0xf bank_mask:0xf\n\t"
        "s_nop 1\n\tv_max_f32_dpp %0, %0, %0 row_half_mirror row_mask:0xf bank_mask:0xf\n\t"
        "s_nop 1\n\tv_max_f32_dpp %0, %0, %0 row_mirror row_mask:0xf bank_mask:0xf\n\t"
        "s_nop 1"
        : "+v"(x));
    return x;
}
__device__ __forceinline__ float red16_sum(float x) {
    asm volatile(
        "s_nop 1\n\tv_add_f32_dpp %0, %0, %0 quad_perm:[1,0,3,2] row_mask:0xf bank_mask:0xf\n\t"
        "s_nop 1\n\tv_add_f32_dpp %0, %0, %0 quad_perm:[2,3,0,1] row_mask:0xf bank_mask:0xf\n\t"
        "s_nop 1\n\tv_add_f32_dpp %0, %0, %0 row_half_mirror row_mask:0xf bank_mask:0xf\n\t"
        "s_nop 1\n\tv_add_f32_dpp %0, %0, %0 row_mirror row_mask:0xf bank_mask:0xf\n\t"
        "s_nop 1"
        : "+v"(x));
    return x;
}
__device__ __forceinline__ u32x4 ld16_sc1(__amdgpu_buffer_rsrc_t rs, unsigned voff, unsigned soff) {
    return __builtin_amdgcn_raw_buffer_load_b128(rs, voff, soff, 16);
}
__device__ __forceinline__ unsigned umax3(unsigned a, unsigned b, unsigned c) { return max(max(a, b), c); }

__device__ __forceinline__ void mfma_a0(f32x4& d, const bf16x8& w, const u32x4& m) {
    asm volatile("v_mfma_f32_16x16x32_bf16 %0, %1, %2, 0" : "=&v"(d) : "a"(w), "v"(m));
}
__device__ __forceinline__ void mfma_a(f32x4& d, const bf16x8& w, const u32x4& m) {
    asm volatile("v_mfma_f32_16x16x32_bf16 %0, %1, %2, %0" : "+v"(d) : "a"(w), "v"(m));
}
__device__ __forceinline__ void mfma_v0(f32x4& d, const bf16x8& w, const u32x4& m) {
    asm volatile("v_mfma_f32_16x16x32_bf16 %0, %1, %2, 0" : "=&v"(d) : "v"(w), "v"(m));
}
__device__ __forceinline__ void mfma_v(f32x4& d, const bf16x8& w, const u32x4& m) {
    asm volatile("v_mfma_f32_16x16x32_bf16 %0, %1, %2, %0" : "+v"(d) : "v"(w), "v"(m));
}
__device__ __forceinline__ void lds_rd16(bf16x8& d, unsigned addr, int off) {
    asm volatile("ds_read_b128 %0, %1 offset:%2" : "=v"(d) : "v"(addr), "n"(off));
}
__device__ __forceinline__ void lds_rd16u(u32x4& d, unsigned addr) {
    asm volatile("ds_read_b128 %0, %1" : "=v"(d) : "v"(addr));
}
template <int N>
__device__ __forceinline__ void wait_lgkm() {
    asm volatile("s_waitcnt lgkmcnt(%0)" ::"n"(N) : "memory");
}
constexpr int sch_n(int q) { return q <= 8 ? 7 + 2 * (q + 1) : (q <= 11 ? 25 + (q - 8) : 28); }
constexpr int sch_m(int i) { return i < 5 ? 2 + i : 7 + 2 * (i - 5); }
constexpr int sch_w(int i) { return i < 2 ? i : (i - 2 <= 8 ? 8 + 2 * (i - 2) : 25 + (i - 11)); }

__device__ __forceinline__ void dma16(const void* gbase, unsigned voff, unsigned lds) {
    unsigned keep;
    asm volatile("s_mov_b32 %0, m0\n\ts_mov_b32 m0, %3\n\ts_nop 0\n\tglobal_load_lds_dwordx4 %1, %2\n\ts_mov_b32 m0, %0"
                 : "=&s"(keep)
                 : "v"(voff), "s"(gbase), "s"(lds)
                 : "memory");
}
__device__ __forceinline__ void dma_x(const u16* x, int s, unsigned xvoff, unsigned lds, int wid) {
    const char* gb = reinterpret_cast<const char*>(x) + (long)s * (SEG * DM * 2) + wid * 7168;
#pragma unroll
    for (int j = 0; j < 7; ++j) dma16(gb + 1024 * j, xvoff, lds + (unsigned)(wid * 7 + j) * 1024u);
}

__device__ __forceinline__ void lds_rd16s(u32x4& d, unsigned addr, unsigned soff) {
    unsigned t;
    asm volatile("v_add_u32 %1, %3, %2\n\tds_read_b128 %0, %1" : "=v"(d), "=&v"(t) : "v"(addr), "s"(soff));
}
__device__ __forceinline__ void tpass(const bf16x8 (&W)[6][14], unsigned wl_addr, unsigned fa, const unsigned (&so)[14],
                                      u32x4 (&mf)[14], bf16x8 (&w6)[3], f32x4 (&acc)[7]) {
#pragma unroll
    for (int q = 0; q < 5; ++q) lds_rd16s(mf[q], fa, so[q]);
#pragma unroll
    for (int q = 0; q < 14; ++q) {
        if (q + 5 < 14) lds_rd16s(mf[q + 5], fa, so[q + 5]);
        if (q + 2 < 14) lds_rd16(w6[(q + 2) % 3], wl_addr, (q + 2) * 1024);
        switch (q) {
#define WM(Q) case Q: wait_lgkm<sch_n(Q) - sch_m(Q) - 1>(); break;
            WM(0) WM(1) WM(2) WM(3) WM(4) WM(5) WM(6) WM(7) WM(8) WM(9) WM(10) WM(11) WM(12) WM(13)
#undef WM
        }
#pragma unroll
        for (int t = 0; t < 4; ++t) {
            if (q == 0) mfma_a0(acc[t], W[t][q], mf[q]);
            else mfma_a(acc[t], W[t][q], mf[q]);
        }
#pragma unroll
        for (int t = 4; t < 6; ++t) {
            if (t == 4 && q < 6) {
                if (q == 0) mfma_a0(acc[t], W[t][q], mf[q]);
                else mfma_a(acc[t], W[t][q], mf[q]);
            } else {
                if (q == 0) mfma_v0(acc[t], W[t][q], mf[q]);
                else mfma_v(acc[t], W[t][q], mf[q]);
            }
        }
        switch (q) {
#define WW(Q) case Q: wait_lgkm<sch_n(Q) - sch_w(Q) - 1>(); break;
            WW(0) WW(1) WW(2) WW(3) WW(4) WW(5) WW(6) WW(7) WW(8) WW(9) WW(10) WW(11) WW(12) WW(13)
#undef WW
        }
        if (q == 0) mfma_v0(acc[6], w6[0], mf[q]);
        else mfma_v(acc[6], w6[q % 3], mf[q]);
    }
    asm volatile("s_nop 7\n\ts_nop 1"
                 : "+v"(acc[0]), "+v"(acc[1]), "+v"(acc[2]), "+v"(acc[3]), "+v"(acc[4]), "+v"(acc[5]), "+v"(acc[6]));
}
__device__ __forceinline__ void tpack(const f32x4 (&acc)[7], bool q3ok, u32x4 (&bv)[4]) {
#pragma unroll
    for (int pr = 0; pr < 3; ++pr)
        bv[pr] = u32x4{pack2(acc[2 * pr][0], acc[2 * pr][1]), pack2(acc[2 * pr][2], acc[2 * pr][3]),
                       pack2(acc[2 * pr + 1][0], acc[2 * pr + 1][1]), pack2(acc[2 * pr + 1][2], acc[2 * pr + 1][3])};
    unsigned t0 = pack2(acc[6][0], acc[6][1]), t1 = pack2(acc[6][2], acc[6][3]);
    bv[3] = q3ok ? u32x4{t0, t1, 0u, 0u} : u32x4{0u, 0u, t0, t1};
}
#define BF8(x) __builtin_bit_cast(bf16x8, x)
__device__ __forceinline__ void schain1(f32x4& d0, const u32x4 (&a0)[4], const u32x4 (&b)[4]) {
    asm volatile(
        "s_nop 1\n\tv_mfma_f32_16x16x32_bf16 %0, %1, %5, 0\n\tv_mfma_f32_16x16x32_bf16 %0, %2, %6, %0\n\t"
        "v_mfma_f32_16x16x32_bf16 %0, %3, %7, %0\n\tv_mfma_f32_16x16x32_bf16 %0, %4, %8, %0\n\ts_nop 7"
        : "=&v"(d0)
        : "v"(a0[0]), "v"(a0[1]), "v"(a0[2]), "v"(a0[3]), "v"(b[0]), "v"(b[1]), "v"(b[2]), "v"(b[3]));
}
__device__ __forceinline__ void schain2(f32x4& d0, f32x4& d1, const u32x4 (&a0)[4], const u32x4 (&a1)[4],
                                        const u32x4 (&b0)[4], const u32x4 (&b1)[4]) {
    asm volatile(
        "s_nop 1\n\tv_mfma_f32_16x16x32_bf16 %0, %2, %10, 0\n\tv_mfma_f32_16x16x32_bf16 %1, %6, %14, 0\n\t"
        "v_mfma_f32_16x16x32_bf16 %0, %3, %11, %0\n\tv_mfma_f32_16x16x32_bf16 %1, %7, %15, %1\n\t"
        "v_mfma_f32_16x16x32_bf16 %0, %4, %12, %0\n\tv_mfma_f32_16x16x32_bf16 %1, %8, %16, %1\n\t"
        "v_mfma_f32_16x16x32_bf16 %0, %5, %13, %0\n\tv_mfma_f32_16x16x32_bf16 %1, %9, %17, %1\n\ts_nop 7"
        : "=&v"(d0), "=&v"(d1)
        : "v"(a0[0]), "v"(a0[1]), "v"(a0[2]), "v"(a0[3]), "v"(a1[0]), "v"(a1[1]), "v"(a1[2]), "v"(a1[3]), "v"(b0[0]),
          "v"(b0[1]), "v"(b0[2]), "v"(b0[3]), "v"(b1[0]), "v"(b1[1]), "v"(b1[2]), "v"(b1[3]));
}
__device__ __forceinline__ void schain3(f32x4& d0, f32x4& d1, f32x4& d2, const u32x4 (&a0)[4], const u32x4 (&a1)[4],
                                        const u32x4 (&a2)[14], const u32x4 (&b)[4]) {
    asm volatile(
        "s_nop 1\n\tv_mfma_f32_16x16x32_bf16 %0, %3, %15, 0\n\tv_mfma_f32_16x16x32_bf16 %1, %7, %15, 0\n\t"
        "v_mfma_f32_16x16x32_bf16 %2, %11, %15, 0\n\t"
        "v_mfma_f32_16x16x32_bf16 %0, %4, %16, %0\n\tv_mfma_f32_16x16x32_bf16 %1, %8, %16, %1\n\t"
        "v_mfma_f32_16x16x32_bf16 %2, %12, %16, %2\n\t"
        "v_mfma_f32_16x16x32_bf16 %0, %5, %17, %0\n\tv_mfma_f32_16x16x32_bf16 %1, %9, %17, %1\n\t"
        "v_mfma_f32_16x16x32_bf16 %2, %13, %17, %2\n\t"
        "v_mfma_f32_16x16x32_bf16 %0, %6, %18, %0\n\tv_mfma_f32_16x16x32_bf16 %1, %10, %18, %1\n\t"
        "v_mfma_f32_16x16x32_bf16 %2, %14, %18, %2\n\ts_nop 7"
        : "=&v"(d0), "=&v"(d1), "=&v"(d2)
        : "v"(a0[0]), "v"(a0[1]), "v"(a0[2]), "v"(a0[3]), "v"(a1[0]), "v"(a1[1]), "v"(a1[2]), "v"(a1[3]), "v"(a2[0]),
          "v"(a2[1]), "v"(a2[2]), "v"(a2[3]), "v"(b[0]), "v"(b[1]), "v"(b[2]), "v"(b[3]));
}

constexpr int AL_W = 0, AL_X = 4 * 14 * 512, AL_S = AL_X + 2 * XIMG;
constexpr int SROW = 44;
constexpr int AL_MEM = AL_S + 4 * 320 * 2;
constexpr int RECA_LDS = (AL_S + 4 * 32 * SROW * 2) * 2;

template <bool ISB>
__device__ __forceinline__ void rec_A_body(const RecParams& p, u16* smem, int h, int wid) {
    float* Sc = reinterpret_cast<float*>(smem + AL_S);
    const int tid = tid_of(wid), lane = tid & 63;
    const int c16 = lane & 15, g = lane >> 4;
    const unsigned xl_base = (unsigned)(uintptr_t)(LAS u16*)(smem + AL_X);
    const unsigned xvoff = (unsigned)lane * 16u;
    dma_x(p.x, 0, xvoff, xl_base, wid);
    dma_x(p.x, 1, xvoff, xl_base + XIMG * 2, wid);
    int kq[14];
#pragma unroll
    for (int q = 0; q < 14; ++q) {
        if (q < 3) kq[q] = 3 * wid + q;
        else if (q == 3) kq[q] = 12 + (wid >> 1);
        else if (q < 13) kq[q] = (q - 4 < 3 * wid) ? (q - 4) : (q - 1);
        else kq[q] = 13 - (wid >> 1);
    }
    bf16x8 W[6][14];
    u16* WL = smem + AL_W + (wid * 14 * 64 + lane) * 8;
#pragma unroll
    for (int t = 0; t < 7; ++t) {
        const int ksp = (t < 6) ? (3 * wid + (t >> 1)) : (12 + (wid >> 1));
        const int half = (t < 6) ? (t & 1) : (wid & 1);
        const int n = 32 * ksp + 8 * (c16 >> 2) + 4 * half + (c16 & 3);
        const u16* ap = p.AmatT + ((long)h * DM + n) * DM + 8 * g;
#pragma unroll
        for (int q = 0; q < 14; ++q) {
            bf16x8 w = *reinterpret_cast<const bf16x8*>(ap + 32 * kq[q]);
            if (t < 6)
                W[t][q] = w;
            else
                *reinterpret_cast<bf16x8*>(WL + q * 512) = w;
        }
    }
    const bool q3ok = (wid & 1) == 0;
    const bool sact = c16 < 10;
    const bool sst = sact && !(c16 & 1);
    const unsigned wl_addr = (unsigned)(uintptr_t)(LAS u16*)WL;
    __amdgpu_buffer_rsrc_t rs2 = __builtin_amdgcn_make_buffer_rsrc(p.G2, 0, 64 * G2_SLOT * 2, 0x00020000);
    const float L2E = 1.4426950408889634f;
    bool dead = false;
    asm volatile("s_waitcnt vmcnt(0)" ::: "memory");
    __syncthreads();
    if constexpr (!ISB) {
        const int srow = 4 * (wid & 1) + g;
        const float* sread = Sc + srow * 40 + (sact ? 4 * c16 : 0);
        float* swr = Sc + wid * 320 + (c16 & 7) * 40 + 8 * g;
        float* swr_mm = Sc + wid * 320 + (c16 & 7) * 40 + 32 + 4 * (g & 1);
        u16* memL = smem + AL_MEM;
        const int ch1 = (tid < 192) ? (256 + tid) : tid;
        u16* mw0 = memL + ((tid >> 1) & 7) * MROW + 16 * (tid >> 4) + 8 * (tid & 1);
        u16* mw1 = memL + ((ch1 >> 1) & 7) * MROW + 16 * (ch1 >> 4) + 8 * (ch1 & 1);
        const unsigned ml_addr = (unsigned)(uintptr_t)(LAS u16*)(memL + (c16 & 7) * MROW + 8 * g);
        unsigned so[14];
#pragma unroll
        for (int q = 0; q < 14; ++q) so[q] = 64 * kq[q];
        __amdgpu_buffer_rsrc_t rs1 = __builtin_amdgcn_make_buffer_rsrc(p.G1, 0, 63 * G1_SLOT * 2, 0x00020000);
        const unsigned g1off = (h * 320 + srow * 40 + 4 * c16) * 2;
        const u16* xrd = smem + AL_X + lane * 8;
        u32x4 xf[2][4];
        auto read_xf = [&](int s) {
#pragma unroll
            for (int tl = 0; tl < 2; ++tl)
#pragma unroll
                for (int q = 0; q < 4; ++q)
                    xf[tl][q] = *reinterpret_cast<const u32x4*>(xrd + (s & 1) * XIMG + (tl * 14 + kq[q]) * 512);
        };
        read_xf(0);
        for (int s = 0; s < NSEG - 1; ++s) {
            u32x4 mf[14];
            bf16x8 w6[3];
            wait_lgkm<0>();
            lds_rd16(w6[0], wl_addr, 0);
            lds_rd16(w6[1], wl_addr, 1024);
            {
                u32x4 a, b;
                for (unsigned spin = 0;; ++spin) {
                    asm volatile("" ::: "memory");
                    a = ld16_sc1(rs2, tid * 16, (unsigned)(s * G2_SLOT * 2));
                    b = ld16_sc1(rs2, ch1 * 16, (unsigned)(s * G2_SLOT * 2));
                    unsigned mx = max(umax3(a[1], a[3], b[1]), b[3]);
                    if (__all(mx != 0xffffffffu) || dead) break;
                    if (spin > SPIN_CAP) dead = true;
                }
                *reinterpret_cast<u32x4*>(mw0) = a;
                *reinterpret_cast<u32x4*>(mw1) = b;
                __syncthreads();
            }
            TS(1);
            f32x4 acc[7];
            tpass(W, wl_addr, ml_addr, so, mf, w6, acc);
            u32x4 bv[4];
            tpack(acc, q3ok, bv);
            f32x4 sm, s0, s1;
            schain3(s0, s1, sm, xf[0], xf[1], mf, bv);
            if (c16 < 8) {
                *reinterpret_cast<f32x4*>(swr) = s0;
                *reinterpret_cast<f32x4*>(swr + 4) = s1;
                if (g < 2) *reinterpret_cast<f32x4*>(swr_mm) = sm;
            }
            TS(2);
            __syncthreads();
            if (wid < 2) {
                f32x4 x0 = *reinterpret_cast<const f32x4*>(sread);
                f32x4 x1 = *reinterpret_cast<const f32x4*>(sread + 320);
                f32x4 x2 = *reinterpret_cast<const f32x4*>(sread + 640);
                f32x4 x3 = *reinterpret_cast<const f32x4*>(sread + 960);
                f32x4 a = (x0 + x1) + (x2 + x3);
                float mx = sact ? __builtin_fmaxf(__builtin_fmaxf(a[0], a[1]), __builtin_fmaxf(a[2], a[3])) : -INFINITY;
                mx = red16_max(mx);
                float nm = -mx * L2E;
                float e0 = __builtin_amdgcn_exp2f(__builtin_fmaf(a[0], L2E, nm));
                float e1 = __builtin_amdgcn_exp2f(__builtin_fmaf(a[1], L2E, nm));
                float e2 = __builtin_amdgcn_exp2f(__builtin_fmaf(a[2], L2E, nm));
                float e3 = __builtin_amdgcn_exp2f(__builtin_fmaf(a[3], L2E, nm));
                float sum = sact ? ((e0 + e1) + (e2 + e3)) : 0.f;
                sum = red16_sum(sum);
                float inv = __builtin_amdgcn_rcpf(sum);
                st_gran8(rs1, g1off, (unsigned)(s * G1_SLOT * 2), sst, pack2(e0 * inv, e1 * inv), pack2(e2 * inv, e3 * inv));
            }
            TS(3);
            asm volatile("" ::: "memory");
            read_xf(s + 1);
            if (s + 2 < NSEG - 1) dma_x(p.x, s + 2, xvoff, xl_base + (s & 1) * (XIMG * 2), wid);
            __builtin_amdgcn_s_sleep(SLP_A);
        }
    } else {
        float* swr = Sc + (wid * 32 + c16) * SROW + 8 * g;
        float* swr_mm = Sc + (wid * 32 + c16) * SROW + 32 + 4 * (g & 1);
        const float* sread = Sc + (wid * 8 + g) * SROW + (sact ? 4 * c16 : 0);
        const int key0 = (c16 < 8) ? 16 * (c16 & 1) + 4 * (c16 >> 1) : -4;
        __amdgpu_buffer_rsrc_t rs3 = __builtin_amdgcn_make_buffer_rsrc(p.G3, 0, 64 * G3_SLOT * 2, 0x00020000);
        const unsigned g3off = ((h * 32 + wid * 8 + g) * 40 + 4 * c16) * 2;
        unsigned so[14];
#pragma unroll
        for (int q = 0; q < 14; ++q) so[q] = 1024 * kq[q];
        unsigned mvoff[4];
#pragma unroll
        for (int q = 0; q < 4; ++q) mvoff[q] = ((2 * kq[q] + (g >> 1)) * 128 + (c16 & 7) * 16 + (g & 1) * 8) * 2;
        for (int s = 0; s < NSEG; ++s) {
            const unsigned xa = xl_base + (s & 1) * (XIMG * 2) + lane * 16;
            u32x4 bv0[4], bv1[4];
            {
                u32x4 mf[14];
                bf16x8 w6[3];
                f32x4 acc[7];
                wait_lgkm<0>();
                lds_rd16(w6[0], wl_addr, 0);
                lds_rd16(w6[1], wl_addr, 1024);
                tpass(W, wl_addr, xa, so, mf, w6, acc);
                tpack(acc, q3ok, bv0);
                const u32x4 m4[4] = {mf[0], mf[1], mf[2], mf[3]};
                f32x4 s00;
                schain1(s00, m4, bv0);
                __syncthreads();
                *reinterpret_cast<f32x4*>(swr) = s00;
            }
            {
                u32x4 mf[14];
                bf16x8 w6[3];
                f32x4 acc[7];
                wait_lgkm<0>();
                lds_rd16(w6[0], wl_addr, 0);
                lds_rd16(w6[1], wl_addr, 1024);
                tpass(W, wl_addr, xa + 14 * 1024, so, mf, w6, acc);
                tpack(acc, q3ok, bv1);
                const u16* xrd = smem + AL_X + (s & 1) * XIMG + lane * 8;
                const u32x4 m4[4] = {mf[0], mf[1], mf[2], mf[3]};
                u32x4 x4[4];
#pragma unroll
                for (int pr = 0; pr < 4; ++pr) x4[pr] = *reinterpret_cast<const u32x4*>(xrd + kq[pr] * 512);
                f32x4 s01, s11;
                schain2(s01, s11, x4, m4, bv1, bv1);
                *reinterpret_cast<f32x4*>(swr + 16 * SROW) = s01;
                *reinterpret_cast<f32x4*>(swr + 16 * SROW + 4) = s11;
            }
            u32x4 mm[4];
            for (unsigned spin = 0;; ++spin) {
                asm volatile("" ::: "memory");
                unsigned mx = 0;
#pragma unroll
                for (int q = 0; q < 4; ++q) mm[q] = ld16_sc1(rs2, mvoff[q], (unsigned)(s * G2_SLOT * 2));
#pragma unroll
                for (int q = 0; q < 4; ++q) mx = umax3(mx, mm[q][1], mm[q][3]);
                if (__all(mx != 0xffffffffu) || dead) break;
                if (spin > SPIN_CAP) dead = true;
                __builtin_amdgcn_s_sleep(8);
            }
            TS(1);
            {
                f32x4 sm0, sm1;
                schain2(sm0, sm1, mm, mm, bv0, bv1);
                if (g < 2) {
                    *reinterpret_cast<f32x4*>(swr_mm) = sm0;
                    *reinterpret_cast<f32x4*>(swr_mm + 16 * SROW) = sm1;
                }
            }
            asm volatile("s_waitcnt vmcnt(0)" ::: "memory");
            __syncthreads();
            TS(2);
            if (s + 2 < NSEG) dma_x(p.x, s + 2, xvoff, xl_base + (s & 1) * (XIMG * 2), wid);
#pragma unroll
            for (int i = 0; i < 2; ++i) {
                const float* sr = sread + 4 * i * SROW;
                f32x4 x0 = *reinterpret_cast<const f32x4*>(sr);
                f32x4 x1 = *reinterpret_cast<const f32x4*>(sr + 32 * SROW);
                f32x4 x2 = *reinterpret_cast<const f32x4*>(sr + 64 * SROW);
                f32x4 x3 = *reinterpret_cast<const f32x4*>(sr + 96 * SROW);
                f32x4 a = (x0 + x1) + (x2 + x3);
                const int row = wid * 8 + 4 * i + g;
#pragma unroll
                for (int r = 0; r < 4; ++r)
                    if (!sact || key0 + r > row) a[r] = -INFINITY;
                float mx = __builtin_fmaxf(__builtin_fmaxf(a[0], a[1]), __builtin_fmaxf(a[2], a[3]));
                mx = red16_max(mx);
                float nm = -mx * L2E;
                float e0 = __builtin_amdgcn_exp2f(__builtin_fmaf(a[0], L2E, nm));
                float e1 = __builtin_amdgcn_exp2f(__builtin_fmaf(a[1], L2E, nm));
                float e2 = __builtin_amdgcn_exp2f(__builtin_fmaf(a[2], L2E, nm));
                float e3 = __builtin_amdgcn_exp2f(__builtin_fmaf(a[3], L2E, nm));
                float sum = red16_sum((e0 + e1) + (e2 + e3));
                float inv = __builtin_amdgcn_rcpf(sum);
                st_gran8(rs3, g3off + i * (4 * 40 * 2), (unsigned)(s * G3_SLOT * 2), sst, pack2(e0 * inv, e1 * inv),
                         pack2(e2 * inv, e3 * inv));
            }
            TS(3);
        }
    }
}

__device__ __forceinline__ void mfma_b0(f32x4& d, const u32x4& a, const bf16x8& w) {
    asm volatile("v_mfma_f32_16x16x32_bf16 %0, %1, %2, 0" : "=&v"(d) : "v"(a), "a"(w));
}
__device__ __forceinline__ void mfma_b(f32x4& d, const u32x4& a, const bf16x8& w) {
    asm volatile("v_mfma_f32_16x16x32_bf16 %0, %1, %2, %0" : "+v"(d) : "v"(a), "a"(w));
}
__device__ __forceinline__ void lds_rd16o(u32x4& d, unsigned addr, int off) {
    asm volatile("ds_read_b128 %0, %1 offset:%2" : "=v"(d) : "v"(addr), "n"(off));
}
__device__ __forceinline__ void chain5(f32x4& d, const u32x4& a0, const u32x4& a1, const u32x4& a2, const u32x4& a3,
                                       const u32x4& a4, const u32x4 (&b)[4], const u32x4& b4) {
    asm volatile(
        "s_nop 1\n\tv_mfma_f32_16x16x32_bf16 %0, %1, %6, 0\n\tv_mfma_f32_16x16x32_bf16 %0, %2, %7, %0\n\t"
        "v_mfma_f32_16x16x32_bf16 %0, %3, %8, %0\n\tv_mfma_f32_16x16x32_bf16 %0, %4, %9, %0\n\t"
        "v_mfma_f32_16x16x32_bf16 %0, %5, %10, %0\n\ts_nop 7"
        : "=&v"(d)
        : "v"(a0), "v"(a1), "v"(a2), "v"(a3), "v"(a4), "v"(b[0]), "v"(b[1]), "v"(b[2]), "v"(b[3]), "v"(b4));
}
constexpr int cmin(int a, int b) { return a < b ? a : b; }
#define SW14(F) switch (ks) { F(0) F(1) F(2) F(3) F(4) F(5) F(6) F(7) F(8) F(9) F(10) F(11) F(12) F(13) }

constexpr int CL_X = 0, CL_MEM = 2 * XIMG, CL_RED = CL_MEM + 8 * MROW, CL_ORED = CL_RED + 4 * 128 * 2;
constexpr int RECC_LDS = (CL_ORED + 4 * 512 * 2) * 2;

__device__ __forceinline__ void rec_C_body(const RecParams& p, u16* smem, int c, int wid) {
    u16* memL = smem + CL_MEM;
    float* red = reinterpret_cast<float*>(smem + CL_RED);
    float* ored = reinterpret_cast<float*>(smem + CL_ORED);
    const int tid = tid_of(wid), lane = tid & 63;
    const int c16 = lane & 15, g = lane >> 4;
    const int ch1 = (tid < 192) ? (256 + tid) : tid;
    if (tid < 32) {
        u32x2 m0 = *reinterpret_cast<const u32x2*>(p.mem0 + (tid >> 2) * DM + 16 * c + 4 * (tid & 3));
        st_gran4(p.G2 + 128 * c + 4 * tid, m0[0], m0[1]);
    }
    const unsigned xl_base = (unsigned)(uintptr_t)(LAS u16*)(smem + CL_X);
    const unsigned xvoff = (unsigned)lane * 16u;
    dma_x(p.x, 0, xvoff, xl_base, wid);
    dma_x(p.x, 1, xvoff, xl_base + XIMG * 2, wid);
    u16* mw0 = memL + ((tid >> 1) & 7) * MROW + 16 * (tid >> 4) + 8 * (tid & 1);
    u16* mw1 = memL + ((ch1 >> 1) & 7) * MROW + 16 * (ch1 >> 4) + 8 * (ch1 & 1);
    const int col = c * 16 + c16;
    bf16x8 W[4][14];
#pragma unroll
    for (int hh = 0; hh < 4; ++hh) {
        const u16* cp = p.CmatT + ((long)(wid * 4 + hh) * DM + col) * DM + 8 * g;
#pragma unroll
        for (int ks = 0; ks < 14; ++ks) W[hh][ks] = *reinterpret_cast<const bf16x8*>(cp + 32 * ks);
    }
    u32x4 Dc[4], Dn[4], Uc;
    auto calc_D = [&](int s, auto&& hook) {
        const unsigned xa = xl_base + (s & 1) * (XIMG * 2) + lane * 16;
        u32x4 fa[14], fb[14];
        f32x4 d0[4], d1[4];
        wait_lgkm<0>();
#pragma unroll
        for (int ks = 0; ks < 3; ++ks) {
            lds_rd16o(fa[ks], xa, ks * 1024);
            lds_rd16o(fb[ks], xa, (14 + ks) * 1024);
        }
#pragma unroll
        for (int ks = 0; ks < 14; ++ks) {
            if (ks + 3 < 14) {
                lds_rd16o(fa[ks + 3], xa, (ks + 3) * 1024);
                lds_rd16o(fb[ks + 3], xa, (17 + ks) * 1024);
            }
#define WD(Q) case Q: wait_lgkm<2 * cmin(Q + 4, 14) - 2 * (Q + 1)>(); break;
            SW14(WD)
#undef WD
#pragma unroll
            for (int hh = 0; hh < 4; ++hh) {
                if (ks == 0) {
                    mfma_b0(d0[hh], fa[ks], W[hh][ks]);
                    mfma_b0(d1[hh], fb[ks], W[hh][ks]);
                } else {
                    mfma_b(d0[hh], fa[ks], W[hh][ks]);
                    mfma_b(d1[hh], fb[ks], W[hh][ks]);
                }
            }
            hook(ks);
        }
        asm volatile("s_nop 7"
                     : "+v"(d0[0]), "+v"(d0[1]), "+v"(d0[2]), "+v"(d0[3]), "+v"(d1[0]), "+v"(d1[1]), "+v"(d1[2]),
                       "+v"(d1[3]));
#pragma unroll
        for (int hh = 0; hh < 4; ++hh)
            Dn[hh] = u32x4{pack2(d0[hh][0], d0[hh][1]), pack2(d0[hh][2], d0[hh][3]), pack2(d1[hh][0], d1[hh][1]),
                           pack2(d1[hh][2], d1[hh][3])};
    };
    asm volatile("s_waitcnt vmcnt(0)" ::: "memory");
    __syncthreads();
    calc_D(0, [&](int) {});
    const int i8 = c16 & 7;
    const unsigned mfa = (unsigned)(uintptr_t)(LAS u16*)(memL + i8 * MROW + 8 * g);
    const unsigned pvoff = ((wid * 4) * 320 + i8 * 40 + 8 * g) * 2;
    const unsigned pvoffx = ((wid * 4 + g) * 320 + i8 * 40 + 32) * 2;
    const unsigned pxoff = ((wid * 4) * 1280 + c16 * 40 + 8 * g) * 2;
    const unsigned pxoffx = ((wid * 4 + g) * 1280 + c16 * 40 + 32) * 2;
    float* rwr = red + wid * 128 + (4 * (g & 1)) * 16 + c16;
    const float* rrd = red + ((lane >> 2) & 7) * 16 + 4 * (lane & 3);
    float* owr = ored + wid * 512 + (4 * g) * 16 + c16;
    const float* ord = ored + ((tid & 127) >> 2) * 16 + 4 * (tid & 3);
    u16* outp = p.out + (long)((tid & 127) >> 2) * 896 + 16 * c + 4 * (tid & 3);
    __amdgpu_buffer_rsrc_t rs1 = __builtin_amdgcn_make_buffer_rsrc(p.G1, 0, 63 * G1_SLOT * 2, 0x00020000);
    __amdgpu_buffer_rsrc_t rs2 = __builtin_amdgcn_make_buffer_rsrc(p.G2, 0, 64 * G2_SLOT * 2, 0x00020000);
    __amdgpu_buffer_rsrc_t rs3 = __builtin_amdgcn_make_buffer_rsrc(p.G3, 0, 64 * G3_SLOT * 2, 0x00020000);
    bool dead = false;
    u32x4 px[10], Up = u32x4{0u, 0u, 0u, 0u};
    Uc = Up;
#pragma unroll
    for (int hh = 0; hh < 4; ++hh) Dc[hh] = Up;
#pragma unroll
    for (int q = 0; q < 10; ++q) px[q] = Up;
    auto load_px1 = [&](int q, int s) {
        const int qt = q / 5, hh = q % 5;
        px[q] = ld16_sc1(rs3, (hh < 4 ? pxoff + hh * 2560 : pxoffx) + qt * 1280, (unsigned)(s * G3_SLOT * 2));
    };
    for (int s = 0;; ++s) {
        if (s < NSEG) {
            u32x4 a, b;
            for (unsigned spin = 0;; ++spin) {
                asm volatile("" ::: "memory");
                a = ld16_sc1(rs2, tid * 16, (unsigned)(s * G2_SLOT * 2));
                b = ld16_sc1(rs2, ch1 * 16, (unsigned)(s * G2_SLOT * 2));
                unsigned mx = max(umax3(a[1], a[3], b[1]), b[3]);
                if (__all(mx != 0xffffffffu) || dead) break;
                if (spin > SPIN_CAP) dead = true;
            }
            *reinterpret_cast<u32x4*>(mw0) = a;
            *reinterpret_cast<u32x4*>(mw1) = b;
        }
        __syncthreads();
        if (s < NSEG) {
            TS(1);
            f32x4 U[4];
            u32x4 mf[14];
            wait_lgkm<0>();
#pragma unroll
            for (int ks = 0; ks < 5; ++ks) lds_rd16o(mf[ks], mfa, ks * 64);
#pragma unroll
            for (int ks = 0; ks < 14; ++ks) {
                if (ks + 5 < 14) lds_rd16o(mf[ks + 5], mfa, (ks + 5) * 64);
#define WU(Q) case Q: wait_lgkm<cmin(Q + 6, 14) - (Q + 1)>(); break;
                SW14(WU)
#undef WU
#pragma unroll
                for (int hh = 0; hh < 4; ++hh) {
                    if (ks == 0) mfma_b0(U[hh], mf[ks], W[hh][ks]);
                    else mfma_b(U[hh], mf[ks], W[hh][ks]);
                }
            }
            asm volatile("s_nop 7" : "+v"(U[0]), "+v"(U[1]), "+v"(U[2]), "+v"(U[3]));
            unsigned pk[4][2];
#pragma unroll
            for (int hh = 0; hh < 4; ++hh) {
                pk[hh][0] = pack2(U[hh][0], U[hh][1]);
                pk[hh][1] = pack2(U[hh][2], U[hh][3]);
            }
            const bool g1 = g & 1, g2 = g & 2;
            unsigned e0 = g2 ? pk[2][0] : pk[0][0], e1 = g2 ? pk[2][1] : pk[0][1];
            unsigned o0 = g2 ? pk[3][0] : pk[1][0], o1 = g2 ? pk[3][1] : pk[1][1];
            unsigned m0 = g1 ? o0 : e0, m1 = g1 ? o1 : e1;
            unsigned t0 = g1 ? e0 : o0, t1 = g1 ? e1 : o1;
            unsigned r0 = (unsigned)__shfl_xor((int)t0, 16), r1 = (unsigned)__shfl_xor((int)t1, 16);
            Uc = g1 ? u32x4{r0, r1, m0, m1} : u32x4{m0, m1, r0, r1};
            TS(7);
        }
        if (s > 0) {
            for (unsigned spin = 0;; ++spin) {
                unsigned mx = 0;
#pragma unroll
                for (int q = 0; q < 10; ++q) mx = umax3(mx, px[q][1], px[q][3]);
                if (__all(mx != 0xffffffffu) || dead) break;
                if (spin > SPIN_CAP) dead = true;
                __builtin_amdgcn_s_sleep(4);
                asm volatile("" ::: "memory");
#pragma unroll
                for (int q = 0; q < 10; ++q) load_px1(q, s - 1);
            }
#pragma unroll
            for (int qt = 0; qt < 2; ++qt) {
                f32x4 o;
                chain5(o, px[qt * 5], px[qt * 5 + 1], px[qt * 5 + 2], px[qt * 5 + 3], px[qt * 5 + 4], Dc, Up);
#pragma unroll
                for (int r = 0; r < 4; ++r) owr[qt * 256 + r * 16] = o[r];
            }
        }
        if (s < NSEG - 1) {
            u32x4 pv[5];
#pragma unroll
            for (int q = 0; q < 5; ++q) pv[q] = u32x4{0u, 0u, 0u, 0u};
            TS(6);
            __builtin_amdgcn_s_sleep(SLP_C1);
            for (unsigned spin = 0;; ++spin) {
                asm volatile("" ::: "memory");
                unsigned mx = 0;
                if (c16 < 8) {
#pragma unroll
                    for (int hh = 0; hh < 4; ++hh) pv[hh] = ld16_sc1(rs1, pvoff + hh * 640, (unsigned)(s * G1_SLOT * 2));
                    pv[4] = ld16_sc1(rs1, pvoffx, (unsigned)(s * G1_SLOT * 2));
#pragma unroll
                    for (int q = 0; q < 5; ++q) mx = umax3(mx, pv[q][1], pv[q][3]);
                }
                if (__all(mx != 0xffffffffu) || dead) break;
                if (spin > SPIN_CAP) dead = true;
            }
            TS(2);
            f32x4 o0;
            chain5(o0, pv[0], pv[1], pv[2], pv[3], pv[4], Dn, Uc);
            if (g < 2) {
#pragma unroll
                for (int r = 0; r < 4; ++r) rwr[r * 16] = o0[r];
            }
        }
        asm volatile("s_waitcnt vmcnt(0)" ::: "memory");
        __syncthreads();
        if (tid < 32 && s < NSEG - 1) {
            f32x4 x0 = *reinterpret_cast<const f32x4*>(rrd);
            f32x4 x1 = *reinterpret_cast<const f32x4*>(rrd + 128);
            f32x4 x2 = *reinterpret_cast<const f32x4*>(rrd + 256);
            f32x4 x3 = *reinterpret_cast<const f32x4*>(rrd + 384);
            f32x4 a = (x0 + x1) + (x2 + x3);
            st_gran8(rs2, (128 * c + 4 * (lane & 31)) * 2, (unsigned)((s + 1) * G2_SLOT * 2), !(lane & 1), pack2(a[0], a[1]),
                     pack2(a[2], a[3]));
        }
        if (tid >= 128 && s > 0) {
            f32x4 x0 = *reinterpret_cast<const f32x4*>(ord);
            f32x4 x1 = *reinterpret_cast<const f32x4*>(ord + 512);
            f32x4 x2 = *reinterpret_cast<const f32x4*>(ord + 1024);
            f32x4 x3 = *reinterpret_cast<const f32x4*>(ord + 1536);
            f32x4 a = (x0 + x1) + (x2 + x3);
            *reinterpret_cast<u32x2*>(outp + (long)(s - 1) * (SEG * 896)) = u32x2{pack2(a[0], a[1]), pack2(a[2], a[3])};
        }
        if (s == NSEG) break;
        TS(3);
        asm volatile("" ::: "memory");
#pragma unroll
        for (int hh = 0; hh < 4; ++hh) Dc[hh] = Dn[hh];
        Up = Uc;
        if (s + 1 < NSEG) {
            const bool more = s + 2 < NSEG;
            const char* gb = reinterpret_cast<const char*>(p.x) + (long)(s + 2) * (SEG * DM * 2) + wid * 7168;
            const unsigned xl = xl_base + (s & 1) * (XIMG * 2) + (unsigned)(wid * 7) * 1024u;
            calc_D(s + 1, [&](int ks) {
                if (ks < 7 && more) dma16(gb + 1024 * ks, xvoff, xl + (unsigned)ks * 1024u);
                if (ks >= 4) load_px1(ks - 4, s);
            });
        } else {
#pragma unroll
            for (int q = 0; q < 10; ++q) px[q] = u32x4{~0u, ~0u, ~0u, ~0u};
        }
        TS(5);
    }
}

template <int BM, int BN, bool NT, bool SC1>
__device__ __forceinline__ void gemm_tile(const u16* __restrict__ A, int lda, const u16* __restrict__ B, int ldb,
                                          u16* __restrict__ C, int ldc, int nk, u16* smem, int ws) {
    constexpr int AROW = 72, BROW = NT ? 72 : BN + 32;
    constexpr int ASZ = BM * AROW, STG = ASZ + (NT ? BN * 72 : 64 * BROW);
    constexpr int ACH = BM / 32, BCH = BN / 32, TM = BM / 64, TN = BN / 64;
    constexpr int BPR = BN / 8;
    constexpr int BRS = 256 / BPR;
    const int tid = tid_of(ws);
    const int lane = tid & 63, wid = ws;
    const int wm = wid >> 1, wn = wid & 1;
    const int r32 = lane & 31, hi = lane >> 5, g = lane >> 4, q4 = (lane & 15) >> 2, p4 = lane & 3;
    f32x16 acc[TM][TN];
#pragma unroll
    for (int i = 0; i < TM; ++i)
#pragma unroll
        for (int j = 0; j < TN; ++j) acc[i][j] = f32x16{};
    u32x4 ra0[ACH], rb0[BCH], ra1[ACH], rb1[BCH];
    const unsigned aoff = ((unsigned)(tid >> 3) * lda + (tid & 7) * 8) * 2u;
    const unsigned boff = NT ? ((unsigned)(tid >> 3) * ldb + (tid & 7) * 8) * 2u
                             : ((unsigned)(tid / BPR) * ldb + (tid % BPR) * 8) * 2u;
    const char* Ac = reinterpret_cast<const char*>(A);
    const char* Bc = reinterpret_cast<const char*>(B);
    u16* aw = smem + (tid >> 3) * AROW + (tid & 7) * 8;
    u16* bw = NT ? (smem + ASZ + (tid >> 3) * BROW + (tid & 7) * 8) : (smem + ASZ + (tid / BPR) * BROW + (tid % BPR) * 8);
    const u16* ard = smem + (wm * (BM / 2) + r32) * AROW + hi * 8;
    const u16* brd = NT ? (smem + ASZ + (wn * (BN / 2) + r32) * BROW + hi * 8)
                        : (smem + ASZ + (8 * (g >> 1) + q4) * BROW + wn * (BN / 2) + 16 * (g & 1) + 4 * p4);
    auto load = [&](u32x4(&ra)[ACH], u32x4(&rb)[BCH], int kt) {
#pragma unroll
        for (int i = 0; i < ACH; ++i)
            ra[i] = *reinterpret_cast<const u32x4*>(Ac + (aoff + (unsigned)(32 * i * lda + kt * 64) * 2u));
#pragma unroll
        for (int i = 0; i < BCH; ++i) {
            if (NT)
                rb[i] = *reinterpret_cast<const u32x4*>(Bc + (boff + (unsigned)(32 * i * ldb + kt * 64) * 2u));
            else
                rb[i] = *reinterpret_cast<const u32x4*>(Bc + (boff + (unsigned)((kt * 64 + BRS * i) * ldb) * 2u));
        }
    };
    auto store = [&](const u32x4(&ra)[ACH], const u32x4(&rb)[BCH], int st) {
#pragma unroll
        for (int i = 0; i < ACH; ++i) *reinterpret_cast<u32x4*>(aw + st * STG + 32 * i * AROW) = ra[i];
#pragma unroll
        for (int i = 0; i < BCH; ++i)
            *reinterpret_cast<u32x4*>(bw + st * STG + (NT ? 32 : BRS) * i * BROW) = rb[i];
    };
    auto compute = [&](int st, int ks) {
        bf16x8 af[TM], bfr[TN];
#pragma unroll
        for (int i = 0; i < TM; ++i)
            af[i] = *reinterpret_cast<const bf16x8*>(ard + st * STG + i * 32 * AROW + ks * 16);
#pragma unroll
        for (int j = 0; j < TN; ++j) {
            if (NT) {
                bfr[j] = *reinterpret_cast<const bf16x8*>(brd + st * STG + j * 32 * BROW + ks * 16);
            } else {
                const u16* pB = brd + st * STG + ks * 16 * BROW + j * 32;
                s16x4 lo = tr_read(pB);
                s16x4 hi4 = tr_read(pB + 4 * BROW);
                bfr[j] = bf16x8{lo[0], lo[1], lo[2], lo[3], hi4[0], hi4[1], hi4[2], hi4[3]};
            }
        }
#pragma unroll
        for (int i = 0; i < TM; ++i)
#pragma unroll
            for (int j = 0; j < TN; ++j) acc[i][j] = MFMA32(bfr[j], af[i], acc[i][j]);
    };
    load(ra0, rb0, 0);
    load(ra1, rb1, 1);
    __syncthreads();
    store(ra0, rb0, 0);
    load(ra0, rb0, 2);
    __syncthreads();
    for (int kt = 0; kt + 1 < nk; kt += 2) {
        compute(0, 0);
        compute(0, 1);
        store(ra1, rb1, 1);
        load(ra1, rb1, min(kt + 3, nk - 1));
        compute(0, 2);
        compute(0, 3);
        __syncthreads();
        compute(1, 0);
        compute(1, 1);
        store(ra0, rb0, 0);
        load(ra0, rb0, min(kt + 4, nk - 1));
        compute(1, 2);
        compute(1, 3);
        __syncthreads();
    }
    if (nk & 1) {
        compute(0, 0);
        compute(0, 1);
        compute(0, 2);
        compute(0, 3);
        __syncthreads();
    }
    constexpr int CROW = BN + 8;
#pragma unroll
    for (int i = 0; i < TM; ++i)
#pragma unroll
        for (int j = 0; j < TN; ++j)
#pragma unroll
            for (int rr = 0; rr < 4; ++rr) {
                u32x2 w;
                w[0] = pack2(acc[i][j][4 * rr], acc[i][j][4 * rr + 1]);
                w[1] = pack2(acc[i][j][4 * rr + 2], acc[i][j][4 * rr + 3]);
                *reinterpret_cast<u32x2*>(smem + (wm * (BM / 2) + 32 * i + r32) * CROW + wn * (BN / 2) + 32 * j +
                                          8 * rr + 4 * hi) = w;
            }
    __syncthreads();
    const int tid2 = tid_of(ws);
    const unsigned coff = ((unsigned)(tid2 / BPR) * ldc + (tid2 % BPR) * 8) * 2u;
    __amdgpu_buffer_rsrc_t rs = __builtin_amdgcn_make_buffer_rsrc(C, 0, 0x7fffffff, 0x00020000);
#pragma unroll
    for (int i = 0; i < BM * BN / 2048; ++i) {
        int row = tid2 / BPR + BRS * i, cc = tid2 % BPR;
        u32x4 v = *reinterpret_cast<const u32x4*>(smem + row * CROW + cc * 8);
        __builtin_amdgcn_raw_buffer_store_b128(v, rs, coff + (unsigned)(BRS * i * ldc) * 2u, 0, SC1 ? 16 : 0);
    }
}

struct MegaParams {
    const u16* B_x;
    const u16* B_ckv;
    const u16* B_wq;
    const u16* B_wuk;
    const u16* B_wuv;
    const u16* B_wo;
    u16* Qb;
    u16* Kb;
    u16* Vb;
    u16* OB;
    u16* out;
    unsigned* ctl;
};
constexpr int NWORK = 256 - NREC;
constexpr int CTL_QA = 0, CTL_KV = 1024, CTL_OC = 1280, CTL_WORDS = 1408;
constexpr unsigned WSPIN_CAP = 2000000u;
#define IDLE_SLP 32

__device__ __forceinline__ int next_ticket(unsigned* q, int* slot, int ws) {
    __syncthreads();
    if (tid_of(ws) == 0) *slot = (int)(__hip_atomic_fetch_add(q, 1u, __ATOMIC_RELAXED, __HIP_MEMORY_SCOPE_AGENT) + 1u);
    __syncthreads();
    return *slot;
}
__device__ __forceinline__ void publish_count(unsigned* c, int ws) {
    asm volatile("s_waitcnt vmcnt(0)" ::: "memory");
    __syncthreads();
    if (tid_of(ws) == 0) __hip_atomic_fetch_add(c, 1u, __ATOMIC_RELAXED, __HIP_MEMORY_SCOPE_AGENT);
}
__device__ __forceinline__ void acquire_agent() {
    __builtin_amdgcn_fence(__ATOMIC_ACQUIRE, "agent");
    asm volatile("s_waitcnt vmcnt(0)" ::: "memory");
}
__device__ __forceinline__ void wait_counts(unsigned* c, int n, unsigned target, int ws) {
    if (ws == 0) {
        const int lane = tid_of(0);
        for (unsigned spin = 0; spin < WSPIN_CAP; ++spin) {
            unsigned v = __hip_atomic_load(c + (lane < n ? lane : 0), __ATOMIC_RELAXED, __HIP_MEMORY_SCOPE_AGENT) + 1u;
            if (__all(v >= target)) break;
            __builtin_amdgcn_s_sleep(32);
        }
        acquire_agent();
    }
    __syncthreads();
}

__device__ __forceinline__ void worker_body(const MegaParams& m, u16* smem, int* slot, int ws) {
    unsigned* ctl = m.ctl;
    WTS(0);
    const int myx = (int)(__builtin_amdgcn_s_getreg((3 << 11) | 20) & 7u);
    for (int k = 0; k < 8; ++k) {
        const int x = (myx + k) & 7;
        for (;;) {
            int t = next_ticket(ctl + CTL_QA + 64 * x, slot, ws);
            if (t >= 128) break;
            KT0
            if (t >= 96) {
                const int idx = t - 96;
                const int qb = 15 - (idx >> 1), h = 2 * x + (idx & 1);
                wait_counts(ctl + CTL_KV + 16 * h, (qb >> 1) + 1, 6u, ws);
                KT1(7);
                attnB_body(m.Qb, m.Kb, m.Vb, m.OB, smem, qb, h, ws);
                publish_count(ctl + CTL_OC + qb, ws);
                KT1(2);
                continue;
            }
            const int rb = t / 12, r = t - rb * 12, mat = r >> 2, cb = 4 * x + (r & 3);
            const int lda = (mat < 2) ? DC : DM;
            const u16* A = ((mat < 2) ? m.B_ckv : m.B_x) + (long)rb * 256 * lda;
            const u16* B = ((mat == 0) ? m.B_wuk : (mat == 1 ? m.B_wuv : m.B_wq)) + cb * 128;
            u16* C = ((mat == 0) ? m.Kb : (mat == 1 ? m.Vb : m.Qb)) + (long)rb * 256 * HD + cb * 128;
            gemm_tile<256, 128, false, true>(A, lda, B, HD, C, HD, lda / 64, smem, ws);
            publish_count(ctl + CTL_KV + 16 * (cb >> 1) + rb, ws);
            KT1(lda == DC ? 1 : 4);
        }
    }
    WTS(1);
    if (blockIdx.x >= 144) {
        const int b = blockIdx.x, x = b & 7, otile = (b - 144) >> 3;
        const int orb = 2 * x + (otile >= 7 ? 1 : 0), cb = otile >= 7 ? otile - 7 : otile;
        wait_counts(ctl + CTL_OC + orb, 1, 16u, ws);
        KT0
        gemm_tile<128, 64, false, false>(m.OB + (long)orb * 128 * HD, HD, m.B_wo + cb * 64, DM,
                                         m.out + (long)orb * 128 * 896 + 448 + cb * 64, 896, HD / 64, smem, ws);
        KT1(9);
        WTS(2);
    }
    WTS(6);
    WTS(7);
}

constexpr int GEMM_LDS = 2 * (256 * 72 + 64 * 160) * 2;
constexpr int cmax(int a, int b) { return a > b ? a : b; }
constexpr int FUSED_LDS = cmax(cmax(ATTN_LDS, RECA_LDS), cmax(GEMM_LDS, RECC_LDS));
static_assert(AL_MEM * 2 + 8 * MROW * 2 <= RECA_LDS, "lds");

__global__ __launch_bounds__(256) void mega_kernel(RecParams p, MegaParams m) {
    __shared__ __attribute__((aligned(16))) u16 smem[FUSED_LDS / 2 + 8];
    const int b = blockIdx.x, ws = __builtin_amdgcn_readfirstlane(threadIdx.x >> 6);
    if (b < NA_BLK) {
        rec_A_body<false>(p, smem, b, ws);
    } else if (b < NA_BLK + NC_BLK) {
        rec_C_body(p, smem, b - NA_BLK, ws);
    } else if (b < NREC) {
        rec_A_body<true>(p, smem, b - NA_BLK - NC_BLK, ws);
    } else {
        worker_body(m, smem, reinterpret_cast<int*>(smem + FUSED_LDS / 2), ws);
    }
}

extern "C" void kernel_launch(void* const* d_in, const int* in_sizes, int n_in, void* d_out, int out_size,
                              void* d_ws, size_t ws_size, hipStream_t stream) {
    const u16* A_x = (const u16*)d_in[0];
    const u16* A_mem0 = (const u16*)d_in[1];
    const u16* A_Wq = (const u16*)d_in[2];
    const u16* A_Wk = (const u16*)d_in[3];
    const u16* A_Wv = (const u16*)d_in[4];
    const u16* A_Wo = (const u16*)d_in[5];
    const u16* B_x = (const u16*)d_in[7];
    const u16* B_ckv = (const u16*)d_in[8];
    const u16* B_wq = (const u16*)d_in[9];
    const u16* B_wuk = (const u16*)d_in[10];
    const u16* B_wuv = (const u16*)d_in[11];
    const u16* B_wo = (const u16*)d_in[12];
    u16* out = (u16*)d_out;

    char* ws = (char*)d_ws;
    const size_t SZ = (size_t)S_ * HD * 2;
    u16* Qb = (u16*)(ws + 0 * SZ);
    u16* Kb = (u16*)(ws + 1 * SZ);
    u16* Vb = (u16*)(ws + 2 * SZ);
    u16* OB = (u16*)(ws + 3 * SZ);
    size_t off = 4 * SZ;
    u16* AmatT = (u16*)(ws + off); off += (size_t)H_ * DM * DM * 2;
    u16* CmatT = (u16*)(ws + off); off += (size_t)H_ * DM * DM * 2;
    u16* XP = (u16*)(ws + off); off += (size_t)S_ * DM * 2;
    char* gbase = ws + off;
    u16* G1 = (u16*)gbase;
    u16* G2 = G1 + 63 * G1_SLOT;
    u16* G3 = G2 + 64 * G2_SLOT;
    unsigned* ctl = (unsigned*)(G3 + 64 * G3_SLOT);
    size_t gbytes = (size_t)(63 * G1_SLOT + 64 * G2_SLOT + 64 * G3_SLOT) * 2 + (size_t)CTL_WORDS * 4;

    hipLaunchKernelGGL(pre_kernel, dim3(224), dim3(256), 0, stream, A_Wq, A_Wk, A_Wv, A_Wo, AmatT, CmatT, (u32x4*)gbase,
                       (int)(gbytes / 16), A_x, (u32x4*)XP);
    RecParams rp;
    rp.x = XP; rp.mem0 = A_mem0; rp.AmatT = AmatT; rp.CmatT = CmatT; rp.G1 = G1; rp.G2 = G2; rp.G3 = G3; rp.out = out;
    MegaParams mp;
    mp.B_x = B_x; mp.B_ckv = B_ckv; mp.B_wq = B_wq; mp.B_wuk = B_wuk; mp.B_wuv = B_wuv; mp.B_wo = B_wo;
    mp.Qb = Qb; mp.Kb = Kb; mp.Vb = Vb; mp.OB = OB;
    mp.out = out; mp.ctl = ctl;
    hipLaunchKernelGGL(mega_kernel, dim3(256), dim3(256), 0, stream, rp, mp);
}
```

```cpp
#include <hip/hip_runtime.h>
#include <hip/hip_bf16.h>
#include <stdint.h>

typedef unsigned short u16;
typedef __attribute__((ext_vector_type(8))) short bf16x8;
typedef __attribute__((ext_vector_type(4))) short s16x4;
typedef __attribute__((ext_vector_type(16))) float f32x16;
typedef __attribute__((ext_vector_type(4))) float f32x4;
typedef __attribute__((ext_vector_type(4))) unsigned u32x4;
typedef __attribute__((ext_vector_type(2))) unsigned u32x2;
typedef short v4i16_t __attribute__((ext_vector_type(4)));

#define LAS __attribute__((address_space(3)))

__device__ __forceinline__ float bf2f(u16 x) { return __uint_as_float(((unsigned)x) << 16); }
__device__ __forceinline__ u16 f2bf(float f) {
    unsigned u = __float_as_uint(f);
    u += 0x7fffu + ((u >> 16) & 1u);
    return (u16)(u >> 16);
}
typedef __bf16 bf16x2_t __attribute__((ext_vector_type(2)));
__device__ __forceinline__ unsigned pack2(float lo, float hi) {
    bf16x2_t r = {(__bf16)lo, (__bf16)hi};
    return __builtin_bit_cast(unsigned, r);
}
__device__ __forceinline__ s16x4 tr_read(const void* p) {
    return __builtin_bit_cast(s16x4, __builtin_amdgcn_ds_read_tr16_b64_v4i16((LAS v4i16_t*)p));
}
#define MFMA32(a, b, c) __builtin_amdgcn_mfma_f32_32x32x16_bf16(a, b, c, 0, 0, 0)
#define MFMA16(a, b, c) __builtin_amdgcn_mfma_f32_16x16x32_bf16(a, b, c, 0, 0, 0)

__device__ __forceinline__ int tid_of(int ws) {
    int l = (int)__builtin_amdgcn_mbcnt_hi(~0u, __builtin_amdgcn_mbcnt_lo(~0u, 0u));
    asm volatile("" : "+v"(l));
    return ws * 64 + l;
}
__device__ __forceinline__ int crow(int r, int hi) { return (r & 3) + 8 * (r >> 2) + 4 * hi; }

constexpr int S_ = 2048, DM = 448, H_ = 16, DH = 256, HD = 4096, MM = 8, SEG = 32, NSEG = 64, DC = 1024;

__global__ __launch_bounds__(256) void pre_kernel(const u16* __restrict__ Wq, const u16* __restrict__ Wk,
                                                  const u16* __restrict__ Wv, const u16* __restrict__ Wo,
                                                  u16* __restrict__ AmatT, u16* __restrict__ CmatT,
                                                  u32x4* __restrict__ gbase, int gchunks,
                                                  const u16* __restrict__ x, u32x4* __restrict__ xp) {
    __shared__ __attribute__((aligned(16))) u16 Bs[256 * 96];
    const int tid = threadIdx.x, lane = tid & 63, wid = tid >> 6;
    const int c16 = lane & 15, g = lane >> 4, q4 = c16 >> 2, p4 = lane & 3;
    const int br = blockIdx.x >> 3, rb = br % 7, hz = (blockIdx.x & 7) + 8 * (br / 7), h = hz & 15;
    const bool isC = hz >= 16;
    u32x4 rbv[8];
#pragma unroll
    for (int i = 0; i < 8; ++i) {
        int c = tid + 256 * i;
        const u16* src = isC ? Wo + (long)(h * DH + (c >> 3)) * DM + rb * 64 + (c & 7) * 8
                             : Wk + (long)(rb * 64 + (c >> 5)) * HD + h * DH + (c & 31) * 8;
        rbv[i] = *reinterpret_cast<const u32x4*>(src);
    }
    const u16* ap = (isC ? Wv : Wq) + (long)(wid * 112 + c16) * HD + h * DH + 8 * g;
    bf16x8 af[7][8];
#pragma unroll
    for (int ks = 0; ks < 8; ++ks)
#pragma unroll
        for (int t = 0; t < 7; ++t) af[t][ks] = *reinterpret_cast<const bf16x8*>(ap + (long)t * 16 * HD + 32 * ks);
    for (int i = blockIdx.x * 256 + tid; i < gchunks; i += 224 * 256)
        gbase[i] = u32x4{0xffffffffu, 0xffffffffu, 0xffffffffu, 0xffffffffu};
#pragma unroll
    for (int i = 0; i < 2; ++i) {
        const int fr = (blockIdx.x * 2 + i) * 4 + wid, rt = fr / 14, kg = fr - rt * 14;
        xp[fr * 64 + lane] = *reinterpret_cast<const u32x4*>(x + (long)(rt * 16 + c16) * DM + 32 * kg + 8 * g);
    }
#pragma unroll
    for (int i = 0; i < 8; ++i) {
        int c = tid + 256 * i;
        u16* dst = isC ? Bs + (c >> 3) * 96 + (c & 7) * 8 : Bs + (c >> 5) * 264 + (c & 31) * 8;
        *reinterpret_cast<u32x4*>(dst) = rbv[i];
    }
    __syncthreads();
    f32x4 acc[7][4];
#pragma unroll
    for (int t = 0; t < 7; ++t)
#pragma unroll
        for (int j = 0; j < 4; ++j) acc[t][j] = f32x4{0.f, 0.f, 0.f, 0.f};
#pragma unroll
    for (int ks = 0; ks < 8; ++ks) {
        bf16x8 bfr[4];
#pragma unroll
        for (int j = 0; j < 4; ++j) {
            if (isC) {
                const u16* pB = Bs + (ks * 32 + 8 * g + q4) * 96 + j * 16 + 4 * p4;
                s16x4 lo = tr_read(pB);
                s16x4 hi4 = tr_read(pB + 4 * 96);
                bfr[j] = bf16x8{lo[0], lo[1], lo[2], lo[3], hi4[0], hi4[1], hi4[2], hi4[3]};
            } else {
                bfr[j] = *reinterpret_cast<const bf16x8*>(Bs + (16 * j + c16) * 264 + ks * 32 + 8 * g);
            }
        }
#pragma unroll
        for (int t = 0; t < 7; ++t)
#pragma unroll
            for (int j = 0; j < 4; ++j) acc[t][j] = MFMA16(af[t][ks], bfr[j], acc[t][j]);
    }
    const float sc = isC ? 1.f : 0.0625f;
    u16* op = (isC ? CmatT : AmatT) + (long)(h * DM + rb * 64 + c16) * DM + wid * 112 + 4 * g;
#pragma unroll
    for (int j = 0; j < 4; ++j)
#pragma unroll
        for (int t = 0; t < 7; ++t) {
            u32x2 w;
            w[0] = pack2(acc[t][j][0] * sc, acc[t][j][1] * sc);
            w[1] = pack2(acc[t][j][2] * sc, acc[t][j][3] * sc);
            *reinterpret_cast<u32x2*>(op + j * 16 * DM + 16 * t) = w;
        }
}

constexpr unsigned WSPIN_CAP = 2000000u;
__device__ __forceinline__ void acquire_agent() {
    __builtin_amdgcn_fence(__ATOMIC_ACQUIRE, "agent");
    asm volatile("s_waitcnt vmcnt(0)" ::: "memory");
}
__device__ __forceinline__ int wait_kv(const unsigned* c, int qr, int need) {
    const int lane = tid_of(0);
    int ready = 8;
    for (unsigned spin = 0; spin < WSPIN_CAP; ++spin) {
        unsigned v = __hip_atomic_load(c + (lane & 15), __ATOMIC_RELAXED, __HIP_MEMORY_SCOPE_AGENT) + 1u;
        unsigned long long ok = __ballot(v >= ((lane & 8) ? 2u : 4u));
        ready = __builtin_ctz(~(unsigned)ok | 256u);
        if (ready >= need && ((ok >> (8 + qr)) & 1)) break;
        __builtin_amdgcn_s_sleep(32);
    }
    acquire_agent();
    return ready;
}

constexpr int KROW = 264;
constexpr int VROW = 288;
constexpr int ATTN_LDS = (64 * KROW + 64 * VROW) * 2;
__device__ __forceinline__ void attnB_body(const u16* __restrict__ Q, const u16* __restrict__ K,
                                           const u16* __restrict__ V, u16* __restrict__ O, u16* smem, int qb, int h,
                                           int ws, const unsigned* cnt, int* slot) {
    u16* Ks = smem;
    u16* Vs = smem + 64 * KROW;
    const int tid = tid_of(ws);
    const int lane = tid & 63, wid = ws;
    const int q0 = qb * 128;
    const int r32 = lane & 31, hi = lane >> 5, g = lane >> 4, q4 = (lane & 15) >> 2, p4 = lane & 3;
    const int qrow = q0 + wid * 32 + r32;
    if (ws == 0) {
        int r = wait_kv(cnt, qb >> 1, 1);
        if (lane == 0) *slot = r;
    }
    __syncthreads();
    int ready = __builtin_amdgcn_readfirstlane(*slot);
    bf16x8 qf[16];
#pragma unroll
    for (int ks = 0; ks < 16; ++ks)
        qf[ks] = *reinterpret_cast<const bf16x8*>(Q + (long)qrow * HD + h * DH + ks * 16 + hi * 8);
    f32x16 o[8];
#pragma unroll
    for (int i = 0; i < 8; ++i) o[i] = f32x16{};
    float mrun = -INFINITY, lrun = 0.f;
    const float c2 = 0.0625f * 1.4426950408889634f;
    const int nt = (q0 + 128) / 64;
    u32x4 rk[8], rv[8];
    auto load_kv = [&](int t) {
#pragma unroll
        for (int i = 0; i < 8; ++i) {
            int c = tid + 256 * i, row = c >> 5, cc = c & 31;
            long off = (long)(t * 64 + row) * HD + h * DH + cc * 8;
            rk[i] = *reinterpret_cast<const u32x4*>(K + off);
            rv[i] = *reinterpret_cast<const u32x4*>(V + off);
        }
    };
    auto store_kv = [&]() {
#pragma unroll
        for (int i = 0; i < 8; ++i) {
            int c = tid + 256 * i, row = c >> 5, cc = c & 31;
            *reinterpret_cast<u32x4*>(Ks + row * KROW + cc * 8) = rk[i];
            *reinterpret_cast<u32x4*>(Vs + row * VROW + cc * 8) = rv[i];
        }
    };
    load_kv(0);
    for (int t = 0; t < nt; ++t) {
        __syncthreads();
        store_kv();
        const bool need = (t + 1 < nt) && ((t + 1) >> 2) >= ready;
        if (need && ws == 0) {
            int r = wait_kv(cnt, qb >> 1, ((t + 1) >> 2) + 1);
            if (lane == 0) *slot = r;
        }
        __syncthreads();
        if (need) ready = __builtin_amdgcn_readfirstlane(*slot);
        if (t + 1 < nt) load_kv(t + 1);
        const int kbase = t * 64;
        if (kbase <= q0 + wid * 32 + 31) {
            f32x16 s0 = f32x16{}, s1 = f32x16{};
#pragma unroll
            for (int ks = 0; ks < 16; ++ks) {
                bf16x8 k0 = *reinterpret_cast<const bf16x8*>(Ks + r32 * KROW + ks * 16 + hi * 8);
                bf16x8 k1 = *reinterpret_cast<const bf16x8*>(Ks + (32 + r32) * KROW + ks * 16 + hi * 8);
                s0 = MFMA32(k0, qf[ks], s0);
                s1 = MFMA32(k1, qf[ks], s1);
                if ((ks & 3) == 3) __builtin_amdgcn_sched_barrier(0);
            }
            if (kbase + 63 > q0 + wid * 32) {
#pragma unroll
                for (int r = 0; r < 16; ++r) {
                    int kk = kbase + crow(r, hi);
                    if (kk > qrow) s0[r] = -INFINITY;
                    if (kk + 32 > qrow) s1[r] = -INFINITY;
                }
            }
            float mx = -INFINITY;
#pragma unroll
            for (int r = 0; r < 16; ++r) mx = fmaxf(mx, fmaxf(s0[r], s1[r]));
            mx = fmaxf(mx, __shfl_xor(mx, 32));
            float mnew = fmaxf(mrun, mx);
            float alpha = __builtin_amdgcn_exp2f((mrun - mnew) * c2);
            float nm = -mnew * c2;
            float psum = 0.f;
#pragma unroll
            for (int r = 0; r < 16; ++r) {
                s0[r] = __builtin_amdgcn_exp2f(fmaf(s0[r], c2, nm));
                s1[r] = __builtin_amdgcn_exp2f(fmaf(s1[r], c2, nm));
                psum += s0[r] + s1[r];
            }
            psum += __shfl_xor(psum, 32);
            lrun = lrun * alpha + psum;
            mrun = mnew;
#pragma unroll
            for (int i = 0; i < 8; ++i)
#pragma unroll
                for (int r = 0; r < 16; ++r) o[i][r] *= alpha;
            bf16x8 pf[4];
#pragma unroll
            for (int s = 0; s < 2; ++s) {
                u32x4 a, b;
#pragma unroll
                for (int j = 0; j < 4; ++j) {
                    a[j] = pack2(s0[8 * s + 2 * j], s0[8 * s + 2 * j + 1]);
                    b[j] = pack2(s1[8 * s + 2 * j], s1[8 * s + 2 * j + 1]);
                }
                pf[s] = __builtin_bit_cast(bf16x8, a);
                pf[2 + s] = __builtin_bit_cast(bf16x8, b);
            }
#pragma unroll
            for (int dt = 0; dt < 8; ++dt) {
#pragma unroll
                for (int s = 0; s < 4; ++s) {
                    const u16* pV = Vs + (16 * s + 4 * hi + q4) * VROW + 32 * dt + 16 * (g & 1) + 4 * p4;
                    s16x4 lo = tr_read(pV);
                    s16x4 hi4 = tr_read(pV + 8 * VROW);
                    bf16x8 vf = bf16x8{lo[0], lo[1], lo[2], lo[3], hi4[0], hi4[1], hi4[2], hi4[3]};
                    o[dt] = MFMA32(vf, pf[s], o[dt]);
                }
                __builtin_amdgcn_sched_barrier(0);
            }
        }
    }
    float inv = 1.f / lrun;
#pragma unroll
    for (int dt = 0; dt < 8; ++dt)
#pragma unroll
        for (int rr = 0; rr < 4; ++rr) {
            u32x2 w;
            w[0] = pack2(o[dt][4 * rr] * inv, o[dt][4 * rr + 1] * inv);
            w[1] = pack2(o[dt][4 * rr + 2] * inv, o[dt][4 * rr + 3] * inv);
            __hip_atomic_store(reinterpret_cast<unsigned long long*>(O + (long)qrow * HD + h * DH + 32 * dt + 8 * rr + 4 * hi),
                               ((unsigned long long)w[1] << 32) | w[0], __ATOMIC_RELAXED, __HIP_MEMORY_SCOPE_AGENT);
        }
}

typedef unsigned long long u64;
__device__ __forceinline__ u64 ld_gran(const u64* p) {
    return __hip_atomic_load(p, __ATOMIC_RELAXED, __HIP_MEMORY_SCOPE_AGENT);
}
__device__ __forceinline__ void st_gran4(u16* p, unsigned lo, unsigned hi) {
    hi = min(hi, 0xfffffffeu);
    __hip_atomic_store(reinterpret_cast<u64*>(p), ((u64)hi << 32) | (u64)lo, __ATOMIC_RELAXED,
                       __HIP_MEMORY_SCOPE_AGENT);
}
__device__ __forceinline__ void st_gran8(__amdgpu_buffer_rsrc_t rs, unsigned voff, unsigned soff, bool act, unsigned lo,
                                         unsigned hi) {
    hi = min(hi, 0xfffffffeu);
    unsigned lo2 = (unsigned)__builtin_amdgcn_mov_dpp((int)lo, 0xF5, 0xf, 0xf, true);
    unsigned hi2 = (unsigned)__builtin_amdgcn_mov_dpp((int)hi, 0xF5, 0xf, 0xf, true);
    if (act) __builtin_amdgcn_raw_buffer_store_b128(u32x4{lo, hi, lo2, hi2}, rs, voff, soff, 16);
}
constexpr int NA_BLK = 16, NB_BLK = 16, NC_BLK = 28, NREC = NA_BLK + NB_BLK + NC_BLK;
constexpr int MROW = 456;
constexpr int G1_SLOT = 16 * 8 * 40;
constexpr int G2_SLOT = 8 * 448;
constexpr int G3_SLOT = 16 * 32 * 40;
constexpr int XIMG = 28 * 512;
constexpr unsigned SPIN_CAP = 400000u;
#define SLP_A 45
#define SLP_C1 2

#define WTS(slot)
#define KT0
#define KT1(k)
#define TS(slot)
struct RecParams {
    const u16* x;
    const u16* mem0;
    const u16* AmatT;
    const u16* CmatT;
    u16* G1;
    u16* G2;
    u16* G3;
    u16* out;
};

__device__ __forceinline__ float red16_max(float x) {
    asm volatile(
        "s_nop 1\n\tv_max_f32_dpp %0, %0, %0 quad_perm:[1,0,3,2] row_mask:0xf bank_mask:0xf\n\t"
        "s_nop 1\n\tv_max_f32_dpp %0, %0, %0 quad_perm:[2,3,0,1] row_mask:0xf bank_mask:0xf\n\t"
        "s_nop 1\n\tv_max_f32_dpp %0, %0, %0 row_half_mirror row_mask:0xf bank_mask:0xf\n\t"
        "s_nop 1\n\tv_max_f32_dpp %0, %0, %0 row_mirror row_mask:0xf bank_mask:0xf\n\t"
        "s_nop 1"
        : "+v"(x));
    return x;
}
__device__ __forceinline__ float red16_sum(float x) {
    asm volatile(
        "s_nop 1\n\tv_add_f32_dpp %0, %0, %0 quad_perm:[1,0,3,2] row_mask:0xf bank_mask:0xf\n\t"
        "s_nop 1\n\tv_add_f32_dpp %0, %0, %0 quad_perm:[2,3,0,1] row_mask:0xf bank_mask:0xf\n\t"
        "s_nop 1\n\tv_add_f32_dpp %0, %0, %0 row_half_mirror row_mask:0xf bank_mask:0xf\n\t"
        "s_nop 1\n\tv_add_f32_dpp %0, %0, %0 row_mirror row_mask:0xf bank_mask:0xf\n\t"
        "s_nop 1"
        : "+v"(x));
    return x;
}
__device__ __forceinline__ u32x4 ld16_sc1(__amdgpu_buffer_rsrc_t rs, unsigned voff, unsigned soff) {
    return __builtin_amdgcn_raw_buffer_load_b128(rs, voff, soff, 16);
}
__device__ __forceinline__ unsigned umax3(unsigned a, unsigned b, unsigned c) { return max(max(a, b), c); }

__device__ __forceinline__ void mfma_a0(f32x4& d, const bf16x8& w, const u32x4& m) {
    asm volatile("v_mfma_f32_16x16x32_bf16 %0, %1, %2, 0" : "=&v"(d) : "a"(w), "v"(m));
}
__device__ __forceinline__ void mfma_a(f32x4& d, const bf16x8& w, const u32x4& m) {
    asm volatile("v_mfma_f32_16x16x32_bf16 %0, %1, %2, %0" : "+v"(d) : "a"(w), "v"(m));
}
__device__ __forceinline__ void mfma_v0(f32x4& d, const bf16x8& w, const u32x4& m) {
    asm volatile("v_mfma_f32_16x16x32_bf16 %0, %1, %2, 0" : "=&v"(d) : "v"(w), "v"(m));
}
__device__ __forceinline__ void mfma_v(f32x4& d, const bf16x8& w, const u32x4& m) {
    asm volatile("v_mfma_f32_16x16x32_bf16 %0, %1, %2, %0" : "+v"(d) : "v"(w), "v"(m));
}
__device__ __forceinline__ void lds_rd16(bf16x8& d, unsigned addr, int off) {
    asm volatile("ds_read_b128 %0, %1 offset:%2" : "=v"(d) : "v"(addr), "n"(off));
}
__device__ __forceinline__ void lds_rd16u(u32x4& d, unsigned addr) {
    asm volatile("ds_read_b128 %0, %1" : "=v"(d) : "v"(addr));
}
template <int N>
__device__ __forceinline__ void wait_lgkm() {
    asm volatile("s_waitcnt lgkmcnt(%0)" ::"n"(N) : "memory");
}
constexpr int sch_n(int q) { return q <= 8 ? 7 + 2 * (q + 1) : (q <= 11 ? 25 + (q - 8) : 28); }
constexpr int sch_m(int i) { return i < 5 ? 2 + i : 7 + 2 * (i - 5); }
constexpr int sch_w(int i) { return i < 2 ? i : (i - 2 <= 8 ? 8 + 2 * (i - 2) : 25 + (i - 11)); }

__device__ __forceinline__ void dma16(const void* gbase, unsigned voff, unsigned lds) {
    unsigned keep;
    asm volatile("s_mov_b32 %0, m0\n\ts_mov_b32 m0, %3\n\ts_nop 0\n\tglobal_load_lds_dwordx4 %1, %2\n\ts_mov_b32 m0, %0"
                 : "=&s"(keep)
                 : "v"(voff), "s"(gbase), "s"(lds)
                 : "memory");
}
__device__ __forceinline__ void dma_x(const u16* x, int s, unsigned xvoff, unsigned lds, int wid) {
    const char* gb = reinterpret_cast<const char*>(x) + (long)s * (SEG * DM * 2) + wid * 7168;
#pragma unroll
    for (int j = 0; j < 7; ++j) dma16(gb + 1024 * j, xvoff, lds + (unsigned)(wid * 7 + j) * 1024u);
}

__device__ __forceinline__ void lds_rd16s(u32x4& d, unsigned addr, unsigned soff) {
    unsigned t;
    asm volatile("v_add_u32 %1, %3, %2\n\tds_read_b128 %0, %1" : "=v"(d), "=&v"(t) : "v"(addr), "s"(soff));
}
__device__ __forceinline__ void tpass(const bf16x8 (&W)[6][14], unsigned wl_addr, unsigned fa, const unsigned (&so)[14],
                                      u32x4 (&mf)[14], bf16x8 (&w6)[3], f32x4 (&acc)[7]) {
#pragma unroll
    for (int q = 0; q < 5; ++q) lds_rd16s(mf[q], fa, so[q]);
#pragma unroll
    for (int q = 0; q < 14; ++q) {
        if (q + 5 < 14) lds_rd16s(mf[q + 5], fa, so[q + 5]);
        if (q + 2 < 14) lds_rd16(w6[(q + 2) % 3], wl_addr, (q + 2) * 1024);
        switch (q) {
#define WM(Q) case Q: wait_lgkm<sch_n(Q) - sch_m(Q) - 1>(); break;
            WM(0) WM(1) WM(2) WM(3) WM(4) WM(5) WM(6) WM(7) WM(8) WM(9) WM(10) WM(11) WM(12) WM(13)
#undef WM
        }
#pragma unroll
        for (int t = 0; t < 4; ++t) {
            if (q == 0) mfma_a0(acc[t], W[t][q], mf[q]);
            else mfma_a(acc[t], W[t][q], mf[q]);
        }
#pragma unroll
        for (int t = 4; t < 6; ++t) {
            if (t == 4 && q < 6) {
                if (q == 0) mfma_a0(acc[t], W[t][q], mf[q]);
                else mfma_a(acc[t], W[t][q], mf[q]);
            } else {
                if (q == 0) mfma_v0(acc[t], W[t][q], mf[q]);
                else mfma_v(acc[t], W[t][q], mf[q]);
            }
        }
        switch (q) {
#define WW(Q) case Q: wait_lgkm<sch_n(Q) - sch_w(Q) - 1>(); break;
            WW(0) WW(1) WW(2) WW(3) WW(4) WW(5) WW(6) WW(7) WW(8) WW(9) WW(10) WW(11) WW(12) WW(13)
#undef WW
        }
        if (q == 0) mfma_v0(acc[6], w6[0], mf[q]);
        else mfma_v(acc[6], w6[q % 3], mf[q]);
    }
    asm volatile("s_nop 7\n\ts_nop 1"
                 : "+v"(acc[0]), "+v"(acc[1]), "+v"(acc[2]), "+v"(acc[3]), "+v"(acc[4]), "+v"(acc[5]), "+v"(acc[6]));
}
__device__ __forceinline__ void tpack(const f32x4 (&acc)[7], bool q3ok, u32x4 (&bv)[4]) {
#pragma unroll
    for (int pr = 0; pr < 3; ++pr)
        bv[pr] = u32x4{pack2(acc[2 * pr][0], acc[2 * pr][1]), pack2(acc[2 * pr][2], acc[2 * pr][3]),
                       pack2(acc[2 * pr + 1][0], acc[2 * pr + 1][1]), pack2(acc[2 * pr + 1][2], acc[2 * pr + 1][3])};
    unsigned t0 = pack2(acc[6][0], acc[6][1]), t1 = pack2(acc[6][2], acc[6][3]);
    bv[3] = q3ok ? u32x4{t0, t1, 0u, 0u} : u32x4{0u, 0u, t0, t1};
}
#define BF8(x) __builtin_bit_cast(bf16x8, x)
__device__ __forceinline__ void schain1(f32x4& d0, const u32x4 (&a0)[4], const u32x4 (&b)[4]) {
    asm volatile(
        "s_nop 1\n\tv_mfma_f32_16x16x32_bf16 %0, %1, %5, 0\n\tv_mfma_f32_16x16x32_bf16 %0, %2, %6, %0\n\t"
        "v_mfma_f32_16x16x32_bf16 %0, %3, %7, %0\n\tv_mfma_f32_16x16x32_bf16 %0, %4, %8, %0\n\ts_nop 7"
        : "=&v"(d0)
        : "v"(a0[0]), "v"(a0[1]), "v"(a0[2]), "v"(a0[3]), "v"(b[0]), "v"(b[1]), "v"(b[2]), "v"(b[3]));
}
__device__ __forceinline__ void schain2(f32x4& d0, f32x4& d1, const u32x4 (&a0)[4], const u32x4 (&a1)[4],
                                        const u32x4 (&b0)[4], const u32x4 (&b1)[4]) {
    asm volatile(
        "s_nop 1\n\tv_mfma_f32_16x16x32_bf16 %0, %2, %10, 0\n\tv_mfma_f32_16x16x32_bf16 %1, %6, %14, 0\n\t"
        "v_mfma_f32_16x16x32_bf16 %0, %3, %11, %0\n\tv_mfma_f32_16x16x32_bf16 %1, %7, %15, %1\n\t"
        "v_mfma_f32_16x16x32_bf16 %0, %4, %12, %0\n\tv_mfma_f32_16x16x32_bf16 %1, %8, %16, %1\n\t"
        "v_mfma_f32_16x16x32_bf16 %0, %5, %13, %0\n\tv_mfma_f32_16x16x32_bf16 %1, %9, %17, %1\n\ts_nop 7"
        : "=&v"(d0), "=&v"(d1)
        : "v"(a0[0]), "v"(a0[1]), "v"(a0[2]), "v"(a0[3]), "v"(a1[0]), "v"(a1[1]), "v"(a1[2]), "v"(a1[3]), "v"(b0[0]),
          "v"(b0[1]), "v"(b0[2]), "v"(b0[3]), "v"(b1[0]), "v"(b1[1]), "v"(b1[2]), "v"(b1[3]));
}
__device__ __forceinline__ void schain3(f32x4& d0, f32x4& d1, f32x4& d2, const u32x4 (&a0)[4], const u32x4 (&a1)[4],
                                        const u32x4 (&a2)[14], const u32x4 (&b)[4]) {
    asm volatile(
        "s_nop 1\n\tv_mfma_f32_16x16x32_bf16 %0, %3, %15, 0\n\tv_mfma_f32_16x16x32_bf16 %1, %7, %15, 0\n\t"
        "v_mfma_f32_16x16x32_bf16 %2, %11, %15, 0\n\t"
        "v_mfma_f32_16x16x32_bf16 %0, %4, %16, %0\n\tv_mfma_f32_16x16x32_bf16 %1, %8, %16, %1\n\t"
        "v_mfma_f32_16x16x32_bf16 %2, %12, %16, %2\n\t"
        "v_mfma_f32_16x16x32_bf16 %0, %5, %17, %0\n\tv_mfma_f32_16x16x32_bf16 %1, %9, %17, %1\n\t"
        "v_mfma_f32_16x16x32_bf16 %2, %13, %17, %2\n\t"
        "v_mfma_f32_16x16x32_bf16 %0, %6, %18, %0\n\tv_mfma_f32_16x16x32_bf16 %1, %10, %18, %1\n\t"
        "v_mfma_f32_16x16x32_bf16 %2, %14, %18, %2\n\ts_nop 7"
        : "=&v"(d0), "=&v"(d1), "=&v"(d2)
        : "v"(a0[0]), "v"(a0[1]), "v"(a0[2]), "v"(a0[3]), "v"(a1[0]), "v"(a1[1]), "v"(a1[2]), "v"(a1[3]), "v"(a2[0]),
          "v"(a2[1]), "v"(a2[2]), "v"(a2[3]), "v"(b[0]), "v"(b[1]), "v"(b[2]), "v"(b[3]));
}

constexpr int AL_W = 0, AL_X = 4 * 14 * 512, AL_S = AL_X + 2 * XIMG;
constexpr int SROW = 44;
constexpr int AL_MEM = AL_S + 4 * 320 * 2;
constexpr int RECA_LDS = (AL_S + 4 * 32 * SROW * 2) * 2;

template <bool ISB>
__device__ __forceinline__ void rec_A_body(const RecParams& p, u16* smem, int h, int wid) {
    float* Sc = reinterpret_cast<float*>(smem + AL_S);
    const int tid = tid_of(wid), lane = tid & 63;
    const int c16 = lane & 15, g = lane >> 4;
    const unsigned xl_base = (unsigned)(uintptr_t)(LAS u16*)(smem + AL_X);
    const unsigned xvoff = (unsigned)lane * 16u;
    dma_x(p.x, 0, xvoff, xl_base, wid);
    dma_x(p.x, 1, xvoff, xl_base + XIMG * 2, wid);
    int kq[14];
#pragma unroll
    for (int q = 0; q < 14; ++q) {
        if (q < 3) kq[q] = 3 * wid + q;
        else if (q == 3) kq[q] = 12 + (wid >> 1);
        else if (q < 13) kq[q] = (q - 4 < 3 * wid) ? (q - 4) : (q - 1);
        else kq[q] = 13 - (wid >> 1);
    }
    bf16x8 W[6][14];
    u16* WL = smem + AL_W + (wid * 14 * 64 + lane) * 8;
#pragma unroll
    for (int t = 0; t < 7; ++t) {
        const int ksp = (t < 6) ? (3 * wid + (t >> 1)) : (12 + (wid >> 1));
        const int half = (t < 6) ? (t & 1) : (wid & 1);
        const int n = 32 * ksp + 8 * (c16 >> 2) + 4 * half + (c16 & 3);
        const u16* ap = p.AmatT + ((long)h * DM + n) * DM + 8 * g;
#pragma unroll
        for (int q = 0; q < 14; ++q) {
            bf16x8 w = *reinterpret_cast<const bf16x8*>(ap + 32 * kq[q]);
            if (t < 6)
                W[t][q] = w;
            else
                *reinterpret_cast<bf16x8*>(WL + q * 512) = w;
        }
    }
    const bool q3ok = (wid & 1) == 0;
    const bool sact = c16 < 10;
    const bool sst = sact && !(c16 & 1);
    const unsigned wl_addr = (unsigned)(uintptr_t)(LAS u16*)WL;
    __amdgpu_buffer_rsrc_t rs2 = __builtin_amdgcn_make_buffer_rsrc(p.G2, 0, 64 * G2_SLOT * 2, 0x00020000);
    const float L2E = 1.4426950408889634f;
    bool dead = false;
    asm volatile("s_waitcnt vmcnt(0)" ::: "memory");
    __syncthreads();
    if constexpr (!ISB) {
        const int srow = 4 * (wid & 1) + g;
        const float* sread = Sc + srow * 40 + (sact ? 4 * c16 : 0);
        float* swr = Sc + wid * 320 + (c16 & 7) * 40 + 8 * g;
        float* swr_mm = Sc + wid * 320 + (c16 & 7) * 40 + 32 + 4 * (g & 1);
        u16* memL = smem + AL_MEM;
        const int ch1 = (tid < 192) ? (256 + tid) : tid;
        u16* mw0 = memL + ((tid >> 1) & 7) * MROW + 16 * (tid >> 4) + 8 * (tid & 1);
        u16* mw1 = memL + ((ch1 >> 1) & 7) * MROW + 16 * (ch1 >> 4) + 8 * (ch1 & 1);
        const unsigned ml_addr = (unsigned)(uintptr_t)(LAS u16*)(memL + (c16 & 7) * MROW + 8 * g);
        unsigned so[14];
#pragma unroll
        for (int q = 0; q < 14; ++q) so[q] = 64 * kq[q];
        __amdgpu_buffer_rsrc_t rs1 = __builtin_amdgcn_make_buffer_rsrc(p.G1, 0, 63 * G1_SLOT * 2, 0x00020000);
        const unsigned g1off = (h * 320 + srow * 40 + 4 * c16) * 2;
        const u16* xrd = smem + AL_X + lane * 8;
        u32x4 xf[2][4];
        auto read_xf = [&](int s) {
#pragma unroll
            for (int tl = 0; tl < 2; ++tl)
#pragma unroll
                for (int q = 0; q < 4; ++q)
                    xf[tl][q] = *reinterpret_cast<const u32x4*>(xrd + (s & 1) * XIMG + (tl * 14 + kq[q]) * 512);
        };
        read_xf(0);
        for (int s = 0; s < NSEG - 1; ++s) {
            u32x4 mf[14];
            bf16x8 w6[3];
            wait_lgkm<0>();
            lds_rd16(w6[0], wl_addr, 0);
            lds_rd16(w6[1], wl_addr, 1024);
            {
                u32x4 a, b;
                for (unsigned spin = 0;; ++spin) {
                    asm volatile("" ::: "memory");
                    a = ld16_sc1(rs2, tid * 16, (unsigned)(s * G2_SLOT * 2));
                    b = ld16_sc1(rs2, ch1 * 16, (unsigned)(s * G2_SLOT * 2));
                    unsigned mx = max(umax3(a[1], a[3], b[1]), b[3]);
                    if (__all(mx != 0xffffffffu) || dead) break;
                    if (spin > SPIN_CAP) dead = true;
                }
                *reinterpret_cast<u32x4*>(mw0) = a;
                *reinterpret_cast<u32x4*>(mw1) = b;
                __syncthreads();
            }
            TS(1);
            f32x4 acc[7];
            tpass(W, wl_addr, ml_addr, so, mf, w6, acc);
            u32x4 bv[4];
            tpack(acc, q3ok, bv);
            f32x4 sm, s0, s1;
            schain3(s0, s1, sm, xf[0], xf[1], mf, bv);
            if (c16 < 8) {
                *reinterpret_cast<f32x4*>(swr) = s0;
                *reinterpret_cast<f32x4*>(swr + 4) = s1;
                if (g < 2) *reinterpret_cast<f32x4*>(swr_mm) = sm;
            }
            TS(2);
            __syncthreads();
            if (wid < 2) {
                f32x4 x0 = *reinterpret_cast<const f32x4*>(sread);
                f32x4 x1 = *reinterpret_cast<const f32x4*>(sread + 320);
                f32x4 x2 = *reinterpret_cast<const f32x4*>(sread + 640);
                f32x4 x3 = *reinterpret_cast<const f32x4*>(sread + 960);
                f32x4 a = (x0 + x1) + (x2 + x3);
                float mx = sact ? __builtin_fmaxf(__builtin_fmaxf(a[0], a[1]), __builtin_fmaxf(a[2], a[3])) : -INFINITY;
                mx = red16_max(mx);
                float nm = -mx * L2E;
                float e0 = __builtin_amdgcn_exp2f(__builtin_fmaf(a[0], L2E, nm));
                float e1 = __builtin_amdgcn_exp2f(__builtin_fmaf(a[1], L2E, nm));
                float e2 = __builtin_amdgcn_exp2f(__builtin_fmaf(a[2], L2E, nm));
                float e3 = __builtin_amdgcn_exp2f(__builtin_fmaf(a[3], L2E, nm));
                float sum = sact ? ((e0 + e1) + (e2 + e3)) : 0.f;
                sum = red16_sum(sum);
                float inv = __builtin_amdgcn_rcpf(sum);
                st_gran8(rs1, g1off, (unsigned)(s * G1_SLOT * 2), sst, pack2(e0 * inv, e1 * inv), pack2(e2 * inv, e3 * inv));
            }
            TS(3);
            asm volatile("" ::: "memory");
            read_xf(s + 1);
            if (s + 2 < NSEG - 1) dma_x(p.x, s + 2, xvoff, xl_base + (s & 1) * (XIMG * 2), wid);
            __builtin_amdgcn_s_sleep(SLP_A);
        }
    } else {
        float* swr = Sc + (wid * 32 + c16) * SROW + 8 * g;
        float* swr_mm = Sc + (wid * 32 + c16) * SROW + 32 + 4 * (g & 1);
        const float* sread = Sc + (wid * 8 + g) * SROW + (sact ? 4 * c16 : 0);
        const int key0 = (c16 < 8) ? 16 * (c16 & 1) + 4 * (c16 >> 1) : -4;
        __amdgpu_buffer_rsrc_t rs3 = __builtin_amdgcn_make_buffer_rsrc(p.G3, 0, 64 * G3_SLOT * 2, 0x00020000);
        const unsigned g3off = ((h * 32 + wid * 8 + g) * 40 + 4 * c16) * 2;
        unsigned so[14];
#pragma unroll
        for (int q = 0; q < 14; ++q) so[q] = 1024 * kq[q];
        unsigned mvoff[4];
#pragma unroll
        for (int q = 0; q < 4; ++q) mvoff[q] = ((2 * kq[q] + (g >> 1)) * 128 + (c16 & 7) * 16 + (g & 1) * 8) * 2;
        for (int s = 0; s < NSEG; ++s) {
            const unsigned xa = xl_base + (s & 1) * (XIMG * 2) + lane * 16;
            u32x4 bv0[4], bv1[4];
            {
                u32x4 mf[14];
                bf16x8 w6[3];
                f32x4 acc[7];
                wait_lgkm<0>();
                lds_rd16(w6[0], wl_addr, 0);
                lds_rd16(w6[1], wl_addr, 1024);
                tpass(W, wl_addr, xa, so, mf, w6, acc);
                tpack(acc, q3ok, bv0);
                const u32x4 m4[4] = {mf[0], mf[1], mf[2], mf[3]};
                f32x4 s00;
                schain1(s00, m4, bv0);
                __syncthreads();
                *reinterpret_cast<f32x4*>(swr) = s00;
            }
            {
                u32x4 mf[14];
                bf16x8 w6[3];
                f32x4 acc[7];
                wait_lgkm<0>();
                lds_rd16(w6[0], wl_addr, 0);
                lds_rd16(w6[1], wl_addr, 1024);
                tpass(W, wl_addr, xa + 14 * 1024, so, mf, w6, acc);
                tpack(acc, q3ok, bv1);
                const u16* xrd = smem + AL_X + (s & 1) * XIMG + lane * 8;
                const u32x4 m4[4] = {mf[0], mf[1], mf[2], mf[3]};
                u32x4 x4[4];
#pragma unroll
                for (int pr = 0; pr < 4; ++pr) x4[pr] = *reinterpret_cast<const u32x4*>(xrd + kq[pr] * 512);
                f32x4 s01, s11;
                schain2(s01, s11, x4, m4, bv1, bv1);
                *reinterpret_cast<f32x4*>(swr + 16 * SROW) = s01;
                *reinterpret_cast<f32x4*>(swr + 16 * SROW + 4) = s11;
            }
            u32x4 mm[4];
            for (unsigned spin = 0;; ++spin) {
                asm volatile("" ::: "memory");
                unsigned mx = 0;
#pragma unroll
                for (int q = 0; q < 4; ++q) mm[q] = ld16_sc1(rs2, mvoff[q], (unsigned)(s * G2_SLOT * 2));
#pragma unroll
                for (int q = 0; q < 4; ++q) mx = umax3(mx, mm[q][1], mm[q][3]);
                if (__all(mx != 0xffffffffu) || dead) break;
                if (spin > SPIN_CAP) dead = true;
                __builtin_amdgcn_s_sleep(8);
            }
            TS(1);
            {
                f32x4 sm0, sm1;
                schain2(sm0, sm1, mm, mm, bv0, bv1);
                if (g < 2) {
                    *reinterpret_cast<f32x4*>(swr_mm) = sm0;
                    *reinterpret_cast<f32x4*>(swr_mm + 16 * SROW) = sm1;
                }
            }
            asm volatile("s_waitcnt vmcnt(0)" ::: "memory");
            __syncthreads();
            TS(2);
            if (s + 2 < NSEG) dma_x(p.x, s + 2, xvoff, xl_base + (s & 1) * (XIMG * 2), wid);
#pragma unroll
            for (int i = 0; i < 2; ++i) {
                const float* sr = sread + 4 * i * SROW;
                f32x4 x0 = *reinterpret_cast<const f32x4*>(sr);
                f32x4 x1 = *reinterpret_cast<const f32x4*>(sr + 32 * SROW);
                f32x4 x2 = *reinterpret_cast<const f32x4*>(sr + 64 * SROW);
                f32x4 x3 = *reinterpret_cast<const f32x4*>(sr + 96 * SROW);
                f32x4 a = (x0 + x1) + (x2 + x3);
                const int row = wid * 8 + 4 * i + g;
#pragma unroll
                for (int r = 0; r < 4; ++r)
                    if (!sact || key0 + r > row) a[r] = -INFINITY;
                float mx = __builtin_fmaxf(__builtin_fmaxf(a[0], a[1]), __builtin_fmaxf(a[2], a[3]));
                mx = red16_max(mx);
                float nm = -mx * L2E;
                float e0 = __builtin_amdgcn_exp2f(__builtin_fmaf(a[0], L2E, nm));
                float e1 = __builtin_amdgcn_exp2f(__builtin_fmaf(a[1], L2E, nm));
                float e2 = __builtin_amdgcn_exp2f(__builtin_fmaf(a[2], L2E, nm));
                float e3 = __builtin_amdgcn_exp2f(__builtin_fmaf(a[3], L2E, nm));
                float sum = red16_sum((e0 + e1) + (e2 + e3));
                float inv = __builtin_amdgcn_rcpf(sum);
                st_gran8(rs3, g3off + i * (4 * 40 * 2), (unsigned)(s * G3_SLOT * 2), sst, pack2(e0 * inv, e1 * inv),
                         pack2(e2 * inv, e3 * inv));
            }
            TS(3);
        }
    }
}

__device__ __forceinline__ void mfma_b0(f32x4& d, const u32x4& a, const bf16x8& w) {
    asm volatile("v_mfma_f32_16x16x32_bf16 %0, %1, %2, 0" : "=&v"(d) : "v"(a), "a"(w));
}
__device__ __forceinline__ void mfma_b(f32x4& d, const u32x4& a, const bf16x8& w) {
    asm volatile("v_mfma_f32_16x16x32_bf16 %0, %1, %2, %0" : "+v"(d) : "v"(a), "a"(w));
}
__device__ __forceinline__ void lds_rd16o(u32x4& d, unsigned addr, int off) {
    asm volatile("ds_read_b128 %0, %1 offset:%2" : "=v"(d) : "v"(addr), "n"(off));
}
__device__ __forceinline__ void chain5(f32x4& d, const u32x4& a0, const u32x4& a1, const u32x4& a2, const u32x4& a3,
                                       const u32x4& a4, const u32x4 (&b)[4], const u32x4& b4) {
    asm volatile(
        "s_nop 1\n\tv_mfma_f32_16x16x32_bf16 %0, %1, %6, 0\n\tv_mfma_f32_16x16x32_bf16 %0, %2, %7, %0\n\t"
        "v_mfma_f32_16x16x32_bf16 %0, %3, %8, %0\n\tv_mfma_f32_16x16x32_bf16 %0, %4, %9, %0\n\t"
        "v_mfma_f32_16x16x32_bf16 %0, %5, %10, %0\n\ts_nop 7"
        : "=&v"(d)
        : "v"(a0), "v"(a1), "v"(a2), "v"(a3), "v"(a4), "v"(b[0]), "v"(b[1]), "v"(b[2]), "v"(b[3]), "v"(b4));
}
constexpr int cmin(int a, int b) { return a < b ? a : b; }
#define SW14(F) switch (ks) { F(0) F(1) F(2) F(3) F(4) F(5) F(6) F(7) F(8) F(9) F(10) F(11) F(12) F(13) }

constexpr int CL_X = 0, CL_MEM = 2 * XIMG, CL_RED = CL_MEM + 8 * MROW, CL_ORED = CL_RED + 4 * 128 * 2;
constexpr int RECC_LDS = (CL_ORED + 4 * 512 * 2) * 2;

__device__ __forceinline__ void rec_C_body(const RecParams& p, u16* smem, int c, int wid) {
    u16* memL = smem + CL_MEM;
    float* red = reinterpret_cast<float*>(smem + CL_RED);
    float* ored = reinterpret_cast<float*>(smem + CL_ORED);
    const int tid = tid_of(wid), lane = tid & 63;
    const int c16 = lane & 15, g = lane >> 4;
    const int ch1 = (tid < 192) ? (256 + tid) : tid;
    if (tid < 32) {
        u32x2 m0 = *reinterpret_cast<const u32x2*>(p.mem0 + (tid >> 2) * DM + 16 * c + 4 * (tid & 3));
        st_gran4(p.G2 + 128 * c + 4 * tid, m0[0], m0[1]);
    }
    const unsigned xl_base = (unsigned)(uintptr_t)(LAS u16*)(smem + CL_X);
    const unsigned xvoff = (unsigned)lane * 16u;
    dma_x(p.x, 0, xvoff, xl_base, wid);
    dma_x(p.x, 1, xvoff, xl_base + XIMG * 2, wid);
    u16* mw0 = memL + ((tid >> 1) & 7) * MROW + 16 * (tid >> 4) + 8 * (tid & 1);
    u16* mw1 = memL + ((ch1 >> 1) & 7) * MROW + 16 * (ch1 >> 4) + 8 * (ch1 & 1);
    const int col = c * 16 + c16;
    bf16x8 W[4][14];
#pragma unroll
    for (int hh = 0; hh < 4; ++hh) {
        const u16* cp = p.CmatT + ((long)(wid * 4 + hh) * DM + col) * DM + 8 * g;
#pragma unroll
        for (int ks = 0; ks < 14; ++ks) W[hh][ks] = *reinterpret_cast<const bf16x8*>(cp + 32 * ks);
    }
    u32x4 Dc[4], Dn[4], Uc;
    auto calc_D = [&](int s, auto&& hook) {
        const unsigned xa = xl_base + (s & 1) * (XIMG * 2) + lane * 16;
        u32x4 fa[14], fb[14];
        f32x4 d0[4], d1[4];
        wait_lgkm<0>();
#pragma unroll
        for (int ks = 0; ks < 3; ++ks) {
            lds_rd16o(fa[ks], xa, ks * 1024);
            lds_rd16o(fb[ks], xa, (14 + ks) * 1024);
        }
#pragma unroll
        for (int ks = 0; ks < 14; ++ks) {
            if (ks + 3 < 14) {
                lds_rd16o(fa[ks + 3], xa, (ks + 3) * 1024);
                lds_rd16o(fb[ks + 3], xa, (17 + ks) * 1024);
            }
#define WD(Q) case Q: wait_lgkm<2 * cmin(Q + 4, 14) - 2 * (Q + 1)>(); break;
            SW14(WD)
#undef WD
#pragma unroll
            for (int hh = 0; hh < 4; ++hh) {
                if (ks == 0) {
                    mfma_b0(d0[hh], fa[ks], W[hh][ks]);
                    mfma_b0(d1[hh], fb[ks], W[hh][ks]);
                } else {
                    mfma_b(d0[hh], fa[ks], W[hh][ks]);
                    mfma_b(d1[hh], fb[ks], W[hh][ks]);
                }
            }
            hook(ks);
        }
        asm volatile("s_nop 7"
                     : "+v"(d0[0]), "+v"(d0[1]), "+v"(d0[2]), "+v"(d0[3]), "+v"(d1[0]), "+v"(d1[1]), "+v"(d1[2]),
                       "+v"(d1[3]));
#pragma unroll
        for (int hh = 0; hh < 4; ++hh)
            Dn[hh] = u32x4{pack2(d0[hh][0], d0[hh][1]), pack2(d0[hh][2], d0[hh][3]), pack2(d1[hh][0], d1[hh][1]),
                           pack2(d1[hh][2], d1[hh][3])};
    };
    asm volatile("s_waitcnt vmcnt(0)" ::: "memory");
    __syncthreads();
    calc_D(0, [&](int) {});
    const int i8 = c16 & 7;
    const unsigned mfa = (unsigned)(uintptr_t)(LAS u16*)(memL + i8 * MROW + 8 * g);
    const unsigned pvoff = ((wid * 4) * 320 + i8 * 40 + 8 * g) * 2;
    const unsigned pvoffx = ((wid * 4 + g) * 320 + i8 * 40 + 32) * 2;
    const unsigned pxoff = ((wid * 4) * 1280 + c16 * 40 + 8 * g) * 2;
    const unsigned pxoffx = ((wid * 4 + g) * 1280 + c16 * 40 + 32) * 2;
    float* rwr = red + wid * 128 + (4 * (g & 1)) * 16 + c16;
    const float* rrd = red + ((lane >> 2) & 7) * 16 + 4 * (lane & 3);
    float* owr = ored + wid * 512 + (4 * g) * 16 + c16;
    const float* ord = ored + ((tid & 127) >> 2) * 16 + 4 * (tid & 3);
    u16* outp = p.out + (long)((tid & 127) >> 2) * 896 + 16 * c + 4 * (tid & 3);
    __amdgpu_buffer_rsrc_t rs1 = __builtin_amdgcn_make_buffer_rsrc(p.G1, 0, 63 * G1_SLOT * 2, 0x00020000);
    __amdgpu_buffer_rsrc_t rs2 = __builtin_amdgcn_make_buffer_rsrc(p.G2, 0, 64 * G2_SLOT * 2, 0x00020000);
    __amdgpu_buffer_rsrc_t rs3 = __builtin_amdgcn_make_buffer_rsrc(p.G3, 0, 64 * G3_SLOT * 2, 0x00020000);
    bool dead = false;
    u32x4 px[10], Up = u32x4{0u, 0u, 0u, 0u};
    Uc = Up;
#pragma unroll
    for (int hh = 0; hh < 4; ++hh) Dc[hh] = Up;
#pragma unroll
    for (int q = 0; q < 10; ++q) px[q] = Up;
    auto load_px1 = [&](int q, int s) {
        const int qt = q / 5, hh = q % 5;
        px[q] = ld16_sc1(rs3, (hh < 4 ? pxoff + hh * 2560 : pxoffx) + qt * 1280, (unsigned)(s * G3_SLOT * 2));
    };
    for (int s = 0;; ++s) {
        if (s < NSEG) {
            u32x4 a, b;
            for (unsigned spin = 0;; ++spin) {
                asm volatile("" ::: "memory");
                a = ld16_sc1(rs2, tid * 16, (unsigned)(s * G2_SLOT * 2));
                b = ld16_sc1(rs2, ch1 * 16, (unsigned)(s * G2_SLOT * 2));
                unsigned mx = max(umax3(a[1], a[3], b[1]), b[3]);
                if (__all(mx != 0xffffffffu) || dead) break;
                if (spin > SPIN_CAP) dead = true;
            }
            *reinterpret_cast<u32x4*>(mw0) = a;
            *reinterpret_cast<u32x4*>(mw1) = b;
        }
        __syncthreads();
        if (s < NSEG) {
            TS(1);
            f32x4 U[4];
            u32x4 mf[14];
            wait_lgkm<0>();
#pragma unroll
            for (int ks = 0; ks < 5; ++ks) lds_rd16o(mf[ks], mfa, ks * 64);
#pragma unroll
            for (int ks = 0; ks < 14; ++ks) {
                if (ks + 5 < 14) lds_rd16o(mf[ks + 5], mfa, (ks + 5) * 64);
#define WU(Q) case Q: wait_lgkm<cmin(Q + 6, 14) - (Q + 1)>(); break;
                SW14(WU)
#undef WU
#pragma unroll
                for (int hh = 0; hh < 4; ++hh) {
                    if (ks == 0) mfma_b0(U[hh], mf[ks], W[hh][ks]);
                    else mfma_b(U[hh], mf[ks], W[hh][ks]);
                }
            }
            asm volatile("s_nop 7" : "+v"(U[0]), "+v"(U[1]), "+v"(U[2]), "+v"(U[3]));
            unsigned pk[4][2];
#pragma unroll
            for (int hh = 0; hh < 4; ++hh) {
                pk[hh][0] = pack2(U[hh][0], U[hh][1]);
                pk[hh][1] = pack2(U[hh][2], U[hh][3]);
            }
            const bool g1 = g & 1, g2 = g & 2;
            unsigned e0 = g2 ? pk[2][0] : pk[0][0], e1 = g2 ? pk[2][1] : pk[0][1];
            unsigned o0 = g2 ? pk[3][0] : pk[1][0], o1 = g2 ? pk[3][1] : pk[1][1];
            unsigned m0 = g1 ? o0 : e0, m1 = g1 ? o1 : e1;
            unsigned t0 = g1 ? e0 : o0, t1 = g1 ? e1 : o1;
            unsigned r0 = (unsigned)__shfl_xor((int)t0, 16), r1 = (unsigned)__shfl_xor((int)t1, 16);
            Uc = g1 ? u32x4{r0, r1, m0, m1} : u32x4{m0, m1, r0, r1};
            TS(7);
        }
        if (s > 0) {
            for (unsigned spin = 0;; ++spin) {
                unsigned mx = 0;
#pragma unroll
                for (int q = 0; q < 10; ++q) mx = umax3(mx, px[q][1], px[q][3]);
                if (__all(mx != 0xffffffffu) || dead) break;
                if (spin > SPIN_CAP) dead = true;
                __builtin_amdgcn_s_sleep(4);
                asm volatile("" ::: "memory");
#pragma unroll
                for (int q = 0; q < 10; ++q) load_px1(q, s - 1);
            }
#pragma unroll
            for (int qt = 0; qt < 2; ++qt) {
                f32x4 o;
                chain5(o, px[qt * 5], px[qt * 5 + 1], px[qt * 5 + 2], px[qt * 5 + 3], px[qt * 5 + 4], Dc, Up);
#pragma unroll
                for (int r = 0; r < 4; ++r) owr[qt * 256 + r * 16] = o[r];
            }
        }
        if (s < NSEG - 1) {
            u32x4 pv[5];
#pragma unroll
            for (int q = 0; q < 5; ++q) pv[q] = u32x4{0u, 0u, 0u, 0u};
            TS(6);
            __builtin_amdgcn_s_sleep(SLP_C1);
            for (unsigned spin = 0;; ++spin) {
                asm volatile("" ::: "memory");
                unsigned mx = 0;
                if (c16 < 8) {
#pragma unroll
                    for (int hh = 0; hh < 4; ++hh) pv[hh] = ld16_sc1(rs1, pvoff + hh * 640, (unsigned)(s * G1_SLOT * 2));
                    pv[4] = ld16_sc1(rs1, pvoffx, (unsigned)(s * G1_SLOT * 2));
#pragma unroll
                    for (int q = 0; q < 5; ++q) mx = umax3(mx, pv[q][1], pv[q][3]);
                }
                if (__all(mx != 0xffffffffu) || dead) break;
                if (spin > SPIN_CAP) dead = true;
            }
            TS(2);
            f32x4 o0;
            chain5(o0, pv[0], pv[1], pv[2], pv[3], pv[4], Dn, Uc);
            if (g < 2) {
#pragma unroll
                for (int r = 0; r < 4; ++r) rwr[r * 16] = o0[r];
            }
        }
        __builtin_amdgcn_s_waitcnt(0x0F70);
        asm volatile("s_waitcnt vmcnt(0)" ::: "memory");
        __syncthreads();
        if (tid < 32 && s < NSEG - 1) {
            f32x4 x0 = *reinterpret_cast<const f32x4*>(rrd);
            f32x4 x1 = *reinterpret_cast<const f32x4*>(rrd + 128);
            f32x4 x2 = *reinterpret_cast<const f32x4*>(rrd + 256);
            f32x4 x3 = *reinterpret_cast<const f32x4*>(rrd + 384);
            f32x4 a = (x0 + x1) + (x2 + x3);
            st_gran8(rs2, (128 * c + 4 * (lane & 31)) * 2, (unsigned)((s + 1) * G2_SLOT * 2), !(lane & 1), pack2(a[0], a[1]),
                     pack2(a[2], a[3]));
        }
        if (tid >= 128 && s > 0) {
            f32x4 x0 = *reinterpret_cast<const f32x4*>(ord);
            f32x4 x1 = *reinterpret_cast<const f32x4*>(ord + 512);
            f32x4 x2 = *reinterpret_cast<const f32x4*>(ord + 1024);
            f32x4 x3 = *reinterpret_cast<const f32x4*>(ord + 1536);
            f32x4 a = (x0 + x1) + (x2 + x3);
            *reinterpret_cast<u32x2*>(outp + (long)(s - 1) * (SEG * 896)) = u32x2{pack2(a[0], a[1]), pack2(a[2], a[3])};
        }
        if (s == NSEG) break;
        TS(3);
        asm volatile("" ::: "memory");
#pragma unroll
        for (int hh = 0; hh < 4; ++hh) Dc[hh] = Dn[hh];
        Up = Uc;
        if (s + 1 < NSEG) {
            const bool more = s + 2 < NSEG;
            const char* gb = reinterpret_cast<const char*>(p.x) + (long)(s + 2) * (SEG * DM * 2) + wid * 7168;
            const unsigned xl = xl_base + (s & 1) * (XIMG * 2) + (unsigned)(wid * 7) * 1024u;
            calc_D(s + 1, [&](int ks) {
                if (ks < 7 && more) dma16(gb + 1024 * ks, xvoff, xl + (unsigned)ks * 1024u);
                if (ks >= 4) load_px1(ks - 4, s);
            });
        } else {
#pragma unroll
            for (int q = 0; q < 10; ++q) px[q] = u32x4{~0u, ~0u, ~0u, ~0u};
        }
        TS(5);
    }
}

template <int BM, int BN, bool NT, bool SC1>
__device__ __forceinline__ void gemm_tile(const u16* __restrict__ A, int lda, const u16* __restrict__ B, int ldb,
                                          u16* __restrict__ C, int ldc, int nk, u16* smem, int ws) {
    constexpr int AROW = 72, BROW = NT ? 72 : BN + 32;
    constexpr int ASZ = BM * AROW, STG = ASZ + (NT ? BN * 72 : 64 * BROW);
    constexpr int ACH = BM / 32, BCH = BN / 32, TM = BM / 64, TN = BN / 64;
    constexpr int BPR = BN / 8;
    constexpr int BRS = 256 / BPR;
    const int tid = tid_of(ws);
    const int lane = tid & 63, wid = ws;
    const int wm = wid >> 1, wn = wid & 1;
    const int r32 = lane & 31, hi = lane >> 5, g = lane >> 4, q4 = (lane & 15) >> 2, p4 = lane & 3;
    f32x16 acc[TM][TN];
#pragma unroll
    for (int i = 0; i < TM; ++i)
#pragma unroll
        for (int j = 0; j < TN; ++j) acc[i][j] = f32x16{};
    u32x4 ra0[ACH], rb0[BCH], ra1[ACH], rb1[BCH];
    const unsigned aoff = ((unsigned)(tid >> 3) * lda + (tid & 7) * 8) * 2u;
    const unsigned boff = NT ? ((unsigned)(tid >> 3) * ldb + (tid & 7) * 8) * 2u
                             : ((unsigned)(tid / BPR) * ldb + (tid % BPR) * 8) * 2u;
    const char* Ac = reinterpret_cast<const char*>(A);
    const char* Bc = reinterpret_cast<const char*>(B);
    u16* aw = smem + (tid >> 3) * AROW + (tid & 7) * 8;
    u16* bw = NT ? (smem + ASZ + (tid >> 3) * BROW + (tid & 7) * 8) : (smem + ASZ + (tid / BPR) * BROW + (tid % BPR) * 8);
    const u16* ard = smem + (wm * (BM / 2) + r32) * AROW + hi * 8;
    const u16* brd = NT ? (smem + ASZ + (wn * (BN / 2) + r32) * BROW + hi * 8)
                        : (smem + ASZ + (8 * (g >> 1) + q4) * BROW + wn * (BN / 2) + 16 * (g & 1) + 4 * p4);
    auto load = [&](u32x4(&ra)[ACH], u32x4(&rb)[BCH], int kt) {
#pragma unroll
        for (int i = 0; i < ACH; ++i)
            ra[i] = *reinterpret_cast<const u32x4*>(Ac + (aoff + (unsigned)(32 * i * lda + kt * 64) * 2u));
#pragma unroll
        for (int i = 0; i < BCH; ++i) {
            if (NT)
                rb[i] = *reinterpret_cast<const u32x4*>(Bc + (boff + (unsigned)(32 * i * ldb + kt * 64) * 2u));
            else
                rb[i] = *reinterpret_cast<const u32x4*>(Bc + (boff + (unsigned)((kt * 64 + BRS * i) * ldb) * 2u));
        }
    };
    auto store = [&](const u32x4(&ra)[ACH], const u32x4(&rb)[BCH], int st) {
#pragma unroll
        for (int i = 0; i < ACH; ++i) *reinterpret_cast<u32x4*>(aw + st * STG + 32 * i * AROW) = ra[i];
#pragma unroll
        for (int i = 0; i < BCH; ++i)
            *reinterpret_cast<u32x4*>(bw + st * STG + (NT ? 32 : BRS) * i * BROW) = rb[i];
    };
    auto compute = [&](int st, int ks) {
        bf16x8 af[TM], bfr[TN];
#pragma unroll
        for (int i = 0; i < TM; ++i)
            af[i] = *reinterpret_cast<const bf16x8*>(ard + st * STG + i * 32 * AROW + ks * 16);
#pragma unroll
        for (int j = 0; j < TN; ++j) {
            if (NT) {
                bfr[j] = *reinterpret_cast<const bf16x8*>(brd + st * STG + j * 32 * BROW + ks * 16);
            } else {
                const u16* pB = brd + st * STG + ks * 16 * BROW + j * 32;
                s16x4 lo = tr_read(pB);
                s16x4 hi4 = tr_read(pB + 4 * BROW);
                bfr[j] = bf16x8{lo[0], lo[1], lo[2], lo[3], hi4[0], hi4[1], hi4[2], hi4[3]};
            }
        }
#pragma unroll
        for (int i = 0; i < TM; ++i)
#pragma unroll
            for (int j = 0; j < TN; ++j) acc[i][j] = MFMA32(bfr[j], af[i], acc[i][j]);
    };
    load(ra0, rb0, 0);
    load(ra1, rb1, 1);
    __syncthreads();
    store(ra0, rb0, 0);
    load(ra0, rb0, 2);
    __syncthreads();
    for (int kt = 0; kt + 1 < nk; kt += 2) {
        compute(0, 0);
        compute(0, 1);
        store(ra1, rb1, 1);
        load(ra1, rb1, min(kt + 3, nk - 1));
        compute(0, 2);
        compute(0, 3);
        __syncthreads();
        compute(1, 0);
        compute(1, 1);
        store(ra0, rb0, 0);
        load(ra0, rb0, min(kt + 4, nk - 1));
        compute(1, 2);
        compute(1, 3);
        __syncthreads();
    }
    if (nk & 1) {
        compute(0, 0);
        compute(0, 1);
        compute(0, 2);
        compute(0, 3);
        __syncthreads();
    }
    constexpr int CROW = BN + 8;
#pragma unroll
    for (int i = 0; i < TM; ++i)
#pragma unroll
        for (int j = 0; j < TN; ++j)
#pragma unroll
            for (int rr = 0; rr < 4; ++rr) {
                u32x2 w;
                w[0] = pack2(acc[i][j][4 * rr], acc[i][j][4 * rr + 1]);
                w[1] = pack2(acc[i][j][4 * rr + 2], acc[i][j][4 * rr + 3]);
                *reinterpret_cast<u32x2*>(smem + (wm * (BM / 2) + 32 * i + r32) * CROW + wn * (BN / 2) + 32 * j +
                                          8 * rr + 4 * hi) = w;
            }
    __syncthreads();
    const int tid2 = tid_of(ws);
    const unsigned coff = ((unsigned)(tid2 / BPR) * ldc + (tid2 % BPR) * 8) * 2u;
    __amdgpu_buffer_rsrc_t rs = __builtin_amdgcn_make_buffer_rsrc(C, 0, 0x7fffffff, 0x00020000);
#pragma unroll
    for (int i = 0; i < BM * BN / 2048; ++i) {
        int row = tid2 / BPR + BRS * i, cc = tid2 % BPR;
        u32x4 v = *reinterpret_cast<const u32x4*>(smem + row * CROW + cc * 8);
        __builtin_amdgcn_raw_buffer_store_b128(v, rs, coff + (unsigned)(BRS * i * ldc) * 2u, 0, SC1 ? 16 : 0);
    }
}

struct MegaParams {
    const u16* B_x;
    const u16* B_ckv;
    const u16* B_wq;
    const u16* B_wuk;
    const u16* B_wuv;
    const u16* B_wo;
    u16* Qb;
    u16* Kb;
    u16* Vb;
    u16* OB;
    u16* out;
    unsigned* ctl;
};
constexpr int NWORK = 256 - NREC;
constexpr int CTL_QA = 0, CTL_KV = 1024, CTL_OC = 1280, CTL_WORDS = 1408;
#define IDLE_SLP 32

__device__ __forceinline__ int next_ticket(unsigned* q, int* slot, int ws) {
    __syncthreads();
    if (tid_of(ws) == 0) *slot = (int)(__hip_atomic_fetch_add(q, 1u, __ATOMIC_RELAXED, __HIP_MEMORY_SCOPE_AGENT) + 1u);
    __syncthreads();
    return *slot;
}
__device__ __forceinline__ void publish_count(unsigned* c, int ws) {
    asm volatile("s_waitcnt vmcnt(0)" ::: "memory");
    __syncthreads();
    if (tid_of(ws) == 0) __hip_atomic_fetch_add(c, 1u, __ATOMIC_RELAXED, __HIP_MEMORY_SCOPE_AGENT);
}
__device__ __forceinline__ void wait_counts(unsigned* c, int n, unsigned target, int ws) {
    if (ws == 0) {
        const int lane = tid_of(0);
        for (unsigned spin = 0; spin < WSPIN_CAP; ++spin) {
            unsigned v = __hip_atomic_load(c + (lane < n ? lane : 0), __ATOMIC_RELAXED, __HIP_MEMORY_SCOPE_AGENT) + 1u;
            if (__all(v >= target)) break;
            __builtin_amdgcn_s_sleep(32);
        }
        acquire_agent();
    }
    __syncthreads();
}

__device__ __forceinline__ void worker_body(const MegaParams& m, u16* smem, int* slot, int ws) {
    unsigned* ctl = m.ctl;
    WTS(0);
    const int myx = (int)(__builtin_amdgcn_s_getreg((3 << 11) | 20) & 7u);
    for (int k = 0; k < 8; ++k) {
        const int x = (myx + k) & 7;
        for (;;) {
            int t = next_ticket(ctl + CTL_QA + 64 * x, slot, ws);
            if (t >= 128) break;
            KT0
            int at = -1, gt;
            if (t < 28) gt = t;
            else if (t < 32) at = t - 28;
            else if (t < 100) gt = t - 4;
            else at = t - 96;
            if (at >= 0) {
                const int qb = 15 - (at >> 1), h = 2 * x + (at & 1);
                attnB_body(m.Qb, m.Kb, m.Vb, m.OB, smem, qb, h, ws, ctl + CTL_KV + 16 * h, slot);
                publish_count(ctl + CTL_OC + qb, ws);
                KT1(2);
                continue;
            }
            int rb, mat;
            if (gt < 4) { rb = 7; mat = 2; }
            else if (gt < 68) { rb = (gt - 4) >> 3; mat = ((gt - 4) >> 2) & 1; }
            else { rb = 6 - ((gt - 68) >> 2); mat = 2; }
            const int cb = 4 * x + (gt & 3);
            const int lda = (mat < 2) ? DC : DM;
            const u16* A = ((mat < 2) ? m.B_ckv : m.B_x) + (long)rb * 256 * lda;
            const u16* B = ((mat == 0) ? m.B_wuk : (mat == 1 ? m.B_wuv : m.B_wq)) + cb * 128;
            u16* C = ((mat == 0) ? m.Kb : (mat == 1 ? m.Vb : m.Qb)) + (long)rb * 256 * HD + cb * 128;
            gemm_tile<256, 128, false, true>(A, lda, B, HD, C, HD, lda / 64, smem, ws);
            publish_count(ctl + CTL_KV + 16 * (cb >> 1) + (mat == 2 ? 8 : 0) + rb, ws);
            KT1(lda == DC ? 1 : 4);
        }
    }
    WTS(1);
    if (blockIdx.x >= 144) {
        const int b = blockIdx.x, x = b & 7, otile = (b - 144) >> 3;
        const int orb = 2 * x + (otile >= 7 ? 1 : 0), cb = otile >= 7 ? otile - 7 : otile;
        wait_counts(ctl + CTL_OC + orb, 1, 16u, ws);
        KT0
        gemm_tile<128, 64, false, false>(m.OB + (long)orb * 128 * HD, HD, m.B_wo + cb * 64, DM,
                                         m.out + (long)orb * 128 * 896 + 448 + cb * 64, 896, HD / 64, smem, ws);
        KT1(9);
        WTS(2);
    }
    WTS(6);
    WTS(7);
}

constexpr int GEMM_LDS = 2 * (256 * 72 + 64 * 160) * 2;
constexpr int cmax(int a, int b) { return a > b ? a : b; }
constexpr int FUSED_LDS = cmax(cmax(ATTN_LDS, RECA_LDS), cmax(GEMM_LDS, RECC_LDS));
static_assert(AL_MEM * 2 + 8 * MROW * 2 <= RECA_LDS, "lds");

__global__ __launch_bounds__(256) void mega_kernel(RecParams p, MegaParams m) {
    __shared__ __attribute__((aligned(16))) u16 smem[FUSED_LDS / 2 + 8];
    const int b = blockIdx.x, ws = __builtin_amdgcn_readfirstlane(threadIdx.x >> 6);
    if (b < NA_BLK) {
        rec_A_body<false>(p, smem, b, ws);
    } else if (b < NA_BLK + NC_BLK) {
        rec_C_body(p, smem, b - NA_BLK, ws);
    } else if (b < NREC) {
        rec_A_body<true>(p, smem, b - NA_BLK - NC_BLK, ws);
    } else {
        worker_body(m, smem, reinterpret_cast<int*>(smem + FUSED_LDS / 2), ws);
    }
}

extern "C" void kernel_launch(void* const* d_in, const int* in_sizes, int n_in, void* d_out, int out_size,
                              void* d_ws, size_t ws_size, hipStream_t stream) {
    const u16* A_x = (const u16*)d_in[0];
    const u16* A_mem0 = (const u16*)d_in[1];
    const u16* A_Wq = (const u16*)d_in[2];
    const u16* A_Wk = (const u16*)d_in[3];
    const u16* A_Wv = (const u16*)d_in[4];
    const u16* A_Wo = (const u16*)d_in[5];
    const u16* B_x = (const u16*)d_in[7];
    const u16* B_ckv = (const u16*)d_in[8];
    const u16* B_wq = (const u16*)d_in[9];
    const u16* B_wuk = (const u16*)d_in[10];
    const u16* B_wuv = (const u16*)d_in[11];
    const u16* B_wo = (const u16*)d_in[12];
    u16* out = (u16*)d_out;

    char* ws = (char*)d_ws;
    const size_t SZ = (size_t)S_ * HD * 2;
    u16* Qb = (u16*)(ws + 0 * SZ);
    u16* Kb = (u16*)(ws + 1 * SZ);
    u16* Vb = (u16*)(ws + 2 * SZ);
    u16* OB = (u16*)(ws + 3 * SZ);
    size_t off = 4 * SZ;
    u16* AmatT = (u16*)(ws + off); off += (size_t)H_ * DM * DM * 2;
    u16* CmatT = (u16*)(ws + off); off += (size_t)H_ * DM * DM * 2;
    u16* XP = (u16*)(ws + off); off += (size_t)S_ * DM * 2;
    char* gbase = ws + off;
    u16* G1 = (u16*)gbase;
    u16* G2 = G1 + 63 * G1_SLOT;
    u16* G3 = G2 + 64 * G2_SLOT;
    unsigned* ctl = (unsigned*)(G3 + 64 * G3_SLOT);
    size_t gbytes = (size_t)(63 * G1_SLOT + 64 * G2_SLOT + 64 * G3_SLOT) * 2 + (size_t)CTL_WORDS * 4;

    hipLaunchKernelGGL(pre_kernel, dim3(224), dim3(256), 0, stream, A_Wq, A_Wk, A_Wv, A_Wo, AmatT, CmatT, (u32x4*)gbase,
                       (int)(gbytes / 16), A_x, (u32x4*)XP);
    RecParams rp;
    rp.x = XP; rp.mem0 = A_mem0; rp.AmatT = AmatT; rp.CmatT = CmatT; rp.G1 = G1; rp.G2 = G2; rp.G3 = G3; rp.out = out;
    MegaParams mp;
    mp.B_x = B_x; mp.B_ckv = B_ckv; mp.B_wq = B_wq; mp.B_wuk = B_wuk; mp.B_wuv = B_wuv; mp.B_wo = B_wo;
    mp.Qb = Qb; mp.Kb = Kb; mp.Vb = Vb; mp.OB = OB;
    mp.out = out; mp.ctl = ctl;
    hipLaunchKernelGGL(mega_kernel, dim3(256), dim3(256), 0, stream, rp, mp);
}
```

```cpp
#include <hip/hip_runtime.h>
#include <hip/hip_bf16.h>
#include <stdint.h>

typedef unsigned short u16;
typedef __attribute__((ext_vector_type(8))) short bf16x8;
typedef __attribute__((ext_vector_type(4))) short s16x4;
typedef __attribute__((ext_vector_type(16))) float f32x16;
typedef __attribute__((ext_vector_type(4))) float f32x4;
typedef __attribute__((ext_vector_type(4))) unsigned u32x4;
typedef __attribute__((ext_vector_type(2))) unsigned u32x2;
typedef short v4i16_t __attribute__((ext_vector_type(4)));

#define LAS __attribute__((address_space(3)))

__device__ __forceinline__ float bf2f(u16 x) { return __uint_as_float(((unsigned)x) << 16); }
__device__ __forceinline__ u16 f2bf(float f) {
    unsigned u = __float_as_uint(f);
    u += 0x7fffu + ((u >> 16) & 1u);
    return (u16)(u >> 16);
}
typedef __bf16 bf16x2_t __attribute__((ext_vector_type(2)));
__device__ __forceinline__ unsigned pack2(float lo, float hi) {
    bf16x2_t r = {(__bf16)lo, (__bf16)hi};
    return __builtin_bit_cast(unsigned, r);
}
__device__ __forceinline__ s16x4 tr_read(const void* p) {
    return __builtin_bit_cast(s16x4, __builtin_amdgcn_ds_read_tr16_b64_v4i16((LAS v4i16_t*)p));
}
#define MFMA32(a, b, c) __builtin_amdgcn_mfma_f32_32x32x16_bf16(a, b, c, 0, 0, 0)
#define MFMA16(a, b, c) __builtin_amdgcn_mfma_f32_16x16x32_bf16(a, b, c, 0, 0, 0)

__device__ __forceinline__ int tid_of(int ws) {
    int l = (int)__builtin_amdgcn_mbcnt_hi(~0u, __builtin_amdgcn_mbcnt_lo(~0u, 0u));
    asm volatile("" : "+v"(l));
    return ws * 64 + l;
}
__device__ __forceinline__ int crow(int r, int hi) { return (r & 3) + 8 * (r >> 2) + 4 * hi; }

constexpr int S_ = 2048, DM = 448, H_ = 16, DH = 256, HD = 4096, MM = 8, SEG = 32, NSEG = 64, DC = 1024;

__global__ __launch_bounds__(256) void pre_kernel(const u16* __restrict__ Wq, const u16* __restrict__ Wk,
                                                  const u16* __restrict__ Wv, const u16* __restrict__ Wo,
                                                  u16* __restrict__ AmatT, u16* __restrict__ CmatT,
                                                  u32x4* __restrict__ gbase, int gchunks,
                                                  const u16* __restrict__ x, u32x4* __restrict__ xp) {
    __shared__ __attribute__((aligned(16))) u16 Bs[256 * 96];
    const int tid = threadIdx.x, lane = tid & 63, wid = tid >> 6;
    const int c16 = lane & 15, g = lane >> 4, q4 = c16 >> 2, p4 = lane & 3;
    const int br = blockIdx.x >> 3, rb = br % 7, hz = (blockIdx.x & 7) + 8 * (br / 7), h = hz & 15;
    const bool isC = hz >= 16;
    u32x4 rbv[8];
#pragma unroll
    for (int i = 0; i < 8; ++i) {
        int c = tid + 256 * i;
        const u16* src = isC ? Wo + (long)(h * DH + (c >> 3)) * DM + rb * 64 + (c & 7) * 8
                             : Wk + (long)(rb * 64 + (c >> 5)) * HD + h * DH + (c & 31) * 8;
        rbv[i] = *reinterpret_cast<const u32x4*>(src);
    }
    const u16* ap = (isC ? Wv : Wq) + (long)(wid * 112 + c16) * HD + h * DH + 8 * g;
    bf16x8 af[7][8];
#pragma unroll
    for (int ks = 0; ks < 8; ++ks)
#pragma unroll
        for (int t = 0; t < 7; ++t) af[t][ks] = *reinterpret_cast<const bf16x8*>(ap + (long)t * 16 * HD + 32 * ks);
    for (int i = blockIdx.x * 256 + tid; i < gchunks; i += 224 * 256)
        gbase[i] = u32x4{0xffffffffu, 0xffffffffu, 0xffffffffu, 0xffffffffu};
#pragma unroll
    for (int i = 0; i < 2; ++i) {
        const int fr = (blockIdx.x * 2 + i) * 4 + wid, rt = fr / 14, kg = fr - rt * 14;
        xp[fr * 64 + lane] = *reinterpret_cast<const u32x4*>(x + (long)(rt * 16 + c16) * DM + 32 * kg + 8 * g);
    }
#pragma unroll
    for (int i = 0; i < 8; ++i) {
        int c = tid + 256 * i;
        u16* dst = isC ? Bs + (c >> 3) * 96 + (c & 7) * 8 : Bs + (c >> 5) * 264 + (c & 31) * 8;
        *reinterpret_cast<u32x4*>(dst) = rbv[i];
    }
    __syncthreads();
    f32x4 acc[7][4];
#pragma unroll
    for (int t = 0; t < 7; ++t)
#pragma unroll
        for (int j = 0; j < 4; ++j) acc[t][j] = f32x4{0.f, 0.f, 0.f, 0.f};
#pragma unroll
    for (int ks = 0; ks < 8; ++ks) {
        bf16x8 bfr[4];
#pragma unroll
        for (int j = 0; j < 4; ++j) {
            if (isC) {
                const u16* pB = Bs + (ks * 32 + 8 * g + q4) * 96 + j * 16 + 4 * p4;
                s16x4 lo = tr_read(pB);
                s16x4 hi4 = tr_read(pB + 4 * 96);
                bfr[j] = bf16x8{lo[0], lo[1], lo[2], lo[3], hi4[0], hi4[1], hi4[2], hi4[3]};
            } else {
                bfr[j] = *reinterpret_cast<const bf16x8*>(Bs + (16 * j + c16) * 264 + ks * 32 + 8 * g);
            }
        }
#pragma unroll
        for (int t = 0; t < 7; ++t)
#pragma unroll
            for (int j = 0; j < 4; ++j) acc[t][j] = MFMA16(af[t][ks], bfr[j], acc[t][j]);
    }
    const float sc = isC ? 1.f : 0.0625f;
    u16* op = (isC ? CmatT : AmatT) + (long)(h * DM + rb * 64 + c16) * DM + wid * 112 + 4 * g;
#pragma unroll
    for (int j = 0; j < 4; ++j)
#pragma unroll
        for (int t = 0; t < 7; ++t) {
            u32x2 w;
            w[0] = pack2(acc[t][j][0] * sc, acc[t][j][1] * sc);
            w[1] = pack2(acc[t][j][2] * sc, acc[t][j][3] * sc);
            *reinterpret_cast<u32x2*>(op + j * 16 * DM + 16 * t) = w;
        }
}

constexpr unsigned WSPIN_CAP = 2000000u;
__device__ __forceinline__ void acquire_agent() {
    __builtin_amdgcn_fence(__ATOMIC_ACQUIRE, "agent");
    asm volatile("s_waitcnt vmcnt(0)" ::: "memory");
}
__device__ __forceinline__ int wait_kv(const unsigned* c, int qr, int need) {
    const int lane = tid_of(0);
    int ready = 8;
    for (unsigned spin = 0; spin < WSPIN_CAP; ++spin) {
        unsigned v = __hip_atomic_load(c + (lane & 15), __ATOMIC_RELAXED, __HIP_MEMORY_SCOPE_AGENT) + 1u;
        unsigned long long ok = __ballot(v >= ((lane & 8) ? 2u : 4u));
        ready = __builtin_ctz(~(unsigned)ok | 256u);
        if (ready >= need && ((ok >> (8 + qr)) & 1)) break;
        __builtin_amdgcn_s_sleep(32);
    }
    acquire_agent();
    return ready;
}

#define ATT_THR 16.0f
constexpr int ATTN_LDS = 4 * 32768;
__device__ __forceinline__ void attnB_body(u16* qkvo, u16* smem, int qb, int h, int ws, const unsigned* cnt) {
    const int lane = tid_of(0);
    if (ws == 0) wait_kv(cnt, qb >> 1, (qb >> 1) + 1);
    __syncthreads();
    __amdgpu_buffer_rsrc_t rs = __builtin_amdgcn_make_buffer_rsrc(qkvo, 0, 4 * S_ * HD * 2, 0x00020000);
    const unsigned lds0 = (unsigned)(uintptr_t)(LAS u16*)smem;
    const unsigned q32 = qb * 4 + ws;
    attn_asm(rs, (unsigned)(h << 20) + q32 * 16384u, 0x1000000u + (unsigned)(h << 20) + ws * 8192u,
             0x3000000u + q32 * 262144u + h * 16384u, 2 * qb + 2, lds0, lds0 + ws * 8192u, __float_as_uint(ATT_THR),
             (unsigned)lane * 16u, ws * 32 + (lane & 31) - 4 * (lane >> 5));
}

typedef unsigned long long u64;
__device__ __forceinline__ u64 ld_gran(const u64* p) {
    return __hip_atomic_load(p, __ATOMIC_RELAXED, __HIP_MEMORY_SCOPE_AGENT);
}
__device__ __forceinline__ void st_gran4(u16* p, unsigned lo, unsigned hi) {
    hi = min(hi, 0xfffffffeu);
    __hip_atomic_store(reinterpret_cast<u64*>(p), ((u64)hi << 32) | (u64)lo, __ATOMIC_RELAXED,
                       __HIP_MEMORY_SCOPE_AGENT);
}
__device__ __forceinline__ void st_gran8(__amdgpu_buffer_rsrc_t rs, unsigned voff, unsigned soff, bool act, unsigned lo,
                                         unsigned hi) {
    hi = min(hi, 0xfffffffeu);
    unsigned lo2 = (unsigned)__builtin_amdgcn_mov_dpp((int)lo, 0xF5, 0xf, 0xf, true);
    unsigned hi2 = (unsigned)__builtin_amdgcn_mov_dpp((int)hi, 0xF5, 0xf, 0xf, true);
    if (act) __builtin_amdgcn_raw_buffer_store_b128(u32x4{lo, hi, lo2, hi2}, rs, voff, soff, 16);
}
__device__ __forceinline__ void st_gran8x2(__amdgpu_buffer_rsrc_t rs, unsigned voff, unsigned soff, unsigned cp, bool act,
                                           unsigned lo, unsigned hi) {
    hi = min(hi, 0xfffffffeu);
    unsigned lo2 = (unsigned)__builtin_amdgcn_mov_dpp((int)lo, 0xF5, 0xf, 0xf, true);
    unsigned hi2 = (unsigned)__builtin_amdgcn_mov_dpp((int)hi, 0xF5, 0xf, 0xf, true);
    if (act) {
        __builtin_amdgcn_raw_buffer_store_b128(u32x4{lo, hi, lo2, hi2}, rs, voff, soff, 16);
        __builtin_amdgcn_raw_buffer_store_b128(u32x4{lo, hi, lo2, hi2}, rs, voff, soff + cp, 16);
    }
}
constexpr int NA_BLK = 16, NB_BLK = 32, NC_BLK = 28, NO_BLK = 14, ND_BLK = 28;
constexpr int NREC = NA_BLK + NB_BLK + NC_BLK + NO_BLK + ND_BLK;
constexpr int MROW = 456;
constexpr int G1_CP = 16 * 8 * 40;
constexpr int G2_CP = 8 * 448;
constexpr int NCP1 = 4, NCP2 = 4;
constexpr int G1_SLOT = NCP1 * G1_CP, G2_SLOT = NCP2 * G2_CP;
constexpr int G3_SLOT = 16 * 32 * 40;
constexpr int XIMG = 28 * 512;
constexpr unsigned SPIN_CAP = 400000u;
#define SLP_A 45
#define SLP_C1 25
#define SLP_C0 2
#define SLP_C2 20

#define WTS(slot)
#define KT0
#define KT1(k)
#define TS(slot)
struct RecParams {
    const u16* x;
    const u16* mem0;
    const u16* AmatT;
    const u16* CmatT;
    u16* G1;
    u16* G2;
    u16* G3;
    u16* out;
    u16* Dg;
    u16* Ug;
    unsigned* dflag;
};

__device__ __forceinline__ float red16_max(float x) {
    asm volatile(
        "s_nop 1\n\tv_max_f32_dpp %0, %0, %0 quad_perm:[1,0,3,2] row_mask:0xf bank_mask:0xf\n\t"
        "s_nop 1\n\tv_max_f32_dpp %0, %0, %0 quad_perm:[2,3,0,1] row_mask:0xf bank_mask:0xf\n\t"
        "s_nop 1\n\tv_max_f32_dpp %0, %0, %0 row_half_mirror row_mask:0xf bank_mask:0xf\n\t"
        "s_nop 1\n\tv_max_f32_dpp %0, %0, %0 row_mirror row_mask:0xf bank_mask:0xf\n\t"
        "s_nop 1"
        : "+v"(x));
    return x;
}
__device__ __forceinline__ float red16_sum(float x) {
    asm volatile(
        "s_nop 1\n\tv_add_f32_dpp %0, %0, %0 quad_perm:[1,0,3,2] row_mask:0xf bank_mask:0xf\n\t"
        "s_nop 1\n\tv_add_f32_dpp %0, %0, %0 quad_perm:[2,3,0,1] row_mask:0xf bank_mask:0xf\n\t"
        "s_nop 1\n\tv_add_f32_dpp %0, %0, %0 row_half_mirror row_mask:0xf bank_mask:0xf\n\t"
        "s_nop 1\n\tv_add_f32_dpp %0, %0, %0 row_mirror row_mask:0xf bank_mask:0xf\n\t"
        "s_nop 1"
        : "+v"(x));
    return x;
}
__device__ __forceinline__ u32x4 ld16_sc1(__amdgpu_buffer_rsrc_t rs, unsigned voff, unsigned soff) {
    return __builtin_amdgcn_raw_buffer_load_b128(rs, voff, soff, 16);
}
__device__ __forceinline__ unsigned umax3(unsigned a, unsigned b, unsigned c) { return max(max(a, b), c); }

__device__ __forceinline__ void mfma_a0(f32x4& d, const bf16x8& w, const u32x4& m) {
    asm volatile("v_mfma_f32_16x16x32_bf16 %0, %1, %2, 0" : "=&v"(d) : "a"(w), "v"(m));
}
__device__ __forceinline__ void mfma_a(f32x4& d, const bf16x8& w, const u32x4& m) {
    asm volatile("v_mfma_f32_16x16x32_bf16 %0, %1, %2, %0" : "+v"(d) : "a"(w), "v"(m));
}
__device__ __forceinline__ void mfma_v0(f32x4& d, const bf16x8& w, const u32x4& m) {
    asm volatile("v_mfma_f32_16x16x32_bf16 %0, %1, %2, 0" : "=&v"(d) : "v"(w), "v"(m));
}
__device__ __forceinline__ void mfma_v(f32x4& d, const bf16x8& w, const u32x4& m) {
    asm volatile("v_mfma_f32_16x16x32_bf16 %0, %1, %2, %0" : "+v"(d) : "v"(w), "v"(m));
}
__device__ __forceinline__ void lds_rd16(bf16x8& d, unsigned addr, int off) {
    asm volatile("ds_read_b128 %0, %1 offset:%2" : "=v"(d) : "v"(addr), "n"(off));
}
__device__ __forceinline__ void lds_rd16u(u32x4& d, unsigned addr) {
    asm volatile("ds_read_b128 %0, %1" : "=v"(d) : "v"(addr));
}
template <int N>
__device__ __forceinline__ void wait_lgkm() {
    asm volatile("s_waitcnt lgkmcnt(%0)" ::"n"(N) : "memory");
}
constexpr int sch_n(int q) { return q <= 8 ? 7 + 2 * (q + 1) : (q <= 11 ? 25 + (q - 8) : 28); }
constexpr int sch_m(int i) { return i < 5 ? 2 + i : 7 + 2 * (i - 5); }
constexpr int sch_w(int i) { return i < 2 ? i : (i - 2 <= 8 ? 8 + 2 * (i - 2) : 25 + (i - 11)); }

__device__ __forceinline__ void dma16(const void* gbase, unsigned voff, unsigned lds) {
    unsigned keep;
    asm volatile("s_mov_b32 %0, m0\n\ts_mov_b32 m0, %3\n\ts_nop 0\n\tglobal_load_lds_dwordx4 %1, %2\n\ts_mov_b32 m0, %0"
                 : "=&s"(keep)
                 : "v"(voff), "s"(gbase), "s"(lds)
                 : "memory");
}
__device__ __forceinline__ void dma_x(const u16* x, int s, unsigned xvoff, unsigned lds, int wid) {
    const char* gb = reinterpret_cast<const char*>(x) + (long)s * (SEG * DM * 2) + wid * 7168;
#pragma unroll
    for (int j = 0; j < 7; ++j) dma16(gb + 1024 * j, xvoff, lds + (unsigned)(wid * 7 + j) * 1024u);
}

__device__ __forceinline__ void lds_rd16s(u32x4& d, unsigned addr, unsigned soff) {
    unsigned t;
    asm volatile("v_add_u32 %1, %3, %2\n\tds_read_b128 %0, %1" : "=v"(d), "=&v"(t) : "v"(addr), "s"(soff));
}
__device__ __forceinline__ void tpass(const bf16x8 (&W)[6][14], unsigned wl_addr, unsigned fa, const unsigned (&so)[14],
                                      u32x4 (&mf)[14], bf16x8 (&w6)[3], f32x4 (&acc)[7]) {
#pragma unroll
    for (int q = 0; q < 5; ++q) lds_rd16s(mf[q], fa, so[q]);
#pragma unroll
    for (int q = 0; q < 14; ++q) {
        if (q + 5 < 14) lds_rd16s(mf[q + 5], fa, so[q + 5]);
        if (q + 2 < 14) lds_rd16(w6[(q + 2) % 3], wl_addr, (q + 2) * 1024);
        switch (q) {
#define WM(Q) case Q: wait_lgkm<sch_n(Q) - sch_m(Q) - 1>(); break;
            WM(0) WM(1) WM(2) WM(3) WM(4) WM(5) WM(6) WM(7) WM(8) WM(9) WM(10) WM(11) WM(12) WM(13)
#undef WM
        }
#pragma unroll
        for (int t = 0; t < 4; ++t) {
            if (q == 0) mfma_a0(acc[t], W[t][q], mf[q]);
            else mfma_a(acc[t], W[t][q], mf[q]);
        }
#pragma unroll
        for (int t = 4; t < 6; ++t) {
            if (t == 4 && q < 6) {
                if (q == 0) mfma_a0(acc[t], W[t][q], mf[q]);
                else mfma_a(acc[t], W[t][q], mf[q]);
            } else {
                if (q == 0) mfma_v0(acc[t], W[t][q], mf[q]);
                else mfma_v(acc[t], W[t][q], mf[q]);
            }
        }
        switch (q) {
#define WW(Q) case Q: wait_lgkm<sch_n(Q) - sch_w(Q) - 1>(); break;
            WW(0) WW(1) WW(2) WW(3) WW(4) WW(5) WW(6) WW(7) WW(8) WW(9) WW(10) WW(11) WW(12) WW(13)
#undef WW
        }
        if (q == 0) mfma_v0(acc[6], w6[0], mf[q]);
        else mfma_v(acc[6], w6[q % 3], mf[q]);
    }
    asm volatile("s_nop 7\n\ts_nop 1"
                 : "+v"(acc[0]), "+v"(acc[1]), "+v"(acc[2]), "+v"(acc[3]), "+v"(acc[4]), "+v"(acc[5]), "+v"(acc[6]));
}
__device__ __forceinline__ void tpack(const f32x4 (&acc)[7], bool q3ok, u32x4 (&bv)[4]) {
#pragma unroll
    for (int pr = 0; pr < 3; ++pr)
        bv[pr] = u32x4{pack2(acc[2 * pr][0], acc[2 * pr][1]), pack2(acc[2 * pr][2], acc[2 * pr][3]),
                       pack2(acc[2 * pr + 1][0], acc[2 * pr + 1][1]), pack2(acc[2 * pr + 1][2], acc[2 * pr + 1][3])};
    unsigned t0 = pack2(acc[6][0], acc[6][1]), t1 = pack2(acc[6][2], acc[6][3]);
    bv[3] = q3ok ? u32x4{t0, t1, 0u, 0u} : u32x4{0u, 0u, t0, t1};
}
#define BF8(x) __builtin_bit_cast(bf16x8, x)
__device__ __forceinline__ void schain1(f32x4& d0, const u32x4 (&a0)[4], const u32x4 (&b)[4]) {
    asm volatile(
        "s_nop 1\n\tv_mfma_f32_16x16x32_bf16 %0, %1, %5, 0\n\tv_mfma_f32_16x16x32_bf16 %0, %2, %6, %0\n\t"
        "v_mfma_f32_16x16x32_bf16 %0, %3, %7, %0\n\tv_mfma_f32_16x16x32_bf16 %0, %4, %8, %0\n\ts_nop 7"
        : "=&v"(d0)
        : "v"(a0[0]), "v"(a0[1]), "v"(a0[2]), "v"(a0[3]), "v"(b[0]), "v"(b[1]), "v"(b[2]), "v"(b[3]));
}
__device__ __forceinline__ void schain2(f32x4& d0, f32x4& d1, const u32x4 (&a0)[4], const u32x4 (&a1)[4],
                                        const u32x4 (&b0)[4], const u32x4 (&b1)[4]) {
    asm volatile(
        "s_nop 1\n\tv_mfma_f32_16x16x32_bf16 %0, %2, %10, 0\n\tv_mfma_f32_16x16x32_bf16 %1, %6, %14, 0\n\t"
        "v_mfma_f32_16x16x32_bf16 %0, %3, %11, %0\n\tv_mfma_f32_16x16x32_bf16 %1, %7, %15, %1\n\t"
        "v_mfma_f32_16x16x32_bf16 %0, %4, %12, %0\n\tv_mfma_f32_16x16x32_bf16 %1, %8, %16, %1\n\t"
        "v_mfma_f32_16x16x32_bf16 %0, %5, %13, %0\n\tv_mfma_f32_16x16x32_bf16 %1, %9, %17, %1\n\ts_nop 7"
        : "=&v"(d0), "=&v"(d1)
        : "v"(a0[0]), "v"(a0[1]), "v"(a0[2]), "v"(a0[3]), "v"(a1[0]), "v"(a1[1]), "v"(a1[2]), "v"(a1[3]), "v"(b0[0]),
          "v"(b0[1]), "v"(b0[2]), "v"(b0[3]), "v"(b1[0]), "v"(b1[1]), "v"(b1[2]), "v"(b1[3]));
}
__device__ __forceinline__ void schain3(f32x4& d0, f32x4& d1, f32x4& d2, const u32x4 (&a0)[4], const u32x4 (&a1)[4],
                                        const u32x4 (&a2)[14], const u32x4 (&b)[4]) {
    asm volatile(
        "s_nop 1\n\tv_mfma_f32_16x16x32_bf16 %0, %3, %15, 0\n\tv_mfma_f32_16x16x32_bf16 %1, %7, %15, 0\n\t"
        "v_mfma_f32_16x16x32_bf16 %2, %11, %15, 0\n\t"
        "v_mfma_f32_16x16x32_bf16 %0, %4, %16, %0\n\tv_mfma_f32_16x16x32_bf16 %1, %8, %16, %1\n\t"
        "v_mfma_f32_16x16x32_bf16 %2, %12, %16, %2\n\t"
        "v_mfma_f32_16x16x32_bf16 %0, %5, %17, %0\n\tv_mfma_f32_16x16x32_bf16 %1, %9, %17, %1\n\t"
        "v_mfma_f32_16x16x32_bf16 %2, %13, %17, %2\n\t"
        "v_mfma_f32_16x16x32_bf16 %0, %6, %18, %0\n\tv_mfma_f32_16x16x32_bf16 %1, %10, %18, %1\n\t"
        "v_mfma_f32_16x16x32_bf16 %2, %14, %18, %2\n\ts_nop 7"
        : "=&v"(d0), "=&v"(d1), "=&v"(d2)
        : "v"(a0[0]), "v"(a0[1]), "v"(a0[2]), "v"(a0[3]), "v"(a1[0]), "v"(a1[1]), "v"(a1[2]), "v"(a1[3]), "v"(a2[0]),
          "v"(a2[1]), "v"(a2[2]), "v"(a2[3]), "v"(b[0]), "v"(b[1]), "v"(b[2]), "v"(b[3]));
}

constexpr int AL_W = 0, AL_X = 4 * 14 * 512, AL_S = AL_X + 2 * XIMG;
constexpr int SROW = 44;
constexpr int AL_MEM = AL_S + 4 * 320 * 2;
constexpr int RECA_LDS = (AL_S + 4 * 32 * SROW * 2) * 2;

template <bool ISB>
__device__ __forceinline__ void rec_A_body(const RecParams& p, u16* smem, int h, int par, int wid) {
    float* Sc = reinterpret_cast<float*>(smem + AL_S);
    const int tid = tid_of(wid), lane = tid & 63;
    const int c16 = lane & 15, g = lane >> 4;
    const unsigned xl_base = (unsigned)(uintptr_t)(LAS u16*)(smem + AL_X);
    const unsigned xvoff = (unsigned)lane * 16u;
    dma_x(p.x, par, xvoff, xl_base, wid);
    dma_x(p.x, ISB ? par + 2 : 1, xvoff, xl_base + XIMG * 2, wid);
    int kq[14];
#pragma unroll
    for (int q = 0; q < 14; ++q) {
        if (q < 3) kq[q] = 3 * wid + q;
        else if (q == 3) kq[q] = 12 + (wid >> 1);
        else if (q < 13) kq[q] = (q - 4 < 3 * wid) ? (q - 4) : (q - 1);
        else kq[q] = 13 - (wid >> 1);
    }
    bf16x8 W[6][14];
    u16* WL = smem + AL_W + (wid * 14 * 64 + lane) * 8;
#pragma unroll
    for (int t = 0; t < 7; ++t) {
        const int ksp = (t < 6) ? (3 * wid + (t >> 1)) : (12 + (wid >> 1));
        const int half = (t < 6) ? (t & 1) : (wid & 1);
        const int n = 32 * ksp + 8 * (c16 >> 2) + 4 * half + (c16 & 3);
        const u16* ap = p.AmatT + ((long)h * DM + n) * DM + 8 * g;
#pragma unroll
        for (int q = 0; q < 14; ++q) {
            bf16x8 w = *reinterpret_cast<const bf16x8*>(ap + 32 * kq[q]);
            if (t < 6)
                W[t][q] = w;
            else
                *reinterpret_cast<bf16x8*>(WL + q * 512) = w;
        }
    }
    const bool q3ok = (wid & 1) == 0;
    const bool sact = c16 < 10;
    const bool sst = sact && !(c16 & 1);
    const unsigned wl_addr = (unsigned)(uintptr_t)(LAS u16*)WL;
    __amdgpu_buffer_rsrc_t rs2 = __builtin_amdgcn_make_buffer_rsrc(p.G2, 0, 64 * G2_SLOT * 2, 0x00020000);
    const float L2E = 1.4426950408889634f;
    bool dead = false;
    asm volatile("s_waitcnt vmcnt(0)" ::: "memory");
    __syncthreads();
    if constexpr (!ISB) {
        const int srow = 4 * (wid & 1) + g;
        const float* sread = Sc + srow * 40 + (sact ? 4 * c16 : 0);
        float* swr = Sc + wid * 320 + (c16 & 7) * 40 + 8 * g;
        float* swr_mm = Sc + wid * 320 + (c16 & 7) * 40 + 32 + 4 * (g & 1);
        u16* memL = smem + AL_MEM;
        const int ch1 = (tid < 192) ? (256 + tid) : tid;
        u16* mw0 = memL + ((tid >> 1) & 7) * MROW + 16 * (tid >> 4) + 8 * (tid & 1);
        u16* mw1 = memL + ((ch1 >> 1) & 7) * MROW + 16 * (ch1 >> 4) + 8 * (ch1 & 1);
        const unsigned ml_addr = (unsigned)(uintptr_t)(LAS u16*)(memL + (c16 & 7) * MROW + 8 * g);
        unsigned so[14];
#pragma unroll
        for (int q = 0; q < 14; ++q) so[q] = 64 * kq[q];
        __amdgpu_buffer_rsrc_t rs1 = __builtin_amdgcn_make_buffer_rsrc(p.G1, 0, 63 * G1_SLOT * 2, 0x00020000);
        const unsigned g1off = (h * 320 + srow * 40 + 4 * c16) * 2;
        const u16* xrd = smem + AL_X + lane * 8;
        u32x4 xf[2][4];
        auto read_xf = [&](int s) {
#pragma unroll
            for (int tl = 0; tl < 2; ++tl)
#pragma unroll
                for (int q = 0; q < 4; ++q)
                    xf[tl][q] = *reinterpret_cast<const u32x4*>(xrd + (s & 1) * XIMG + (tl * 14 + kq[q]) * 512);
        };
        read_xf(0);
        for (int s = 0; s < NSEG - 1; ++s) {
            u32x4 mf[14];
            bf16x8 w6[3];
            wait_lgkm<0>();
            lds_rd16(w6[0], wl_addr, 0);
            lds_rd16(w6[1], wl_addr, 1024);
            {
                u32x4 a, b;
                for (unsigned spin = 0;; ++spin) {
                    asm volatile("" ::: "memory");
                    a = ld16_sc1(rs2, tid * 16, (unsigned)((s * G2_SLOT + (h & 1) * G2_CP) * 2));
                    b = ld16_sc1(rs2, ch1 * 16, (unsigned)((s * G2_SLOT + (h & 1) * G2_CP) * 2));
                    unsigned mx = max(umax3(a[1], a[3], b[1]), b[3]);
                    if (__all(mx != 0xffffffffu) || dead) break;
                    if (spin > SPIN_CAP) dead = true;
                }
                *reinterpret_cast<u32x4*>(mw0) = a;
                *reinterpret_cast<u32x4*>(mw1) = b;
                __syncthreads();
            }
            TS(1);
            f32x4 acc[7];
            tpass(W, wl_addr, ml_addr, so, mf, w6, acc);
            u32x4 bv[4];
            tpack(acc, q3ok, bv);
            f32x4 sm, s0, s1;
            schain3(s0, s1, sm, xf[0], xf[1], mf, bv);
            if (c16 < 8) {
                *reinterpret_cast<f32x4*>(swr) = s0;
                *reinterpret_cast<f32x4*>(swr + 4) = s1;
                if (g < 2) *reinterpret_cast<f32x4*>(swr_mm) = sm;
            }
            TS(2);
            __syncthreads();
            {
                f32x4 x0 = *reinterpret_cast<const f32x4*>(sread);
                f32x4 x1 = *reinterpret_cast<const f32x4*>(sread + 320);
                f32x4 x2 = *reinterpret_cast<const f32x4*>(sread + 640);
                f32x4 x3 = *reinterpret_cast<const f32x4*>(sread + 960);
                f32x4 a = (x0 + x1) + (x2 + x3);
                float mx = sact ? __builtin_fmaxf(__builtin_fmaxf(a[0], a[1]), __builtin_fmaxf(a[2], a[3])) : -INFINITY;
                mx = red16_max(mx);
                float nm = -mx * L2E;
                float e0 = __builtin_amdgcn_exp2f(__builtin_fmaf(a[0], L2E, nm));
                float e1 = __builtin_amdgcn_exp2f(__builtin_fmaf(a[1], L2E, nm));
                float e2 = __builtin_amdgcn_exp2f(__builtin_fmaf(a[2], L2E, nm));
                float e3 = __builtin_amdgcn_exp2f(__builtin_fmaf(a[3], L2E, nm));
                float sum = sact ? ((e0 + e1) + (e2 + e3)) : 0.f;
                sum = red16_sum(sum);
                float inv = __builtin_amdgcn_rcpf(sum);
                st_gran8x2(rs1, g1off, (unsigned)((s * G1_SLOT + (wid >> 1) * 2 * G1_CP) * 2), G1_CP * 2, sst,
                           pack2(e0 * inv, e1 * inv), pack2(e2 * inv, e3 * inv));
            }
            TS(3);
            asm volatile("" ::: "memory");
            read_xf(s + 1);
            if (s + 2 < NSEG - 1) dma_x(p.x, s + 2, xvoff, xl_base + (s & 1) * (XIMG * 2), wid);
            __builtin_amdgcn_s_sleep(SLP_A);
        }
    } else {
        float* swr = Sc + (wid * 32 + c16) * SROW + 8 * g;
        float* swr_mm = Sc + (wid * 32 + c16) * SROW + 32 + 4 * (g & 1);
        const float* sread = Sc + (wid * 8 + g) * SROW + (sact ? 4 * c16 : 0);
        const int key0 = (c16 < 8) ? 16 * (c16 & 1) + 4 * (c16 >> 1) : -4;
        __amdgpu_buffer_rsrc_t rs3 = __builtin_amdgcn_make_buffer_rsrc(p.G3, 0, 64 * G3_SLOT * 2, 0x00020000);
        const unsigned g3off = ((h * 32 + wid * 8 + g) * 40 + 4 * c16) * 2;
        unsigned so[14];
#pragma unroll
        for (int q = 0; q < 14; ++q) so[q] = 1024 * kq[q];
        unsigned mvoff[4];
#pragma unroll
        for (int q = 0; q < 4; ++q) mvoff[q] = ((2 * kq[q] + (g >> 1)) * 128 + (c16 & 7) * 16 + (g & 1) * 8) * 2;
        for (int s = par; s < NSEG; s += 2) {
            const int xb = (s >> 1) & 1;
            const unsigned xa = xl_base + xb * (XIMG * 2) + lane * 16;
            u32x4 bv0[4], bv1[4];
            {
                u32x4 mf[14];
                bf16x8 w6[3];
                f32x4 acc[7];
                wait_lgkm<0>();
                lds_rd16(w6[0], wl_addr, 0);
                lds_rd16(w6[1], wl_addr, 1024);
                tpass(W, wl_addr, xa, so, mf, w6, acc);
                tpack(acc, q3ok, bv0);
                const u32x4 m4[4] = {mf[0], mf[1], mf[2], mf[3]};
                f32x4 s00;
                schain1(s00, m4, bv0);
                __syncthreads();
                *reinterpret_cast<f32x4*>(swr) = s00;
            }
            {
                u32x4 mf[14];
                bf16x8 w6[3];
                f32x4 acc[7];
                wait_lgkm<0>();
                lds_rd16(w6[0], wl_addr, 0);
                lds_rd16(w6[1], wl_addr, 1024);
                tpass(W, wl_addr, xa + 14 * 1024, so, mf, w6, acc);
                tpack(acc, q3ok, bv1);
                const u16* xrd = smem + AL_X + xb * XIMG + lane * 8;
                const u32x4 m4[4] = {mf[0], mf[1], mf[2], mf[3]};
                u32x4 x4[4];
#pragma unroll
                for (int pr = 0; pr < 4; ++pr) x4[pr] = *reinterpret_cast<const u32x4*>(xrd + kq[pr] * 512);
                f32x4 s01, s11;
                schain2(s01, s11, x4, m4, bv1, bv1);
                *reinterpret_cast<f32x4*>(swr + 16 * SROW) = s01;
                *reinterpret_cast<f32x4*>(swr + 16 * SROW + 4) = s11;
            }
            u32x4 mm[4];
            for (unsigned spin = 0;; ++spin) {
                asm volatile("" ::: "memory");
                unsigned mx = 0;
#pragma unroll
                for (int q = 0; q < 4; ++q) mm[q] = ld16_sc1(rs2, mvoff[q], (unsigned)((s * G2_SLOT + 3 * G2_CP) * 2));
#pragma unroll
                for (int q = 0; q < 4; ++q) mx = umax3(mx, mm[q][1], mm[q][3]);
                if (__all(mx != 0xffffffffu) || dead) break;
                if (spin > SPIN_CAP) dead = true;
                __builtin_amdgcn_s_sleep(8);
            }
            TS(1);
            {
                f32x4 sm0, sm1;
                schain2(sm0, sm1, mm, mm, bv0, bv1);
                if (g < 2) {
                    *reinterpret_cast<f32x4*>(swr_mm) = sm0;
                    *reinterpret_cast<f32x4*>(swr_mm + 16 * SROW) = sm1;
                }
            }
            asm volatile("s_waitcnt vmcnt(0)" ::: "memory");
            __syncthreads();
            TS(2);
            if (s + 4 < NSEG) dma_x(p.x, s + 4, xvoff, xl_base + xb * (XIMG * 2), wid);
#pragma unroll
            for (int i = 0; i < 2; ++i) {
                const float* sr = sread + 4 * i * SROW;
                f32x4 x0 = *reinterpret_cast<const f32x4*>(sr);
                f32x4 x1 = *reinterpret_cast<const f32x4*>(sr + 32 * SROW);
                f32x4 x2 = *reinterpret_cast<const f32x4*>(sr + 64 * SROW);
                f32x4 x3 = *reinterpret_cast<const f32x4*>(sr + 96 * SROW);
                f32x4 a = (x0 + x1) + (x2 + x3);
                const int row = wid * 8 + 4 * i + g;
#pragma unroll
                for (int r = 0; r < 4; ++r)
                    if (!sact || key0 + r > row) a[r] = -INFINITY;
                float mx = __builtin_fmaxf(__builtin_fmaxf(a[0], a[1]), __builtin_fmaxf(a[2], a[3]));
                mx = red16_max(mx);
                float nm = -mx * L2E;
                float e0 = __builtin_amdgcn_exp2f(__builtin_fmaf(a[0], L2E, nm));
                float e1 = __builtin_amdgcn_exp2f(__builtin_fmaf(a[1], L2E, nm));
                float e2 = __builtin_amdgcn_exp2f(__builtin_fmaf(a[2], L2E, nm));
                float e3 = __builtin_amdgcn_exp2f(__builtin_fmaf(a[3], L2E, nm));
                float sum = red16_sum((e0 + e1) + (e2 + e3));
                float inv = __builtin_amdgcn_rcpf(sum);
                st_gran8(rs3, g3off + i * (4 * 40 * 2), (unsigned)(s * G3_SLOT * 2), sst, pack2(e0 * inv, e1 * inv),
                         pack2(e2 * inv, e3 * inv));
            }
            TS(3);
        }
    }
}

__device__ __forceinline__ void mfma_b0(f32x4& d, const u32x4& a, const bf16x8& w) {
    asm volatile("v_mfma_f32_16x16x32_bf16 %0, %1, %2, 0" : "=&v"(d) : "v"(a), "a"(w));
}
__device__ __forceinline__ void mfma_b(f32x4& d, const u32x4& a, const bf16x8& w) {
    asm volatile("v_mfma_f32_16x16x32_bf16 %0, %1, %2, %0" : "+v"(d) : "v"(a), "a"(w));
}
__device__ __forceinline__ void lds_rd16o(u32x4& d, unsigned addr, int off) {
    asm volatile("ds_read_b128 %0, %1 offset:%2" : "=v"(d) : "v"(addr), "n"(off));
}
__device__ __forceinline__ void chain5(f32x4& d, const u32x4& a0, const u32x4& a1, const u32x4& a2, const u32x4& a3,
                                       const u32x4& a4, const u32x4 (&b)[4], const u32x4& b4) {
    asm volatile(
        "s_nop 1\n\tv_mfma_f32_16x16x32_bf16 %0, %1, %6, 0\n\tv_mfma_f32_16x16x32_bf16 %0, %2, %7, %0\n\t"
        "v_mfma_f32_16x16x32_bf16 %0, %3, %8, %0\n\tv_mfma_f32_16x16x32_bf16 %0, %4, %9, %0\n\t"
        "v_mfma_f32_16x16x32_bf16 %0, %5, %10, %0\n\ts_nop 7"
        : "=&v"(d)
        : "v"(a0), "v"(a1), "v"(a2), "v"(a3), "v"(a4), "v"(b[0]), "v"(b[1]), "v"(b[2]), "v"(b[3]), "v"(b4));
}
constexpr int cmin(int a, int b) { return a < b ? a : b; }
#define SW14(F) switch (ks) { F(0) F(1) F(2) F(3) F(4) F(5) F(6) F(7) F(8) F(9) F(10) F(11) F(12) F(13) }

constexpr int CL_MEM = 0, CL_RED = CL_MEM + 8 * MROW, CL_ORED = CL_RED + 4 * 128 * 2;
constexpr int RECC_LDS = 2 * 128 * 68 * 4;
constexpr int DG_SEG = 28 * 4 * 4 * 512;
constexpr int UG_SEG = 28 * 4 * 512;

__device__ __forceinline__ void rec_D_body(const RecParams& p, u16* smem, int c, int wid) {
    const int tid = tid_of(wid), lane = tid & 63;
    const int c16 = lane & 15, g = lane >> 4;
    const unsigned xl_base = (unsigned)(uintptr_t)(LAS u16*)smem;
    const unsigned xvoff = (unsigned)lane * 16u;
    dma_x(p.x, 0, xvoff, xl_base, wid);
    const int col = c * 16 + c16;
    bf16x8 W[4][14];
#pragma unroll
    for (int hh = 0; hh < 4; ++hh) {
        const u16* cp = p.CmatT + ((long)(wid * 4 + hh) * DM + col) * DM + 8 * g;
#pragma unroll
        for (int ks = 0; ks < 14; ++ks) W[hh][ks] = *reinterpret_cast<const bf16x8*>(cp + 32 * ks);
    }
    __amdgpu_buffer_rsrc_t rsd = __builtin_amdgcn_make_buffer_rsrc(p.Dg, 0, 64 * DG_SEG * 2, 0x00020000);
    const unsigned dvoff = ((c * 4 + wid) * 2048 + lane * 8) * 2;
    unsigned* fl = p.dflag + (c * 4 + wid) * 64;
    for (int s = 0; s < NSEG; ++s) {
        __builtin_amdgcn_s_waitcnt(0x0F70);
        asm volatile("s_waitcnt vmcnt(0)" ::: "memory");
        if (s > 0 && lane == 0) __hip_atomic_store(fl, (unsigned)(s - 1), __ATOMIC_RELAXED, __HIP_MEMORY_SCOPE_AGENT);
        __syncthreads();
        const bool more = s + 1 < NSEG;
        const char* gb = reinterpret_cast<const char*>(p.x) + (long)(s + 1) * (SEG * DM * 2) + wid * 7168;
        const unsigned xl = xl_base + ((s + 1) & 1) * (XIMG * 2) + (unsigned)(wid * 7) * 1024u;
        const unsigned xa = xl_base + (s & 1) * (XIMG * 2) + lane * 16;
        u32x4 fa[14], fb[14];
        f32x4 d0[4], d1[4];
        wait_lgkm<0>();
#pragma unroll
        for (int ks = 0; ks < 3; ++ks) {
            lds_rd16o(fa[ks], xa, ks * 1024);
            lds_rd16o(fb[ks], xa, (14 + ks) * 1024);
        }
#pragma unroll
        for (int ks = 0; ks < 14; ++ks) {
            if (ks + 3 < 14) {
                lds_rd16o(fa[ks + 3], xa, (ks + 3) * 1024);
                lds_rd16o(fb[ks + 3], xa, (17 + ks) * 1024);
            }
#define WD(Q) case Q: wait_lgkm<2 * cmin(Q + 4, 14) - 2 * (Q + 1)>(); break;
            SW14(WD)
#undef WD
#pragma unroll
            for (int hh = 0; hh < 4; ++hh) {
                if (ks == 0) {
                    mfma_b0(d0[hh], fa[ks], W[hh][ks]);
                    mfma_b0(d1[hh], fb[ks], W[hh][ks]);
                } else {
                    mfma_b(d0[hh], fa[ks], W[hh][ks]);
                    mfma_b(d1[hh], fb[ks], W[hh][ks]);
                }
            }
            if (ks < 7 && more) dma16(gb + 1024 * ks, xvoff, xl + (unsigned)ks * 1024u);
        }
        asm volatile("s_nop 7"
                     : "+v"(d0[0]), "+v"(d0[1]), "+v"(d0[2]), "+v"(d0[3]), "+v"(d1[0]), "+v"(d1[1]), "+v"(d1[2]),
                       "+v"(d1[3]));
#pragma unroll
        for (int hh = 0; hh < 4; ++hh)
            __builtin_amdgcn_raw_buffer_store_b128(u32x4{pack2(d0[hh][0], d0[hh][1]), pack2(d0[hh][2], d0[hh][3]),
                                                         pack2(d1[hh][0], d1[hh][1]), pack2(d1[hh][2], d1[hh][3])},
                                                   rsd, dvoff + hh * 1024, (unsigned)(s * DG_SEG * 2), 16);
    }
    asm volatile("s_waitcnt vmcnt(0)" ::: "memory");
    if (lane == 0) __hip_atomic_store(fl, (unsigned)(NSEG - 1), __ATOMIC_RELAXED, __HIP_MEMORY_SCOPE_AGENT);
}

__device__ __forceinline__ void rec_C_body(const RecParams& p, u16* smem, int c, int wid) {
    u16* memL = smem + CL_MEM;
    float* red = reinterpret_cast<float*>(smem + CL_RED);
    const int tid = tid_of(wid), lane = tid & 63;
    const int c16 = lane & 15, g = lane >> 4;
    const int ch1 = (tid < 192) ? (256 + tid) : tid;
    if (tid < 128) {
        u32x2 m0 = *reinterpret_cast<const u32x2*>(p.mem0 + ((tid & 31) >> 2) * DM + 16 * c + 4 * (tid & 3));
        st_gran4(p.G2 + (tid >> 5) * G2_CP + 128 * c + 4 * (tid & 31), m0[0], m0[1]);
    }
    u16* mw0 = memL + ((tid >> 1) & 7) * MROW + 16 * (tid >> 4) + 8 * (tid & 1);
    u16* mw1 = memL + ((ch1 >> 1) & 7) * MROW + 16 * (ch1 >> 4) + 8 * (ch1 & 1);
    const int col = c * 16 + c16;
    bf16x8 W[4][14];
#pragma unroll
    for (int hh = 0; hh < 4; ++hh) {
        const u16* cp = p.CmatT + ((long)(wid * 4 + hh) * DM + col) * DM + 8 * g;
#pragma unroll
        for (int ks = 0; ks < 14; ++ks) W[hh][ks] = *reinterpret_cast<const bf16x8*>(cp + 32 * ks);
    }
    u32x4 Dn[4], Uc;
    const int i8 = c16 & 7;
    const unsigned mfa = (unsigned)(uintptr_t)(LAS u16*)(memL + i8 * MROW + 8 * g);
    const unsigned pvoff = ((c & 3) * G1_CP + (wid * 4) * 320 + i8 * 40 + 8 * g) * 2;
    const unsigned pvoffx = ((c & 3) * G1_CP + (wid * 4 + g) * 320 + i8 * 40 + 32) * 2;
    float* rwr = red + wid * 128 + (4 * (g & 1)) * 16 + c16;
    const float* rrd = red + ((lane >> 2) & 7) * 16 + 4 * (lane & 3);
    __amdgpu_buffer_rsrc_t rs1 = __builtin_amdgcn_make_buffer_rsrc(p.G1, 0, 63 * G1_SLOT * 2, 0x00020000);
    __amdgpu_buffer_rsrc_t rs2 = __builtin_amdgcn_make_buffer_rsrc(p.G2, 0, 64 * G2_SLOT * 2, 0x00020000);
    __amdgpu_buffer_rsrc_t rsd = __builtin_amdgcn_make_buffer_rsrc(p.Dg, 0, 64 * DG_SEG * 2, 0x00020000);
    __amdgpu_buffer_rsrc_t rsu = __builtin_amdgcn_make_buffer_rsrc(p.Ug, 0, 64 * UG_SEG * 2, 0x00020000);
    const unsigned dvoff = ((c * 4 + wid) * 2048 + lane * 8) * 2;
    const unsigned uvoff = ((c * 4 + wid) * 512 + lane * 8) * 2;
    const unsigned* fl = p.dflag + (c * 4 + wid) * 64;
    unsigned* ufl = p.dflag + (c * 4 + wid) * 64 + 32;
    bool dead = false;
    int dready = -1;
    auto load_D = [&](int s) {
        for (unsigned spin = 0; dready < s && !dead; ++spin) {
            dready = __builtin_amdgcn_readfirstlane(
                (int)__hip_atomic_load(fl, __ATOMIC_RELAXED, __HIP_MEMORY_SCOPE_AGENT));
            if (spin > SPIN_CAP) dead = true;
        }
        asm volatile("" ::: "memory");
#pragma unroll
        for (int hh = 0; hh < 4; ++hh) Dn[hh] = ld16_sc1(rsd, dvoff + hh * 1024, (unsigned)(s * DG_SEG * 2));
    };
    load_D(0);
    for (int s = 0;; ++s) {
        {
            u32x4 a, b;
            for (unsigned spin = 0;; ++spin) {
                asm volatile("" ::: "memory");
                a = ld16_sc1(rs2, tid * 16, (unsigned)((s * G2_SLOT + 2 * G2_CP) * 2));
                b = ld16_sc1(rs2, ch1 * 16, (unsigned)((s * G2_SLOT + 2 * G2_CP) * 2));
                unsigned mx = max(umax3(a[1], a[3], b[1]), b[3]);
                if (__all(mx != 0xffffffffu) || dead) break;
                if (spin > SPIN_CAP) dead = true;
                __builtin_amdgcn_s_sleep(SLP_C0);
            }
            *reinterpret_cast<u32x4*>(mw0) = a;
            *reinterpret_cast<u32x4*>(mw1) = b;
        }
        __syncthreads();
        {
            TS(1);
            f32x4 U[4];
            u32x4 mf[14];
            wait_lgkm<0>();
#pragma unroll
            for (int ks = 0; ks < 5; ++ks) lds_rd16o(mf[ks], mfa, ks * 64);
#pragma unroll
            for (int ks = 0; ks < 14; ++ks) {
                if (ks + 5 < 14) lds_rd16o(mf[ks + 5], mfa, (ks + 5) * 64);
#define WU(Q) case Q: wait_lgkm<cmin(Q + 6, 14) - (Q + 1)>(); break;
                SW14(WU)
#undef WU
#pragma unroll
                for (int hh = 0; hh < 4; ++hh) {
                    if (ks == 0) mfma_b0(U[hh], mf[ks], W[hh][ks]);
                    else mfma_b(U[hh], mf[ks], W[hh][ks]);
                }
            }
            asm volatile("s_nop 7" : "+v"(U[0]), "+v"(U[1]), "+v"(U[2]), "+v"(U[3]));
            unsigned pk[4][2];
#pragma unroll
            for (int hh = 0; hh < 4; ++hh) {
                pk[hh][0] = pack2(U[hh][0], U[hh][1]);
                pk[hh][1] = pack2(U[hh][2], U[hh][3]);
            }
            const bool g1 = g & 1, g2 = g & 2;
            unsigned e0 = g2 ? pk[2][0] : pk[0][0], e1 = g2 ? pk[2][1] : pk[0][1];
            unsigned o0 = g2 ? pk[3][0] : pk[1][0], o1 = g2 ? pk[3][1] : pk[1][1];
            unsigned m0 = g1 ? o0 : e0, m1 = g1 ? o1 : e1;
            unsigned t0 = g1 ? e0 : o0, t1 = g1 ? e1 : o1;
            unsigned r0 = (unsigned)__shfl_xor((int)t0, 16), r1 = (unsigned)__shfl_xor((int)t1, 16);
            Uc = g1 ? u32x4{r0, r1, m0, m1} : u32x4{m0, m1, r0, r1};
            __builtin_amdgcn_raw_buffer_store_b128(Uc, rsu, uvoff, (unsigned)(s * UG_SEG * 2), 16);
            TS(7);
        }
        if (s == NSEG - 1) break;
        {
            u32x4 pv[5];
#pragma unroll
            for (int q = 0; q < 5; ++q) pv[q] = u32x4{0u, 0u, 0u, 0u};
            TS(6);
            __builtin_amdgcn_s_sleep(SLP_C1);
            for (unsigned spin = 0;; ++spin) {
                asm volatile("" ::: "memory");
                unsigned mx = 0;
                if (c16 < 8) {
#pragma unroll
                    for (int hh = 0; hh < 4; ++hh) pv[hh] = ld16_sc1(rs1, pvoff + hh * 640, (unsigned)(s * G1_SLOT * 2));
                    pv[4] = ld16_sc1(rs1, pvoffx, (unsigned)(s * G1_SLOT * 2));
#pragma unroll
                    for (int q = 0; q < 5; ++q) mx = umax3(mx, pv[q][1], pv[q][3]);
                }
                if (__all(mx != 0xffffffffu) || dead) break;
                if (spin > SPIN_CAP) dead = true;
            }
            TS(2);
            f32x4 o0;
            chain5(o0, pv[0], pv[1], pv[2], pv[3], pv[4], Dn, Uc);
            if (g < 2) {
#pragma unroll
                for (int r = 0; r < 4; ++r) rwr[r * 16] = o0[r];
            }
        }
        asm volatile("s_waitcnt vmcnt(0)" ::: "memory");
        __syncthreads();
        if (tid < 128) {
            f32x4 x0 = *reinterpret_cast<const f32x4*>(rrd);
            f32x4 x1 = *reinterpret_cast<const f32x4*>(rrd + 128);
            f32x4 x2 = *reinterpret_cast<const f32x4*>(rrd + 256);
            f32x4 x3 = *reinterpret_cast<const f32x4*>(rrd + 384);
            f32x4 a = (x0 + x1) + (x2 + x3);
            st_gran8(rs2, ((tid >> 5) * G2_CP + 128 * c + 4 * (lane & 31)) * 2, (unsigned)((s + 1) * G2_SLOT * 2),
                     !(lane & 1), pack2(a[0], a[1]), pack2(a[2], a[3]));
        }
        TS(3);
        asm volatile("" ::: "memory");
        if (lane == 0) __hip_atomic_store(ufl, (unsigned)s, __ATOMIC_RELAXED, __HIP_MEMORY_SCOPE_AGENT);
        if (s + 2 < NSEG) load_D(s + 1);
        TS(5);
        __builtin_amdgcn_s_sleep(SLP_C2);
    }
    asm volatile("s_waitcnt vmcnt(0)" ::: "memory");
    if (lane == 0) __hip_atomic_store(ufl, (unsigned)(NSEG - 1), __ATOMIC_RELAXED, __HIP_MEMORY_SCOPE_AGENT);
}

constexpr int OROW = 68;
__device__ __forceinline__ void rec_O_body(const RecParams& p, u16* smem, int cg, int par, int wid) {
    float* ored = reinterpret_cast<float*>(smem);
    const int tid = tid_of(wid), lane = tid & 63;
    const int c16 = lane & 15, g = lane >> 4;
    const unsigned pxoff = ((wid * 4) * 1280 + c16 * 40 + 8 * g) * 2;
    const unsigned pxoffx = ((wid * 4 + g) * 1280 + c16 * 40 + 32) * 2;
    float* owr = ored + (wid * 32 + 4 * g) * OROW + c16;
    const float* ord = ored + (tid >> 3) * OROW + 8 * (tid & 7);
    u16* outp = p.out + (long)(tid >> 3) * 896 + 64 * cg + 8 * (tid & 7);
    __amdgpu_buffer_rsrc_t rs3 = __builtin_amdgcn_make_buffer_rsrc(p.G3, 0, 64 * G3_SLOT * 2, 0x00020000);
    __amdgpu_buffer_rsrc_t rsd = __builtin_amdgcn_make_buffer_rsrc(p.Dg, 0, 64 * DG_SEG * 2, 0x00020000);
    __amdgpu_buffer_rsrc_t rsu = __builtin_amdgcn_make_buffer_rsrc(p.Ug, 0, 64 * UG_SEG * 2, 0x00020000);
    const unsigned dvoff = ((cg * 16 + wid) * 2048 + lane * 8) * 2;
    const unsigned uvoff = ((cg * 16 + wid) * 512 + lane * 8) * 2;
    const unsigned* fl = p.dflag + ((cg * 4 + (lane & 3)) * 4 + wid) * 64;
    bool dead = false;
    auto wait_fl = [&](int w, int s) {
        for (unsigned spin = 0; !dead; ++spin) {
            int v = (int)__hip_atomic_load(fl + w, __ATOMIC_RELAXED, __HIP_MEMORY_SCOPE_AGENT);
            if (__all(v >= s)) break;
            if (spin > SPIN_CAP) dead = true;
            __builtin_amdgcn_s_sleep(8);
        }
        asm volatile("" ::: "memory");
    };
    for (int s = par; s < NSEG; s += 2) {
        u32x4 D[4][4], U[4], px[10];
        wait_fl(0, s);
#pragma unroll
        for (int cb = 0; cb < 4; ++cb)
#pragma unroll
            for (int hh = 0; hh < 4; ++hh)
                D[cb][hh] = ld16_sc1(rsd, dvoff + cb * 16384 + hh * 1024, (unsigned)(s * DG_SEG * 2));
        wait_fl(32, s);
#pragma unroll
        for (int cb = 0; cb < 4; ++cb) U[cb] = ld16_sc1(rsu, uvoff + cb * 4096, (unsigned)(s * UG_SEG * 2));
        for (unsigned spin = 0;; ++spin) {
            asm volatile("" ::: "memory");
            unsigned mx = 0;
#pragma unroll
            for (int q = 0; q < 10; ++q)
                px[q] = ld16_sc1(rs3, ((q % 5) < 4 ? pxoff + (q % 5) * 2560 : pxoffx) + (q / 5) * 1280,
                                 (unsigned)(s * G3_SLOT * 2));
#pragma unroll
            for (int q = 0; q < 10; ++q) mx = umax3(mx, px[q][1], px[q][3]);
            if (__all(mx != 0xffffffffu) || dead) break;
            if (spin > SPIN_CAP) dead = true;
            __builtin_amdgcn_s_sleep(4);
        }
        const int bo = ((s >> 1) & 1) * (128 * OROW);
#pragma unroll
        for (int cb = 0; cb < 4; ++cb)
#pragma unroll
            for (int qt = 0; qt < 2; ++qt) {
                f32x4 o;
                chain5(o, px[qt * 5], px[qt * 5 + 1], px[qt * 5 + 2], px[qt * 5 + 3], px[qt * 5 + 4], D[cb], U[cb]);
#pragma unroll
                for (int r = 0; r < 4; ++r) owr[bo + (qt * 16 + r) * OROW + cb * 16] = o[r];
            }
        __syncthreads();
        f32x4 a0 = f32x4{0.f, 0.f, 0.f, 0.f}, a1 = a0;
        {
            f32x4 y[4][2];
#pragma unroll
            for (int w = 0; w < 4; ++w) {
                y[w][0] = *reinterpret_cast<const f32x4*>(ord + bo + w * 32 * OROW);
                y[w][1] = *reinterpret_cast<const f32x4*>(ord + bo + w * 32 * OROW + 4);
            }
            a0 = (y[0][0] + y[1][0]) + (y[2][0] + y[3][0]);
            a1 = (y[0][1] + y[1][1]) + (y[2][1] + y[3][1]);
        }
        *reinterpret_cast<u32x4*>(outp + (long)s * (SEG * 896)) =
            u32x4{pack2(a0[0], a0[1]), pack2(a0[2], a0[3]), pack2(a1[0], a1[1]), pack2(a1[2], a1[3])};
    }
}

template <int BM, int BN, int MODE>
__device__ __forceinline__ void gemm_tile(const u16* __restrict__ A, int lda, const u16* __restrict__ B, int ldb,
                                          u16* __restrict__ C, int ldc, int nk, u16* smem, int ws) {
    constexpr bool AF = MODE == 2;
    constexpr int AROW = AF ? 64 : 72, BROW = BN + 32;
    constexpr int ASZ = BM * AROW, STG = ASZ + 64 * BROW;
    constexpr int ACH = BM / 32, BCH = BN / 32, TM = BM / 64, TN = BN / 64;
    constexpr int BPR = BN / 8;
    constexpr int BRS = 256 / BPR;
    const int tid = tid_of(ws);
    const int lane = tid & 63, wid = ws;
    const int wm = wid >> 1, wn = wid & 1;
    const int r32 = lane & 31, hi = lane >> 5, g = lane >> 4, q4 = (lane & 15) >> 2, p4 = lane & 3;
    f32x16 acc[TM][TN];
#pragma unroll
    for (int i = 0; i < TM; ++i)
#pragma unroll
        for (int j = 0; j < TN; ++j) acc[i][j] = f32x16{};
    u32x4 ra0[ACH], rb0[BCH], ra1[ACH], rb1[BCH];
    const unsigned aoff = AF ? (unsigned)tid * 16u : ((unsigned)(tid >> 3) * lda + (tid & 7) * 8) * 2u;
    const unsigned boff = ((unsigned)(tid / BPR) * ldb + (tid % BPR) * 8) * 2u;
    const char* Ac = reinterpret_cast<const char*>(A);
    const char* Bc = reinterpret_cast<const char*>(B);
    u16* aw = smem + (tid >> 3) * AROW + (tid & 7) * 8;
    u16* bw = smem + ASZ + (tid / BPR) * BROW + (tid % BPR) * 8;
    const u16* ard = AF ? smem + wm * (BM / 2) * AROW + lane * 8 : smem + (wm * (BM / 2) + r32) * AROW + hi * 8;
    const u16* brd = smem + ASZ + ((AF ? 4 : 8) * (g >> 1) + q4) * BROW + wn * (BN / 2) + 16 * (g & 1) + 4 * p4;
    auto load = [&](u32x4(&ra)[ACH], u32x4(&rb)[BCH], int kt) {
#pragma unroll
        for (int i = 0; i < ACH; ++i)
            ra[i] = *reinterpret_cast<const u32x4*>(Ac + (aoff + (unsigned)(32 * i * lda + kt * (AF ? 2048 : 64)) * 2u));
#pragma unroll
        for (int i = 0; i < BCH; ++i)
            rb[i] = *reinterpret_cast<const u32x4*>(Bc + (boff + (unsigned)((kt * 64 + BRS * i) * ldb) * 2u));
    };
    auto store = [&](const u32x4(&ra)[ACH], const u32x4(&rb)[BCH], int st) {
#pragma unroll
        for (int i = 0; i < ACH; ++i) *reinterpret_cast<u32x4*>(aw + st * STG + 32 * i * AROW) = ra[i];
#pragma unroll
        for (int i = 0; i < BCH; ++i) *reinterpret_cast<u32x4*>(bw + st * STG + BRS * i * BROW) = rb[i];
    };
    auto compute = [&](int st, int ks) {
        bf16x8 af[TM], bfr[TN];
#pragma unroll
        for (int i = 0; i < TM; ++i)
            af[i] = *reinterpret_cast<const bf16x8*>(ard + st * STG + i * 32 * AROW + ks * (AF ? 512 : 16));
#pragma unroll
        for (int j = 0; j < TN; ++j) {
            const u16* pB = brd + st * STG + ks * 16 * BROW + j * 32;
            s16x4 lo = tr_read(pB);
            s16x4 hi4 = tr_read(pB + (AF ? 8 : 4) * BROW);
            bfr[j] = bf16x8{lo[0], lo[1], lo[2], lo[3], hi4[0], hi4[1], hi4[2], hi4[3]};
        }
#pragma unroll
        for (int i = 0; i < TM; ++i)
#pragma unroll
            for (int j = 0; j < TN; ++j)
                acc[i][j] = MODE == 1 ? MFMA32(af[i], bfr[j], acc[i][j]) : MFMA32(bfr[j], af[i], acc[i][j]);
    };
    load(ra0, rb0, 0);
    load(ra1, rb1, 1);
    __syncthreads();
    store(ra0, rb0, 0);
    load(ra0, rb0, 2);
    __syncthreads();
    for (int kt = 0; kt + 1 < nk; kt += 2) {
        compute(0, 0);
        compute(0, 1);
        store(ra1, rb1, 1);
        load(ra1, rb1, min(kt + 3, nk - 1));
        compute(0, 2);
        compute(0, 3);
        __syncthreads();
        compute(1, 0);
        compute(1, 1);
        store(ra0, rb0, 0);
        load(ra0, rb0, min(kt + 4, nk - 1));
        compute(1, 2);
        compute(1, 3);
        __syncthreads();
    }
    if (nk & 1) {
        compute(0, 0);
        compute(0, 1);
        compute(0, 2);
        compute(0, 3);
        __syncthreads();
    }
    const int tid2 = tid_of(ws);
    __amdgpu_buffer_rsrc_t rs = __builtin_amdgcn_make_buffer_rsrc(C, 0, 0x7fffffff, 0x00020000);
    if constexpr (MODE < 2) {
        const bool isq = ldc != 0;
        const float sc = isq ? 0.0625f * 1.4426950408889634f : 1.f;
        const unsigned voff = (unsigned)(tid2 & 63) * 16u;
#pragma unroll
        for (int i = 0; i < TM; ++i)
#pragma unroll
            for (int j = 0; j < TN; ++j)
#pragma unroll
                for (int sh = 0; sh < 2; ++sh) {
                    u32x4 w;
#pragma unroll
                    for (int r = 0; r < 4; ++r)
                        w[r] = pack2(acc[i][j][8 * sh + 2 * r] * sc, acc[i][j][8 * sh + 2 * r + 1] * sc);
                    unsigned so;
                    if (MODE == 1) so = (wm * 2 + (i >> 1)) * 32768 + ((2 * (i & 1) + sh) * 8 + wn * 2 + j) * 1024;
                    else if (isq) so = (wm * 4 + i) * 16384 + (wn * 4 + 2 * j + sh) * 1024;
                    else so = (wm * 2 + (i >> 1)) * 32768 + ((wn * 4 + 2 * j + sh) * 2 + (i & 1)) * 1024;
                    __builtin_amdgcn_raw_buffer_store_b128(w, rs, voff, so, 16);
                }
    } else {
        constexpr int CROW = BN + 8;
#pragma unroll
        for (int i = 0; i < TM; ++i)
#pragma unroll
            for (int j = 0; j < TN; ++j)
#pragma unroll
                for (int rr = 0; rr < 4; ++rr) {
                    u32x2 w;
                    w[0] = pack2(acc[i][j][4 * rr], acc[i][j][4 * rr + 1]);
                    w[1] = pack2(acc[i][j][4 * rr + 2], acc[i][j][4 * rr + 3]);
                    *reinterpret_cast<u32x2*>(smem + (wm * (BM / 2) + 32 * i + r32) * CROW + wn * (BN / 2) + 32 * j +
                                              8 * rr + 4 * hi) = w;
                }
        __syncthreads();
        const unsigned coff = ((unsigned)(tid2 / BPR) * ldc + (tid2 % BPR) * 8) * 2u;
#pragma unroll
        for (int i = 0; i < BM * BN / 2048; ++i) {
            int row = tid2 / BPR + BRS * i, cc = tid2 % BPR;
            u32x4 v = *reinterpret_cast<const u32x4*>(smem + row * CROW + cc * 8);
            __builtin_amdgcn_raw_buffer_store_b128(v, rs, coff + (unsigned)(BRS * i * ldc) * 2u, 0, 0);
        }
    }
}

struct MegaParams {
    const u16* B_x;
    const u16* B_ckv;
    const u16* B_wq;
    const u16* B_wuk;
    const u16* B_wuv;
    const u16* B_wo;
    u16* Qb;
    u16* Kb;
    u16* Vb;
    u16* OB;
    u16* out;
    unsigned* ctl;
};
constexpr int NWORK = 256 - NREC;
constexpr int CTL_QA = 0, CTL_KV = 1024, CTL_OC = 1280, CTL_DF = 1408, CTL_WORDS = 1408 + 112 * 64;
#define IDLE_SLP 32

__device__ __forceinline__ int next_ticket(unsigned* q, int* slot, int ws) {
    __syncthreads();
    if (tid_of(ws) == 0) *slot = (int)(__hip_atomic_fetch_add(q, 1u, __ATOMIC_RELAXED, __HIP_MEMORY_SCOPE_AGENT) + 1u);
    __syncthreads();
    return *slot;
}
__device__ __forceinline__ void publish_count(unsigned* c, int ws) {
    asm volatile("s_waitcnt vmcnt(0)" ::: "memory");
    __syncthreads();
    if (tid_of(ws) == 0) __hip_atomic_fetch_add(c, 1u, __ATOMIC_RELAXED, __HIP_MEMORY_SCOPE_AGENT);
}
__device__ __forceinline__ void wait_counts(unsigned* c, int n, unsigned target, int ws) {
    if (ws == 0) {
        const int lane = tid_of(0);
        for (unsigned spin = 0; spin < WSPIN_CAP; ++spin) {
            unsigned v = __hip_atomic_load(c + (lane < n ? lane : 0), __ATOMIC_RELAXED, __HIP_MEMORY_SCOPE_AGENT) + 1u;
            if (__all(v >= target)) break;
            __builtin_amdgcn_s_sleep(32);
        }
        acquire_agent();
    }
    __syncthreads();
}

__device__ __forceinline__ void worker_body(const MegaParams& m, u16* smem, int* slot, int ws) {
    unsigned* ctl = m.ctl;
    WTS(0);
    const int myx = (int)(__builtin_amdgcn_s_getreg((3 << 11) | 20) & 7u);
    for (int k = 0; k < 8; ++k) {
        const int x = (myx + k) & 7;
        for (;;) {
            int t = next_ticket(ctl + CTL_QA + 64 * x, slot, ws);
            if (t >= 128) break;
            KT0
            if (t >= 96) {
                const int at = t - 96, qb = 15 - (at >> 1), h = 2 * x + (at & 1);
                attnB_body(m.Qb, smem, qb, h, ws, ctl + CTL_KV + 16 * h);
                publish_count(ctl + CTL_OC + qb, ws);
                KT1(2);
                continue;
            }
            int rb, mat;
            if (t < 64) { rb = t >> 3; mat = (t >> 2) & 1; }
            else { rb = 7 - ((t - 64) >> 2); mat = 2; }
            const int cb = 4 * x + (t & 3);
            const int lda = (mat < 2) ? DC : DM;
            const u16* A = ((mat < 2) ? m.B_ckv : m.B_x) + (long)rb * 256 * lda;
            const u16* B = ((mat == 0) ? m.B_wuk : (mat == 1 ? m.B_wuv : m.B_wq)) + cb * 128;
            u16* C = ((mat == 0) ? m.Kb : (mat == 1 ? m.Vb : m.Qb)) + (long)(cb >> 1) * 524288 + rb * 65536 +
                     (cb & 1) * (mat == 0 ? 8192 : (mat == 1 ? 2048 : 4096));
            if (mat == 1) gemm_tile<256, 128, 1>(A, lda, B, HD, C, 0, lda / 64, smem, ws);
            else gemm_tile<256, 128, 0>(A, lda, B, HD, C, mat == 2, lda / 64, smem, ws);
            publish_count(ctl + CTL_KV + 16 * (cb >> 1) + (mat == 2 ? 8 : 0) + rb, ws);
            KT1(lda == DC ? 1 : 4);
        }
    }
    WTS(1);
    if (blockIdx.x >= 144) {
        const int b = blockIdx.x, x = b & 7, otile = (b - 144) >> 3;
        const int orb = 2 * x + (otile >= 7 ? 1 : 0), cb = otile >= 7 ? otile - 7 : otile;
        wait_counts(ctl + CTL_OC + orb, 1, 16u, ws);
        KT0
        gemm_tile<128, 64, 2>(m.OB + (long)orb * 128 * HD, HD, m.B_wo + cb * 64, DM,
                                         m.out + (long)orb * 128 * 896 + 448 + cb * 64, 896, HD / 64, smem, ws);
        KT1(9);
        WTS(2);
    }
    WTS(6);
    WTS(7);
}

constexpr int GEMM_LDS = 2 * (256 * 72 + 64 * 160) * 2;
constexpr int cmax(int a, int b) { return a > b ? a : b; }
constexpr int FUSED_LDS = cmax(cmax(ATTN_LDS, RECA_LDS), cmax(GEMM_LDS, RECC_LDS));
static_assert(AL_MEM * 2 + 8 * MROW * 2 <= RECA_LDS, "lds");

__global__ __launch_bounds__(256) void mega_kernel(RecParams p, MegaParams m) {
    __shared__ __attribute__((aligned(16))) u16 smem[FUSED_LDS / 2 + 8];
    const int b = blockIdx.x, ws = __builtin_amdgcn_readfirstlane(threadIdx.x >> 6);
    if (b < NA_BLK) {
        rec_A_body<false>(p, smem, b, 0, ws);
    } else if (b < NA_BLK + NC_BLK) {
        rec_C_body(p, smem, b - NA_BLK, ws);
    } else if (b < NA_BLK + NC_BLK + NB_BLK) {
        rec_A_body<true>(p, smem, (b - NA_BLK - NC_BLK) & 15, (b - NA_BLK - NC_BLK) >> 4, ws);
    } else if (b < NA_BLK + NC_BLK + NB_BLK + NO_BLK) {
        rec_O_body(p, smem, (b - NA_BLK - NC_BLK - NB_BLK) >> 1, b & 1, ws);
    } else {
        if (b < NREC) rec_D_body(p, smem, b - (NREC - ND_BLK), ws);
        worker_body(m, smem, reinterpret_cast<int*>(smem + FUSED_LDS / 2), ws);
    }
}

extern "C" void kernel_launch(void* const* d_in, const int* in_sizes, int n_in, void* d_out, int out_size,
                              void* d_ws, size_t ws_size, hipStream_t stream) {
    const u16* A_x = (const u16*)d_in[0];
    const u16* A_mem0 = (const u16*)d_in[1];
    const u16* A_Wq = (const u16*)d_in[2];
    const u16* A_Wk = (const u16*)d_in[3];
    const u16* A_Wv = (const u16*)d_in[4];
    const u16* A_Wo = (const u16*)d_in[5];
    const u16* B_x = (const u16*)d_in[7];
    const u16* B_ckv = (const u16*)d_in[8];
    const u16* B_wq = (const u16*)d_in[9];
    const u16* B_wuk = (const u16*)d_in[10];
    const u16* B_wuv = (const u16*)d_in[11];
    const u16* B_wo = (const u16*)d_in[12];
    u16* out = (u16*)d_out;

    char* ws = (char*)d_ws;
    const size_t SZ = (size_t)S_ * HD * 2;
    u16* Qb = (u16*)(ws + 0 * SZ);
    u16* Kb = (u16*)(ws + 1 * SZ);
    u16* Vb = (u16*)(ws + 2 * SZ);
    u16* OB = (u16*)(ws + 3 * SZ);
    size_t off = 4 * SZ;
    u16* AmatT = (u16*)(ws + off); off += (size_t)H_ * DM * DM * 2;
    u16* CmatT = (u16*)(ws + off); off += (size_t)H_ * DM * DM * 2;
    u16* XP = (u16*)(ws + off); off += (size_t)S_ * DM * 2;
    u16* Dg = (u16*)(ws + off); off += (size_t)64 * DG_SEG * 2;
    u16* Ug = (u16*)(ws + off); off += (size_t)64 * UG_SEG * 2;
    char* gbase = ws + off;
    u16* G1 = (u16*)gbase;
    u16* G2 = G1 + 63 * G1_SLOT;
    u16* G3 = G2 + 64 * G2_SLOT;
    unsigned* ctl = (unsigned*)(G3 + 64 * G3_SLOT);
    size_t gbytes = (size_t)(63 * G1_SLOT + 64 * G2_SLOT + 64 * G3_SLOT) * 2 + (size_t)CTL_WORDS * 4;

    hipLaunchKernelGGL(pre_kernel, dim3(224), dim3(256), 0, stream, A_Wq, A_Wk, A_Wv, A_Wo, AmatT, CmatT, (u32x4*)gbase,
                       (int)(gbytes / 16), A_x, (u32x4*)XP);
    RecParams rp;
    rp.x = XP; rp.mem0 = A_mem0; rp.AmatT = AmatT; rp.CmatT = CmatT; rp.G1 = G1; rp.G2 = G2; rp.G3 = G3; rp.out = out;
    rp.Dg = Dg; rp.Ug = Ug; rp.dflag = ctl + CTL_DF;
    MegaParams mp;
    mp.B_x = B_x; mp.B_ckv = B_ckv; mp.B_wq = B_wq; mp.B_wuk = B_wuk; mp.B_wuv = B_wuv; mp.B_wo = B_wo;
    mp.Qb = Qb; mp.Kb = Kb; mp.Vb = Vb; mp.OB = OB;
    mp.out = out; mp.ctl = ctl;
    hipLaunchKernelGGL(mega_kernel, dim3(256), dim3(256), 0, stream, rp, mp);
}
```

```cpp
#include <hip/hip_runtime.h>
#include <hip/hip_bf16.h>
#include <stdint.h>

typedef unsigned short u16;
typedef __attribute__((ext_vector_type(8))) short bf16x8;
typedef __attribute__((ext_vector_type(4))) short s16x4;
typedef __attribute__((ext_vector_type(16))) float f32x16;
typedef __attribute__((ext_vector_type(4))) float f32x4;
typedef __attribute__((ext_vector_type(2))) float f32x2;
typedef __attribute__((ext_vector_type(4))) unsigned u32x4;
typedef __attribute__((ext_vector_type(2))) unsigned u32x2;
typedef short v4i16_t __attribute__((ext_vector_type(4)));

#define LAS __attribute__((address_space(3)))

__device__ __forceinline__ float bf2f(u16 x) { return __uint_as_float(((unsigned)x) << 16); }
__device__ __forceinline__ u16 f2bf(float f) {
    unsigned u = __float_as_uint(f);
    u += 0x7fffu + ((u >> 16) & 1u);
    return (u16)(u >> 16);
}
typedef __bf16 bf16x2_t __attribute__((ext_vector_type(2)));
__device__ __forceinline__ unsigned pack2(float lo, float hi) {
    bf16x2_t r = {(__bf16)lo, (__bf16)hi};
    return __builtin_bit_cast(unsigned, r);
}
__device__ __forceinline__ s16x4 tr_read(const void* p) {
    return __builtin_bit_cast(s16x4, __builtin_amdgcn_ds_read_tr16_b64_v4i16((LAS v4i16_t*)p));
}
#define MFMA32(a, b, c) __builtin_amdgcn_mfma_f32_32x32x16_bf16(a, b, c, 0, 0, 0)
#define MFMA16(a, b, c) __builtin_amdgcn_mfma_f32_16x16x32_bf16(a, b, c, 0, 0, 0)

__device__ __forceinline__ int tid_of(int ws) {
    int l = (int)__builtin_amdgcn_mbcnt_hi(~0u, __builtin_amdgcn_mbcnt_lo(~0u, 0u));
    asm volatile("" : "+v"(l));
    return ws * 64 + l;
}
__device__ __forceinline__ int crow(int r, int hi) { return (r & 3) + 8 * (r >> 2) + 4 * hi; }

constexpr int S_ = 2048, DM = 448, H_ = 16, DH = 256, HD = 4096, MM = 8, SEG = 32, NSEG = 64, DC = 1024;

__device__ __forceinline__ int tile_col(int w, int t, int nl) {
    const int ga = nl >> 2, jj = nl & 3;
    const int base = t < 4 ? 64 * w + 16 * ga + 8 * (t >> 1) : (t < 6 ? 256 + 32 * w : 384 + 32 * (w >> 1)) + 8 * ga;
    return base + 4 * (jj >> 1) + 2 * (t < 6 ? (t & 1) : (w & 1)) + (jj & 1);
}
#define PTS(k)
#define PRE_E 4
#define WT_MOD "sc1"
__device__ __forceinline__ void st_wt(u32x4* p, u32x4 v) {
    asm volatile("global_store_dwordx4 %0, %1, off " WT_MOD "\n\ts_nop 1" ::"v"(p), "v"(v) : "memory");
}
template <bool isC>
__device__ __forceinline__ void pre_body(const u16* __restrict__ Wa, const u16* __restrict__ Wb, u32x4* __restrict__ F,
                                         u32x4* __restrict__ gbase, int gchunks, const u16* __restrict__ x,
                                         u32x4* __restrict__ xp, u16* Bs) {
    const int tid = threadIdx.x, lane = tid & 63, wid = __builtin_amdgcn_readfirstlane(tid >> 6);
    const int c16 = lane & 15, g = lane >> 4, q4 = c16 >> 2, p4 = lane & 3;
    const int bj = blockIdx.x >> 3, h = (2 * (blockIdx.x & 7) + (bj >> 3)) & 15;
    const int kh = bj & 1, q = (bj >> 1) & 3;
    u32x4 rbv[7];
    PTS(0);
#pragma unroll
    for (int i = 0; i < 7; ++i) {
        const int c = tid + 512 * i, e = c / 14;
        const u16* src = isC ? Wb + (long)(h * DH + e) * DM + q * 112 + (c - 14 * e) * 8
                             : Wb + (long)tile_col(q, i, (tid >> 5) & 15) * HD + h * DH + (tid & 31) * 8;
        rbv[i] = *reinterpret_cast<const u32x4*>(src);
    }
    const int kg = 7 * kh + wid;
    __amdgpu_buffer_rsrc_t rsa =
        __builtin_amdgcn_make_buffer_rsrc(const_cast<u16*>(wid < 7 ? Wa : x), 0, wid < 7 ? DM * HD * 2 : S_ * DM * 2, 0x00020000);
    const unsigned ao = wid < 7 ? ((32 * kg + 8 * q4 + p4) * HD + h * DH + 8 * g) * 2
                                : (((blockIdx.x >> 1) * 16 + c16) * DM + 224 * (blockIdx.x & 1) + 8 * g) * 2;
    const unsigned ab1 = wid < 7 ? 8 * HD : 0;
    bf16x8 af[2][8];
#pragma unroll
    for (int es = 0; es < PRE_E; ++es) {
        af[0][es] = __builtin_bit_cast(bf16x8, __builtin_amdgcn_raw_buffer_load_b128(rsa, ao + 64 * es, 0, 0));
        af[1][es] = __builtin_bit_cast(bf16x8, __builtin_amdgcn_raw_buffer_load_b128(rsa, ao + 64 * es, ab1, 0));
    }
    asm volatile("" ::: "memory");
#pragma unroll
    for (int i = 0; i < 7; ++i) {
        const int c = tid + 512 * i, e = c / 14;
        u16* dst = isC ? Bs + e * 144 + (c - 14 * e) * 8 : Bs + (c >> 5) * 264 + (c & 31) * 8;
        *reinterpret_cast<u32x4*>(dst) = rbv[i];
    }
    asm volatile("" ::: "memory");
#pragma unroll
    for (int es = PRE_E; es < 8; ++es) {
        af[0][es] = __builtin_bit_cast(bf16x8, __builtin_amdgcn_raw_buffer_load_b128(rsa, ao + 64 * es, 0, 0));
        af[1][es] = __builtin_bit_cast(bf16x8, __builtin_amdgcn_raw_buffer_load_b128(rsa, ao + 64 * es, ab1, 0));
    }
    asm volatile("" ::: "memory");
    __syncthreads();
    if (wid == 7) {
        for (int i = blockIdx.x * 64 + lane; i < gchunks; i += 256 * 64)
            st_wt(gbase + i, u32x4{0xffffffffu, 0xffffffffu, 0xffffffffu, 0xffffffffu});
#pragma unroll
        for (int es = 0; es < 7; ++es) st_wt(xp + (blockIdx.x * 7 + es) * 64 + lane, __builtin_bit_cast(u32x4, af[0][es]));
        return;
    }
    PTS(1);
    f32x4 acc[7][2];
#pragma unroll
    for (int t = 0; t < 7; ++t) acc[t][0] = acc[t][1] = f32x4{0.f, 0.f, 0.f, 0.f};
    bf16x8 bfr[2][7];
    auto rdB = [&](int es) {
#pragma unroll
        for (int t = 0; t < 7; ++t) {
            if (isC) {
                const u16* pB = Bs + (es * 32 + 8 * g + q4) * 144 + t * 16 + 4 * p4;
                s16x4 lo = tr_read(pB);
                s16x4 hi4 = tr_read(pB + 4 * 144);
                bfr[es & 1][t] = bf16x8{lo[0], lo[1], lo[2], lo[3], hi4[0], hi4[1], hi4[2], hi4[3]};
            } else {
                bfr[es & 1][t] = *reinterpret_cast<const bf16x8*>(Bs + (16 * t + c16) * 264 + es * 32 + 8 * g);
            }
        }
    };
    rdB(0);
#pragma unroll
    for (int es = 0; es < 8; ++es) {
        if (es < 7) rdB(es + 1);
        __builtin_amdgcn_sched_barrier(0);
#pragma unroll
        for (int t = 0; t < 7; ++t) {
            acc[t][0] = MFMA16(af[0][es], bfr[es & 1][t], acc[t][0]);
            acc[t][1] = MFMA16(af[1][es], bfr[es & 1][t], acc[t][1]);
        }
        __builtin_amdgcn_sched_barrier(0);
    }
    PTS(2);
    const float sc = isC ? 1.f : 0.0625f * 1.4426950408889634f;
    u32x4* op = F + (isC ? (7 * q * 16 + h) * 14 + kg : (h * 4 + q) * 98 + kg) * 64 + lane;
#pragma unroll
    for (int t = 0; t < 7; ++t)
        st_wt(op + t * (isC ? 16 * 14 * 64 : 14 * 64),
              u32x4{pack2(acc[t][0][0] * sc, acc[t][0][1] * sc), pack2(acc[t][0][2] * sc, acc[t][0][3] * sc),
                    pack2(acc[t][1][0] * sc, acc[t][1][1] * sc), pack2(acc[t][1][2] * sc, acc[t][1][3] * sc)});
    PTS(3);
}
__global__ __launch_bounds__(512) void pre_kernel(const u16* __restrict__ Wq, const u16* __restrict__ Wk,
                                                  const u16* __restrict__ Wv, const u16* __restrict__ Wo,
                                                  u32x4* __restrict__ AF, u32x4* __restrict__ CF,
                                                  u32x4* __restrict__ gbase, int gchunks,
                                                  const u16* __restrict__ x, u32x4* __restrict__ xp) {
    __shared__ __attribute__((aligned(16))) u16 Bs[256 * 144];
    if (blockIdx.x >= 128) pre_body<true>(Wv, Wo, CF, gbase, gchunks, x, xp, Bs);
    else pre_body<false>(Wq, Wk, AF, gbase, gchunks, x, xp, Bs);
}

constexpr unsigned WSPIN_CAP = 2000000u;
__device__ __forceinline__ void acquire_agent() {
    __builtin_amdgcn_fence(__ATOMIC_ACQUIRE, "agent");
    asm volatile("s_waitcnt vmcnt(0)" ::: "memory");
}
__device__ __forceinline__ int wait_kv(const unsigned* c, int qr, int need) {
    const int lane = tid_of(0);
    int ready = 8;
    for (unsigned spin = 0; spin < WSPIN_CAP; ++spin) {
        unsigned v = __hip_atomic_load(c + (lane & 15), __ATOMIC_RELAXED, __HIP_MEMORY_SCOPE_AGENT) + 1u;
        unsigned long long ok = __ballot(v >= ((lane & 8) ? 2u : 4u));
        ready = __builtin_ctz(~(unsigned)ok | 256u);
        if (ready >= need && ((ok >> (8 + qr)) & 1)) break;
        __builtin_amdgcn_s_sleep(32);
    }
    acquire_agent();
    return ready;
}

__device__ __forceinline__ void attn_asm(__amdgpu_buffer_rsrc_t rs, unsigned qo, unsigned ko, unsigned oo, unsigned nt,
                                         unsigned lds0, unsigned ldsw, unsigned thr, unsigned l16, int lim) {
    asm volatile(
        "s_mov_b32 s71, m0\n\t"
        "v_add_u32_e32 v178, 0x1000, %[l16]\n\t"
        "v_add_u32_e32 v176, %[lds0], %[l16]\n\t"
        "v_add_u32_e32 v177, 0x10000, v176\n\t"
        "s_mov_b32 s80, %[qo]\n\t"
        "buffer_load_dwordx4 a[128:131], %[l16], %[rs], s80 offen offset:0\n\t"
        "buffer_load_dwordx4 a[132:135], %[l16], %[rs], s80 offen offset:1024\n\t"
        "buffer_load_dwordx4 a[136:139], %[l16], %[rs], s80 offen offset:2048\n\t"
        "buffer_load_dwordx4 a[140:143], %[l16], %[rs], s80 offen offset:3072\n\t"
        "s_add_u32 s80, s80, 0x1000\n\t"
        "buffer_load_dwordx4 a[144:147], %[l16], %[rs], s80 offen offset:0\n\t"
        "buffer_load_dwordx4 a[148:151], %[l16], %[rs], s80 offen offset:1024\n\t"
        "buffer_load_dwordx4 a[152:155], %[l16], %[rs], s80 offen offset:2048\n\t"
        "buffer_load_dwordx4 a[156:159], %[l16], %[rs], s80 offen offset:3072\n\t"
        "s_add_u32 s80, s80, 0x1000\n\t"
        "buffer_load_dwordx4 a[160:163], %[l16], %[rs], s80 offen offset:0\n\t"
        "buffer_load_dwordx4 a[164:167], %[l16], %[rs], s80 offen offset:1024\n\t"
        "buffer_load_dwordx4 a[168:171], %[l16], %[rs], s80 offen offset:2048\n\t"
        "buffer_load_dwordx4 a[172:175], %[l16], %[rs], s80 offen offset:3072\n\t"
        "s_add_u32 s80, s80, 0x1000\n\t"
        "buffer_load_dwordx4 a[176:179], %[l16], %[rs], s80 offen offset:0\n\t"
        "buffer_load_dwordx4 a[180:183], %[l16], %[rs], s80 offen offset:1024\n\t"
        "buffer_load_dwordx4 a[184:187], %[l16], %[rs], s80 offen offset:2048\n\t"
        "buffer_load_dwordx4 a[188:191], %[l16], %[rs], s80 offen offset:3072\n\t"
        "s_mov_b32 s64, %[ko]\n\t"
        "s_add_u32 s66, %[ko], 0x1000000\n\t"
        "s_sub_u32 s80, %[nt], 1\n\t"
        "s_lshl_b32 s80, s80, 15\n\t"
        "s_add_u32 s65, s64, s80\n\t"
        "s_add_u32 s67, s66, s80\n\t"
        "s_add_u32 m0, %[ldsw], 0\n\t"
        "s_nop 0\n\t"
        "buffer_load_dwordx4 %[l16], %[rs], s64 offen offset:0 lds\n\t"
        "buffer_load_dwordx4 %[l16], %[rs], s64 offen offset:1024 lds\n\t"
        "buffer_load_dwordx4 %[l16], %[rs], s64 offen offset:2048 lds\n\t"
        "buffer_load_dwordx4 %[l16], %[rs], s64 offen offset:3072 lds\n\t"
        "s_add_u32 m0, %[ldsw], 4096\n\t"
        "s_nop 0\n\t"
        "buffer_load_dwordx4 v178, %[rs], s64 offen offset:0 lds\n\t"
        "buffer_load_dwordx4 v178, %[rs], s64 offen offset:1024 lds\n\t"
        "buffer_load_dwordx4 v178, %[rs], s64 offen offset:2048 lds\n\t"
        "buffer_load_dwordx4 v178, %[rs], s64 offen offset:3072 lds\n\t"
        "s_add_u32 s64, s64, 0x8000\n\t"
        "s_min_u32 s64, s64, s65\n\t"
        "s_add_u32 m0, %[ldsw], 32768\n\t"
        "s_nop 0\n\t"
        "buffer_load_dwordx4 %[l16], %[rs], s64 offen offset:0 lds\n\t"
        "buffer_load_dwordx4 %[l16], %[rs], s64 offen offset:1024 lds\n\t"
        "buffer_load_dwordx4 %[l16], %[rs], s64 offen offset:2048 lds\n\t"
        "buffer_load_dwordx4 %[l16], %[rs], s64 offen offset:3072 lds\n\t"
        "s_add_u32 m0, %[ldsw], 36864\n\t"
        "s_nop 0\n\t"
        "buffer_load_dwordx4 v178, %[rs], s64 offen offset:0 lds\n\t"
        "buffer_load_dwordx4 v178, %[rs], s64 offen offset:1024 lds\n\t"
        "buffer_load_dwordx4 v178, %[rs], s64 offen offset:2048 lds\n\t"
        "buffer_load_dwordx4 v178, %[rs], s64 offen offset:3072 lds\n\t"
        "s_add_u32 s64, s64, 0x8000\n\t"
        "s_min_u32 s64, s64, s65\n\t"
        "s_add_u32 m0, %[ldsw], 65536\n\t"
        "s_nop 0\n\t"
        "buffer_load_dwordx4 %[l16], %[rs], s66 offen offset:0 lds\n\t"
        "buffer_load_dwordx4 %[l16], %[rs], s66 offen offset:1024 lds\n\t"
        "buffer_load_dwordx4 %[l16], %[rs], s66 offen offset:2048 lds\n\t"
        "buffer_load_dwordx4 %[l16], %[rs], s66 offen offset:3072 lds\n\t"
        "s_add_u32 m0, %[ldsw], 69632\n\t"
        "s_nop 0\n\t"
        "buffer_load_dwordx4 v178, %[rs], s66 offen offset:0 lds\n\t"
        "buffer_load_dwordx4 v178, %[rs], s66 offen offset:1024 lds\n\t"
        "buffer_load_dwordx4 v178, %[rs], s66 offen offset:2048 lds\n\t"
        "buffer_load_dwordx4 v178, %[rs], s66 offen offset:3072 lds\n\t"
        "s_add_u32 s66, s66, 0x8000\n\t"
        "s_min_u32 s66, s66, s67\n\t"
        "s_add_u32 s68, %[nt], 1\n\t"
        "s_mov_b32 s69, 0xff800000\n\t"
        "s_mov_b32 s70, 0xff800000\n\t"
        "v_mov_b32_e32 v166, 0xff800000\n\t"
        "v_mov_b32_e32 v160, 0\n\t"
        "v_mov_b32_e32 v161, 0\n\t"
        "v_mov_b32_e32 v64, 0\n\t"
        "v_mov_b32_e32 v65, 0\n\t"
        "v_mov_b32_e32 v66, 0\n\t"
        "v_mov_b32_e32 v67, 0\n\t"
        "v_mov_b32_e32 v68, 0\n\t"
        "v_mov_b32_e32 v69, 0\n\t"
        "v_mov_b32_e32 v70, 0\n\t"
        "v_mov_b32_e32 v71, 0\n\t"
        "v_mov_b32_e32 v72, 0\n\t"
        "v_mov_b32_e32 v73, 0\n\t"
        "v_mov_b32_e32 v74, 0\n\t"
        "v_mov_b32_e32 v75, 0\n\t"
        "v_mov_b32_e32 v76, 0\n\t"
        "v_mov_b32_e32 v77, 0\n\t"
        "v_mov_b32_e32 v78, 0\n\t"
        "v_mov_b32_e32 v79, 0\n\t"
        "v_accvgpr_write_b32 a0, 0\n\t"
        "v_accvgpr_write_b32 a1, 0\n\t"
        "v_accvgpr_write_b32 a2, 0\n\t"
        "v_accvgpr_write_b32 a3, 0\n\t"
        "v_accvgpr_write_b32 a4, 0\n\t"
        "v_accvgpr_write_b32 a5, 0\n\t"
        "v_accvgpr_write_b32 a6, 0\n\t"
        "v_accvgpr_write_b32 a7, 0\n\t"
        "v_accvgpr_write_b32 a8, 0\n\t"
        "v_accvgpr_write_b32 a9, 0\n\t"
        "v_accvgpr_write_b32 a10, 0\n\t"
        "v_accvgpr_write_b32 a11, 0\n\t"
        "v_accvgpr_write_b32 a12, 0\n\t"
        "v_accvgpr_write_b32 a13, 0\n\t"
        "v_accvgpr_write_b32 a14, 0\n\t"
        "v_accvgpr_write_b32 a15, 0\n\t"
        "v_accvgpr_write_b32 a16, 0\n\t"
        "v_accvgpr_write_b32 a17, 0\n\t"
        "v_accvgpr_write_b32 a18, 0\n\t"
        "v_accvgpr_write_b32 a19, 0\n\t"
        "v_accvgpr_write_b32 a20, 0\n\t"
        "v_accvgpr_write_b32 a21, 0\n\t"
        "v_accvgpr_write_b32 a22, 0\n\t"
        "v_accvgpr_write_b32 a23, 0\n\t"
        "v_accvgpr_write_b32 a24, 0\n\t"
        "v_accvgpr_write_b32 a25, 0\n\t"
        "v_accvgpr_write_b32 a26, 0\n\t"
        "v_accvgpr_write_b32 a27, 0\n\t"
        "v_accvgpr_write_b32 a28, 0\n\t"
        "v_accvgpr_write_b32 a29, 0\n\t"
        "v_accvgpr_write_b32 a30, 0\n\t"
        "v_accvgpr_write_b32 a31, 0\n\t"
        "v_accvgpr_write_b32 a32, 0\n\t"
        "v_accvgpr_write_b32 a33, 0\n\t"
        "v_accvgpr_write_b32 a34, 0\n\t"
        "v_accvgpr_write_b32 a35, 0\n\t"
        "v_accvgpr_write_b32 a36, 0\n\t"
        "v_accvgpr_write_b32 a37, 0\n\t"
        "v_accvgpr_write_b32 a38, 0\n\t"
        "v_accvgpr_write_b32 a39, 0\n\t"
        "v_accvgpr_write_b32 a40, 0\n\t"
        "v_accvgpr_write_b32 a41, 0\n\t"
        "v_accvgpr_write_b32 a42, 0\n\t"
        "v_accvgpr_write_b32 a43, 0\n\t"
        "v_accvgpr_write_b32 a44, 0\n\t"
        "v_accvgpr_write_b32 a45, 0\n\t"
        "v_accvgpr_write_b32 a46, 0\n\t"
        "v_accvgpr_write_b32 a47, 0\n\t"
        "v_accvgpr_write_b32 a48, 0\n\t"
        "v_accvgpr_write_b32 a49, 0\n\t"
        "v_accvgpr_write_b32 a50, 0\n\t"
        "v_accvgpr_write_b32 a51, 0\n\t"
        "v_accvgpr_write_b32 a52, 0\n\t"
        "v_accvgpr_write_b32 a53, 0\n\t"
        "v_accvgpr_write_b32 a54, 0\n\t"
        "v_accvgpr_write_b32 a55, 0\n\t"
        "v_accvgpr_write_b32 a56, 0\n\t"
        "v_accvgpr_write_b32 a57, 0\n\t"
        "v_accvgpr_write_b32 a58, 0\n\t"
        "v_accvgpr_write_b32 a59, 0\n\t"
        "v_accvgpr_write_b32 a60, 0\n\t"
        "v_accvgpr_write_b32 a61, 0\n\t"
        "v_accvgpr_write_b32 a62, 0\n\t"
        "v_accvgpr_write_b32 a63, 0\n\t"
        "v_accvgpr_write_b32 a64, 0\n\t"
        "v_accvgpr_write_b32 a65, 0\n\t"
        "v_accvgpr_write_b32 a66, 0\n\t"
        "v_accvgpr_write_b32 a67, 0\n\t"
        "v_accvgpr_write_b32 a68, 0\n\t"
        "v_accvgpr_write_b32 a69, 0\n\t"
        "v_accvgpr_write_b32 a70, 0\n\t"
        "v_accvgpr_write_b32 a71, 0\n\t"
        "v_accvgpr_write_b32 a72, 0\n\t"
        "v_accvgpr_write_b32 a73, 0\n\t"
        "v_accvgpr_write_b32 a74, 0\n\t"
        "v_accvgpr_write_b32 a75, 0\n\t"
        "v_accvgpr_write_b32 a76, 0\n\t"
        "v_accvgpr_write_b32 a77, 0\n\t"
        "v_accvgpr_write_b32 a78, 0\n\t"
        "v_accvgpr_write_b32 a79, 0\n\t"
        "v_accvgpr_write_b32 a80, 0\n\t"
        "v_accvgpr_write_b32 a81, 0\n\t"
        "v_accvgpr_write_b32 a82, 0\n\t"
        "v_accvgpr_write_b32 a83, 0\n\t"
        "v_accvgpr_write_b32 a84, 0\n\t"
        "v_accvgpr_write_b32 a85, 0\n\t"
        "v_accvgpr_write_b32 a86, 0\n\t"
        "v_accvgpr_write_b32 a87, 0\n\t"
        "v_accvgpr_write_b32 a88, 0\n\t"
        "v_accvgpr_write_b32 a89, 0\n\t"
        "v_accvgpr_write_b32 a90, 0\n\t"
        "v_accvgpr_write_b32 a91, 0\n\t"
        "v_accvgpr_write_b32 a92, 0\n\t"
        "v_accvgpr_write_b32 a93, 0\n\t"
        "v_accvgpr_write_b32 a94, 0\n\t"
        "v_accvgpr_write_b32 a95, 0\n\t"
        "v_accvgpr_write_b32 a96, 0\n\t"
        "v_accvgpr_write_b32 a97, 0\n\t"
        "v_accvgpr_write_b32 a98, 0\n\t"
        "v_accvgpr_write_b32 a99, 0\n\t"
        "v_accvgpr_write_b32 a100, 0\n\t"
        "v_accvgpr_write_b32 a101, 0\n\t"
        "v_accvgpr_write_b32 a102, 0\n\t"
        "v_accvgpr_write_b32 a103, 0\n\t"
        "v_accvgpr_write_b32 a104, 0\n\t"
        "v_accvgpr_write_b32 a105, 0\n\t"
        "v_accvgpr_write_b32 a106, 0\n\t"
        "v_accvgpr_write_b32 a107, 0\n\t"
        "v_accvgpr_write_b32 a108, 0\n\t"
        "v_accvgpr_write_b32 a109, 0\n\t"
        "v_accvgpr_write_b32 a110, 0\n\t"
        "v_accvgpr_write_b32 a111, 0\n\t"
        "v_accvgpr_write_b32 a112, 0\n\t"
        "v_accvgpr_write_b32 a113, 0\n\t"
        "v_accvgpr_write_b32 a114, 0\n\t"
        "v_accvgpr_write_b32 a115, 0\n\t"
        "v_accvgpr_write_b32 a116, 0\n\t"
        "v_accvgpr_write_b32 a117, 0\n\t"
        "v_accvgpr_write_b32 a118, 0\n\t"
        "v_accvgpr_write_b32 a119, 0\n\t"
        "v_accvgpr_write_b32 a120, 0\n\t"
        "v_accvgpr_write_b32 a121, 0\n\t"
        "v_accvgpr_write_b32 a122, 0\n\t"
        "v_accvgpr_write_b32 a123, 0\n\t"
        "v_accvgpr_write_b32 a124, 0\n\t"
        "v_accvgpr_write_b32 a125, 0\n\t"
        "v_accvgpr_write_b32 a126, 0\n\t"
        "v_accvgpr_write_b32 a127, 0\n\t"
        "s_waitcnt vmcnt(16)\n\t"
        "s_barrier\n\t"
        "ds_read_b128 v[96:99], v176 offset:0\n\t"
        "ds_read_b128 v[100:103], v176 offset:1024\n\t"
        "ds_read_b128 v[104:107], v176 offset:2048\n\t"
        "ds_read_b128 v[108:111], v176 offset:3072\n\t"
        "ds_read_b128 v[112:115], v176 offset:4096\n\t"
        "ds_read_b128 v[116:119], v176 offset:5120\n\t"
        "ds_read_b128 v[120:123], v176 offset:6144\n\t"
        "ds_read_b128 v[124:127], v176 offset:7168\n\t"
        "ds_read_b128 v[128:131], v176 offset:8192\n\t"
        "ds_read_b128 v[132:135], v176 offset:9216\n\t"
        "ds_read_b128 v[136:139], v176 offset:10240\n\t"
        "ds_read_b128 v[140:143], v176 offset:11264\n\t"
        "s_waitcnt lgkmcnt(10)\n\t"
        "v_mfma_f32_32x32x16_bf16 v[0:15], v[96:99], a[128:131], v[64:79]\n\t"
        "ds_read_b128 v[144:147], v176 offset:12288\n\t"
        "v_mfma_f32_32x32x16_bf16 v[16:31], v[100:103], a[128:131], v[64:79]\n\t"
        "ds_read_b128 v[148:151], v176 offset:13312\n\t"
        "s_waitcnt lgkmcnt(10)\n\t"
        "v_mfma_f32_32x32x16_bf16 v[0:15], v[104:107], a[132:135], v[0:15]\n\t"
        "ds_read_b128 v[152:155], v176 offset:14336\n\t"
        "s_add_u32 m0, %[ldsw], 98304\n\t"
        "s_nop 0\n\t"
        "buffer_load_dwordx4 %[l16], %[rs], s64 offen offset:0 lds\n\t"
        "v_mfma_f32_32x32x16_bf16 v[16:31], v[108:111], a[132:135], v[16:31]\n\t"
        "ds_read_b128 v[156:159], v176 offset:15360\n\t"
        "s_waitcnt lgkmcnt(10)\n\t"
        "v_mfma_f32_32x32x16_bf16 v[0:15], v[112:115], a[136:139], v[0:15]\n\t"
        "ds_read_b128 v[96:99], v176 offset:16384\n\t"
        "v_mfma_f32_32x32x16_bf16 v[16:31], v[116:119], a[136:139], v[16:31]\n\t"
        "ds_read_b128 v[100:103], v176 offset:17408\n\t"
        "buffer_load_dwordx4 %[l16], %[rs], s64 offen offset:1024 lds\n\t"
        "s_waitcnt lgkmcnt(10)\n\t"
        "v_mfma_f32_32x32x16_bf16 v[0:15], v[120:123], a[140:143], v[0:15]\n\t"
        "ds_read_b128 v[104:107], v176 offset:18432\n\t"
        "v_mfma_f32_32x32x16_bf16 v[16:31], v[124:127], a[140:143], v[16:31]\n\t"
        "ds_read_b128 v[108:111], v176 offset:19456\n\t"
        "s_waitcnt lgkmcnt(10)\n\t"
        "v_mfma_f32_32x32x16_bf16 v[0:15], v[128:131], a[144:147], v[0:15]\n\t"
        "ds_read_b128 v[112:115], v176 offset:20480\n\t"
        "buffer_load_dwordx4 %[l16], %[rs], s64 offen offset:2048 lds\n\t"
        "v_mfma_f32_32x32x16_bf16 v[16:31], v[132:135], a[144:147], v[16:31]\n\t"
        "ds_read_b128 v[116:119], v176 offset:21504\n\t"
        "s_waitcnt lgkmcnt(10)\n\t"
        "v_mfma_f32_32x32x16_bf16 v[0:15], v[136:139], a[148:151], v[0:15]\n\t"
        "ds_read_b128 v[120:123], v176 offset:22528\n\t"
        "v_mfma_f32_32x32x16_bf16 v[16:31], v[140:143], a[148:151], v[16:31]\n\t"
        "ds_read_b128 v[124:127], v176 offset:23552\n\t"
        "buffer_load_dwordx4 %[l16], %[rs], s64 offen offset:3072 lds\n\t"
        "s_waitcnt lgkmcnt(10)\n\t"
        "v_mfma_f32_32x32x16_bf16 v[0:15], v[144:147], a[152:155], v[0:15]\n\t"
        "ds_read_b128 v[128:131], v176 offset:24576\n\t"
        "v_mfma_f32_32x32x16_bf16 v[16:31], v[148:151], a[152:155], v[16:31]\n\t"
        "ds_read_b128 v[132:135], v176 offset:25600\n\t"
        "s_waitcnt lgkmcnt(10)\n\t"
        "v_mfma_f32_32x32x16_bf16 v[0:15], v[152:155], a[156:159], v[0:15]\n\t"
        "ds_read_b128 v[136:139], v176 offset:26624\n\t"
        "s_add_u32 m0, %[ldsw], 102400\n\t"
        "s_nop 0\n\t"
        "buffer_load_dwordx4 v178, %[rs], s64 offen offset:0 lds\n\t"
        "v_mfma_f32_32x32x16_bf16 v[16:31], v[156:159], a[156:159], v[16:31]\n\t"
        "ds_read_b128 v[140:143], v176 offset:27648\n\t"
        "s_waitcnt lgkmcnt(10)\n\t"
        "v_mfma_f32_32x32x16_bf16 v[0:15], v[96:99], a[160:163], v[0:15]\n\t"
        "ds_read_b128 v[144:147], v176 offset:28672\n\t"
        "v_mfma_f32_32x32x16_bf16 v[16:31], v[100:103], a[160:163], v[16:31]\n\t"
        "ds_read_b128 v[148:151], v176 offset:29696\n\t"
        "buffer_load_dwordx4 v178, %[rs], s64 offen offset:1024 lds\n\t"
        "s_waitcnt lgkmcnt(10)\n\t"
        "v_mfma_f32_32x32x16_bf16 v[0:15], v[104:107], a[164:167], v[0:15]\n\t"
        "ds_read_b128 v[152:155], v176 offset:30720\n\t"
        "v_mfma_f32_32x32x16_bf16 v[16:31], v[108:111], a[164:167], v[16:31]\n\t"
        "ds_read_b128 v[156:159], v176 offset:31744\n\t"
        "s_waitcnt lgkmcnt(10)\n\t"
        "v_mfma_f32_32x32x16_bf16 v[0:15], v[112:115], a[168:171], v[0:15]\n\t"
        "buffer_load_dwordx4 v178, %[rs], s64 offen offset:2048 lds\n\t"
        "v_mfma_f32_32x32x16_bf16 v[16:31], v[116:119], a[168:171], v[16:31]\n\t"
        "s_waitcnt lgkmcnt(8)\n\t"
        "v_mfma_f32_32x32x16_bf16 v[0:15], v[120:123], a[172:175], v[0:15]\n\t"
        "v_mfma_f32_32x32x16_bf16 v[16:31], v[124:127], a[172:175], v[16:31]\n\t"
        "buffer_load_dwordx4 v178, %[rs], s64 offen offset:3072 lds\n\t"
        "s_add_u32 s64, s64, 0x8000\n\t"
        "s_min_u32 s64, s64, s65\n\t"
        "s_waitcnt lgkmcnt(6)\n\t"
        "v_mfma_f32_32x32x16_bf16 v[0:15], v[128:131], a[176:179], v[0:15]\n\t"
        "v_mfma_f32_32x32x16_bf16 v[16:31], v[132:135], a[176:179], v[16:31]\n\t"
        "s_waitcnt vmcnt(16) lgkmcnt(0)\n\t"
        "s_barrier\n\t"
        "ds_read_b128 v[96:99], v176 offset:32768\n\t"
        "ds_read_b128 v[100:103], v176 offset:33792\n\t"
        "v_mfma_f32_32x32x16_bf16 v[0:15], v[136:139], a[180:183], v[0:15]\n\t"
        "ds_read_b128 v[104:107], v176 offset:34816\n\t"
        "ds_read_b128 v[108:111], v176 offset:35840\n\t"
        "v_mfma_f32_32x32x16_bf16 v[16:31], v[140:143], a[180:183], v[16:31]\n\t"
        "ds_read_b128 v[112:115], v176 offset:36864\n\t"
        "ds_read_b128 v[116:119], v176 offset:37888\n\t"
        "v_mfma_f32_32x32x16_bf16 v[0:15], v[144:147], a[184:187], v[0:15]\n\t"
        "ds_read_b128 v[120:123], v176 offset:38912\n\t"
        "ds_read_b128 v[124:127], v176 offset:39936\n\t"
        "v_mfma_f32_32x32x16_bf16 v[16:31], v[148:151], a[184:187], v[16:31]\n\t"
        "ds_read_b128 v[128:131], v176 offset:40960\n\t"
        "ds_read_b128 v[132:135], v176 offset:41984\n\t"
        "v_mfma_f32_32x32x16_bf16 v[0:15], v[152:155], a[188:191], v[0:15]\n\t"
        "ds_read_b128 v[136:139], v176 offset:43008\n\t"
        "ds_read_b128 v[140:143], v176 offset:44032\n\t"
        "v_mfma_f32_32x32x16_bf16 v[16:31], v[156:159], a[188:191], v[16:31]\n\t"
        "s_nop 7\n\t"
        "s_nop 3\n\t"
        "v_max3_f32 v162, v0, v1, v2\n\t"
        "v_max3_f32 v163, v3, v4, v5\n\t"
        "v_max3_f32 v162, v162, v6, v7\n\t"
        "v_max3_f32 v163, v163, v8, v9\n\t"
        "v_max3_f32 v162, v162, v10, v11\n\t"
        "v_max3_f32 v163, v163, v12, v13\n\t"
        "v_max3_f32 v162, v162, v14, v15\n\t"
        "v_max3_f32 v163, v163, v16, v17\n\t"
        "v_max3_f32 v162, v162, v18, v19\n\t"
        "v_max3_f32 v163, v163, v20, v21\n\t"
        "v_max3_f32 v162, v162, v22, v23\n\t"
        "v_max3_f32 v163, v163, v24, v25\n\t"
        "v_max3_f32 v162, v162, v26, v27\n\t"
        "v_max3_f32 v163, v163, v28, v29\n\t"
        "v_max3_f32 v162, v162, v30, v31\n\t"
        "v_max_f32_e32 v162, v162, v163\n\t"
        "v_cmp_lt_f32_e32 vcc, s69, v162\n\t"
        "s_branch LTO_%=\n\t"
        ".p2align 6\n\t"
        "LOOP_%=:\n\t"
        "s_waitcnt lgkmcnt(10)\n\t"
        "v_mfma_f32_32x32x16_bf16 v[32:47], v[96:99], a[128:131], v[64:79]\n\t"
        "ds_read_b128 v[144:147], v176 offset:45056\n\t"
        "v_exp_f32_e32 v0, v0\n\t"
        "v_mfma_f32_32x32x16_bf16 v[48:63], v[100:103], a[128:131], v[64:79]\n\t"
        "ds_read_b128 v[148:151], v176 offset:46080\n\t"
        "v_exp_f32_e32 v1, v1\n\t"
        "v_add_f32_e32 v160, v160, v0\n\t"
        "s_waitcnt lgkmcnt(10)\n\t"
        "v_mfma_f32_32x32x16_bf16 v[32:47], v[104:107], a[132:135], v[32:47]\n\t"
        "ds_read_b128 v[152:155], v176 offset:47104\n\t"
        "v_add_f32_e32 v161, v161, v1\n\t"
        "v_cvt_pk_bf16_f32 v80, v0, v1\n\t"
        "s_add_u32 m0, %[ldsw], 0\n\t"
        "s_nop 0\n\t"
        "buffer_load_dwordx4 %[l16], %[rs], s66 offen offset:0 lds\n\t"
        "v_mfma_f32_32x32x16_bf16 v[48:63], v[108:111], a[132:135], v[48:63]\n\t"
        "ds_read_b128 v[156:159], v176 offset:48128\n\t"
        "v_exp_f32_e32 v2, v2\n\t"
        "s_waitcnt lgkmcnt(10)\n\t"
        "v_mfma_f32_32x32x16_bf16 v[32:47], v[112:115], a[136:139], v[32:47]\n\t"
        "ds_read_b128 v[96:99], v176 offset:49152\n\t"
        "v_exp_f32_e32 v3, v3\n\t"
        "v_add_f32_e32 v160, v160, v2\n\t"
        "v_mfma_f32_32x32x16_bf16 v[48:63], v[116:119], a[136:139], v[48:63]\n\t"
        "ds_read_b128 v[100:103], v176 offset:50176\n\t"
        "v_add_f32_e32 v161, v161, v3\n\t"
        "v_cvt_pk_bf16_f32 v81, v2, v3\n\t"
        "buffer_load_dwordx4 %[l16], %[rs], s66 offen offset:1024 lds\n\t"
        "s_waitcnt lgkmcnt(10)\n\t"
        "v_mfma_f32_32x32x16_bf16 v[32:47], v[120:123], a[140:143], v[32:47]\n\t"
        "ds_read_b128 v[104:107], v176 offset:51200\n\t"
        "v_exp_f32_e32 v4, v4\n\t"
        "v_mfma_f32_32x32x16_bf16 v[48:63], v[124:127], a[140:143], v[48:63]\n\t"
        "ds_read_b128 v[108:111], v176 offset:52224\n\t"
        "v_exp_f32_e32 v5, v5\n\t"
        "v_add_f32_e32 v160, v160, v4\n\t"
        "s_waitcnt lgkmcnt(10)\n\t"
        "v_mfma_f32_32x32x16_bf16 v[32:47], v[128:131], a[144:147], v[32:47]\n\t"
        "ds_read_b128 v[112:115], v176 offset:53248\n\t"
        "v_add_f32_e32 v161, v161, v5\n\t"
        "v_cvt_pk_bf16_f32 v82, v4, v5\n\t"
        "buffer_load_dwordx4 %[l16], %[rs], s66 offen offset:2048 lds\n\t"
        "v_mfma_f32_32x32x16_bf16 v[48:63], v[132:135], a[144:147], v[48:63]\n\t"
        "ds_read_b128 v[116:119], v176 offset:54272\n\t"
        "v_exp_f32_e32 v6, v6\n\t"
        "s_waitcnt lgkmcnt(10)\n\t"
        "v_mfma_f32_32x32x16_bf16 v[32:47], v[136:139], a[148:151], v[32:47]\n\t"
        "ds_read_b128 v[120:123], v176 offset:55296\n\t"
        "v_exp_f32_e32 v7, v7\n\t"
        "v_add_f32_e32 v160, v160, v6\n\t"
        "v_mfma_f32_32x32x16_bf16 v[48:63], v[140:143], a[148:151], v[48:63]\n\t"
        "ds_read_b128 v[124:127], v176 offset:56320\n\t"
        "v_add_f32_e32 v161, v161, v7\n\t"
        "v_cvt_pk_bf16_f32 v83, v6, v7\n\t"
        "buffer_load_dwordx4 %[l16], %[rs], s66 offen offset:3072 lds\n\t"
        "s_waitcnt lgkmcnt(10)\n\t"
        "v_mfma_f32_32x32x16_bf16 v[32:47], v[144:147], a[152:155], v[32:47]\n\t"
        "ds_read_b128 v[128:131], v176 offset:57344\n\t"
        "v_exp_f32_e32 v8, v8\n\t"
        "v_mfma_f32_32x32x16_bf16 v[48:63], v[148:151], a[152:155], v[48:63]\n\t"
        "ds_read_b128 v[132:135], v176 offset:58368\n\t"
        "v_exp_f32_e32 v9, v9\n\t"
        "v_add_f32_e32 v160, v160, v8\n\t"
        "s_waitcnt lgkmcnt(10)\n\t"
        "v_mfma_f32_32x32x16_bf16 v[32:47], v[152:155], a[156:159], v[32:47]\n\t"
        "ds_read_b128 v[136:139], v176 offset:59392\n\t"
        "v_add_f32_e32 v161, v161, v9\n\t"
        "v_cvt_pk_bf16_f32 v84, v8, v9\n\t"
        "s_add_u32 m0, %[ldsw], 4096\n\t"
        "s_nop 0\n\t"
        "buffer_load_dwordx4 v178, %[rs], s66 offen offset:0 lds\n\t"
        "v_mfma_f32_32x32x16_bf16 v[48:63], v[156:159], a[156:159], v[48:63]\n\t"
        "ds_read_b128 v[140:143], v176 offset:60416\n\t"
        "v_exp_f32_e32 v10, v10\n\t"
        "s_waitcnt lgkmcnt(10)\n\t"
        "v_mfma_f32_32x32x16_bf16 v[32:47], v[96:99], a[160:163], v[32:47]\n\t"
        "ds_read_b128 v[144:147], v176 offset:61440\n\t"
        "v_exp_f32_e32 v11, v11\n\t"
        "v_add_f32_e32 v160, v160, v10\n\t"
        "v_mfma_f32_32x32x16_bf16 v[48:63], v[100:103], a[160:163], v[48:63]\n\t"
        "ds_read_b128 v[148:151], v176 offset:62464\n\t"
        "v_add_f32_e32 v161, v161, v11\n\t"
        "v_cvt_pk_bf16_f32 v85, v10, v11\n\t"
        "buffer_load_dwordx4 v178, %[rs], s66 offen offset:1024 lds\n\t"
        "s_waitcnt lgkmcnt(10)\n\t"
        "v_mfma_f32_32x32x16_bf16 v[32:47], v[104:107], a[164:167], v[32:47]\n\t"
        "ds_read_b128 v[152:155], v176 offset:63488\n\t"
        "v_exp_f32_e32 v12, v12\n\t"
        "v_mfma_f32_32x32x16_bf16 v[48:63], v[108:111], a[164:167], v[48:63]\n\t"
        "ds_read_b128 v[156:159], v176 offset:64512\n\t"
        "v_exp_f32_e32 v13, v13\n\t"
        "v_add_f32_e32 v160, v160, v12\n\t"
        "s_waitcnt lgkmcnt(10)\n\t"
        "v_mfma_f32_32x32x16_bf16 v[32:47], v[112:115], a[168:171], v[32:47]\n\t"
        "v_add_f32_e32 v161, v161, v13\n\t"
        "v_cvt_pk_bf16_f32 v86, v12, v13\n\t"
        "buffer_load_dwordx4 v178, %[rs], s66 offen offset:2048 lds\n\t"
        "v_mfma_f32_32x32x16_bf16 v[48:63], v[116:119], a[168:171], v[48:63]\n\t"
        "v_exp_f32_e32 v14, v14\n\t"
        "s_waitcnt lgkmcnt(8)\n\t"
        "v_mfma_f32_32x32x16_bf16 v[32:47], v[120:123], a[172:175], v[32:47]\n\t"
        "v_exp_f32_e32 v15, v15\n\t"
        "v_add_f32_e32 v160, v160, v14\n\t"
        "v_mfma_f32_32x32x16_bf16 v[48:63], v[124:127], a[172:175], v[48:63]\n\t"
        "v_add_f32_e32 v161, v161, v15\n\t"
        "v_cvt_pk_bf16_f32 v87, v14, v15\n\t"
        "buffer_load_dwordx4 v178, %[rs], s66 offen offset:3072 lds\n\t"
        "s_add_u32 s66, s66, 0x8000\n\t"
        "s_min_u32 s66, s66, s67\n\t"
        "s_waitcnt lgkmcnt(6)\n\t"
        "v_mfma_f32_32x32x16_bf16 v[32:47], v[128:131], a[176:179], v[32:47]\n\t"
        "v_exp_f32_e32 v16, v16\n\t"
        "v_mfma_f32_32x32x16_bf16 v[48:63], v[132:135], a[176:179], v[48:63]\n\t"
        "v_exp_f32_e32 v17, v17\n\t"
        "v_add_f32_e32 v160, v160, v16\n\t"
        "s_waitcnt vmcnt(16) lgkmcnt(0)\n\t"
        "s_barrier\n\t"
        "ds_read_b128 v[96:99], v177 offset:0\n\t"
        "ds_read_b128 v[100:103], v177 offset:1024\n\t"
        "v_mfma_f32_32x32x16_bf16 v[32:47], v[136:139], a[180:183], v[32:47]\n\t"
        "v_add_f32_e32 v161, v161, v17\n\t"
        "v_cvt_pk_bf16_f32 v88, v16, v17\n\t"
        "ds_read_b128 v[104:107], v177 offset:2048\n\t"
        "ds_read_b128 v[108:111], v177 offset:3072\n\t"
        "v_mfma_f32_32x32x16_bf16 v[48:63], v[140:143], a[180:183], v[48:63]\n\t"
        "v_exp_f32_e32 v18, v18\n\t"
        "ds_read_b128 v[112:115], v177 offset:4096\n\t"
        "ds_read_b128 v[116:119], v177 offset:5120\n\t"
        "v_mfma_f32_32x32x16_bf16 v[32:47], v[144:147], a[184:187], v[32:47]\n\t"
        "v_exp_f32_e32 v19, v19\n\t"
        "v_add_f32_e32 v160, v160, v18\n\t"
        "ds_read_b128 v[120:123], v177 offset:6144\n\t"
        "ds_read_b128 v[124:127], v177 offset:7168\n\t"
        "v_mfma_f32_32x32x16_bf16 v[48:63], v[148:151], a[184:187], v[48:63]\n\t"
        "v_add_f32_e32 v161, v161, v19\n\t"
        "v_cvt_pk_bf16_f32 v89, v18, v19\n\t"
        "ds_read_b128 v[128:131], v177 offset:8192\n\t"
        "ds_read_b128 v[132:135], v177 offset:9216\n\t"
        "v_mfma_f32_32x32x16_bf16 v[32:47], v[152:155], a[188:191], v[32:47]\n\t"
        "v_exp_f32_e32 v20, v20\n\t"
        "ds_read_b128 v[136:139], v177 offset:10240\n\t"
        "ds_read_b128 v[140:143], v177 offset:11264\n\t"
        "v_mfma_f32_32x32x16_bf16 v[48:63], v[156:159], a[188:191], v[48:63]\n\t"
        "v_exp_f32_e32 v21, v21\n\t"
        "v_add_f32_e32 v160, v160, v20\n\t"
        "s_waitcnt lgkmcnt(10)\n\t"
        "v_mfma_f32_32x32x16_bf16 a[0:15], v[96:99], v[80:83], a[0:15]\n\t"
        "ds_read_b128 v[144:147], v177 offset:12288\n\t"
        "v_add_f32_e32 v161, v161, v21\n\t"
        "v_cvt_pk_bf16_f32 v90, v20, v21\n\t"
        "s_add_u32 m0, %[ldsw], 32768\n\t"
        "s_nop 0\n\t"
        "buffer_load_dwordx4 %[l16], %[rs], s64 offen offset:0 lds\n\t"
        "v_mfma_f32_32x32x16_bf16 a[16:31], v[100:103], v[80:83], a[16:31]\n\t"
        "ds_read_b128 v[148:151], v177 offset:13312\n\t"
        "v_exp_f32_e32 v22, v22\n\t"
        "s_waitcnt lgkmcnt(10)\n\t"
        "v_mfma_f32_32x32x16_bf16 a[32:47], v[104:107], v[80:83], a[32:47]\n\t"
        "ds_read_b128 v[152:155], v177 offset:14336\n\t"
        "v_exp_f32_e32 v23, v23\n\t"
        "v_add_f32_e32 v160, v160, v22\n\t"
        "v_mfma_f32_32x32x16_bf16 a[48:63], v[108:111], v[80:83], a[48:63]\n\t"
        "ds_read_b128 v[156:159], v177 offset:15360\n\t"
        "v_add_f32_e32 v161, v161, v23\n\t"
        "v_cvt_pk_bf16_f32 v91, v22, v23\n\t"
        "buffer_load_dwordx4 %[l16], %[rs], s64 offen offset:1024 lds\n\t"
        "s_waitcnt lgkmcnt(10)\n\t"
        "v_mfma_f32_32x32x16_bf16 a[64:79], v[112:115], v[80:83], a[64:79]\n\t"
        "ds_read_b128 v[96:99], v177 offset:16384\n\t"
        "v_exp_f32_e32 v24, v24\n\t"
        "v_mfma_f32_32x32x16_bf16 a[80:95], v[116:119], v[80:83], a[80:95]\n\t"
        "ds_read_b128 v[100:103], v177 offset:17408\n\t"
        "v_exp_f32_e32 v25, v25\n\t"
        "v_add_f32_e32 v160, v160, v24\n\t"
        "s_waitcnt lgkmcnt(10)\n\t"
        "v_mfma_f32_32x32x16_bf16 a[96:111], v[120:123], v[80:83], a[96:111]\n\t"
        "ds_read_b128 v[104:107], v177 offset:18432\n\t"
        "v_add_f32_e32 v161, v161, v25\n\t"
        "v_cvt_pk_bf16_f32 v92, v24, v25\n\t"
        "buffer_load_dwordx4 %[l16], %[rs], s64 offen offset:2048 lds\n\t"
        "v_mfma_f32_32x32x16_bf16 a[112:127], v[124:127], v[80:83], a[112:127]\n\t"
        "ds_read_b128 v[108:111], v177 offset:19456\n\t"
        "v_exp_f32_e32 v26, v26\n\t"
        "s_waitcnt lgkmcnt(10)\n\t"
        "v_mfma_f32_32x32x16_bf16 a[0:15], v[128:131], v[84:87], a[0:15]\n\t"
        "ds_read_b128 v[112:115], v177 offset:20480\n\t"
        "v_exp_f32_e32 v27, v27\n\t"
        "v_add_f32_e32 v160, v160, v26\n\t"
        "v_mfma_f32_32x32x16_bf16 a[16:31], v[132:135], v[84:87], a[16:31]\n\t"
        "ds_read_b128 v[116:119], v177 offset:21504\n\t"
        "v_add_f32_e32 v161, v161, v27\n\t"
        "v_cvt_pk_bf16_f32 v93, v26, v27\n\t"
        "buffer_load_dwordx4 %[l16], %[rs], s64 offen offset:3072 lds\n\t"
        "s_waitcnt lgkmcnt(10)\n\t"
        "v_mfma_f32_32x32x16_bf16 a[32:47], v[136:139], v[84:87], a[32:47]\n\t"
        "ds_read_b128 v[120:123], v177 offset:22528\n\t"
        "v_exp_f32_e32 v28, v28\n\t"
        "v_max3_f32 v162, v32, v33, v34\n\t"
        "v_mfma_f32_32x32x16_bf16 a[48:63], v[140:143], v[84:87], a[48:63]\n\t"
        "ds_read_b128 v[124:127], v177 offset:23552\n\t"
        "v_exp_f32_e32 v29, v29\n\t"
        "v_add_f32_e32 v160, v160, v28\n\t"
        "s_waitcnt lgkmcnt(10)\n\t"
        "v_mfma_f32_32x32x16_bf16 a[64:79], v[144:147], v[84:87], a[64:79]\n\t"
        "ds_read_b128 v[128:131], v177 offset:24576\n\t"
        "v_add_f32_e32 v161, v161, v29\n\t"
        "v_cvt_pk_bf16_f32 v94, v28, v29\n\t"
        "s_add_u32 m0, %[ldsw], 36864\n\t"
        "s_nop 0\n\t"
        "buffer_load_dwordx4 v178, %[rs], s64 offen offset:0 lds\n\t"
        "v_mfma_f32_32x32x16_bf16 a[80:95], v[148:151], v[84:87], a[80:95]\n\t"
        "ds_read_b128 v[132:135], v177 offset:25600\n\t"
        "v_exp_f32_e32 v30, v30\n\t"
        "v_max3_f32 v163, v35, v36, v37\n\t"
        "s_waitcnt lgkmcnt(10)\n\t"
        "v_mfma_f32_32x32x16_bf16 a[96:111], v[152:155], v[84:87], a[96:111]\n\t"
        "ds_read_b128 v[136:139], v177 offset:26624\n\t"
        "v_exp_f32_e32 v31, v31\n\t"
        "v_add_f32_e32 v160, v160, v30\n\t"
        "v_mfma_f32_32x32x16_bf16 a[112:127], v[156:159], v[84:87], a[112:127]\n\t"
        "ds_read_b128 v[140:143], v177 offset:27648\n\t"
        "v_add_f32_e32 v161, v161, v31\n\t"
        "v_cvt_pk_bf16_f32 v95, v30, v31\n\t"
        "buffer_load_dwordx4 v178, %[rs], s64 offen offset:1024 lds\n\t"
        "s_waitcnt lgkmcnt(10)\n\t"
        "v_mfma_f32_32x32x16_bf16 a[0:15], v[96:99], v[88:91], a[0:15]\n\t"
        "ds_read_b128 v[144:147], v177 offset:28672\n\t"
        "v_max3_f32 v162, v162, v38, v39\n\t"
        "v_mfma_f32_32x32x16_bf16 a[16:31], v[100:103], v[88:91], a[16:31]\n\t"
        "ds_read_b128 v[148:151], v177 offset:29696\n\t"
        "v_max3_f32 v163, v163, v40, v41\n\t"
        "s_waitcnt lgkmcnt(10)\n\t"
        "v_mfma_f32_32x32x16_bf16 a[32:47], v[104:107], v[88:91], a[32:47]\n\t"
        "ds_read_b128 v[152:155], v177 offset:30720\n\t"
        "v_max3_f32 v162, v162, v42, v43\n\t"
        "buffer_load_dwordx4 v178, %[rs], s64 offen offset:2048 lds\n\t"
        "v_mfma_f32_32x32x16_bf16 a[48:63], v[108:111], v[88:91], a[48:63]\n\t"
        "ds_read_b128 v[156:159], v177 offset:31744\n\t"
        "v_max3_f32 v163, v163, v44, v45\n\t"
        "s_waitcnt lgkmcnt(10)\n\t"
        "v_mfma_f32_32x32x16_bf16 a[64:79], v[112:115], v[88:91], a[64:79]\n\t"
        "v_max3_f32 v162, v162, v46, v47\n\t"
        "v_mfma_f32_32x32x16_bf16 a[80:95], v[116:119], v[88:91], a[80:95]\n\t"
        "v_max3_f32 v163, v163, v48, v49\n\t"
        "buffer_load_dwordx4 v178, %[rs], s64 offen offset:3072 lds\n\t"
        "s_add_u32 s64, s64, 0x8000\n\t"
        "s_min_u32 s64, s64, s65\n\t"
        "s_waitcnt lgkmcnt(8)\n\t"
        "v_mfma_f32_32x32x16_bf16 a[96:111], v[120:123], v[88:91], a[96:111]\n\t"
        "v_max3_f32 v162, v162, v50, v51\n\t"
        "v_mfma_f32_32x32x16_bf16 a[112:127], v[124:127], v[88:91], a[112:127]\n\t"
        "v_max3_f32 v163, v163, v52, v53\n\t"
        "s_waitcnt lgkmcnt(6)\n\t"
        "v_mfma_f32_32x32x16_bf16 a[0:15], v[128:131], v[92:95], a[0:15]\n\t"
        "v_max3_f32 v162, v162, v54, v55\n\t"
        "v_mfma_f32_32x32x16_bf16 a[16:31], v[132:135], v[92:95], a[16:31]\n\t"
        "s_waitcnt vmcnt(16) lgkmcnt(0)\n\t"
        "s_barrier\n\t"
        "ds_read_b128 v[96:99], v177 offset:32768\n\t"
        "ds_read_b128 v[100:103], v177 offset:33792\n\t"
        "v_mfma_f32_32x32x16_bf16 a[32:47], v[136:139], v[92:95], a[32:47]\n\t"
        "v_max3_f32 v163, v163, v56, v57\n\t"
        "ds_read_b128 v[104:107], v177 offset:34816\n\t"
        "ds_read_b128 v[108:111], v177 offset:35840\n\t"
        "v_mfma_f32_32x32x16_bf16 a[48:63], v[140:143], v[92:95], a[48:63]\n\t"
        "v_max3_f32 v162, v162, v58, v59\n\t"
        "ds_read_b128 v[112:115], v177 offset:36864\n\t"
        "ds_read_b128 v[116:119], v177 offset:37888\n\t"
        "v_mfma_f32_32x32x16_bf16 a[64:79], v[144:147], v[92:95], a[64:79]\n\t"
        "v_max3_f32 v163, v163, v60, v61\n\t"
        "ds_read_b128 v[120:123], v177 offset:38912\n\t"
        "ds_read_b128 v[124:127], v177 offset:39936\n\t"
        "v_mfma_f32_32x32x16_bf16 a[80:95], v[148:151], v[92:95], a[80:95]\n\t"
        "v_max3_f32 v162, v162, v62, v63\n\t"
        "ds_read_b128 v[128:131], v177 offset:40960\n\t"
        "ds_read_b128 v[132:135], v177 offset:41984\n\t"
        "v_mfma_f32_32x32x16_bf16 a[96:111], v[152:155], v[92:95], a[96:111]\n\t"
        "v_max_f32_e32 v162, v162, v163\n\t"
        "ds_read_b128 v[136:139], v177 offset:43008\n\t"
        "ds_read_b128 v[140:143], v177 offset:44032\n\t"
        "v_mfma_f32_32x32x16_bf16 a[112:127], v[156:159], v[92:95], a[112:127]\n\t"
        "v_cmp_lt_f32_e32 vcc, s69, v162\n\t"
        "s_cmp_eq_u32 s68, 2\n\t"
        "s_cbranch_scc1 LME_%=\n\t"
        "LCE_%=:\n\t"
        "s_cbranch_vccnz LSE_%=\n\t"
        "LRE_%=:\n\t"
        "s_sub_u32 s68, s68, 1\n\t"
        "s_waitcnt lgkmcnt(10)\n\t"
        "v_mfma_f32_32x32x16_bf16 v[0:15], v[96:99], a[128:131], v[64:79]\n\t"
        "ds_read_b128 v[144:147], v177 offset:45056\n\t"
        "v_exp_f32_e32 v32, v32\n\t"
        "v_mfma_f32_32x32x16_bf16 v[16:31], v[100:103], a[128:131], v[64:79]\n\t"
        "ds_read_b128 v[148:151], v177 offset:46080\n\t"
        "v_exp_f32_e32 v33, v33\n\t"
        "v_add_f32_e32 v160, v160, v32\n\t"
        "s_waitcnt lgkmcnt(10)\n\t"
        "v_mfma_f32_32x32x16_bf16 v[0:15], v[104:107], a[132:135], v[0:15]\n\t"
        "ds_read_b128 v[152:155], v177 offset:47104\n\t"
        "v_add_f32_e32 v161, v161, v33\n\t"
        "v_cvt_pk_bf16_f32 v80, v32, v33\n\t"
        "s_add_u32 m0, %[ldsw], 65536\n\t"
        "s_nop 0\n\t"
        "buffer_load_dwordx4 %[l16], %[rs], s66 offen offset:0 lds\n\t"
        "v_mfma_f32_32x32x16_bf16 v[16:31], v[108:111], a[132:135], v[16:31]\n\t"
        "ds_read_b128 v[156:159], v177 offset:48128\n\t"
        "v_exp_f32_e32 v34, v34\n\t"
        "s_waitcnt lgkmcnt(10)\n\t"
        "v_mfma_f32_32x32x16_bf16 v[0:15], v[112:115], a[136:139], v[0:15]\n\t"
        "ds_read_b128 v[96:99], v177 offset:49152\n\t"
        "v_exp_f32_e32 v35, v35\n\t"
        "v_add_f32_e32 v160, v160, v34\n\t"
        "v_mfma_f32_32x32x16_bf16 v[16:31], v[116:119], a[136:139], v[16:31]\n\t"
        "ds_read_b128 v[100:103], v177 offset:50176\n\t"
        "v_add_f32_e32 v161, v161, v35\n\t"
        "v_cvt_pk_bf16_f32 v81, v34, v35\n\t"
        "buffer_load_dwordx4 %[l16], %[rs], s66 offen offset:1024 lds\n\t"
        "s_waitcnt lgkmcnt(10)\n\t"
        "v_mfma_f32_32x32x16_bf16 v[0:15], v[120:123], a[140:143], v[0:15]\n\t"
        "ds_read_b128 v[104:107], v177 offset:51200\n\t"
        "v_exp_f32_e32 v36, v36\n\t"
        "v_mfma_f32_32x32x16_bf16 v[16:31], v[124:127], a[140:143], v[16:31]\n\t"
        "ds_read_b128 v[108:111], v177 offset:52224\n\t"
        "v_exp_f32_e32 v37, v37\n\t"
        "v_add_f32_e32 v160, v160, v36\n\t"
        "s_waitcnt lgkmcnt(10)\n\t"
        "v_mfma_f32_32x32x16_bf16 v[0:15], v[128:131], a[144:147], v[0:15]\n\t"
        "ds_read_b128 v[112:115], v177 offset:53248\n\t"
        "v_add_f32_e32 v161, v161, v37\n\t"
        "v_cvt_pk_bf16_f32 v82, v36, v37\n\t"
        "buffer_load_dwordx4 %[l16], %[rs], s66 offen offset:2048 lds\n\t"
        "v_mfma_f32_32x32x16_bf16 v[16:31], v[132:135], a[144:147], v[16:31]\n\t"
        "ds_read_b128 v[116:119], v177 offset:54272\n\t"
        "v_exp_f32_e32 v38, v38\n\t"
        "s_waitcnt lgkmcnt(10)\n\t"
        "v_mfma_f32_32x32x16_bf16 v[0:15], v[136:139], a[148:151], v[0:15]\n\t"
        "ds_read_b128 v[120:123], v177 offset:55296\n\t"
        "v_exp_f32_e32 v39, v39\n\t"
        "v_add_f32_e32 v160, v160, v38\n\t"
        "v_mfma_f32_32x32x16_bf16 v[16:31], v[140:143], a[148:151], v[16:31]\n\t"
        "ds_read_b128 v[124:127], v177 offset:56320\n\t"
        "v_add_f32_e32 v161, v161, v39\n\t"
        "v_cvt_pk_bf16_f32 v83, v38, v39\n\t"
        "buffer_load_dwordx4 %[l16], %[rs], s66 offen offset:3072 lds\n\t"
        "s_waitcnt lgkmcnt(10)\n\t"
        "v_mfma_f32_32x32x16_bf16 v[0:15], v[144:147], a[152:155], v[0:15]\n\t"
        "ds_read_b128 v[128:131], v177 offset:57344\n\t"
        "v_exp_f32_e32 v40, v40\n\t"
        "v_mfma_f32_32x32x16_bf16 v[16:31], v[148:151], a[152:155], v[16:31]\n\t"
        "ds_read_b128 v[132:135], v177 offset:58368\n\t"
        "v_exp_f32_e32 v41, v41\n\t"
        "v_add_f32_e32 v160, v160, v40\n\t"
        "s_waitcnt lgkmcnt(10)\n\t"
        "v_mfma_f32_32x32x16_bf16 v[0:15], v[152:155], a[156:159], v[0:15]\n\t"
        "ds_read_b128 v[136:139], v177 offset:59392\n\t"
        "v_add_f32_e32 v161, v161, v41\n\t"
        "v_cvt_pk_bf16_f32 v84, v40, v41\n\t"
        "s_add_u32 m0, %[ldsw], 69632\n\t"
        "s_nop 0\n\t"
        "buffer_load_dwordx4 v178, %[rs], s66 offen offset:0 lds\n\t"
        "v_mfma_f32_32x32x16_bf16 v[16:31], v[156:159], a[156:159], v[16:31]\n\t"
        "ds_read_b128 v[140:143], v177 offset:60416\n\t"
        "v_exp_f32_e32 v42, v42\n\t"
        "s_waitcnt lgkmcnt(10)\n\t"
        "v_mfma_f32_32x32x16_bf16 v[0:15], v[96:99], a[160:163], v[0:15]\n\t"
        "ds_read_b128 v[144:147], v177 offset:61440\n\t"
        "v_exp_f32_e32 v43, v43\n\t"
        "v_add_f32_e32 v160, v160, v42\n\t"
        "v_mfma_f32_32x32x16_bf16 v[16:31], v[100:103], a[160:163], v[16:31]\n\t"
        "ds_read_b128 v[148:151], v177 offset:62464\n\t"
        "v_add_f32_e32 v161, v161, v43\n\t"
        "v_cvt_pk_bf16_f32 v85, v42, v43\n\t"
        "buffer_load_dwordx4 v178, %[rs], s66 offen offset:1024 lds\n\t"
        "s_waitcnt lgkmcnt(10)\n\t"
        "v_mfma_f32_32x32x16_bf16 v[0:15], v[104:107], a[164:167], v[0:15]\n\t"
        "ds_read_b128 v[152:155], v177 offset:63488\n\t"
        "v_exp_f32_e32 v44, v44\n\t"
        "v_mfma_f32_32x32x16_bf16 v[16:31], v[108:111], a[164:167], v[16:31]\n\t"
        "ds_read_b128 v[156:159], v177 offset:64512\n\t"
        "v_exp_f32_e32 v45, v45\n\t"
        "v_add_f32_e32 v160, v160, v44\n\t"
        "s_waitcnt lgkmcnt(10)\n\t"
        "v_mfma_f32_32x32x16_bf16 v[0:15], v[112:115], a[168:171], v[0:15]\n\t"
        "v_add_f32_e32 v161, v161, v45\n\t"
        "v_cvt_pk_bf16_f32 v86, v44, v45\n\t"
        "buffer_load_dwordx4 v178, %[rs], s66 offen offset:2048 lds\n\t"
        "v_mfma_f32_32x32x16_bf16 v[16:31], v[116:119], a[168:171], v[16:31]\n\t"
        "v_exp_f32_e32 v46, v46\n\t"
        "s_waitcnt lgkmcnt(8)\n\t"
        "v_mfma_f32_32x32x16_bf16 v[0:15], v[120:123], a[172:175], v[0:15]\n\t"
        "v_exp_f32_e32 v47, v47\n\t"
        "v_add_f32_e32 v160, v160, v46\n\t"
        "v_mfma_f32_32x32x16_bf16 v[16:31], v[124:127], a[172:175], v[16:31]\n\t"
        "v_add_f32_e32 v161, v161, v47\n\t"
        "v_cvt_pk_bf16_f32 v87, v46, v47\n\t"
        "buffer_load_dwordx4 v178, %[rs], s66 offen offset:3072 lds\n\t"
        "s_add_u32 s66, s66, 0x8000\n\t"
        "s_min_u32 s66, s66, s67\n\t"
        "s_waitcnt lgkmcnt(6)\n\t"
        "v_mfma_f32_32x32x16_bf16 v[0:15], v[128:131], a[176:179], v[0:15]\n\t"
        "v_exp_f32_e32 v48, v48\n\t"
        "v_mfma_f32_32x32x16_bf16 v[16:31], v[132:135], a[176:179], v[16:31]\n\t"
        "v_exp_f32_e32 v49, v49\n\t"
        "v_add_f32_e32 v160, v160, v48\n\t"
        "s_waitcnt vmcnt(16) lgkmcnt(0)\n\t"
        "s_barrier\n\t"
        "ds_read_b128 v[96:99], v176 offset:0\n\t"
        "ds_read_b128 v[100:103], v176 offset:1024\n\t"
        "v_mfma_f32_32x32x16_bf16 v[0:15], v[136:139], a[180:183], v[0:15]\n\t"
        "v_add_f32_e32 v161, v161, v49\n\t"
        "v_cvt_pk_bf16_f32 v88, v48, v49\n\t"
        "ds_read_b128 v[104:107], v176 offset:2048\n\t"
        "ds_read_b128 v[108:111], v176 offset:3072\n\t"
        "v_mfma_f32_32x32x16_bf16 v[16:31], v[140:143], a[180:183], v[16:31]\n\t"
        "v_exp_f32_e32 v50, v50\n\t"
        "ds_read_b128 v[112:115], v176 offset:4096\n\t"
        "ds_read_b128 v[116:119], v176 offset:5120\n\t"
        "v_mfma_f32_32x32x16_bf16 v[0:15], v[144:147], a[184:187], v[0:15]\n\t"
        "v_exp_f32_e32 v51, v51\n\t"
        "v_add_f32_e32 v160, v160, v50\n\t"
        "ds_read_b128 v[120:123], v176 offset:6144\n\t"
        "ds_read_b128 v[124:127], v176 offset:7168\n\t"
        "v_mfma_f32_32x32x16_bf16 v[16:31], v[148:151], a[184:187], v[16:31]\n\t"
        "v_add_f32_e32 v161, v161, v51\n\t"
        "v_cvt_pk_bf16_f32 v89, v50, v51\n\t"
        "ds_read_b128 v[128:131], v176 offset:8192\n\t"
        "ds_read_b128 v[132:135], v176 offset:9216\n\t"
        "v_mfma_f32_32x32x16_bf16 v[0:15], v[152:155], a[188:191], v[0:15]\n\t"
        "v_exp_f32_e32 v52, v52\n\t"
        "ds_read_b128 v[136:139], v176 offset:10240\n\t"
        "ds_read_b128 v[140:143], v176 offset:11264\n\t"
        "v_mfma_f32_32x32x16_bf16 v[16:31], v[156:159], a[188:191], v[16:31]\n\t"
        "v_exp_f32_e32 v53, v53\n\t"
        "v_add_f32_e32 v160, v160, v52\n\t"
        "s_waitcnt lgkmcnt(10)\n\t"
        "v_mfma_f32_32x32x16_bf16 a[0:15], v[96:99], v[80:83], a[0:15]\n\t"
        "ds_read_b128 v[144:147], v176 offset:12288\n\t"
        "v_add_f32_e32 v161, v161, v53\n\t"
        "v_cvt_pk_bf16_f32 v90, v52, v53\n\t"
        "s_add_u32 m0, %[ldsw], 98304\n\t"
        "s_nop 0\n\t"
        "buffer_load_dwordx4 %[l16], %[rs], s64 offen offset:0 lds\n\t"
        "v_mfma_f32_32x32x16_bf16 a[16:31], v[100:103], v[80:83], a[16:31]\n\t"
        "ds_read_b128 v[148:151], v176 offset:13312\n\t"
        "v_exp_f32_e32 v54, v54\n\t"
        "s_waitcnt lgkmcnt(10)\n\t"
        "v_mfma_f32_32x32x16_bf16 a[32:47], v[104:107], v[80:83], a[32:47]\n\t"
        "ds_read_b128 v[152:155], v176 offset:14336\n\t"
        "v_exp_f32_e32 v55, v55\n\t"
        "v_add_f32_e32 v160, v160, v54\n\t"
        "v_mfma_f32_32x32x16_bf16 a[48:63], v[108:111], v[80:83], a[48:63]\n\t"
        "ds_read_b128 v[156:159], v176 offset:15360\n\t"
        "v_add_f32_e32 v161, v161, v55\n\t"
        "v_cvt_pk_bf16_f32 v91, v54, v55\n\t"
        "buffer_load_dwordx4 %[l16], %[rs], s64 offen offset:1024 lds\n\t"
        "s_waitcnt lgkmcnt(10)\n\t"
        "v_mfma_f32_32x32x16_bf16 a[64:79], v[112:115], v[80:83], a[64:79]\n\t"
        "ds_read_b128 v[96:99], v176 offset:16384\n\t"
        "v_exp_f32_e32 v56, v56\n\t"
        "v_mfma_f32_32x32x16_bf16 a[80:95], v[116:119], v[80:83], a[80:95]\n\t"
        "ds_read_b128 v[100:103], v176 offset:17408\n\t"
        "v_exp_f32_e32 v57, v57\n\t"
        "v_add_f32_e32 v160, v160, v56\n\t"
        "s_waitcnt lgkmcnt(10)\n\t"
        "v_mfma_f32_32x32x16_bf16 a[96:111], v[120:123], v[80:83], a[96:111]\n\t"
        "ds_read_b128 v[104:107], v176 offset:18432\n\t"
        "v_add_f32_e32 v161, v161, v57\n\t"
        "v_cvt_pk_bf16_f32 v92, v56, v57\n\t"
        "buffer_load_dwordx4 %[l16], %[rs], s64 offen offset:2048 lds\n\t"
        "v_mfma_f32_32x32x16_bf16 a[112:127], v[124:127], v[80:83], a[112:127]\n\t"
        "ds_read_b128 v[108:111], v176 offset:19456\n\t"
        "v_exp_f32_e32 v58, v58\n\t"
        "s_waitcnt lgkmcnt(10)\n\t"
        "v_mfma_f32_32x32x16_bf16 a[0:15], v[128:131], v[84:87], a[0:15]\n\t"
        "ds_read_b128 v[112:115], v176 offset:20480\n\t"
        "v_exp_f32_e32 v59, v59\n\t"
        "v_add_f32_e32 v160, v160, v58\n\t"
        "v_mfma_f32_32x32x16_bf16 a[16:31], v[132:135], v[84:87], a[16:31]\n\t"
        "ds_read_b128 v[116:119], v176 offset:21504\n\t"
        "v_add_f32_e32 v161, v161, v59\n\t"
        "v_cvt_pk_bf16_f32 v93, v58, v59\n\t"
        "buffer_load_dwordx4 %[l16], %[rs], s64 offen offset:3072 lds\n\t"
        "s_waitcnt lgkmcnt(10)\n\t"
        "v_mfma_f32_32x32x16_bf16 a[32:47], v[136:139], v[84:87], a[32:47]\n\t"
        "ds_read_b128 v[120:123], v176 offset:22528\n\t"
        "v_exp_f32_e32 v60, v60\n\t"
        "v_max3_f32 v162, v0, v1, v2\n\t"
        "v_mfma_f32_32x32x16_bf16 a[48:63], v[140:143], v[84:87], a[48:63]\n\t"
        "ds_read_b128 v[124:127], v176 offset:23552\n\t"
        "v_exp_f32_e32 v61, v61\n\t"
        "v_add_f32_e32 v160, v160, v60\n\t"
        "s_waitcnt lgkmcnt(10)\n\t"
        "v_mfma_f32_32x32x16_bf16 a[64:79], v[144:147], v[84:87], a[64:79]\n\t"
        "ds_read_b128 v[128:131], v176 offset:24576\n\t"
        "v_add_f32_e32 v161, v161, v61\n\t"
        "v_cvt_pk_bf16_f32 v94, v60, v61\n\t"
        "s_add_u32 m0, %[ldsw], 102400\n\t"
        "s_nop 0\n\t"
        "buffer_load_dwordx4 v178, %[rs], s64 offen offset:0 lds\n\t"
        "v_mfma_f32_32x32x16_bf16 a[80:95], v[148:151], v[84:87], a[80:95]\n\t"
        "ds_read_b128 v[132:135], v176 offset:25600\n\t"
        "v_exp_f32_e32 v62, v62\n\t"
        "v_max3_f32 v163, v3, v4, v5\n\t"
        "s_waitcnt lgkmcnt(10)\n\t"
        "v_mfma_f32_32x32x16_bf16 a[96:111], v[152:155], v[84:87], a[96:111]\n\t"
        "ds_read_b128 v[136:139], v176 offset:26624\n\t"
        "v_exp_f32_e32 v63, v63\n\t"
        "v_add_f32_e32 v160, v160, v62\n\t"
        "v_mfma_f32_32x32x16_bf16 a[112:127], v[156:159], v[84:87], a[112:127]\n\t"
        "ds_read_b128 v[140:143], v176 offset:27648\n\t"
        "v_add_f32_e32 v161, v161, v63\n\t"
        "v_cvt_pk_bf16_f32 v95, v62, v63\n\t"
        "buffer_load_dwordx4 v178, %[rs], s64 offen offset:1024 lds\n\t"
        "s_waitcnt lgkmcnt(10)\n\t"
        "v_mfma_f32_32x32x16_bf16 a[0:15], v[96:99], v[88:91], a[0:15]\n\t"
        "ds_read_b128 v[144:147], v176 offset:28672\n\t"
        "v_max3_f32 v162, v162, v6, v7\n\t"
        "v_mfma_f32_32x32x16_bf16 a[16:31], v[100:103], v[88:91], a[16:31]\n\t"
        "ds_read_b128 v[148:151], v176 offset:29696\n\t"
        "v_max3_f32 v163, v163, v8, v9\n\t"
        "s_waitcnt lgkmcnt(10)\n\t"
        "v_mfma_f32_32x32x16_bf16 a[32:47], v[104:107], v[88:91], a[32:47]\n\t"
        "ds_read_b128 v[152:155], v176 offset:30720\n\t"
        "v_max3_f32 v162, v162, v10, v11\n\t"
        "buffer_load_dwordx4 v178, %[rs], s64 offen offset:2048 lds\n\t"
        "v_mfma_f32_32x32x16_bf16 a[48:63], v[108:111], v[88:91], a[48:63]\n\t"
        "ds_read_b128 v[156:159], v176 offset:31744\n\t"
        "v_max3_f32 v163, v163, v12, v13\n\t"
        "s_waitcnt lgkmcnt(10)\n\t"
        "v_mfma_f32_32x32x16_bf16 a[64:79], v[112:115], v[88:91], a[64:79]\n\t"
        "v_max3_f32 v162, v162, v14, v15\n\t"
        "v_mfma_f32_32x32x16_bf16 a[80:95], v[116:119], v[88:91], a[80:95]\n\t"
        "v_max3_f32 v163, v163, v16, v17\n\t"
        "buffer_load_dwordx4 v178, %[rs], s64 offen offset:3072 lds\n\t"
        "s_add_u32 s64, s64, 0x8000\n\t"
        "s_min_u32 s64, s64, s65\n\t"
        "s_waitcnt lgkmcnt(8)\n\t"
        "v_mfma_f32_32x32x16_bf16 a[96:111], v[120:123], v[88:91], a[96:111]\n\t"
        "v_max3_f32 v162, v162, v18, v19\n\t"
        "v_mfma_f32_32x32x16_bf16 a[112:127], v[124:127], v[88:91], a[112:127]\n\t"
        "v_max3_f32 v163, v163, v20, v21\n\t"
        "s_waitcnt lgkmcnt(6)\n\t"
        "v_mfma_f32_32x32x16_bf16 a[0:15], v[128:131], v[92:95], a[0:15]\n\t"
        "v_max3_f32 v162, v162, v22, v23\n\t"
        "v_mfma_f32_32x32x16_bf16 a[16:31], v[132:135], v[92:95], a[16:31]\n\t"
        "s_waitcnt vmcnt(16) lgkmcnt(0)\n\t"
        "s_barrier\n\t"
        "ds_read_b128 v[96:99], v176 offset:32768\n\t"
        "ds_read_b128 v[100:103], v176 offset:33792\n\t"
        "v_mfma_f32_32x32x16_bf16 a[32:47], v[136:139], v[92:95], a[32:47]\n\t"
        "v_max3_f32 v163, v163, v24, v25\n\t"
        "ds_read_b128 v[104:107], v176 offset:34816\n\t"
        "ds_read_b128 v[108:111], v176 offset:35840\n\t"
        "v_mfma_f32_32x32x16_bf16 a[48:63], v[140:143], v[92:95], a[48:63]\n\t"
        "v_max3_f32 v162, v162, v26, v27\n\t"
        "ds_read_b128 v[112:115], v176 offset:36864\n\t"
        "ds_read_b128 v[116:119], v176 offset:37888\n\t"
        "v_mfma_f32_32x32x16_bf16 a[64:79], v[144:147], v[92:95], a[64:79]\n\t"
        "v_max3_f32 v163, v163, v28, v29\n\t"
        "ds_read_b128 v[120:123], v176 offset:38912\n\t"
        "ds_read_b128 v[124:127], v176 offset:39936\n\t"
        "v_mfma_f32_32x32x16_bf16 a[80:95], v[148:151], v[92:95], a[80:95]\n\t"
        "v_max3_f32 v162, v162, v30, v31\n\t"
        "ds_read_b128 v[128:131], v176 offset:40960\n\t"
        "ds_read_b128 v[132:135], v176 offset:41984\n\t"
        "v_mfma_f32_32x32x16_bf16 a[96:111], v[152:155], v[92:95], a[96:111]\n\t"
        "v_max_f32_e32 v162, v162, v163\n\t"
        "ds_read_b128 v[136:139], v176 offset:43008\n\t"
        "ds_read_b128 v[140:143], v176 offset:44032\n\t"
        "v_mfma_f32_32x32x16_bf16 a[112:127], v[156:159], v[92:95], a[112:127]\n\t"
        "v_cmp_lt_f32_e32 vcc, s69, v162\n\t"
        "s_cmp_eq_u32 s68, 1\n\t"
        "s_cbranch_scc1 LEND_%=\n\t"
        "LTO_%=:\n\t"
        "s_cmp_eq_u32 s68, 3\n\t"
        "s_cbranch_scc1 LMO_%=\n\t"
        "LCO_%=:\n\t"
        "s_cbranch_vccnz LSO_%=\n\t"
        "LRO_%=:\n\t"
        "s_sub_u32 s68, s68, 1\n\t"
        "s_branch LOOP_%=\n\t"
        "LME_%=:\n\t"
        "v_subrev_u32_e32 v167, 64, %[lim]\n\t"
        "v_cmp_gt_i32_e64 s[72:73], 0, v167\n\t"
        "v_cmp_gt_i32_e64 s[74:75], 1, v167\n\t"
        "v_cmp_gt_i32_e64 s[76:77], 2, v167\n\t"
        "v_cmp_gt_i32_e64 s[78:79], 3, v167\n\t"
        "v_cndmask_b32_e64 v32, v32, v166, s[72:73]\n\t"
        "v_cndmask_b32_e64 v33, v33, v166, s[74:75]\n\t"
        "v_cndmask_b32_e64 v34, v34, v166, s[76:77]\n\t"
        "v_cndmask_b32_e64 v35, v35, v166, s[78:79]\n\t"
        "v_cmp_gt_i32_e64 s[72:73], 8, v167\n\t"
        "v_cmp_gt_i32_e64 s[74:75], 9, v167\n\t"
        "v_cmp_gt_i32_e64 s[76:77], 10, v167\n\t"
        "v_cmp_gt_i32_e64 s[78:79], 11, v167\n\t"
        "v_cndmask_b32_e64 v36, v36, v166, s[72:73]\n\t"
        "v_cndmask_b32_e64 v37, v37, v166, s[74:75]\n\t"
        "v_cndmask_b32_e64 v38, v38, v166, s[76:77]\n\t"
        "v_cndmask_b32_e64 v39, v39, v166, s[78:79]\n\t"
        "v_cmp_gt_i32_e64 s[72:73], 16, v167\n\t"
        "v_cmp_gt_i32_e64 s[74:75], 17, v167\n\t"
        "v_cmp_gt_i32_e64 s[76:77], 18, v167\n\t"
        "v_cmp_gt_i32_e64 s[78:79], 19, v167\n\t"
        "v_cndmask_b32_e64 v40, v40, v166, s[72:73]\n\t"
        "v_cndmask_b32_e64 v41, v41, v166, s[74:75]\n\t"
        "v_cndmask_b32_e64 v42, v42, v166, s[76:77]\n\t"
        "v_cndmask_b32_e64 v43, v43, v166, s[78:79]\n\t"
        "v_cmp_gt_i32_e64 s[72:73], 24, v167\n\t"
        "v_cmp_gt_i32_e64 s[74:75], 25, v167\n\t"
        "v_cmp_gt_i32_e64 s[76:77], 26, v167\n\t"
        "v_cmp_gt_i32_e64 s[78:79], 27, v167\n\t"
        "v_cndmask_b32_e64 v44, v44, v166, s[72:73]\n\t"
        "v_cndmask_b32_e64 v45, v45, v166, s[74:75]\n\t"
        "v_cndmask_b32_e64 v46, v46, v166, s[76:77]\n\t"
        "v_cndmask_b32_e64 v47, v47, v166, s[78:79]\n\t"
        "v_cmp_gt_i32_e64 s[72:73], 32, v167\n\t"
        "v_cmp_gt_i32_e64 s[74:75], 33, v167\n\t"
        "v_cmp_gt_i32_e64 s[76:77], 34, v167\n\t"
        "v_cmp_gt_i32_e64 s[78:79], 35, v167\n\t"
        "v_cndmask_b32_e64 v48, v48, v166, s[72:73]\n\t"
        "v_cndmask_b32_e64 v49, v49, v166, s[74:75]\n\t"
        "v_cndmask_b32_e64 v50, v50, v166, s[76:77]\n\t"
        "v_cndmask_b32_e64 v51, v51, v166, s[78:79]\n\t"
        "v_cmp_gt_i32_e64 s[72:73], 40, v167\n\t"
        "v_cmp_gt_i32_e64 s[74:75], 41, v167\n\t"
        "v_cmp_gt_i32_e64 s[76:77], 42, v167\n\t"
        "v_cmp_gt_i32_e64 s[78:79], 43, v167\n\t"
        "v_cndmask_b32_e64 v52, v52, v166, s[72:73]\n\t"
        "v_cndmask_b32_e64 v53, v53, v166, s[74:75]\n\t"
        "v_cndmask_b32_e64 v54, v54, v166, s[76:77]\n\t"
        "v_cndmask_b32_e64 v55, v55, v166, s[78:79]\n\t"
        "v_cmp_gt_i32_e64 s[72:73], 48, v167\n\t"
        "v_cmp_gt_i32_e64 s[74:75], 49, v167\n\t"
        "v_cmp_gt_i32_e64 s[76:77], 50, v167\n\t"
        "v_cmp_gt_i32_e64 s[78:79], 51, v167\n\t"
        "v_cndmask_b32_e64 v56, v56, v166, s[72:73]\n\t"
        "v_cndmask_b32_e64 v57, v57, v166, s[74:75]\n\t"
        "v_cndmask_b32_e64 v58, v58, v166, s[76:77]\n\t"
        "v_cndmask_b32_e64 v59, v59, v166, s[78:79]\n\t"
        "v_cmp_gt_i32_e64 s[72:73], 56, v167\n\t"
        "v_cmp_gt_i32_e64 s[74:75], 57, v167\n\t"
        "v_cmp_gt_i32_e64 s[76:77], 58, v167\n\t"
        "v_cmp_gt_i32_e64 s[78:79], 59, v167\n\t"
        "v_cndmask_b32_e64 v60, v60, v166, s[72:73]\n\t"
        "v_cndmask_b32_e64 v61, v61, v166, s[74:75]\n\t"
        "v_cndmask_b32_e64 v62, v62, v166, s[76:77]\n\t"
        "v_cndmask_b32_e64 v63, v63, v166, s[78:79]\n\t"
        "v_max3_f32 v162, v32, v33, v34\n\t"
        "v_max3_f32 v163, v35, v36, v37\n\t"
        "v_max3_f32 v162, v162, v38, v39\n\t"
        "v_max3_f32 v163, v163, v40, v41\n\t"
        "v_max3_f32 v162, v162, v42, v43\n\t"
        "v_max3_f32 v163, v163, v44, v45\n\t"
        "v_max3_f32 v162, v162, v46, v47\n\t"
        "v_max3_f32 v163, v163, v48, v49\n\t"
        "v_max3_f32 v162, v162, v50, v51\n\t"
        "v_max3_f32 v163, v163, v52, v53\n\t"
        "v_max3_f32 v162, v162, v54, v55\n\t"
        "v_max3_f32 v163, v163, v56, v57\n\t"
        "v_max3_f32 v162, v162, v58, v59\n\t"
        "v_max3_f32 v163, v163, v60, v61\n\t"
        "v_max3_f32 v162, v162, v62, v63\n\t"
        "v_max_f32_e32 v162, v162, v163\n\t"
        "v_cmp_lt_f32_e32 vcc, s69, v162\n\t"
        "s_branch LCE_%=\n\t"
        "LMO_%=:\n\t"
        "v_mov_b32_e32 v167, %[lim]\n\t"
        "v_cmp_gt_i32_e64 s[72:73], 0, v167\n\t"
        "v_cmp_gt_i32_e64 s[74:75], 1, v167\n\t"
        "v_cmp_gt_i32_e64 s[76:77], 2, v167\n\t"
        "v_cmp_gt_i32_e64 s[78:79], 3, v167\n\t"
        "v_cndmask_b32_e64 v0, v0, v166, s[72:73]\n\t"
        "v_cndmask_b32_e64 v1, v1, v166, s[74:75]\n\t"
        "v_cndmask_b32_e64 v2, v2, v166, s[76:77]\n\t"
        "v_cndmask_b32_e64 v3, v3, v166, s[78:79]\n\t"
        "v_cmp_gt_i32_e64 s[72:73], 8, v167\n\t"
        "v_cmp_gt_i32_e64 s[74:75], 9, v167\n\t"
        "v_cmp_gt_i32_e64 s[76:77], 10, v167\n\t"
        "v_cmp_gt_i32_e64 s[78:79], 11, v167\n\t"
        "v_cndmask_b32_e64 v4, v4, v166, s[72:73]\n\t"
        "v_cndmask_b32_e64 v5, v5, v166, s[74:75]\n\t"
        "v_cndmask_b32_e64 v6, v6, v166, s[76:77]\n\t"
        "v_cndmask_b32_e64 v7, v7, v166, s[78:79]\n\t"
        "v_cmp_gt_i32_e64 s[72:73], 16, v167\n\t"
        "v_cmp_gt_i32_e64 s[74:75], 17, v167\n\t"
        "v_cmp_gt_i32_e64 s[76:77], 18, v167\n\t"
        "v_cmp_gt_i32_e64 s[78:79], 19, v167\n\t"
        "v_cndmask_b32_e64 v8, v8, v166, s[72:73]\n\t"
        "v_cndmask_b32_e64 v9, v9, v166, s[74:75]\n\t"
        "v_cndmask_b32_e64 v10, v10, v166, s[76:77]\n\t"
        "v_cndmask_b32_e64 v11, v11, v166, s[78:79]\n\t"
        "v_cmp_gt_i32_e64 s[72:73], 24, v167\n\t"
        "v_cmp_gt_i32_e64 s[74:75], 25, v167\n\t"
        "v_cmp_gt_i32_e64 s[76:77], 26, v167\n\t"
        "v_cmp_gt_i32_e64 s[78:79], 27, v167\n\t"
        "v_cndmask_b32_e64 v12, v12, v166, s[72:73]\n\t"
        "v_cndmask_b32_e64 v13, v13, v166, s[74:75]\n\t"
        "v_cndmask_b32_e64 v14, v14, v166, s[76:77]\n\t"
        "v_cndmask_b32_e64 v15, v15, v166, s[78:79]\n\t"
        "v_cmp_gt_i32_e64 s[72:73], 32, v167\n\t"
        "v_cmp_gt_i32_e64 s[74:75], 33, v167\n\t"
        "v_cmp_gt_i32_e64 s[76:77], 34, v167\n\t"
        "v_cmp_gt_i32_e64 s[78:79], 35, v167\n\t"
        "v_cndmask_b32_e64 v16, v16, v166, s[72:73]\n\t"
        "v_cndmask_b32_e64 v17, v17, v166, s[74:75]\n\t"
        "v_cndmask_b32_e64 v18, v18, v166, s[76:77]\n\t"
        "v_cndmask_b32_e64 v19, v19, v166, s[78:79]\n\t"
        "v_cmp_gt_i32_e64 s[72:73], 40, v167\n\t"
        "v_cmp_gt_i32_e64 s[74:75], 41, v167\n\t"
        "v_cmp_gt_i32_e64 s[76:77], 42, v167\n\t"
        "v_cmp_gt_i32_e64 s[78:79], 43, v167\n\t"
        "v_cndmask_b32_e64 v20, v20, v166, s[72:73]\n\t"
        "v_cndmask_b32_e64 v21, v21, v166, s[74:75]\n\t"
        "v_cndmask_b32_e64 v22, v22, v166, s[76:77]\n\t"
        "v_cndmask_b32_e64 v23, v23, v166, s[78:79]\n\t"
        "v_cmp_gt_i32_e64 s[72:73], 48, v167\n\t"
        "v_cmp_gt_i32_e64 s[74:75], 49, v167\n\t"
        "v_cmp_gt_i32_e64 s[76:77], 50, v167\n\t"
        "v_cmp_gt_i32_e64 s[78:79], 51, v167\n\t"
        "v_cndmask_b32_e64 v24, v24, v166, s[72:73]\n\t"
        "v_cndmask_b32_e64 v25, v25, v166, s[74:75]\n\t"
        "v_cndmask_b32_e64 v26, v26, v166, s[76:77]\n\t"
        "v_cndmask_b32_e64 v27, v27, v166, s[78:79]\n\t"
        "v_cmp_gt_i32_e64 s[72:73], 56, v167\n\t"
        "v_cmp_gt_i32_e64 s[74:75], 57, v167\n\t"
        "v_cmp_gt_i32_e64 s[76:77], 58, v167\n\t"
        "v_cmp_gt_i32_e64 s[78:79], 59, v167\n\t"
        "v_cndmask_b32_e64 v28, v28, v166, s[72:73]\n\t"
        "v_cndmask_b32_e64 v29, v29, v166, s[74:75]\n\t"
        "v_cndmask_b32_e64 v30, v30, v166, s[76:77]\n\t"
        "v_cndmask_b32_e64 v31, v31, v166, s[78:79]\n\t"
        "v_max3_f32 v162, v0, v1, v2\n\t"
        "v_max3_f32 v163, v3, v4, v5\n\t"
        "v_max3_f32 v162, v162, v6, v7\n\t"
        "v_max3_f32 v163, v163, v8, v9\n\t"
        "v_max3_f32 v162, v162, v10, v11\n\t"
        "v_max3_f32 v163, v163, v12, v13\n\t"
        "v_max3_f32 v162, v162, v14, v15\n\t"
        "v_max3_f32 v163, v163, v16, v17\n\t"
        "v_max3_f32 v162, v162, v18, v19\n\t"
        "v_max3_f32 v163, v163, v20, v21\n\t"
        "v_max3_f32 v162, v162, v22, v23\n\t"
        "v_max3_f32 v163, v163, v24, v25\n\t"
        "v_max3_f32 v162, v162, v26, v27\n\t"
        "v_max3_f32 v163, v163, v28, v29\n\t"
        "v_max3_f32 v162, v162, v30, v31\n\t"
        "v_max_f32_e32 v162, v162, v163\n\t"
        "v_cmp_lt_f32_e32 vcc, s69, v162\n\t"
        "s_branch LCO_%=\n\t"
        "LSE_%=:\n\t"
        "v_mov_b32_e32 v164, v162\n\t"
        "s_nop 1\n\t"
        "v_permlane32_swap_b32_e32 v162, v164\n\t"
        "s_nop 1\n\t"
        "v_max_f32_e32 v162, v162, v164\n\t"
        "v_max_f32_e32 v164, s70, v162\n\t"
        "v_max_f32_e32 v165, 0, v164\n\t"
        "v_exp_f32_e64 v165, -v165\n\t"
        "v_sub_f32_e32 v32, v32, v164\n\t"
        "v_sub_f32_e32 v33, v33, v164\n\t"
        "v_sub_f32_e32 v34, v34, v164\n\t"
        "v_sub_f32_e32 v35, v35, v164\n\t"
        "v_sub_f32_e32 v36, v36, v164\n\t"
        "v_sub_f32_e32 v37, v37, v164\n\t"
        "v_sub_f32_e32 v38, v38, v164\n\t"
        "v_sub_f32_e32 v39, v39, v164\n\t"
        "v_sub_f32_e32 v40, v40, v164\n\t"
        "v_sub_f32_e32 v41, v41, v164\n\t"
        "v_sub_f32_e32 v42, v42, v164\n\t"
        "v_sub_f32_e32 v43, v43, v164\n\t"
        "v_sub_f32_e32 v44, v44, v164\n\t"
        "v_sub_f32_e32 v45, v45, v164\n\t"
        "v_sub_f32_e32 v46, v46, v164\n\t"
        "v_sub_f32_e32 v47, v47, v164\n\t"
        "v_sub_f32_e32 v48, v48, v164\n\t"
        "v_sub_f32_e32 v49, v49, v164\n\t"
        "v_sub_f32_e32 v50, v50, v164\n\t"
        "v_sub_f32_e32 v51, v51, v164\n\t"
        "v_sub_f32_e32 v52, v52, v164\n\t"
        "v_sub_f32_e32 v53, v53, v164\n\t"
        "v_sub_f32_e32 v54, v54, v164\n\t"
        "v_sub_f32_e32 v55, v55, v164\n\t"
        "v_sub_f32_e32 v56, v56, v164\n\t"
        "v_sub_f32_e32 v57, v57, v164\n\t"
        "v_sub_f32_e32 v58, v58, v164\n\t"
        "v_sub_f32_e32 v59, v59, v164\n\t"
        "v_sub_f32_e32 v60, v60, v164\n\t"
        "v_sub_f32_e32 v61, v61, v164\n\t"
        "v_sub_f32_e32 v62, v62, v164\n\t"
        "v_sub_f32_e32 v63, v63, v164\n\t"
        "v_sub_f32_e32 v64, v64, v164\n\t"
        "v_sub_f32_e32 v65, v65, v164\n\t"
        "v_sub_f32_e32 v66, v66, v164\n\t"
        "v_sub_f32_e32 v67, v67, v164\n\t"
        "v_sub_f32_e32 v68, v68, v164\n\t"
        "v_sub_f32_e32 v69, v69, v164\n\t"
        "v_sub_f32_e32 v70, v70, v164\n\t"
        "v_sub_f32_e32 v71, v71, v164\n\t"
        "v_sub_f32_e32 v72, v72, v164\n\t"
        "v_sub_f32_e32 v73, v73, v164\n\t"
        "v_sub_f32_e32 v74, v74, v164\n\t"
        "v_sub_f32_e32 v75, v75, v164\n\t"
        "v_sub_f32_e32 v76, v76, v164\n\t"
        "v_sub_f32_e32 v77, v77, v164\n\t"
        "v_sub_f32_e32 v78, v78, v164\n\t"
        "v_sub_f32_e32 v79, v79, v164\n\t"
        "v_mul_f32_e32 v160, v160, v165\n\t"
        "v_mul_f32_e32 v161, v161, v165\n\t"
        "s_cmp_lg_u32 s70, 0\n\t"
        "s_cbranch_scc1 LSKE_%=\n\t"
        "v_accvgpr_read_b32 v168, a0\n\t"
        "v_accvgpr_read_b32 v169, a1\n\t"
        "v_accvgpr_read_b32 v170, a2\n\t"
        "v_accvgpr_read_b32 v171, a3\n\t"
        "v_accvgpr_read_b32 v172, a4\n\t"
        "v_accvgpr_read_b32 v173, a5\n\t"
        "v_accvgpr_read_b32 v174, a6\n\t"
        "v_accvgpr_read_b32 v175, a7\n\t"
        "v_mul_f32_e32 v168, v168, v165\n\t"
        "v_mul_f32_e32 v169, v169, v165\n\t"
        "v_mul_f32_e32 v170, v170, v165\n\t"
        "v_mul_f32_e32 v171, v171, v165\n\t"
        "v_mul_f32_e32 v172, v172, v165\n\t"
        "v_mul_f32_e32 v173, v173, v165\n\t"
        "v_mul_f32_e32 v174, v174, v165\n\t"
        "v_mul_f32_e32 v175, v175, v165\n\t"
        "v_accvgpr_write_b32 a0, v168\n\t"
        "v_accvgpr_write_b32 a1, v169\n\t"
        "v_accvgpr_write_b32 a2, v170\n\t"
        "v_accvgpr_write_b32 a3, v171\n\t"
        "v_accvgpr_write_b32 a4, v172\n\t"
        "v_accvgpr_write_b32 a5, v173\n\t"
        "v_accvgpr_write_b32 a6, v174\n\t"
        "v_accvgpr_write_b32 a7, v175\n\t"
        "v_accvgpr_read_b32 v168, a8\n\t"
        "v_accvgpr_read_b32 v169, a9\n\t"
        "v_accvgpr_read_b32 v170, a10\n\t"
        "v_accvgpr_read_b32 v171, a11\n\t"
        "v_accvgpr_read_b32 v172, a12\n\t"
        "v_accvgpr_read_b32 v173, a13\n\t"
        "v_accvgpr_read_b32 v174, a14\n\t"
        "v_accvgpr_read_b32 v175, a15\n\t"
        "v_mul_f32_e32 v168, v168, v165\n\t"
        "v_mul_f32_e32 v169, v169, v165\n\t"
        "v_mul_f32_e32 v170, v170, v165\n\t"
        "v_mul_f32_e32 v171, v171, v165\n\t"
        "v_mul_f32_e32 v172, v172, v165\n\t"
        "v_mul_f32_e32 v173, v173, v165\n\t"
        "v_mul_f32_e32 v174, v174, v165\n\t"
        "v_mul_f32_e32 v175, v175, v165\n\t"
        "v_accvgpr_write_b32 a8, v168\n\t"
        "v_accvgpr_write_b32 a9, v169\n\t"
        "v_accvgpr_write_b32 a10, v170\n\t"
        "v_accvgpr_write_b32 a11, v171\n\t"
        "v_accvgpr_write_b32 a12, v172\n\t"
        "v_accvgpr_write_b32 a13, v173\n\t"
        "v_accvgpr_write_b32 a14, v174\n\t"
        "v_accvgpr_write_b32 a15, v175\n\t"
        "v_accvgpr_read_b32 v168, a16\n\t"
        "v_accvgpr_read_b32 v169, a17\n\t"
        "v_accvgpr_read_b32 v170, a18\n\t"
        "v_accvgpr_read_b32 v171, a19\n\t"
        "v_accvgpr_read_b32 v172, a20\n\t"
        "v_accvgpr_read_b32 v173, a21\n\t"
        "v_accvgpr_read_b32 v174, a22\n\t"
        "v_accvgpr_read_b32 v175, a23\n\t"
        "v_mul_f32_e32 v168, v168, v165\n\t"
        "v_mul_f32_e32 v169, v169, v165\n\t"
        "v_mul_f32_e32 v170, v170, v165\n\t"
        "v_mul_f32_e32 v171, v171, v165\n\t"
        "v_mul_f32_e32 v172, v172, v165\n\t"
        "v_mul_f32_e32 v173, v173, v165\n\t"
        "v_mul_f32_e32 v174, v174, v165\n\t"
        "v_mul_f32_e32 v175, v175, v165\n\t"
        "v_accvgpr_write_b32 a16, v168\n\t"
        "v_accvgpr_write_b32 a17, v169\n\t"
        "v_accvgpr_write_b32 a18, v170\n\t"
        "v_accvgpr_write_b32 a19, v171\n\t"
        "v_accvgpr_write_b32 a20, v172\n\t"
        "v_accvgpr_write_b32 a21, v173\n\t"
        "v_accvgpr_write_b32 a22, v174\n\t"
        "v_accvgpr_write_b32 a23, v175\n\t"
        "v_accvgpr_read_b32 v168, a24\n\t"
        "v_accvgpr_read_b32 v169, a25\n\t"
        "v_accvgpr_read_b32 v170, a26\n\t"
        "v_accvgpr_read_b32 v171, a27\n\t"
        "v_accvgpr_read_b32 v172, a28\n\t"
        "v_accvgpr_read_b32 v173, a29\n\t"
        "v_accvgpr_read_b32 v174, a30\n\t"
        "v_accvgpr_read_b32 v175, a31\n\t"
        "v_mul_f32_e32 v168, v168, v165\n\t"
        "v_mul_f32_e32 v169, v169, v165\n\t"
        "v_mul_f32_e32 v170, v170, v165\n\t"
        "v_mul_f32_e32 v171, v171, v165\n\t"
        "v_mul_f32_e32 v172, v172, v165\n\t"
        "v_mul_f32_e32 v173, v173, v165\n\t"
        "v_mul_f32_e32 v174, v174, v165\n\t"
        "v_mul_f32_e32 v175, v175, v165\n\t"
        "v_accvgpr_write_b32 a24, v168\n\t"
        "v_accvgpr_write_b32 a25, v169\n\t"
        "v_accvgpr_write_b32 a26, v170\n\t"
        "v_accvgpr_write_b32 a27, v171\n\t"
        "v_accvgpr_write_b32 a28, v172\n\t"
        "v_accvgpr_write_b32 a29, v173\n\t"
        "v_accvgpr_write_b32 a30, v174\n\t"
        "v_accvgpr_write_b32 a31, v175\n\t"
        "v_accvgpr_read_b32 v168, a32\n\t"
        "v_accvgpr_read_b32 v169, a33\n\t"
        "v_accvgpr_read_b32 v170, a34\n\t"
        "v_accvgpr_read_b32 v171, a35\n\t"
        "v_accvgpr_read_b32 v172, a36\n\t"
        "v_accvgpr_read_b32 v173, a37\n\t"
        "v_accvgpr_read_b32 v174, a38\n\t"
        "v_accvgpr_read_b32 v175, a39\n\t"
        "v_mul_f32_e32 v168, v168, v165\n\t"
        "v_mul_f32_e32 v169, v169, v165\n\t"
        "v_mul_f32_e32 v170, v170, v165\n\t"
        "v_mul_f32_e32 v171, v171, v165\n\t"
        "v_mul_f32_e32 v172, v172, v165\n\t"
        "v_mul_f32_e32 v173, v173, v165\n\t"
        "v_mul_f32_e32 v174, v174, v165\n\t"
        "v_mul_f32_e32 v175, v175, v165\n\t"
        "v_accvgpr_write_b32 a32, v168\n\t"
        "v_accvgpr_write_b32 a33, v169\n\t"
        "v_accvgpr_write_b32 a34, v170\n\t"
        "v_accvgpr_write_b32 a35, v171\n\t"
        "v_accvgpr_write_b32 a36, v172\n\t"
        "v_accvgpr_write_b32 a37, v173\n\t"
        "v_accvgpr_write_b32 a38, v174\n\t"
        "v_accvgpr_write_b32 a39, v175\n\t"
        "v_accvgpr_read_b32 v168, a40\n\t"
        "v_accvgpr_read_b32 v169, a41\n\t"
        "v_accvgpr_read_b32 v170, a42\n\t"
        "v_accvgpr_read_b32 v171, a43\n\t"
        "v_accvgpr_read_b32 v172, a44\n\t"
        "v_accvgpr_read_b32 v173, a45\n\t"
        "v_accvgpr_read_b32 v174, a46\n\t"
        "v_accvgpr_read_b32 v175, a47\n\t"
        "v_mul_f32_e32 v168, v168, v165\n\t"
        "v_mul_f32_e32 v169, v169, v165\n\t"
        "v_mul_f32_e32 v170, v170, v165\n\t"
        "v_mul_f32_e32 v171, v171, v165\n\t"
        "v_mul_f32_e32 v172, v172, v165\n\t"
        "v_mul_f32_e32 v173, v173, v165\n\t"
        "v_mul_f32_e32 v174, v174, v165\n\t"
        "v_mul_f32_e32 v175, v175, v165\n\t"
        "v_accvgpr_write_b32 a40, v168\n\t"
        "v_accvgpr_write_b32 a41, v169\n\t"
        "v_accvgpr_write_b32 a42, v170\n\t"
        "v_accvgpr_write_b32 a43, v171\n\t"
        "v_accvgpr_write_b32 a44, v172\n\t"
        "v_accvgpr_write_b32 a45, v173\n\t"
        "v_accvgpr_write_b32 a46, v174\n\t"
        "v_accvgpr_write_b32 a47, v175\n\t"
        "v_accvgpr_read_b32 v168, a48\n\t"
        "v_accvgpr_read_b32 v169, a49\n\t"
        "v_accvgpr_read_b32 v170, a50\n\t"
        "v_accvgpr_read_b32 v171, a51\n\t"
        "v_accvgpr_read_b32 v172, a52\n\t"
        "v_accvgpr_read_b32 v173, a53\n\t"
        "v_accvgpr_read_b32 v174, a54\n\t"
        "v_accvgpr_read_b32 v175, a55\n\t"
        "v_mul_f32_e32 v168, v168, v165\n\t"
        "v_mul_f32_e32 v169, v169, v165\n\t"
        "v_mul_f32_e32 v170, v170, v165\n\t"
        "v_mul_f32_e32 v171, v171, v165\n\t"
        "v_mul_f32_e32 v172, v172, v165\n\t"
        "v_mul_f32_e32 v173, v173, v165\n\t"
        "v_mul_f32_e32 v174, v174, v165\n\t"
        "v_mul_f32_e32 v175, v175, v165\n\t"
        "v_accvgpr_write_b32 a48, v168\n\t"
        "v_accvgpr_write_b32 a49, v169\n\t"
        "v_accvgpr_write_b32 a50, v170\n\t"
        "v_accvgpr_write_b32 a51, v171\n\t"
        "v_accvgpr_write_b32 a52, v172\n\t"
        "v_accvgpr_write_b32 a53, v173\n\t"
        "v_accvgpr_write_b32 a54, v174\n\t"
        "v_accvgpr_write_b32 a55, v175\n\t"
        "v_accvgpr_read_b32 v168, a56\n\t"
        "v_accvgpr_read_b32 v169, a57\n\t"
        "v_accvgpr_read_b32 v170, a58\n\t"
        "v_accvgpr_read_b32 v171, a59\n\t"
        "v_accvgpr_read_b32 v172, a60\n\t"
        "v_accvgpr_read_b32 v173, a61\n\t"
        "v_accvgpr_read_b32 v174, a62\n\t"
        "v_accvgpr_read_b32 v175, a63\n\t"
        "v_mul_f32_e32 v168, v168, v165\n\t"
        "v_mul_f32_e32 v169, v169, v165\n\t"
        "v_mul_f32_e32 v170, v170, v165\n\t"
        "v_mul_f32_e32 v171, v171, v165\n\t"
        "v_mul_f32_e32 v172, v172, v165\n\t"
        "v_mul_f32_e32 v173, v173, v165\n\t"
        "v_mul_f32_e32 v174, v174, v165\n\t"
        "v_mul_f32_e32 v175, v175, v165\n\t"
        "v_accvgpr_write_b32 a56, v168\n\t"
        "v_accvgpr_write_b32 a57, v169\n\t"
        "v_accvgpr_write_b32 a58, v170\n\t"
        "v_accvgpr_write_b32 a59, v171\n\t"
        "v_accvgpr_write_b32 a60, v172\n\t"
        "v_accvgpr_write_b32 a61, v173\n\t"
        "v_accvgpr_write_b32 a62, v174\n\t"
        "v_accvgpr_write_b32 a63, v175\n\t"
        "v_accvgpr_read_b32 v168, a64\n\t"
        "v_accvgpr_read_b32 v169, a65\n\t"
        "v_accvgpr_read_b32 v170, a66\n\t"
        "v_accvgpr_read_b32 v171, a67\n\t"
        "v_accvgpr_read_b32 v172, a68\n\t"
        "v_accvgpr_read_b32 v173, a69\n\t"
        "v_accvgpr_read_b32 v174, a70\n\t"
        "v_accvgpr_read_b32 v175, a71\n\t"
        "v_mul_f32_e32 v168, v168, v165\n\t"
        "v_mul_f32_e32 v169, v169, v165\n\t"
        "v_mul_f32_e32 v170, v170, v165\n\t"
        "v_mul_f32_e32 v171, v171, v165\n\t"
        "v_mul_f32_e32 v172, v172, v165\n\t"
        "v_mul_f32_e32 v173, v173, v165\n\t"
        "v_mul_f32_e32 v174, v174, v165\n\t"
        "v_mul_f32_e32 v175, v175, v165\n\t"
        "v_accvgpr_write_b32 a64, v168\n\t"
        "v_accvgpr_write_b32 a65, v169\n\t"
        "v_accvgpr_write_b32 a66, v170\n\t"
        "v_accvgpr_write_b32 a67, v171\n\t"
        "v_accvgpr_write_b32 a68, v172\n\t"
        "v_accvgpr_write_b32 a69, v173\n\t"
        "v_accvgpr_write_b32 a70, v174\n\t"
        "v_accvgpr_write_b32 a71, v175\n\t"
        "v_accvgpr_read_b32 v168, a72\n\t"
        "v_accvgpr_read_b32 v169, a73\n\t"
        "v_accvgpr_read_b32 v170, a74\n\t"
        "v_accvgpr_read_b32 v171, a75\n\t"
        "v_accvgpr_read_b32 v172, a76\n\t"
        "v_accvgpr_read_b32 v173, a77\n\t"
        "v_accvgpr_read_b32 v174, a78\n\t"
        "v_accvgpr_read_b32 v175, a79\n\t"
        "v_mul_f32_e32 v168, v168, v165\n\t"
        "v_mul_f32_e32 v169, v169, v165\n\t"
        "v_mul_f32_e32 v170, v170, v165\n\t"
        "v_mul_f32_e32 v171, v171, v165\n\t"
        "v_mul_f32_e32 v172, v172, v165\n\t"
        "v_mul_f32_e32 v173, v173, v165\n\t"
        "v_mul_f32_e32 v174, v174, v165\n\t"
        "v_mul_f32_e32 v175, v175, v165\n\t"
        "v_accvgpr_write_b32 a72, v168\n\t"
        "v_accvgpr_write_b32 a73, v169\n\t"
        "v_accvgpr_write_b32 a74, v170\n\t"
        "v_accvgpr_write_b32 a75, v171\n\t"
        "v_accvgpr_write_b32 a76, v172\n\t"
        "v_accvgpr_write_b32 a77, v173\n\t"
        "v_accvgpr_write_b32 a78, v174\n\t"
        "v_accvgpr_write_b32 a79, v175\n\t"
        "v_accvgpr_read_b32 v168, a80\n\t"
        "v_accvgpr_read_b32 v169, a81\n\t"
        "v_accvgpr_read_b32 v170, a82\n\t"
        "v_accvgpr_read_b32 v171, a83\n\t"
        "v_accvgpr_read_b32 v172, a84\n\t"
        "v_accvgpr_read_b32 v173, a85\n\t"
        "v_accvgpr_read_b32 v174, a86\n\t"
        "v_accvgpr_read_b32 v175, a87\n\t"
        "v_mul_f32_e32 v168, v168, v165\n\t"
        "v_mul_f32_e32 v169, v169, v165\n\t"
        "v_mul_f32_e32 v170, v170, v165\n\t"
        "v_mul_f32_e32 v171, v171, v165\n\t"
        "v_mul_f32_e32 v172, v172, v165\n\t"
        "v_mul_f32_e32 v173, v173, v165\n\t"
        "v_mul_f32_e32 v174, v174, v165\n\t"
        "v_mul_f32_e32 v175, v175, v165\n\t"
        "v_accvgpr_write_b32 a80, v168\n\t"
        "v_accvgpr_write_b32 a81, v169\n\t"
        "v_accvgpr_write_b32 a82, v170\n\t"
        "v_accvgpr_write_b32 a83, v171\n\t"
        "v_accvgpr_write_b32 a84, v172\n\t"
        "v_accvgpr_write_b32 a85, v173\n\t"
        "v_accvgpr_write_b32 a86, v174\n\t"
        "v_accvgpr_write_b32 a87, v175\n\t"
        "v_accvgpr_read_b32 v168, a88\n\t"
        "v_accvgpr_read_b32 v169, a89\n\t"
        "v_accvgpr_read_b32 v170, a90\n\t"
        "v_accvgpr_read_b32 v171, a91\n\t"
        "v_accvgpr_read_b32 v172, a92\n\t"
        "v_accvgpr_read_b32 v173, a93\n\t"
        "v_accvgpr_read_b32 v174, a94\n\t"
        "v_accvgpr_read_b32 v175, a95\n\t"
        "v_mul_f32_e32 v168, v168, v165\n\t"
        "v_mul_f32_e32 v169, v169, v165\n\t"
        "v_mul_f32_e32 v170, v170, v165\n\t"
        "v_mul_f32_e32 v171, v171, v165\n\t"
        "v_mul_f32_e32 v172, v172, v165\n\t"
        "v_mul_f32_e32 v173, v173, v165\n\t"
        "v_mul_f32_e32 v174, v174, v165\n\t"
        "v_mul_f32_e32 v175, v175, v165\n\t"
        "v_accvgpr_write_b32 a88, v168\n\t"
        "v_accvgpr_write_b32 a89, v169\n\t"
        "v_accvgpr_write_b32 a90, v170\n\t"
        "v_accvgpr_write_b32 a91, v171\n\t"
        "v_accvgpr_write_b32 a92, v172\n\t"
        "v_accvgpr_write_b32 a93, v173\n\t"
        "v_accvgpr_write_b32 a94, v174\n\t"
        "v_accvgpr_write_b32 a95, v175\n\t"
        "v_accvgpr_read_b32 v168, a96\n\t"
        "v_accvgpr_read_b32 v169, a97\n\t"
        "v_accvgpr_read_b32 v170, a98\n\t"
        "v_accvgpr_read_b32 v171, a99\n\t"
        "v_accvgpr_read_b32 v172, a100\n\t"
        "v_accvgpr_read_b32 v173, a101\n\t"
        "v_accvgpr_read_b32 v174, a102\n\t"
        "v_accvgpr_read_b32 v175, a103\n\t"
        "v_mul_f32_e32 v168, v168, v165\n\t"
        "v_mul_f32_e32 v169, v169, v165\n\t"
        "v_mul_f32_e32 v170, v170, v165\n\t"
        "v_mul_f32_e32 v171, v171, v165\n\t"
        "v_mul_f32_e32 v172, v172, v165\n\t"
        "v_mul_f32_e32 v173, v173, v165\n\t"
        "v_mul_f32_e32 v174, v174, v165\n\t"
        "v_mul_f32_e32 v175, v175, v165\n\t"
        "v_accvgpr_write_b32 a96, v168\n\t"
        "v_accvgpr_write_b32 a97, v169\n\t"
        "v_accvgpr_write_b32 a98, v170\n\t"
        "v_accvgpr_write_b32 a99, v171\n\t"
        "v_accvgpr_write_b32 a100, v172\n\t"
        "v_accvgpr_write_b32 a101, v173\n\t"
        "v_accvgpr_write_b32 a102, v174\n\t"
        "v_accvgpr_write_b32 a103, v175\n\t"
        "v_accvgpr_read_b32 v168, a104\n\t"
        "v_accvgpr_read_b32 v169, a105\n\t"
        "v_accvgpr_read_b32 v170, a106\n\t"
        "v_accvgpr_read_b32 v171, a107\n\t"
        "v_accvgpr_read_b32 v172, a108\n\t"
        "v_accvgpr_read_b32 v173, a109\n\t"
        "v_accvgpr_read_b32 v174, a110\n\t"
        "v_accvgpr_read_b32 v175, a111\n\t"
        "v_mul_f32_e32 v168, v168, v165\n\t"
        "v_mul_f32_e32 v169, v169, v165\n\t"
        "v_mul_f32_e32 v170, v170, v165\n\t"
        "v_mul_f32_e32 v171, v171, v165\n\t"
        "v_mul_f32_e32 v172, v172, v165\n\t"
        "v_mul_f32_e32 v173, v173, v165\n\t"
        "v_mul_f32_e32 v174, v174, v165\n\t"
        "v_mul_f32_e32 v175, v175, v165\n\t"
        "v_accvgpr_write_b32 a104, v168\n\t"
        "v_accvgpr_write_b32 a105, v169\n\t"
        "v_accvgpr_write_b32 a106, v170\n\t"
        "v_accvgpr_write_b32 a107, v171\n\t"
        "v_accvgpr_write_b32 a108, v172\n\t"
        "v_accvgpr_write_b32 a109, v173\n\t"
        "v_accvgpr_write_b32 a110, v174\n\t"
        "v_accvgpr_write_b32 a111, v175\n\t"
        "v_accvgpr_read_b32 v168, a112\n\t"
        "v_accvgpr_read_b32 v169, a113\n\t"
        "v_accvgpr_read_b32 v170, a114\n\t"
        "v_accvgpr_read_b32 v171, a115\n\t"
        "v_accvgpr_read_b32 v172, a116\n\t"
        "v_accvgpr_read_b32 v173, a117\n\t"
        "v_accvgpr_read_b32 v174, a118\n\t"
        "v_accvgpr_read_b32 v175, a119\n\t"
        "v_mul_f32_e32 v168, v168, v165\n\t"
        "v_mul_f32_e32 v169, v169, v165\n\t"
        "v_mul_f32_e32 v170, v170, v165\n\t"
        "v_mul_f32_e32 v171, v171, v165\n\t"
        "v_mul_f32_e32 v172, v172, v165\n\t"
        "v_mul_f32_e32 v173, v173, v165\n\t"
        "v_mul_f32_e32 v174, v174, v165\n\t"
        "v_mul_f32_e32 v175, v175, v165\n\t"
        "v_accvgpr_write_b32 a112, v168\n\t"
        "v_accvgpr_write_b32 a113, v169\n\t"
        "v_accvgpr_write_b32 a114, v170\n\t"
        "v_accvgpr_write_b32 a115, v171\n\t"
        "v_accvgpr_write_b32 a116, v172\n\t"
        "v_accvgpr_write_b32 a117, v173\n\t"
        "v_accvgpr_write_b32 a118, v174\n\t"
        "v_accvgpr_write_b32 a119, v175\n\t"
        "v_accvgpr_read_b32 v168, a120\n\t"
        "v_accvgpr_read_b32 v169, a121\n\t"
        "v_accvgpr_read_b32 v170, a122\n\t"
        "v_accvgpr_read_b32 v171, a123\n\t"
        "v_accvgpr_read_b32 v172, a124\n\t"
        "v_accvgpr_read_b32 v173, a125\n\t"
        "v_accvgpr_read_b32 v174, a126\n\t"
        "v_accvgpr_read_b32 v175, a127\n\t"
        "v_mul_f32_e32 v168, v168, v165\n\t"
        "v_mul_f32_e32 v169, v169, v165\n\t"
        "v_mul_f32_e32 v170, v170, v165\n\t"
        "v_mul_f32_e32 v171, v171, v165\n\t"
        "v_mul_f32_e32 v172, v172, v165\n\t"
        "v_mul_f32_e32 v173, v173, v165\n\t"
        "v_mul_f32_e32 v174, v174, v165\n\t"
        "v_mul_f32_e32 v175, v175, v165\n\t"
        "v_accvgpr_write_b32 a120, v168\n\t"
        "v_accvgpr_write_b32 a121, v169\n\t"
        "v_accvgpr_write_b32 a122, v170\n\t"
        "v_accvgpr_write_b32 a123, v171\n\t"
        "v_accvgpr_write_b32 a124, v172\n\t"
        "v_accvgpr_write_b32 a125, v173\n\t"
        "v_accvgpr_write_b32 a126, v174\n\t"
        "v_accvgpr_write_b32 a127, v175\n\t"
        "LSKE_%=:\n\t"
        "s_mov_b32 s70, 0\n\t"
        "s_mov_b32 s69, %[thr]\n\t"
        "s_nop 1\n\t"
        "s_branch LRE_%=\n\t"
        "LSO_%=:\n\t"
        "v_mov_b32_e32 v164, v162\n\t"
        "s_nop 1\n\t"
        "v_permlane32_swap_b32_e32 v162, v164\n\t"
        "s_nop 1\n\t"
        "v_max_f32_e32 v162, v162, v164\n\t"
        "v_max_f32_e32 v164, s70, v162\n\t"
        "v_max_f32_e32 v165, 0, v164\n\t"
        "v_exp_f32_e64 v165, -v165\n\t"
        "v_sub_f32_e32 v0, v0, v164\n\t"
        "v_sub_f32_e32 v1, v1, v164\n\t"
        "v_sub_f32_e32 v2, v2, v164\n\t"
        "v_sub_f32_e32 v3, v3, v164\n\t"
        "v_sub_f32_e32 v4, v4, v164\n\t"
        "v_sub_f32_e32 v5, v5, v164\n\t"
        "v_sub_f32_e32 v6, v6, v164\n\t"
        "v_sub_f32_e32 v7, v7, v164\n\t"
        "v_sub_f32_e32 v8, v8, v164\n\t"
        "v_sub_f32_e32 v9, v9, v164\n\t"
        "v_sub_f32_e32 v10, v10, v164\n\t"
        "v_sub_f32_e32 v11, v11, v164\n\t"
        "v_sub_f32_e32 v12, v12, v164\n\t"
        "v_sub_f32_e32 v13, v13, v164\n\t"
        "v_sub_f32_e32 v14, v14, v164\n\t"
        "v_sub_f32_e32 v15, v15, v164\n\t"
        "v_sub_f32_e32 v16, v16, v164\n\t"
        "v_sub_f32_e32 v17, v17, v164\n\t"
        "v_sub_f32_e32 v18, v18, v164\n\t"
        "v_sub_f32_e32 v19, v19, v164\n\t"
        "v_sub_f32_e32 v20, v20, v164\n\t"
        "v_sub_f32_e32 v21, v21, v164\n\t"
        "v_sub_f32_e32 v22, v22, v164\n\t"
        "v_sub_f32_e32 v23, v23, v164\n\t"
        "v_sub_f32_e32 v24, v24, v164\n\t"
        "v_sub_f32_e32 v25, v25, v164\n\t"
        "v_sub_f32_e32 v26, v26, v164\n\t"
        "v_sub_f32_e32 v27, v27, v164\n\t"
        "v_sub_f32_e32 v28, v28, v164\n\t"
        "v_sub_f32_e32 v29, v29, v164\n\t"
        "v_sub_f32_e32 v30, v30, v164\n\t"
        "v_sub_f32_e32 v31, v31, v164\n\t"
        "v_sub_f32_e32 v64, v64, v164\n\t"
        "v_sub_f32_e32 v65, v65, v164\n\t"
        "v_sub_f32_e32 v66, v66, v164\n\t"
        "v_sub_f32_e32 v67, v67, v164\n\t"
        "v_sub_f32_e32 v68, v68, v164\n\t"
        "v_sub_f32_e32 v69, v69, v164\n\t"
        "v_sub_f32_e32 v70, v70, v164\n\t"
        "v_sub_f32_e32 v71, v71, v164\n\t"
        "v_sub_f32_e32 v72, v72, v164\n\t"
        "v_sub_f32_e32 v73, v73, v164\n\t"
        "v_sub_f32_e32 v74, v74, v164\n\t"
        "v_sub_f32_e32 v75, v75, v164\n\t"
        "v_sub_f32_e32 v76, v76, v164\n\t"
        "v_sub_f32_e32 v77, v77, v164\n\t"
        "v_sub_f32_e32 v78, v78, v164\n\t"
        "v_sub_f32_e32 v79, v79, v164\n\t"
        "v_mul_f32_e32 v160, v160, v165\n\t"
        "v_mul_f32_e32 v161, v161, v165\n\t"
        "s_cmp_lg_u32 s70, 0\n\t"
        "s_cbranch_scc1 LSKO_%=\n\t"
        "v_accvgpr_read_b32 v168, a0\n\t"
        "v_accvgpr_read_b32 v169, a1\n\t"
        "v_accvgpr_read_b32 v170, a2\n\t"
        "v_accvgpr_read_b32 v171, a3\n\t"
        "v_accvgpr_read_b32 v172, a4\n\t"
        "v_accvgpr_read_b32 v173, a5\n\t"
        "v_accvgpr_read_b32 v174, a6\n\t"
        "v_accvgpr_read_b32 v175, a7\n\t"
        "v_mul_f32_e32 v168, v168, v165\n\t"
        "v_mul_f32_e32 v169, v169, v165\n\t"
        "v_mul_f32_e32 v170, v170, v165\n\t"
        "v_mul_f32_e32 v171, v171, v165\n\t"
        "v_mul_f32_e32 v172, v172, v165\n\t"
        "v_mul_f32_e32 v173, v173, v165\n\t"
        "v_mul_f32_e32 v174, v174, v165\n\t"
        "v_mul_f32_e32 v175, v175, v165\n\t"
        "v_accvgpr_write_b32 a0, v168\n\t"
        "v_accvgpr_write_b32 a1, v169\n\t"
        "v_accvgpr_write_b32 a2, v170\n\t"
        "v_accvgpr_write_b32 a3, v171\n\t"
        "v_accvgpr_write_b32 a4, v172\n\t"
        "v_accvgpr_write_b32 a5, v173\n\t"
        "v_accvgpr_write_b32 a6, v174\n\t"
        "v_accvgpr_write_b32 a7, v175\n\t"
        "v_accvgpr_read_b32 v168, a8\n\t"
        "v_accvgpr_read_b32 v169, a9\n\t"
        "v_accvgpr_read_b32 v170, a10\n\t"
        "v_accvgpr_read_b32 v171, a11\n\t"
        "v_accvgpr_read_b32 v172, a12\n\t"
        "v_accvgpr_read_b32 v173, a13\n\t"
        "v_accvgpr_read_b32 v174, a14\n\t"
        "v_accvgpr_read_b32 v175, a15\n\t"
        "v_mul_f32_e32 v168, v168, v165\n\t"
        "v_mul_f32_e32 v169, v169, v165\n\t"
        "v_mul_f32_e32 v170, v170, v165\n\t"
        "v_mul_f32_e32 v171, v171, v165\n\t"
        "v_mul_f32_e32 v172, v172, v165\n\t"
        "v_mul_f32_e32 v173, v173, v165\n\t"
        "v_mul_f32_e32 v174, v174, v165\n\t"
        "v_mul_f32_e32 v175, v175, v165\n\t"
        "v_accvgpr_write_b32 a8, v168\n\t"
        "v_accvgpr_write_b32 a9, v169\n\t"
        "v_accvgpr_write_b32 a10, v170\n\t"
        "v_accvgpr_write_b32 a11, v171\n\t"
        "v_accvgpr_write_b32 a12, v172\n\t"
        "v_accvgpr_write_b32 a13, v173\n\t"
        "v_accvgpr_write_b32 a14, v174\n\t"
        "v_accvgpr_write_b32 a15, v175\n\t"
        "v_accvgpr_read_b32 v168, a16\n\t"
        "v_accvgpr_read_b32 v169, a17\n\t"
        "v_accvgpr_read_b32 v170, a18\n\t"
        "v_accvgpr_read_b32 v171, a19\n\t"
        "v_accvgpr_read_b32 v172, a20\n\t"
        "v_accvgpr_read_b32 v173, a21\n\t"
        "v_accvgpr_read_b32 v174, a22\n\t"
        "v_accvgpr_read_b32 v175, a23\n\t"
        "v_mul_f32_e32 v168, v168, v165\n\t"
        "v_mul_f32_e32 v169, v169, v165\n\t"
        "v_mul_f32_e32 v170, v170, v165\n\t"
        "v_mul_f32_e32 v171, v171, v165\n\t"
        "v_mul_f32_e32 v172, v172, v165\n\t"
        "v_mul_f32_e32 v173, v173, v165\n\t"
        "v_mul_f32_e32 v174, v174, v165\n\t"
        "v_mul_f32_e32 v175, v175, v165\n\t"
        "v_accvgpr_write_b32 a16, v168\n\t"
        "v_accvgpr_write_b32 a17, v169\n\t"
        "v_accvgpr_write_b32 a18, v170\n\t"
        "v_accvgpr_write_b32 a19, v171\n\t"
        "v_accvgpr_write_b32 a20, v172\n\t"
        "v_accvgpr_write_b32 a21, v173\n\t"
        "v_accvgpr_write_b32 a22, v174\n\t"
        "v_accvgpr_write_b32 a23, v175\n\t"
        "v_accvgpr_read_b32 v168, a24\n\t"
        "v_accvgpr_read_b32 v169, a25\n\t"
        "v_accvgpr_read_b32 v170, a26\n\t"
        "v_accvgpr_read_b32 v171, a27\n\t"
        "v_accvgpr_read_b32 v172, a28\n\t"
        "v_accvgpr_read_b32 v173, a29\n\t"
        "v_accvgpr_read_b32 v174, a30\n\t"
        "v_accvgpr_read_b32 v175, a31\n\t"
        "v_mul_f32_e32 v168, v168, v165\n\t"
        "v_mul_f32_e32 v169, v169, v165\n\t"
        "v_mul_f32_e32 v170, v170, v165\n\t"
        "v_mul_f32_e32 v171, v171, v165\n\t"
        "v_mul_f32_e32 v172, v172, v165\n\t"
        "v_mul_f32_e32 v173, v173, v165\n\t"
        "v_mul_f32_e32 v174, v174, v165\n\t"
        "v_mul_f32_e32 v175, v175, v165\n\t"
        "v_accvgpr_write_b32 a24, v168\n\t"
        "v_accvgpr_write_b32 a25, v169\n\t"
        "v_accvgpr_write_b32 a26, v170\n\t"
        "v_accvgpr_write_b32 a27, v171\n\t"
        "v_accvgpr_write_b32 a28, v172\n\t"
        "v_accvgpr_write_b32 a29, v173\n\t"
        "v_accvgpr_write_b32 a30, v174\n\t"
        "v_accvgpr_write_b32 a31, v175\n\t"
        "v_accvgpr_read_b32 v168, a32\n\t"
        "v_accvgpr_read_b32 v169, a33\n\t"
        "v_accvgpr_read_b32 v170, a34\n\t"
        "v_accvgpr_read_b32 v171, a35\n\t"
        "v_accvgpr_read_b32 v172, a36\n\t"
        "v_accvgpr_read_b32 v173, a37\n\t"
        "v_accvgpr_read_b32 v174, a38\n\t"
        "v_accvgpr_read_b32 v175, a39\n\t"
        "v_mul_f32_e32 v168, v168, v165\n\t"
        "v_mul_f32_e32 v169, v169, v165\n\t"
        "v_mul_f32_e32 v170, v170, v165\n\t"
        "v_mul_f32_e32 v171, v171, v165\n\t"
        "v_mul_f32_e32 v172, v172, v165\n\t"
        "v_mul_f32_e32 v173, v173, v165\n\t"
        "v_mul_f32_e32 v174, v174, v165\n\t"
        "v_mul_f32_e32 v175, v175, v165\n\t"
        "v_accvgpr_write_b32 a32, v168\n\t"
        "v_accvgpr_write_b32 a33, v169\n\t"
        "v_accvgpr_write_b32 a34, v170\n\t"
        "v_accvgpr_write_b32 a35, v171\n\t"
        "v_accvgpr_write_b32 a36, v172\n\t"
        "v_accvgpr_write_b32 a37, v173\n\t"
        "v_accvgpr_write_b32 a38, v174\n\t"
        "v_accvgpr_write_b32 a39, v175\n\t"
        "v_accvgpr_read_b32 v168, a40\n\t"
        "v_accvgpr_read_b32 v169, a41\n\t"
        "v_accvgpr_read_b32 v170, a42\n\t"
        "v_accvgpr_read_b32 v171, a43\n\t"
        "v_accvgpr_read_b32 v172, a44\n\t"
        "v_accvgpr_read_b32 v173, a45\n\t"
        "v_accvgpr_read_b32 v174, a46\n\t"
        "v_accvgpr_read_b32 v175, a47\n\t"
        "v_mul_f32_e32 v168, v168, v165\n\t"
        "v_mul_f32_e32 v169, v169, v165\n\t"
        "v_mul_f32_e32 v170, v170, v165\n\t"
        "v_mul_f32_e32 v171, v171, v165\n\t"
        "v_mul_f32_e32 v172, v172, v165\n\t"
        "v_mul_f32_e32 v173, v173, v165\n\t"
        "v_mul_f32_e32 v174, v174, v165\n\t"
        "v_mul_f32_e32 v175, v175, v165\n\t"
        "v_accvgpr_write_b32 a40, v168\n\t"
        "v_accvgpr_write_b32 a41, v169\n\t"
        "v_accvgpr_write_b32 a42, v170\n\t"
        "v_accvgpr_write_b32 a43, v171\n\t"
        "v_accvgpr_write_b32 a44, v172\n\t"
        "v_accvgpr_write_b32 a45, v173\n\t"
        "v_accvgpr_write_b32 a46, v174\n\t"
        "v_accvgpr_write_b32 a47, v175\n\t"
        "v_accvgpr_read_b32 v168, a48\n\t"
        "v_accvgpr_read_b32 v169, a49\n\t"
        "v_accvgpr_read_b32 v170, a50\n\t"
        "v_accvgpr_read_b32 v171, a51\n\t"
        "v_accvgpr_read_b32 v172, a52\n\t"
        "v_accvgpr_read_b32 v173, a53\n\t"
        "v_accvgpr_read_b32 v174, a54\n\t"
        "v_accvgpr_read_b32 v175, a55\n\t"
        "v_mul_f32_e32 v168, v168, v165\n\t"
        "v_mul_f32_e32 v169, v169, v165\n\t"
        "v_mul_f32_e32 v170, v170, v165\n\t"
        "v_mul_f32_e32 v171, v171, v165\n\t"
        "v_mul_f32_e32 v172, v172, v165\n\t"
        "v_mul_f32_e32 v173, v173, v165\n\t"
        "v_mul_f32_e32 v174, v174, v165\n\t"
        "v_mul_f32_e32 v175, v175, v165\n\t"
        "v_accvgpr_write_b32 a48, v168\n\t"
        "v_accvgpr_write_b32 a49, v169\n\t"
        "v_accvgpr_write_b32 a50, v170\n\t"
        "v_accvgpr_write_b32 a51, v171\n\t"
        "v_accvgpr_write_b32 a52, v172\n\t"
        "v_accvgpr_write_b32 a53, v173\n\t"
        "v_accvgpr_write_b32 a54, v174\n\t"
        "v_accvgpr_write_b32 a55, v175\n\t"
        "v_accvgpr_read_b32 v168, a56\n\t"
        "v_accvgpr_read_b32 v169, a57\n\t"
        "v_accvgpr_read_b32 v170, a58\n\t"
        "v_accvgpr_read_b32 v171, a59\n\t"
        "v_accvgpr_read_b32 v172, a60\n\t"
        "v_accvgpr_read_b32 v173, a61\n\t"
        "v_accvgpr_read_b32 v174, a62\n\t"
        "v_accvgpr_read_b32 v175, a63\n\t"
        "v_mul_f32_e32 v168, v168, v165\n\t"
        "v_mul_f32_e32 v169, v169, v165\n\t"
        "v_mul_f32_e32 v170, v170, v165\n\t"
        "v_mul_f32_e32 v171, v171, v165\n\t"
        "v_mul_f32_e32 v172, v172, v165\n\t"
        "v_mul_f32_e32 v173, v173, v165\n\t"
        "v_mul_f32_e32 v174, v174, v165\n\t"
        "v_mul_f32_e32 v175, v175, v165\n\t"
        "v_accvgpr_write_b32 a56, v168\n\t"
        "v_accvgpr_write_b32 a57, v169\n\t"
        "v_accvgpr_write_b32 a58, v170\n\t"
        "v_accvgpr_write_b32 a59, v171\n\t"
        "v_accvgpr_write_b32 a60, v172\n\t"
        "v_accvgpr_write_b32 a61, v173\n\t"
        "v_accvgpr_write_b32 a62, v174\n\t"
        "v_accvgpr_write_b32 a63, v175\n\t"
        "v_accvgpr_read_b32 v168, a64\n\t"
        "v_accvgpr_read_b32 v169, a65\n\t"
        "v_accvgpr_read_b32 v170, a66\n\t"
        "v_accvgpr_read_b32 v171, a67\n\t"
        "v_accvgpr_read_b32 v172, a68\n\t"
        "v_accvgpr_read_b32 v173, a69\n\t"
        "v_accvgpr_read_b32 v174, a70\n\t"
        "v_accvgpr_read_b32 v175, a71\n\t"
        "v_mul_f32_e32 v168, v168, v165\n\t"
        "v_mul_f32_e32 v169, v169, v165\n\t"
        "v_mul_f32_e32 v170, v170, v165\n\t"
        "v_mul_f32_e32 v171, v171, v165\n\t"
        "v_mul_f32_e32 v172, v172, v165\n\t"
        "v_mul_f32_e32 v173, v173, v165\n\t"
        "v_mul_f32_e32 v174, v174, v165\n\t"
        "v_mul_f32_e32 v175, v175, v165\n\t"
        "v_accvgpr_write_b32 a64, v168\n\t"
        "v_accvgpr_write_b32 a65, v169\n\t"
        "v_accvgpr_write_b32 a66, v170\n\t"
        "v_accvgpr_write_b32 a67, v171\n\t"
        "v_accvgpr_write_b32 a68, v172\n\t"
        "v_accvgpr_write_b32 a69, v173\n\t"
        "v_accvgpr_write_b32 a70, v174\n\t"
        "v_accvgpr_write_b32 a71, v175\n\t"
        "v_accvgpr_read_b32 v168, a72\n\t"
        "v_accvgpr_read_b32 v169, a73\n\t"
        "v_accvgpr_read_b32 v170, a74\n\t"
        "v_accvgpr_read_b32 v171, a75\n\t"
        "v_accvgpr_read_b32 v172, a76\n\t"
        "v_accvgpr_read_b32 v173, a77\n\t"
        "v_accvgpr_read_b32 v174, a78\n\t"
        "v_accvgpr_read_b32 v175, a79\n\t"
        "v_mul_f32_e32 v168, v168, v165\n\t"
        "v_mul_f32_e32 v169, v169, v165\n\t"
        "v_mul_f32_e32 v170, v170, v165\n\t"
        "v_mul_f32_e32 v171, v171, v165\n\t"
        "v_mul_f32_e32 v172, v172, v165\n\t"
        "v_mul_f32_e32 v173, v173, v165\n\t"
        "v_mul_f32_e32 v174, v174, v165\n\t"
        "v_mul_f32_e32 v175, v175, v165\n\t"
        "v_accvgpr_write_b32 a72, v168\n\t"
        "v_accvgpr_write_b32 a73, v169\n\t"
        "v_accvgpr_write_b32 a74, v170\n\t"
        "v_accvgpr_write_b32 a75, v171\n\t"
        "v_accvgpr_write_b32 a76, v172\n\t"
        "v_accvgpr_write_b32 a77, v173\n\t"
        "v_accvgpr_write_b32 a78, v174\n\t"
        "v_accvgpr_write_b32 a79, v175\n\t"
        "v_accvgpr_read_b32 v168, a80\n\t"
        "v_accvgpr_read_b32 v169, a81\n\t"
        "v_accvgpr_read_b32 v170, a82\n\t"
        "v_accvgpr_read_b32 v171, a83\n\t"
        "v_accvgpr_read_b32 v172, a84\n\t"
        "v_accvgpr_read_b32 v173, a85\n\t"
        "v_accvgpr_read_b32 v174, a86\n\t"
        "v_accvgpr_read_b32 v175, a87\n\t"
        "v_mul_f32_e32 v168, v168, v165\n\t"
        "v_mul_f32_e32 v169, v169, v165\n\t"
        "v_mul_f32_e32 v170, v170, v165\n\t"
        "v_mul_f32_e32 v171, v171, v165\n\t"
        "v_mul_f32_e32 v172, v172, v165\n\t"
        "v_mul_f32_e32 v173, v173, v165\n\t"
        "v_mul_f32_e32 v174, v174, v165\n\t"
        "v_mul_f32_e32 v175, v175, v165\n\t"
        "v_accvgpr_write_b32 a80, v168\n\t"
        "v_accvgpr_write_b32 a81, v169\n\t"
        "v_accvgpr_write_b32 a82, v170\n\t"
        "v_accvgpr_write_b32 a83, v171\n\t"
        "v_accvgpr_write_b32 a84, v172\n\t"
        "v_accvgpr_write_b32 a85, v173\n\t"
        "v_accvgpr_write_b32 a86, v174\n\t"
        "v_accvgpr_write_b32 a87, v175\n\t"
        "v_accvgpr_read_b32 v168, a88\n\t"
        "v_accvgpr_read_b32 v169, a89\n\t"
        "v_accvgpr_read_b32 v170, a90\n\t"
        "v_accvgpr_read_b32 v171, a91\n\t"
        "v_accvgpr_read_b32 v172, a92\n\t"
        "v_accvgpr_read_b32 v173, a93\n\t"
        "v_accvgpr_read_b32 v174, a94\n\t"
        "v_accvgpr_read_b32 v175, a95\n\t"
        "v_mul_f32_e32 v168, v168, v165\n\t"
        "v_mul_f32_e32 v169, v169, v165\n\t"
        "v_mul_f32_e32 v170, v170, v165\n\t"
        "v_mul_f32_e32 v171, v171, v165\n\t"
        "v_mul_f32_e32 v172, v172, v165\n\t"
        "v_mul_f32_e32 v173, v173, v165\n\t"
        "v_mul_f32_e32 v174, v174, v165\n\t"
        "v_mul_f32_e32 v175, v175, v165\n\t"
        "v_accvgpr_write_b32 a88, v168\n\t"
        "v_accvgpr_write_b32 a89, v169\n\t"
        "v_accvgpr_write_b32 a90, v170\n\t"
        "v_accvgpr_write_b32 a91, v171\n\t"
        "v_accvgpr_write_b32 a92, v172\n\t"
        "v_accvgpr_write_b32 a93, v173\n\t"
        "v_accvgpr_write_b32 a94, v174\n\t"
        "v_accvgpr_write_b32 a95, v175\n\t"
        "v_accvgpr_read_b32 v168, a96\n\t"
        "v_accvgpr_read_b32 v169, a97\n\t"
        "v_accvgpr_read_b32 v170, a98\n\t"
        "v_accvgpr_read_b32 v171, a99\n\t"
        "v_accvgpr_read_b32 v172, a100\n\t"
        "v_accvgpr_read_b32 v173, a101\n\t"
        "v_accvgpr_read_b32 v174, a102\n\t"
        "v_accvgpr_read_b32 v175, a103\n\t"
        "v_mul_f32_e32 v168, v168, v165\n\t"
        "v_mul_f32_e32 v169, v169, v165\n\t"
        "v_mul_f32_e32 v170, v170, v165\n\t"
        "v_mul_f32_e32 v171, v171, v165\n\t"
        "v_mul_f32_e32 v172, v172, v165\n\t"
        "v_mul_f32_e32 v173, v173, v165\n\t"
        "v_mul_f32_e32 v174, v174, v165\n\t"
        "v_mul_f32_e32 v175, v175, v165\n\t"
        "v_accvgpr_write_b32 a96, v168\n\t"
        "v_accvgpr_write_b32 a97, v169\n\t"
        "v_accvgpr_write_b32 a98, v170\n\t"
        "v_accvgpr_write_b32 a99, v171\n\t"
        "v_accvgpr_write_b32 a100, v172\n\t"
        "v_accvgpr_write_b32 a101, v173\n\t"
        "v_accvgpr_write_b32 a102, v174\n\t"
        "v_accvgpr_write_b32 a103, v175\n\t"
        "v_accvgpr_read_b32 v168, a104\n\t"
        "v_accvgpr_read_b32 v169, a105\n\t"
        "v_accvgpr_read_b32 v170, a106\n\t"
        "v_accvgpr_read_b32 v171, a107\n\t"
        "v_accvgpr_read_b32 v172, a108\n\t"
        "v_accvgpr_read_b32 v173, a109\n\t"
        "v_accvgpr_read_b32 v174, a110\n\t"
        "v_accvgpr_read_b32 v175, a111\n\t"
        "v_mul_f32_e32 v168, v168, v165\n\t"
        "v_mul_f32_e32 v169, v169, v165\n\t"
        "v_mul_f32_e32 v170, v170, v165\n\t"
        "v_mul_f32_e32 v171, v171, v165\n\t"
        "v_mul_f32_e32 v172, v172, v165\n\t"
        "v_mul_f32_e32 v173, v173, v165\n\t"
        "v_mul_f32_e32 v174, v174, v165\n\t"
        "v_mul_f32_e32 v175, v175, v165\n\t"
        "v_accvgpr_write_b32 a104, v168\n\t"
        "v_accvgpr_write_b32 a105, v169\n\t"
        "v_accvgpr_write_b32 a106, v170\n\t"
        "v_accvgpr_write_b32 a107, v171\n\t"
        "v_accvgpr_write_b32 a108, v172\n\t"
        "v_accvgpr_write_b32 a109, v173\n\t"
        "v_accvgpr_write_b32 a110, v174\n\t"
        "v_accvgpr_write_b32 a111, v175\n\t"
        "v_accvgpr_read_b32 v168, a112\n\t"
        "v_accvgpr_read_b32 v169, a113\n\t"
        "v_accvgpr_read_b32 v170, a114\n\t"
        "v_accvgpr_read_b32 v171, a115\n\t"
        "v_accvgpr_read_b32 v172, a116\n\t"
        "v_accvgpr_read_b32 v173, a117\n\t"
        "v_accvgpr_read_b32 v174, a118\n\t"
        "v_accvgpr_read_b32 v175, a119\n\t"
        "v_mul_f32_e32 v168, v168, v165\n\t"
        "v_mul_f32_e32 v169, v169, v165\n\t"
        "v_mul_f32_e32 v170, v170, v165\n\t"
        "v_mul_f32_e32 v171, v171, v165\n\t"
        "v_mul_f32_e32 v172, v172, v165\n\t"
        "v_mul_f32_e32 v173, v173, v165\n\t"
        "v_mul_f32_e32 v174, v174, v165\n\t"
        "v_mul_f32_e32 v175, v175, v165\n\t"
        "v_accvgpr_write_b32 a112, v168\n\t"
        "v_accvgpr_write_b32 a113, v169\n\t"
        "v_accvgpr_write_b32 a114, v170\n\t"
        "v_accvgpr_write_b32 a115, v171\n\t"
        "v_accvgpr_write_b32 a116, v172\n\t"
        "v_accvgpr_write_b32 a117, v173\n\t"
        "v_accvgpr_write_b32 a118, v174\n\t"
        "v_accvgpr_write_b32 a119, v175\n\t"
        "v_accvgpr_read_b32 v168, a120\n\t"
        "v_accvgpr_read_b32 v169, a121\n\t"
        "v_accvgpr_read_b32 v170, a122\n\t"
        "v_accvgpr_read_b32 v171, a123\n\t"
        "v_accvgpr_read_b32 v172, a124\n\t"
        "v_accvgpr_read_b32 v173, a125\n\t"
        "v_accvgpr_read_b32 v174, a126\n\t"
        "v_accvgpr_read_b32 v175, a127\n\t"
        "v_mul_f32_e32 v168, v168, v165\n\t"
        "v_mul_f32_e32 v169, v169, v165\n\t"
        "v_mul_f32_e32 v170, v170, v165\n\t"
        "v_mul_f32_e32 v171, v171, v165\n\t"
        "v_mul_f32_e32 v172, v172, v165\n\t"
        "v_mul_f32_e32 v173, v173, v165\n\t"
        "v_mul_f32_e32 v174, v174, v165\n\t"
        "v_mul_f32_e32 v175, v175, v165\n\t"
        "v_accvgpr_write_b32 a120, v168\n\t"
        "v_accvgpr_write_b32 a121, v169\n\t"
        "v_accvgpr_write_b32 a122, v170\n\t"
        "v_accvgpr_write_b32 a123, v171\n\t"
        "v_accvgpr_write_b32 a124, v172\n\t"
        "v_accvgpr_write_b32 a125, v173\n\t"
        "v_accvgpr_write_b32 a126, v174\n\t"
        "v_accvgpr_write_b32 a127, v175\n\t"
        "LSKO_%=:\n\t"
        "s_mov_b32 s70, 0\n\t"
        "s_mov_b32 s69, %[thr]\n\t"
        "s_nop 1\n\t"
        "s_branch LRO_%=\n\t"
        "LEND_%=:\n\t"
        "v_add_f32_e32 v160, v160, v161\n\t"
        "s_mov_b32 m0, s71\n\t"
        "v_mov_b32_e32 v164, v160\n\t"
        "s_nop 1\n\t"
        "v_permlane32_swap_b32_e32 v160, v164\n\t"
        "s_nop 1\n\t"
        "v_add_f32_e32 v160, v160, v164\n\t"
        "v_rcp_f32_e32 v165, v160\n\t"
        "s_waitcnt lgkmcnt(0)\n\t"
        "s_mov_b32 s80, %[oo]\n\t"
        "s_nop 3\n\t"
        "v_accvgpr_read_b32 v0, a0\n\t"
        "v_accvgpr_read_b32 v1, a1\n\t"
        "v_accvgpr_read_b32 v2, a2\n\t"
        "v_accvgpr_read_b32 v3, a3\n\t"
        "v_accvgpr_read_b32 v4, a4\n\t"
        "v_accvgpr_read_b32 v5, a5\n\t"
        "v_accvgpr_read_b32 v6, a6\n\t"
        "v_accvgpr_read_b32 v7, a7\n\t"
        "v_mul_f32_e32 v0, v0, v165\n\t"
        "v_mul_f32_e32 v1, v1, v165\n\t"
        "v_mul_f32_e32 v2, v2, v165\n\t"
        "v_mul_f32_e32 v3, v3, v165\n\t"
        "v_mul_f32_e32 v4, v4, v165\n\t"
        "v_mul_f32_e32 v5, v5, v165\n\t"
        "v_mul_f32_e32 v6, v6, v165\n\t"
        "v_mul_f32_e32 v7, v7, v165\n\t"
        "v_cvt_pk_bf16_f32 v96, v0, v1\n\t"
        "v_cvt_pk_bf16_f32 v97, v2, v3\n\t"
        "v_cvt_pk_bf16_f32 v98, v4, v5\n\t"
        "v_cvt_pk_bf16_f32 v99, v6, v7\n\t"
        "buffer_store_dwordx4 v[96:99], %[l16], %[rs], s80 offen offset:0 sc1\n\t"
        "v_accvgpr_read_b32 v8, a8\n\t"
        "v_accvgpr_read_b32 v9, a9\n\t"
        "v_accvgpr_read_b32 v10, a10\n\t"
        "v_accvgpr_read_b32 v11, a11\n\t"
        "v_accvgpr_read_b32 v12, a12\n\t"
        "v_accvgpr_read_b32 v13, a13\n\t"
        "v_accvgpr_read_b32 v14, a14\n\t"
        "v_accvgpr_read_b32 v15, a15\n\t"
        "v_mul_f32_e32 v8, v8, v165\n\t"
        "v_mul_f32_e32 v9, v9, v165\n\t"
        "v_mul_f32_e32 v10, v10, v165\n\t"
        "v_mul_f32_e32 v11, v11, v165\n\t"
        "v_mul_f32_e32 v12, v12, v165\n\t"
        "v_mul_f32_e32 v13, v13, v165\n\t"
        "v_mul_f32_e32 v14, v14, v165\n\t"
        "v_mul_f32_e32 v15, v15, v165\n\t"
        "v_cvt_pk_bf16_f32 v100, v8, v9\n\t"
        "v_cvt_pk_bf16_f32 v101, v10, v11\n\t"
        "v_cvt_pk_bf16_f32 v102, v12, v13\n\t"
        "v_cvt_pk_bf16_f32 v103, v14, v15\n\t"
        "buffer_store_dwordx4 v[100:103], %[l16], %[rs], s80 offen offset:1024 sc1\n\t"
        "v_accvgpr_read_b32 v16, a16\n\t"
        "v_accvgpr_read_b32 v17, a17\n\t"
        "v_accvgpr_read_b32 v18, a18\n\t"
        "v_accvgpr_read_b32 v19, a19\n\t"
        "v_accvgpr_read_b32 v20, a20\n\t"
        "v_accvgpr_read_b32 v21, a21\n\t"
        "v_accvgpr_read_b32 v22, a22\n\t"
        "v_accvgpr_read_b32 v23, a23\n\t"
        "v_mul_f32_e32 v16, v16, v165\n\t"
        "v_mul_f32_e32 v17, v17, v165\n\t"
        "v_mul_f32_e32 v18, v18, v165\n\t"
        "v_mul_f32_e32 v19, v19, v165\n\t"
        "v_mul_f32_e32 v20, v20, v165\n\t"
        "v_mul_f32_e32 v21, v21, v165\n\t"
        "v_mul_f32_e32 v22, v22, v165\n\t"
        "v_mul_f32_e32 v23, v23, v165\n\t"
        "v_cvt_pk_bf16_f32 v104, v16, v17\n\t"
        "v_cvt_pk_bf16_f32 v105, v18, v19\n\t"
        "v_cvt_pk_bf16_f32 v106, v20, v21\n\t"
        "v_cvt_pk_bf16_f32 v107, v22, v23\n\t"
        "buffer_store_dwordx4 v[104:107], %[l16], %[rs], s80 offen offset:2048 sc1\n\t"
        "v_accvgpr_read_b32 v24, a24\n\t"
        "v_accvgpr_read_b32 v25, a25\n\t"
        "v_accvgpr_read_b32 v26, a26\n\t"
        "v_accvgpr_read_b32 v27, a27\n\t"
        "v_accvgpr_read_b32 v28, a28\n\t"
        "v_accvgpr_read_b32 v29, a29\n\t"
        "v_accvgpr_read_b32 v30, a30\n\t"
        "v_accvgpr_read_b32 v31, a31\n\t"
        "v_mul_f32_e32 v24, v24, v165\n\t"
        "v_mul_f32_e32 v25, v25, v165\n\t"
        "v_mul_f32_e32 v26, v26, v165\n\t"
        "v_mul_f32_e32 v27, v27, v165\n\t"
        "v_mul_f32_e32 v28, v28, v165\n\t"
        "v_mul_f32_e32 v29, v29, v165\n\t"
        "v_mul_f32_e32 v30, v30, v165\n\t"
        "v_mul_f32_e32 v31, v31, v165\n\t"
        "v_cvt_pk_bf16_f32 v108, v24, v25\n\t"
        "v_cvt_pk_bf16_f32 v109, v26, v27\n\t"
        "v_cvt_pk_bf16_f32 v110, v28, v29\n\t"
        "v_cvt_pk_bf16_f32 v111, v30, v31\n\t"
        "buffer_store_dwordx4 v[108:111], %[l16], %[rs], s80 offen offset:3072 sc1\n\t"
        "v_accvgpr_read_b32 v32, a32\n\t"
        "v_accvgpr_read_b32 v33, a33\n\t"
        "v_accvgpr_read_b32 v34, a34\n\t"
        "v_accvgpr_read_b32 v35, a35\n\t"
        "v_accvgpr_read_b32 v36, a36\n\t"
        "v_accvgpr_read_b32 v37, a37\n\t"
        "v_accvgpr_read_b32 v38, a38\n\t"
        "v_accvgpr_read_b32 v39, a39\n\t"
        "v_mul_f32_e32 v32, v32, v165\n\t"
        "v_mul_f32_e32 v33, v33, v165\n\t"
        "v_mul_f32_e32 v34, v34, v165\n\t"
        "v_mul_f32_e32 v35, v35, v165\n\t"
        "v_mul_f32_e32 v36, v36, v165\n\t"
        "v_mul_f32_e32 v37, v37, v165\n\t"
        "v_mul_f32_e32 v38, v38, v165\n\t"
        "v_mul_f32_e32 v39, v39, v165\n\t"
        "v_cvt_pk_bf16_f32 v112, v32, v33\n\t"
        "v_cvt_pk_bf16_f32 v113, v34, v35\n\t"
        "v_cvt_pk_bf16_f32 v114, v36, v37\n\t"
        "v_cvt_pk_bf16_f32 v115, v38, v39\n\t"
        "s_add_u32 s80, s80, 0x1000\n\t"
        "buffer_store_dwordx4 v[112:115], %[l16], %[rs], s80 offen offset:0 sc1\n\t"
        "v_accvgpr_read_b32 v40, a40\n\t"
        "v_accvgpr_read_b32 v41, a41\n\t"
        "v_accvgpr_read_b32 v42, a42\n\t"
        "v_accvgpr_read_b32 v43, a43\n\t"
        "v_accvgpr_read_b32 v44, a44\n\t"
        "v_accvgpr_read_b32 v45, a45\n\t"
        "v_accvgpr_read_b32 v46, a46\n\t"
        "v_accvgpr_read_b32 v47, a47\n\t"
        "v_mul_f32_e32 v40, v40, v165\n\t"
        "v_mul_f32_e32 v41, v41, v165\n\t"
        "v_mul_f32_e32 v42, v42, v165\n\t"
        "v_mul_f32_e32 v43, v43, v165\n\t"
        "v_mul_f32_e32 v44, v44, v165\n\t"
        "v_mul_f32_e32 v45, v45, v165\n\t"
        "v_mul_f32_e32 v46, v46, v165\n\t"
        "v_mul_f32_e32 v47, v47, v165\n\t"
        "v_cvt_pk_bf16_f32 v116, v40, v41\n\t"
        "v_cvt_pk_bf16_f32 v117, v42, v43\n\t"
        "v_cvt_pk_bf16_f32 v118, v44, v45\n\t"
        "v_cvt_pk_bf16_f32 v119, v46, v47\n\t"
        "buffer_store_dwordx4 v[116:119], %[l16], %[rs], s80 offen offset:1024 sc1\n\t"
        "v_accvgpr_read_b32 v48, a48\n\t"
        "v_accvgpr_read_b32 v49, a49\n\t"
        "v_accvgpr_read_b32 v50, a50\n\t"
        "v_accvgpr_read_b32 v51, a51\n\t"
        "v_accvgpr_read_b32 v52, a52\n\t"
        "v_accvgpr_read_b32 v53, a53\n\t"
        "v_accvgpr_read_b32 v54, a54\n\t"
        "v_accvgpr_read_b32 v55, a55\n\t"
        "v_mul_f32_e32 v48, v48, v165\n\t"
        "v_mul_f32_e32 v49, v49, v165\n\t"
        "v_mul_f32_e32 v50, v50, v165\n\t"
        "v_mul_f32_e32 v51, v51, v165\n\t"
        "v_mul_f32_e32 v52, v52, v165\n\t"
        "v_mul_f32_e32 v53, v53, v165\n\t"
        "v_mul_f32_e32 v54, v54, v165\n\t"
        "v_mul_f32_e32 v55, v55, v165\n\t"
        "v_cvt_pk_bf16_f32 v120, v48, v49\n\t"
        "v_cvt_pk_bf16_f32 v121, v50, v51\n\t"
        "v_cvt_pk_bf16_f32 v122, v52, v53\n\t"
        "v_cvt_pk_bf16_f32 v123, v54, v55\n\t"
        "buffer_store_dwordx4 v[120:123], %[l16], %[rs], s80 offen offset:2048 sc1\n\t"
        "v_accvgpr_read_b32 v56, a56\n\t"
        "v_accvgpr_read_b32 v57, a57\n\t"
        "v_accvgpr_read_b32 v58, a58\n\t"
        "v_accvgpr_read_b32 v59, a59\n\t"
        "v_accvgpr_read_b32 v60, a60\n\t"
        "v_accvgpr_read_b32 v61, a61\n\t"
        "v_accvgpr_read_b32 v62, a62\n\t"
        "v_accvgpr_read_b32 v63, a63\n\t"
        "v_mul_f32_e32 v56, v56, v165\n\t"
        "v_mul_f32_e32 v57, v57, v165\n\t"
        "v_mul_f32_e32 v58, v58, v165\n\t"
        "v_mul_f32_e32 v59, v59, v165\n\t"
        "v_mul_f32_e32 v60, v60, v165\n\t"
        "v_mul_f32_e32 v61, v61, v165\n\t"
        "v_mul_f32_e32 v62, v62, v165\n\t"
        "v_mul_f32_e32 v63, v63, v165\n\t"
        "v_cvt_pk_bf16_f32 v124, v56, v57\n\t"
        "v_cvt_pk_bf16_f32 v125, v58, v59\n\t"
        "v_cvt_pk_bf16_f32 v126, v60, v61\n\t"
        "v_cvt_pk_bf16_f32 v127, v62, v63\n\t"
        "buffer_store_dwordx4 v[124:127], %[l16], %[rs], s80 offen offset:3072 sc1\n\t"
        "v_accvgpr_read_b32 v0, a64\n\t"
        "v_accvgpr_read_b32 v1, a65\n\t"
        "v_accvgpr_read_b32 v2, a66\n\t"
        "v_accvgpr_read_b32 v3, a67\n\t"
        "v_accvgpr_read_b32 v4, a68\n\t"
        "v_accvgpr_read_b32 v5, a69\n\t"
        "v_accvgpr_read_b32 v6, a70\n\t"
        "v_accvgpr_read_b32 v7, a71\n\t"
        "v_mul_f32_e32 v0, v0, v165\n\t"
        "v_mul_f32_e32 v1, v1, v165\n\t"
        "v_mul_f32_e32 v2, v2, v165\n\t"
        "v_mul_f32_e32 v3, v3, v165\n\t"
        "v_mul_f32_e32 v4, v4, v165\n\t"
        "v_mul_f32_e32 v5, v5, v165\n\t"
        "v_mul_f32_e32 v6, v6, v165\n\t"
        "v_mul_f32_e32 v7, v7, v165\n\t"
        "v_cvt_pk_bf16_f32 v128, v0, v1\n\t"
        "v_cvt_pk_bf16_f32 v129, v2, v3\n\t"
        "v_cvt_pk_bf16_f32 v130, v4, v5\n\t"
        "v_cvt_pk_bf16_f32 v131, v6, v7\n\t"
        "s_add_u32 s80, s80, 0x1000\n\t"
        "buffer_store_dwordx4 v[128:131], %[l16], %[rs], s80 offen offset:0 sc1\n\t"
        "v_accvgpr_read_b32 v8, a72\n\t"
        "v_accvgpr_read_b32 v9, a73\n\t"
        "v_accvgpr_read_b32 v10, a74\n\t"
        "v_accvgpr_read_b32 v11, a75\n\t"
        "v_accvgpr_read_b32 v12, a76\n\t"
        "v_accvgpr_read_b32 v13, a77\n\t"
        "v_accvgpr_read_b32 v14, a78\n\t"
        "v_accvgpr_read_b32 v15, a79\n\t"
        "v_mul_f32_e32 v8, v8, v165\n\t"
        "v_mul_f32_e32 v9, v9, v165\n\t"
        "v_mul_f32_e32 v10, v10, v165\n\t"
        "v_mul_f32_e32 v11, v11, v165\n\t"
        "v_mul_f32_e32 v12, v12, v165\n\t"
        "v_mul_f32_e32 v13, v13, v165\n\t"
        "v_mul_f32_e32 v14, v14, v165\n\t"
        "v_mul_f32_e32 v15, v15, v165\n\t"
        "v_cvt_pk_bf16_f32 v132, v8, v9\n\t"
        "v_cvt_pk_bf16_f32 v133, v10, v11\n\t"
        "v_cvt_pk_bf16_f32 v134, v12, v13\n\t"
        "v_cvt_pk_bf16_f32 v135, v14, v15\n\t"
        "buffer_store_dwordx4 v[132:135], %[l16], %[rs], s80 offen offset:1024 sc1\n\t"
        "v_accvgpr_read_b32 v16, a80\n\t"
        "v_accvgpr_read_b32 v17, a81\n\t"
        "v_accvgpr_read_b32 v18, a82\n\t"
        "v_accvgpr_read_b32 v19, a83\n\t"
        "v_accvgpr_read_b32 v20, a84\n\t"
        "v_accvgpr_read_b32 v21, a85\n\t"
        "v_accvgpr_read_b32 v22, a86\n\t"
        "v_accvgpr_read_b32 v23, a87\n\t"
        "v_mul_f32_e32 v16, v16, v165\n\t"
        "v_mul_f32_e32 v17, v17, v165\n\t"
        "v_mul_f32_e32 v18, v18, v165\n\t"
        "v_mul_f32_e32 v19, v19, v165\n\t"
        "v_mul_f32_e32 v20, v20, v165\n\t"
        "v_mul_f32_e32 v21, v21, v165\n\t"
        "v_mul_f32_e32 v22, v22, v165\n\t"
        "v_mul_f32_e32 v23, v23, v165\n\t"
        "v_cvt_pk_bf16_f32 v136, v16, v17\n\t"
        "v_cvt_pk_bf16_f32 v137, v18, v19\n\t"
        "v_cvt_pk_bf16_f32 v138, v20, v21\n\t"
        "v_cvt_pk_bf16_f32 v139, v22, v23\n\t"
        "buffer_store_dwordx4 v[136:139], %[l16], %[rs], s80 offen offset:2048 sc1\n\t"
        "v_accvgpr_read_b32 v24, a88\n\t"
        "v_accvgpr_read_b32 v25, a89\n\t"
        "v_accvgpr_read_b32 v26, a90\n\t"
        "v_accvgpr_read_b32 v27, a91\n\t"
        "v_accvgpr_read_b32 v28, a92\n\t"
        "v_accvgpr_read_b32 v29, a93\n\t"
        "v_accvgpr_read_b32 v30, a94\n\t"
        "v_accvgpr_read_b32 v31, a95\n\t"
        "v_mul_f32_e32 v24, v24, v165\n\t"
        "v_mul_f32_e32 v25, v25, v165\n\t"
        "v_mul_f32_e32 v26, v26, v165\n\t"
        "v_mul_f32_e32 v27, v27, v165\n\t"
        "v_mul_f32_e32 v28, v28, v165\n\t"
        "v_mul_f32_e32 v29, v29, v165\n\t"
        "v_mul_f32_e32 v30, v30, v165\n\t"
        "v_mul_f32_e32 v31, v31, v165\n\t"
        "v_cvt_pk_bf16_f32 v140, v24, v25\n\t"
        "v_cvt_pk_bf16_f32 v141, v26, v27\n\t"
        "v_cvt_pk_bf16_f32 v142, v28, v29\n\t"
        "v_cvt_pk_bf16_f32 v143, v30, v31\n\t"
        "buffer_store_dwordx4 v[140:143], %[l16], %[rs], s80 offen offset:3072 sc1\n\t"
        "v_accvgpr_read_b32 v32, a96\n\t"
        "v_accvgpr_read_b32 v33, a97\n\t"
        "v_accvgpr_read_b32 v34, a98\n\t"
        "v_accvgpr_read_b32 v35, a99\n\t"
        "v_accvgpr_read_b32 v36, a100\n\t"
        "v_accvgpr_read_b32 v37, a101\n\t"
        "v_accvgpr_read_b32 v38, a102\n\t"
        "v_accvgpr_read_b32 v39, a103\n\t"
        "v_mul_f32_e32 v32, v32, v165\n\t"
        "v_mul_f32_e32 v33, v33, v165\n\t"
        "v_mul_f32_e32 v34, v34, v165\n\t"
        "v_mul_f32_e32 v35, v35, v165\n\t"
        "v_mul_f32_e32 v36, v36, v165\n\t"
        "v_mul_f32_e32 v37, v37, v165\n\t"
        "v_mul_f32_e32 v38, v38, v165\n\t"
        "v_mul_f32_e32 v39, v39, v165\n\t"
        "v_cvt_pk_bf16_f32 v144, v32, v33\n\t"
        "v_cvt_pk_bf16_f32 v145, v34, v35\n\t"
        "v_cvt_pk_bf16_f32 v146, v36, v37\n\t"
        "v_cvt_pk_bf16_f32 v147, v38, v39\n\t"
        "s_add_u32 s80, s80, 0x1000\n\t"
        "buffer_store_dwordx4 v[144:147], %[l16], %[rs], s80 offen offset:0 sc1\n\t"
        "v_accvgpr_read_b32 v40, a104\n\t"
        "v_accvgpr_read_b32 v41, a105\n\t"
        "v_accvgpr_read_b32 v42, a106\n\t"
        "v_accvgpr_read_b32 v43, a107\n\t"
        "v_accvgpr_read_b32 v44, a108\n\t"
        "v_accvgpr_read_b32 v45, a109\n\t"
        "v_accvgpr_read_b32 v46, a110\n\t"
        "v_accvgpr_read_b32 v47, a111\n\t"
        "v_mul_f32_e32 v40, v40, v165\n\t"
        "v_mul_f32_e32 v41, v41, v165\n\t"
        "v_mul_f32_e32 v42, v42, v165\n\t"
        "v_mul_f32_e32 v43, v43, v165\n\t"
        "v_mul_f32_e32 v44, v44, v165\n\t"
        "v_mul_f32_e32 v45, v45, v165\n\t"
        "v_mul_f32_e32 v46, v46, v165\n\t"
        "v_mul_f32_e32 v47, v47, v165\n\t"
        "v_cvt_pk_bf16_f32 v148, v40, v41\n\t"
        "v_cvt_pk_bf16_f32 v149, v42, v43\n\t"
        "v_cvt_pk_bf16_f32 v150, v44, v45\n\t"
        "v_cvt_pk_bf16_f32 v151, v46, v47\n\t"
        "buffer_store_dwordx4 v[148:151], %[l16], %[rs], s80 offen offset:1024 sc1\n\t"
        "v_accvgpr_read_b32 v48, a112\n\t"
        "v_accvgpr_read_b32 v49, a113\n\t"
        "v_accvgpr_read_b32 v50, a114\n\t"
        "v_accvgpr_read_b32 v51, a115\n\t"
        "v_accvgpr_read_b32 v52, a116\n\t"
        "v_accvgpr_read_b32 v53, a117\n\t"
        "v_accvgpr_read_b32 v54, a118\n\t"
        "v_accvgpr_read_b32 v55, a119\n\t"
        "v_mul_f32_e32 v48, v48, v165\n\t"
        "v_mul_f32_e32 v49, v49, v165\n\t"
        "v_mul_f32_e32 v50, v50, v165\n\t"
        "v_mul_f32_e32 v51, v51, v165\n\t"
        "v_mul_f32_e32 v52, v52, v165\n\t"
        "v_mul_f32_e32 v53, v53, v165\n\t"
        "v_mul_f32_e32 v54, v54, v165\n\t"
        "v_mul_f32_e32 v55, v55, v165\n\t"
        "v_cvt_pk_bf16_f32 v152, v48, v49\n\t"
        "v_cvt_pk_bf16_f32 v153, v50, v51\n\t"
        "v_cvt_pk_bf16_f32 v154, v52, v53\n\t"
        "v_cvt_pk_bf16_f32 v155, v54, v55\n\t"
        "buffer_store_dwordx4 v[152:155], %[l16], %[rs], s80 offen offset:2048 sc1\n\t"
        "v_accvgpr_read_b32 v56, a120\n\t"
        "v_accvgpr_read_b32 v57, a121\n\t"
        "v_accvgpr_read_b32 v58, a122\n\t"
        "v_accvgpr_read_b32 v59, a123\n\t"
        "v_accvgpr_read_b32 v60, a124\n\t"
        "v_accvgpr_read_b32 v61, a125\n\t"
        "v_accvgpr_read_b32 v62, a126\n\t"
        "v_accvgpr_read_b32 v63, a127\n\t"
        "v_mul_f32_e32 v56, v56, v165\n\t"
        "v_mul_f32_e32 v57, v57, v165\n\t"
        "v_mul_f32_e32 v58, v58, v165\n\t"
        "v_mul_f32_e32 v59, v59, v165\n\t"
        "v_mul_f32_e32 v60, v60, v165\n\t"
        "v_mul_f32_e32 v61, v61, v165\n\t"
        "v_mul_f32_e32 v62, v62, v165\n\t"
        "v_mul_f32_e32 v63, v63, v165\n\t"
        "v_cvt_pk_bf16_f32 v156, v56, v57\n\t"
        "v_cvt_pk_bf16_f32 v157, v58, v59\n\t"
        "v_cvt_pk_bf16_f32 v158, v60, v61\n\t"
        "v_cvt_pk_bf16_f32 v159, v62, v63\n\t"
        "buffer_store_dwordx4 v[156:159], %[l16], %[rs], s80 offen offset:3072 sc1\n\t"
        "s_nop 1\n\t"
        :
        : [rs] "s"(rs), [qo] "s"(qo), [ko] "s"(ko), [oo] "s"(oo), [nt] "s"(nt), [lds0] "s"(lds0), [ldsw] "s"(ldsw),
          [thr] "s"(thr), [l16] "v"(l16), [lim] "v"(lim)
        :
          "v0", "v1", "v2", "v3", "v4", "v5", "v6", "v7", "v8", "v9", "v10", "v11", "v12", "v13", "v14", "v15",
          "v16", "v17", "v18", "v19", "v20", "v21", "v22", "v23", "v24", "v25", "v26", "v27", "v28", "v29", "v30",
          "v31", "v32", "v33", "v34", "v35", "v36", "v37", "v38", "v39", "v40", "v41", "v42", "v43", "v44", "v45",
          "v46", "v47", "v48", "v49", "v50", "v51", "v52", "v53", "v54", "v55", "v56", "v57", "v58", "v59", "v60",
          "v61", "v62", "v63", "v64", "v65", "v66", "v67", "v68", "v69", "v70", "v71", "v72", "v73", "v74", "v75",
          "v76", "v77", "v78", "v79", "v80", "v81", "v82", "v83", "v84", "v85", "v86", "v87", "v88", "v89", "v90",
          "v91", "v92", "v93", "v94", "v95", "v96", "v97", "v98", "v99", "v100", "v101", "v102", "v103", "v104",
          "v105", "v106", "v107", "v108", "v109", "v110", "v111", "v112", "v113", "v114", "v115", "v116", "v117",
          "v118", "v119", "v120", "v121", "v122", "v123", "v124", "v125", "v126", "v127", "v128", "v129", "v130",
          "v131", "v132", "v133", "v134", "v135", "v136", "v137", "v138", "v139", "v140", "v141", "v142", "v143",
          "v144", "v145", "v146", "v147", "v148", "v149", "v150", "v151", "v152", "v153", "v154", "v155", "v156",
          "v157", "v158", "v159", "v160", "v161", "v162", "v163", "v164", "v165", "v166", "v167", "v168", "v169",
          "v170", "v171", "v172", "v173", "v174", "v175", "v176", "v177", "v178", "v179", "a0", "a1", "a2", "a3",
          "a4", "a5", "a6", "a7", "a8", "a9", "a10", "a11", "a12", "a13", "a14", "a15", "a16", "a17", "a18", "a19",
          "a20", "a21", "a22", "a23", "a24", "a25", "a26", "a27", "a28", "a29", "a30", "a31", "a32", "a33", "a34",
          "a35", "a36", "a37", "a38", "a39", "a40", "a41", "a42", "a43", "a44", "a45", "a46", "a47", "a48", "a49",
          "a50", "a51", "a52", "a53", "a54", "a55", "a56", "a57", "a58", "a59", "a60", "a61", "a62", "a63", "a64",
          "a65", "a66", "a67", "a68", "a69", "a70", "a71", "a72", "a73", "a74", "a75", "a76", "a77", "a78", "a79",
          "a80", "a81", "a82", "a83", "a84", "a85", "a86", "a87", "a88", "a89", "a90", "a91", "a92", "a93", "a94",
          "a95", "a96", "a97", "a98", "a99", "a100", "a101", "a102", "a103", "a104", "a105", "a106", "a107", "a108",
          "a109", "a110", "a111", "a112", "a113", "a114", "a115", "a116", "a117", "a118", "a119", "a120", "a121",
          "a122", "a123", "a124", "a125", "a126", "a127", "a128", "a129", "a130", "a131", "a132", "a133", "a134",
          "a135", "a136", "a137", "a138", "a139", "a140", "a141", "a142", "a143", "a144", "a145", "a146", "a147",
          "a148", "a149", "a150", "a151", "a152", "a153", "a154", "a155", "a156", "a157", "a158", "a159", "a160",
          "a161", "a162", "a163", "a164", "a165", "a166", "a167", "a168", "a169", "a170", "a171", "a172", "a173",
          "a174", "a175", "a176", "a177", "a178", "a179", "a180", "a181", "a182", "a183", "a184", "a185", "a186",
          "a187", "a188", "a189", "a190", "a191", "s64", "s65", "s66", "s67", "s68", "s69", "s70", "s71", "s72",
          "s73", "s74", "s75", "s76", "s77", "s78", "s79", "s80", "s81", "vcc", "scc", "memory" );
}
#define ATT_THR 16.0f
constexpr int ATTN_LDS = 4 * 32768;
__device__ __forceinline__ void attnB_body(u16* qkvo, u16* smem, int qb, int h, int ws, const unsigned* cnt) {
    const int lane = tid_of(0);
    if (ws == 0) wait_kv(cnt, qb >> 1, (qb >> 1) + 1);
    __syncthreads();
    __amdgpu_buffer_rsrc_t rs = __builtin_amdgcn_make_buffer_rsrc(qkvo, 0, 4 * S_ * HD * 2, 0x00020000);
    const unsigned lds0 = (unsigned)(uintptr_t)(LAS u16*)smem;
    const unsigned q32 = qb * 4 + ws;
    attn_asm(rs, (unsigned)(h << 20) + q32 * 16384u, 0x1000000u + (unsigned)(h << 20) + ws * 8192u,
             0x3000000u + q32 * 262144u + h * 16384u, 2 * qb + 2, lds0, lds0 + ws * 8192u, __float_as_uint(ATT_THR),
             (unsigned)lane * 16u, ws * 32 + (lane & 31) - 4 * (lane >> 5));
}

typedef unsigned long long u64;
__device__ __forceinline__ u64 ld_gran(const u64* p) {
    return __hip_atomic_load(p, __ATOMIC_RELAXED, __HIP_MEMORY_SCOPE_AGENT);
}
__device__ __forceinline__ void st_gran4(u16* p, unsigned lo, unsigned hi) {
    hi = min(hi, 0xfffffffeu);
    __hip_atomic_store(reinterpret_cast<u64*>(p), ((u64)hi << 32) | (u64)lo, __ATOMIC_RELAXED,
                       __HIP_MEMORY_SCOPE_AGENT);
}
__device__ __forceinline__ void st_gran8(__amdgpu_buffer_rsrc_t rs, unsigned voff, unsigned soff, bool act, unsigned lo,
                                         unsigned hi) {
    hi = min(hi, 0xfffffffeu);
    unsigned lo2 = (unsigned)__builtin_amdgcn_mov_dpp((int)lo, 0xF5, 0xf, 0xf, true);
    unsigned hi2 = (unsigned)__builtin_amdgcn_mov_dpp((int)hi, 0xF5, 0xf, 0xf, true);
    if (act) __builtin_amdgcn_raw_buffer_store_b128(u32x4{lo, hi, lo2, hi2}, rs, voff, soff, 16);
}
constexpr int NA_BLK = 16, NB_BLK = 32, NC_BLK = 28, NO_BLK = 14, ND_BLK = 28;
constexpr int NREC = NA_BLK + NB_BLK + NC_BLK + NO_BLK + ND_BLK;
__device__ __forceinline__ int lid() {
    const int x = blockIdx.x & 7, j = blockIdx.x >> 3;
    if (x == 0) return j < 16 ? j : (j < 30 ? 60 + j : 14 + j);
    if (x == 1) return j < 28 ? 16 + j : 18 + j;
    const int idx = x - 2 + 6 * j;
    return idx < 26 ? 50 + idx : 64 + idx;
}
constexpr int MROW = 456;
constexpr int G1_CP = 16 * 8 * 40;
constexpr int G2_CP = 8 * 448;
constexpr int NCP1 = 1, NCP2 = 3;
constexpr int G1_SLOT = NCP1 * G1_CP, G2_SLOT = NCP2 * G2_CP;
constexpr int G3_SLOT = 16 * 32 * 40;
constexpr int XIMG = 28 * 512;
constexpr unsigned SPIN_CAP = 400000u;
#define SLP_A 33
#define SLP_C1 3
#define SLP_C0 4
#define SLP_C2 14
#define D_NEAR 64
#define LD_LATE 1
#define SLP_Q 6

#define WTS(slot)
#define KT0
#define KT1(k)
#define TS(slot)
#define TSC(slot)
struct RecParams {
    const u16* x;
    const u16* mem0;
    const u16* AmatT;
    const u16* CmatT;
    u16* G1;
    u16* G2;
    u16* G3;
    u16* out;
    u16* Dg;
    u16* Ug;
    unsigned* dflag;
};

__device__ __forceinline__ float red16_max(float x) {
    asm volatile(
        "s_nop 1\n\tv_max_f32_dpp %0, %0, %0 quad_perm:[1,0,3,2] row_mask:0xf bank_mask:0xf\n\t"
        "s_nop 1\n\tv_max_f32_dpp %0, %0, %0 quad_perm:[2,3,0,1] row_mask:0xf bank_mask:0xf\n\t"
        "s_nop 1\n\tv_max_f32_dpp %0, %0, %0 row_half_mirror row_mask:0xf bank_mask:0xf\n\t"
        "s_nop 1\n\tv_max_f32_dpp %0, %0, %0 row_mirror row_mask:0xf bank_mask:0xf\n\t"
        "s_nop 1"
        : "+v"(x));
    return x;
}
__device__ __forceinline__ float red16_sum(float x) {
    asm volatile(
        "s_nop 1\n\tv_add_f32_dpp %0, %0, %0 quad_perm:[1,0,3,2] row_mask:0xf bank_mask:0xf\n\t"
        "s_nop 1\n\tv_add_f32_dpp %0, %0, %0 quad_perm:[2,3,0,1] row_mask:0xf bank_mask:0xf\n\t"
        "s_nop 1\n\tv_add_f32_dpp %0, %0, %0 row_half_mirror row_mask:0xf bank_mask:0xf\n\t"
        "s_nop 1\n\tv_add_f32_dpp %0, %0, %0 row_mirror row_mask:0xf bank_mask:0xf\n\t"
        "s_nop 1"
        : "+v"(x));
    return x;
}
__device__ __forceinline__ u32x4 ld16_sc1(__amdgpu_buffer_rsrc_t rs, unsigned voff, unsigned soff) {
    return __builtin_amdgcn_raw_buffer_load_b128(rs, voff, soff, 16);
}
__device__ __forceinline__ unsigned umax3(unsigned a, unsigned b, unsigned c) { return max(max(a, b), c); }

__device__ __forceinline__ void mfma_a0(f32x4& d, const bf16x8& w, const u32x4& m) {
    asm volatile("v_mfma_f32_16x16x32_bf16 %0, %1, %2, 0" : "=&v"(d) : "a"(w), "v"(m));
}
__device__ __forceinline__ void mfma_a(f32x4& d, const bf16x8& w, const u32x4& m) {
    asm volatile("v_mfma_f32_16x16x32_bf16 %0, %1, %2, %0" : "+v"(d) : "a"(w), "v"(m));
}
__device__ __forceinline__ void mfma_v0(f32x4& d, const bf16x8& w, const u32x4& m) {
    asm volatile("v_mfma_f32_16x16x32_bf16 %0, %1, %2, 0" : "=&v"(d) : "v"(w), "v"(m));
}
__device__ __forceinline__ void mfma_v(f32x4& d, const bf16x8& w, const u32x4& m) {
    asm volatile("v_mfma_f32_16x16x32_bf16 %0, %1, %2, %0" : "+v"(d) : "v"(w), "v"(m));
}
__device__ __forceinline__ void lds_rd16(bf16x8& d, unsigned addr, int off) {
    asm volatile("ds_read_b128 %0, %1 offset:%2" : "=v"(d) : "v"(addr), "n"(off));
}
__device__ __forceinline__ void lds_rd16u(u32x4& d, unsigned addr) {
    asm volatile("ds_read_b128 %0, %1" : "=v"(d) : "v"(addr));
}
template <int N>
__device__ __forceinline__ void wait_lgkm() {
    asm volatile("s_waitcnt lgkmcnt(%0)" ::"n"(N) : "memory");
}
constexpr int sch_n(int q) { return q <= 8 ? 7 + 2 * (q + 1) : (q <= 11 ? 25 + (q - 8) : 28); }
constexpr int sch_m(int i) { return i < 5 ? 2 + i : 7 + 2 * (i - 5); }
constexpr int sch_w(int i) { return i < 2 ? i : (i - 2 <= 8 ? 8 + 2 * (i - 2) : 25 + (i - 11)); }

__device__ __forceinline__ void dma16(const void* gbase, unsigned voff, unsigned lds) {
    unsigned keep;
    asm volatile("s_mov_b32 %0, m0\n\ts_mov_b32 m0, %3\n\ts_nop 0\n\tglobal_load_lds_dwordx4 %1, %2\n\ts_mov_b32 m0, %0"
                 : "=&s"(keep)
                 : "v"(voff), "s"(gbase), "s"(lds)
                 : "memory");
}
__device__ __forceinline__ void dma_x(const u16* x, int s, unsigned xvoff, unsigned lds, int wid) {
    const char* gb = reinterpret_cast<const char*>(x) + (long)s * (SEG * DM * 2) + wid * 7168;
#pragma unroll
    for (int j = 0; j < 7; ++j) dma16(gb + 1024 * j, xvoff, lds + (unsigned)(wid * 7 + j) * 1024u);
}

__device__ __forceinline__ void lds_rd16s(u32x4& d, unsigned addr, unsigned soff) {
    unsigned t;
    asm volatile("v_add_u32 %1, %3, %2\n\tds_read_b128 %0, %1" : "=v"(d), "=&v"(t) : "v"(addr), "s"(soff));
}
__device__ __forceinline__ void tpass(const bf16x8 (&W)[6][14], unsigned wl_addr, unsigned fa, const unsigned (&so)[14],
                                      u32x4 (&mf)[14], bf16x8 (&w6)[3], f32x4 (&acc)[7]) {
#pragma unroll
    for (int q = 0; q < 5; ++q) lds_rd16s(mf[q], fa, so[q]);
#pragma unroll
    for (int q = 0; q < 14; ++q) {
        if (q + 5 < 14) lds_rd16s(mf[q + 5], fa, so[q + 5]);
        if (q + 2 < 14) lds_rd16(w6[(q + 2) % 3], wl_addr, (q + 2) * 1024);
        switch (q) {
#define WM(Q) case Q: wait_lgkm<sch_n(Q) - sch_m(Q) - 1>(); break;
            WM(0) WM(1) WM(2) WM(3) WM(4) WM(5) WM(6) WM(7) WM(8) WM(9) WM(10) WM(11) WM(12) WM(13)
#undef WM
        }
#pragma unroll
        for (int t = 0; t < 4; ++t) {
            if (q == 0) mfma_a0(acc[t], W[t][q], mf[q]);
            else mfma_a(acc[t], W[t][q], mf[q]);
        }
#pragma unroll
        for (int t = 4; t < 6; ++t) {
            if (t == 4 && q < 6) {
                if (q == 0) mfma_a0(acc[t], W[t][q], mf[q]);
                else mfma_a(acc[t], W[t][q], mf[q]);
            } else {
                if (q == 0) mfma_v0(acc[t], W[t][q], mf[q]);
                else mfma_v(acc[t], W[t][q], mf[q]);
            }
        }
        switch (q) {
#define WW(Q) case Q: wait_lgkm<sch_n(Q) - sch_w(Q) - 1>(); break;
            WW(0) WW(1) WW(2) WW(3) WW(4) WW(5) WW(6) WW(7) WW(8) WW(9) WW(10) WW(11) WW(12) WW(13)
#undef WW
        }
        if (q == 0) mfma_v0(acc[6], w6[0], mf[q]);
        else mfma_v(acc[6], w6[q % 3], mf[q]);
    }
    asm volatile("s_nop 7\n\ts_nop 1"
                 : "+v"(acc[0]), "+v"(acc[1]), "+v"(acc[2]), "+v"(acc[3]), "+v"(acc[4]), "+v"(acc[5]), "+v"(acc[6]));
}
__device__ __forceinline__ void tpack(const f32x4 (&acc)[7], bool q3ok, u32x4 (&bv)[4]) {
#pragma unroll
    for (int pr = 0; pr < 3; ++pr)
        bv[pr] = u32x4{pack2(acc[2 * pr][0], acc[2 * pr][1]), pack2(acc[2 * pr + 1][0], acc[2 * pr + 1][1]),
                       pack2(acc[2 * pr][2], acc[2 * pr][3]), pack2(acc[2 * pr + 1][2], acc[2 * pr + 1][3])};
    unsigned t0 = pack2(acc[6][0], acc[6][1]), t1 = pack2(acc[6][2], acc[6][3]);
    bv[3] = q3ok ? u32x4{t0, 0u, t1, 0u} : u32x4{0u, t0, 0u, t1};
}
#define BF8(x) __builtin_bit_cast(bf16x8, x)
__device__ __forceinline__ void schain1(f32x4& d0, const u32x4 (&a0)[4], const u32x4 (&b)[4]) {
    asm volatile(
        "s_nop 1\n\tv_mfma_f32_16x16x32_bf16 %0, %1, %5, 0\n\tv_mfma_f32_16x16x32_bf16 %0, %2, %6, %0\n\t"
        "v_mfma_f32_16x16x32_bf16 %0, %3, %7, %0\n\tv_mfma_f32_16x16x32_bf16 %0, %4, %8, %0\n\ts_nop 7"
        : "=&v"(d0)
        : "v"(a0[0]), "v"(a0[1]), "v"(a0[2]), "v"(a0[3]), "v"(b[0]), "v"(b[1]), "v"(b[2]), "v"(b[3]));
}
__device__ __forceinline__ void schain2(f32x4& d0, f32x4& d1, const u32x4 (&a0)[4], const u32x4 (&a1)[4],
                                        const u32x4 (&b0)[4], const u32x4 (&b1)[4]) {
    asm volatile(
        "s_nop 1\n\tv_mfma_f32_16x16x32_bf16 %0, %2, %10, 0\n\tv_mfma_f32_16x16x32_bf16 %1, %6, %14, 0\n\t"
        "v_mfma_f32_16x16x32_bf16 %0, %3, %11, %0\n\tv_mfma_f32_16x16x32_bf16 %1, %7, %15, %1\n\t"
        "v_mfma_f32_16x16x32_bf16 %0, %4, %12, %0\n\tv_mfma_f32_16x16x32_bf16 %1, %8, %16, %1\n\t"
        "v_mfma_f32_16x16x32_bf16 %0, %5, %13, %0\n\tv_mfma_f32_16x16x32_bf16 %1, %9, %17, %1\n\ts_nop 7"
        : "=&v"(d0), "=&v"(d1)
        : "v"(a0[0]), "v"(a0[1]), "v"(a0[2]), "v"(a0[3]), "v"(a1[0]), "v"(a1[1]), "v"(a1[2]), "v"(a1[3]), "v"(b0[0]),
          "v"(b0[1]), "v"(b0[2]), "v"(b0[3]), "v"(b1[0]), "v"(b1[1]), "v"(b1[2]), "v"(b1[3]));
}

constexpr int AL_W = 0, AL_X = 4 * 14 * 512, AL_S = AL_X + 2 * XIMG;
constexpr int SROW = 44;
constexpr int RECA_LDS = (AL_S + 4 * 32 * SROW * 2) * 2;

__device__ __forceinline__ void rec_B_body(const RecParams& p, u16* smem, int h, int par, int wid) {
    float* Sc = reinterpret_cast<float*>(smem + AL_S);
    const int tid = tid_of(wid), lane = tid & 63;
    const int c16 = lane & 15, g = lane >> 4;
    const unsigned xl_base = (unsigned)(uintptr_t)(LAS u16*)(smem + AL_X);
    const unsigned xvoff = (unsigned)lane * 16u;
    dma_x(p.x, par, xvoff, xl_base, wid);
    dma_x(p.x, par + 2, xvoff, xl_base + XIMG * 2, wid);
    int sck[4];
#pragma unroll
    for (int pr = 0; pr < 4; ++pr) sck[pr] = tile_col(wid, 2 * pr, 4 * g) >> 3;
    bf16x8 W[6][14];
    u16* WL = smem + AL_W + (wid * 14 * 64 + lane) * 8;
    {
        const u16* ap = p.AmatT + (long)(h * 4 + wid) * (98 * 512) + lane * 8;
#pragma unroll
        for (int t = 0; t < 7; ++t)
#pragma unroll
            for (int q = 0; q < 14; ++q) {
                bf16x8 w = *reinterpret_cast<const bf16x8*>(ap + (t * 14 + q) * 512);
                if (t < 6)
                    W[t][q] = w;
                else
                    *reinterpret_cast<bf16x8*>(WL + q * 512) = w;
            }
    }
    const bool q3ok = (wid & 1) == 0;
    const bool sact = c16 < 10;
    const bool sst = sact && !(c16 & 1);
    const unsigned wl_addr = (unsigned)(uintptr_t)(LAS u16*)WL;
    __amdgpu_buffer_rsrc_t rs2 = __builtin_amdgcn_make_buffer_rsrc(p.G2, 0, 64 * G2_SLOT * 2, 0x00020000);
    bool dead = false;
    asm volatile("s_waitcnt vmcnt(0)" ::: "memory");
    __syncthreads();
    {
        float* swr = Sc + (wid * 32 + c16) * SROW + 8 * g;
        float* swr_mm = Sc + (wid * 32 + c16) * SROW + 32 + 4 * (g & 1);
        const float* sread = Sc + (wid * 8 + g) * SROW + (sact ? 4 * c16 : 0);
        const int key0 = (c16 < 8) ? 16 * (c16 & 1) + 4 * (c16 >> 1) : -4;
        __amdgpu_buffer_rsrc_t rs3 = __builtin_amdgcn_make_buffer_rsrc(p.G3, 0, 64 * G3_SLOT * 2, 0x00020000);
        const unsigned g3off = ((h * 32 + wid * 8 + g) * 40 + 4 * c16) * 2;
        unsigned so[14];
#pragma unroll
        for (int q = 0; q < 14; ++q) so[q] = 1024 * q;
        unsigned mvoff[4];
#pragma unroll
        for (int q = 0; q < 4; ++q) mvoff[q] = ((sck[q] >> 1) * 128 + (c16 & 7) * 16 + (sck[q] & 1) * 8) * 2;
        for (int s = par; s < NSEG; s += 2) {
            const int xb = (s >> 1) & 1;
            const unsigned xa = xl_base + xb * (XIMG * 2) + lane * 16;
            u32x4 bv0[4], bv1[4];
            {
                u32x4 mf[14];
                bf16x8 w6[3];
                f32x4 acc[7];
                wait_lgkm<0>();
                lds_rd16(w6[0], wl_addr, 0);
                lds_rd16(w6[1], wl_addr, 1024);
                tpass(W, wl_addr, xa, so, mf, w6, acc);
                tpack(acc, q3ok, bv0);
                const u16* xrd = smem + AL_X + xb * XIMG + c16 * 8;
                u32x4 m4[4];
#pragma unroll
                for (int pr = 0; pr < 4; ++pr) m4[pr] = *reinterpret_cast<const u32x4*>(xrd + sck[pr] * 128);
                f32x4 s00;
                schain1(s00, m4, bv0);
                __syncthreads();
                *reinterpret_cast<f32x4*>(swr) = s00;
            }
            {
                u32x4 mf[14];
                bf16x8 w6[3];
                f32x4 acc[7];
                wait_lgkm<0>();
                lds_rd16(w6[0], wl_addr, 0);
                lds_rd16(w6[1], wl_addr, 1024);
                tpass(W, wl_addr, xa + 14 * 1024, so, mf, w6, acc);
                tpack(acc, q3ok, bv1);
                const u16* xrd = smem + AL_X + xb * XIMG + c16 * 8;
                u32x4 m4[4], x4[4];
#pragma unroll
                for (int pr = 0; pr < 4; ++pr) {
                    x4[pr] = *reinterpret_cast<const u32x4*>(xrd + sck[pr] * 128);
                    m4[pr] = *reinterpret_cast<const u32x4*>(xrd + 14 * 512 + sck[pr] * 128);
                }
                f32x4 s01, s11;
                schain2(s01, s11, x4, m4, bv1, bv1);
                *reinterpret_cast<f32x4*>(swr + 16 * SROW) = s01;
                *reinterpret_cast<f32x4*>(swr + 16 * SROW + 4) = s11;
            }
            u32x4 mm[4];
            for (unsigned spin = 0;; ++spin) {
                asm volatile("" ::: "memory");
                unsigned mx = 0;
#pragma unroll
                for (int q = 0; q < 4; ++q) mm[q] = ld16_sc1(rs2, mvoff[q], (unsigned)((s * G2_SLOT + 2 * G2_CP) * 2));
#pragma unroll
                for (int q = 0; q < 4; ++q) mx = umax3(mx, mm[q][1], mm[q][3]);
                if (__all(mx != 0xffffffffu) || dead) break;
                if (spin > SPIN_CAP) dead = true;
                __builtin_amdgcn_s_sleep(8);
            }
            TS(1);
            {
                f32x4 sm0, sm1;
                schain2(sm0, sm1, mm, mm, bv0, bv1);
                if (g < 2) {
                    *reinterpret_cast<f32x4*>(swr_mm) = sm0;
                    *reinterpret_cast<f32x4*>(swr_mm + 16 * SROW) = sm1;
                }
            }
            asm volatile("s_waitcnt vmcnt(0)" ::: "memory");
            __syncthreads();
            TS(2);
            if (s + 4 < NSEG) dma_x(p.x, s + 4, xvoff, xl_base + xb * (XIMG * 2), wid);
#pragma unroll
            for (int i = 0; i < 2; ++i) {
                const float* sr = sread + 4 * i * SROW;
                f32x4 x0 = *reinterpret_cast<const f32x4*>(sr);
                f32x4 x1 = *reinterpret_cast<const f32x4*>(sr + 32 * SROW);
                f32x4 x2 = *reinterpret_cast<const f32x4*>(sr + 64 * SROW);
                f32x4 x3 = *reinterpret_cast<const f32x4*>(sr + 96 * SROW);
                f32x4 a = (x0 + x1) + (x2 + x3);
                const int row = wid * 8 + 4 * i + g;
#pragma unroll
                for (int r = 0; r < 4; ++r)
                    if (!sact || key0 + r > row) a[r] = -INFINITY;
                float mx = __builtin_fmaxf(__builtin_fmaxf(a[0], a[1]), __builtin_fmaxf(a[2], a[3]));
                mx = red16_max(mx);
                float e0 = __builtin_amdgcn_exp2f(a[0] - mx);
                float e1 = __builtin_amdgcn_exp2f(a[1] - mx);
                float e2 = __builtin_amdgcn_exp2f(a[2] - mx);
                float e3 = __builtin_amdgcn_exp2f(a[3] - mx);
                float sum = red16_sum((e0 + e1) + (e2 + e3));
                float inv = __builtin_amdgcn_rcpf(sum);
                st_gran8(rs3, g3off + i * (4 * 40 * 2), (unsigned)(s * G3_SLOT * 2), sst, pack2(e0 * inv, e1 * inv),
                         pack2(e2 * inv, e3 * inv));
            }
            TS(3);
        }
    }
}

#define RECA_DBG_ARGS int
#define RECA_DBG_IN
#define RECA_DBG_PASS 0
__device__ __forceinline__ void reca_asm(__amdgpu_buffer_rsrc_t rs, unsigned wo, unsigned g2o, unsigned g1o, unsigned xo,
                                         unsigned xl, unsigned al, unsigned at, unsigned ac, unsigned amk1, unsigned amk2,
                                         unsigned axb0, unsigned axb1, unsigned asw, unsigned aswm, unsigned asr,
                                         unsigned ag1, unsigned idx, u32x2 sel, u32x2 sel6, RECA_DBG_ARGS) {
    asm volatile(
        "s_mov_b32 s74, m0\n\t"
        "s_mov_b32 s75, %[wo]\n\t"
        "buffer_load_dwordx4 a[0:3], %[al], %[rs], s75 offen offset:0\n\t"
        "buffer_load_dwordx4 a[4:7], %[al], %[rs], s75 offen offset:1024\n\t"
        "buffer_load_dwordx4 a[8:11], %[al], %[rs], s75 offen offset:2048\n\t"
        "buffer_load_dwordx4 a[12:15], %[al], %[rs], s75 offen offset:3072\n\t"
        "s_add_u32 s75, s75, 0x1000\n\t"
        "buffer_load_dwordx4 a[16:19], %[al], %[rs], s75 offen offset:0\n\t"
        "buffer_load_dwordx4 a[20:23], %[al], %[rs], s75 offen offset:1024\n\t"
        "buffer_load_dwordx4 a[24:27], %[al], %[rs], s75 offen offset:2048\n\t"
        "buffer_load_dwordx4 a[28:31], %[al], %[rs], s75 offen offset:3072\n\t"
        "s_add_u32 s75, s75, 0x1000\n\t"
        "buffer_load_dwordx4 a[32:35], %[al], %[rs], s75 offen offset:0\n\t"
        "buffer_load_dwordx4 a[36:39], %[al], %[rs], s75 offen offset:1024\n\t"
        "buffer_load_dwordx4 a[40:43], %[al], %[rs], s75 offen offset:2048\n\t"
        "buffer_load_dwordx4 a[44:47], %[al], %[rs], s75 offen offset:3072\n\t"
        "s_add_u32 s75, s75, 0x1000\n\t"
        "buffer_load_dwordx4 a[48:51], %[al], %[rs], s75 offen offset:0\n\t"
        "buffer_load_dwordx4 a[52:55], %[al], %[rs], s75 offen offset:1024\n\t"
        "buffer_load_dwordx4 a[56:59], %[al], %[rs], s75 offen offset:2048\n\t"
        "buffer_load_dwordx4 a[60:63], %[al], %[rs], s75 offen offset:3072\n\t"
        "s_add_u32 s75, s75, 0x1000\n\t"
        "buffer_load_dwordx4 a[64:67], %[al], %[rs], s75 offen offset:0\n\t"
        "buffer_load_dwordx4 a[68:71], %[al], %[rs], s75 offen offset:1024\n\t"
        "buffer_load_dwordx4 a[72:75], %[al], %[rs], s75 offen offset:2048\n\t"
        "buffer_load_dwordx4 a[76:79], %[al], %[rs], s75 offen offset:3072\n\t"
        "s_add_u32 s75, s75, 0x1000\n\t"
        "buffer_load_dwordx4 a[80:83], %[al], %[rs], s75 offen offset:0\n\t"
        "buffer_load_dwordx4 a[84:87], %[al], %[rs], s75 offen offset:1024\n\t"
        "buffer_load_dwordx4 a[88:91], %[al], %[rs], s75 offen offset:2048\n\t"
        "buffer_load_dwordx4 a[92:95], %[al], %[rs], s75 offen offset:3072\n\t"
        "s_add_u32 s75, s75, 0x1000\n\t"
        "buffer_load_dwordx4 a[96:99], %[al], %[rs], s75 offen offset:0\n\t"
        "buffer_load_dwordx4 a[100:103], %[al], %[rs], s75 offen offset:1024\n\t"
        "buffer_load_dwordx4 a[104:107], %[al], %[rs], s75 offen offset:2048\n\t"
        "buffer_load_dwordx4 a[108:111], %[al], %[rs], s75 offen offset:3072\n\t"
        "s_add_u32 s75, s75, 0x1000\n\t"
        "buffer_load_dwordx4 a[112:115], %[al], %[rs], s75 offen offset:0\n\t"
        "buffer_load_dwordx4 a[116:119], %[al], %[rs], s75 offen offset:1024\n\t"
        "buffer_load_dwordx4 a[120:123], %[al], %[rs], s75 offen offset:2048\n\t"
        "buffer_load_dwordx4 a[124:127], %[al], %[rs], s75 offen offset:3072\n\t"
        "s_add_u32 s75, s75, 0x1000\n\t"
        "buffer_load_dwordx4 a[128:131], %[al], %[rs], s75 offen offset:0\n\t"
        "buffer_load_dwordx4 a[132:135], %[al], %[rs], s75 offen offset:1024\n\t"
        "buffer_load_dwordx4 a[136:139], %[al], %[rs], s75 offen offset:2048\n\t"
        "buffer_load_dwordx4 a[140:143], %[al], %[rs], s75 offen offset:3072\n\t"
        "s_add_u32 s75, s75, 0x1000\n\t"
        "buffer_load_dwordx4 a[144:147], %[al], %[rs], s75 offen offset:0\n\t"
        "buffer_load_dwordx4 a[148:151], %[al], %[rs], s75 offen offset:1024\n\t"
        "buffer_load_dwordx4 a[152:155], %[al], %[rs], s75 offen offset:2048\n\t"
        "buffer_load_dwordx4 a[156:159], %[al], %[rs], s75 offen offset:3072\n\t"
        "s_add_u32 s75, s75, 0x1000\n\t"
        "s_waitcnt vmcnt(32)\n\t"
        "buffer_load_dwordx4 a[160:163], %[al], %[rs], s75 offen offset:0\n\t"
        "buffer_load_dwordx4 a[164:167], %[al], %[rs], s75 offen offset:1024\n\t"
        "buffer_load_dwordx4 a[168:171], %[al], %[rs], s75 offen offset:2048\n\t"
        "buffer_load_dwordx4 a[172:175], %[al], %[rs], s75 offen offset:3072\n\t"
        "s_add_u32 s75, s75, 0x1000\n\t"
        "buffer_load_dwordx4 a[176:179], %[al], %[rs], s75 offen offset:0\n\t"
        "buffer_load_dwordx4 a[180:183], %[al], %[rs], s75 offen offset:1024\n\t"
        "buffer_load_dwordx4 a[184:187], %[al], %[rs], s75 offen offset:2048\n\t"
        "buffer_load_dwordx4 a[188:191], %[al], %[rs], s75 offen offset:3072\n\t"
        "s_add_u32 s75, s75, 0x1000\n\t"
        "s_waitcnt vmcnt(32)\n\t"
        "buffer_load_dwordx4 a[192:195], %[al], %[rs], s75 offen offset:0\n\t"
        "buffer_load_dwordx4 a[196:199], %[al], %[rs], s75 offen offset:1024\n\t"
        "buffer_load_dwordx4 a[200:203], %[al], %[rs], s75 offen offset:2048\n\t"
        "buffer_load_dwordx4 a[204:207], %[al], %[rs], s75 offen offset:3072\n\t"
        "s_add_u32 s75, s75, 0x1000\n\t"
        "buffer_load_dwordx4 a[208:211], %[al], %[rs], s75 offen offset:0\n\t"
        "buffer_load_dwordx4 a[212:215], %[al], %[rs], s75 offen offset:1024\n\t"
        "buffer_load_dwordx4 a[216:219], %[al], %[rs], s75 offen offset:2048\n\t"
        "buffer_load_dwordx4 a[220:223], %[al], %[rs], s75 offen offset:3072\n\t"
        "s_add_u32 s75, s75, 0x1000\n\t"
        "s_waitcnt vmcnt(32)\n\t"
        "buffer_load_dwordx4 a[224:227], %[al], %[rs], s75 offen offset:0\n\t"
        "buffer_load_dwordx4 a[228:231], %[al], %[rs], s75 offen offset:1024\n\t"
        "buffer_load_dwordx4 a[232:235], %[al], %[rs], s75 offen offset:2048\n\t"
        "buffer_load_dwordx4 a[236:239], %[al], %[rs], s75 offen offset:3072\n\t"
        "s_add_u32 s75, s75, 0x1000\n\t"
        "buffer_load_dwordx4 a[240:243], %[al], %[rs], s75 offen offset:0\n\t"
        "buffer_load_dwordx4 a[244:247], %[al], %[rs], s75 offen offset:1024\n\t"
        "buffer_load_dwordx4 a[248:251], %[al], %[rs], s75 offen offset:2048\n\t"
        "buffer_load_dwordx4 a[252:255], %[al], %[rs], s75 offen offset:3072\n\t"
        "s_add_u32 s75, s75, 0x1000\n\t"
        "s_waitcnt vmcnt(32)\n\t"
        "buffer_load_dwordx4 v[0:3], %[al], %[rs], s75 offen offset:0\n\t"
        "buffer_load_dwordx4 v[4:7], %[al], %[rs], s75 offen offset:1024\n\t"
        "buffer_load_dwordx4 v[8:11], %[al], %[rs], s75 offen offset:2048\n\t"
        "buffer_load_dwordx4 v[12:15], %[al], %[rs], s75 offen offset:3072\n\t"
        "s_add_u32 s75, s75, 0x1000\n\t"
        "buffer_load_dwordx4 v[16:19], %[al], %[rs], s75 offen offset:0\n\t"
        "buffer_load_dwordx4 v[20:23], %[al], %[rs], s75 offen offset:1024\n\t"
        "buffer_load_dwordx4 v[24:27], %[al], %[rs], s75 offen offset:2048\n\t"
        "buffer_load_dwordx4 v[28:31], %[al], %[rs], s75 offen offset:3072\n\t"
        "s_add_u32 s75, s75, 0x1000\n\t"
        "s_waitcnt vmcnt(32)\n\t"
        "buffer_load_dwordx4 v[32:35], %[al], %[rs], s75 offen offset:0\n\t"
        "buffer_load_dwordx4 v[36:39], %[al], %[rs], s75 offen offset:1024\n\t"
        "buffer_load_dwordx4 v[40:43], %[al], %[rs], s75 offen offset:2048\n\t"
        "buffer_load_dwordx4 v[44:47], %[al], %[rs], s75 offen offset:3072\n\t"
        "s_add_u32 s75, s75, 0x1000\n\t"
        "buffer_load_dwordx4 v[48:51], %[al], %[rs], s75 offen offset:0\n\t"
        "buffer_load_dwordx4 v[52:55], %[al], %[rs], s75 offen offset:1024\n\t"
        "buffer_load_dwordx4 v[56:59], %[al], %[rs], s75 offen offset:2048\n\t"
        "buffer_load_dwordx4 v[60:63], %[al], %[rs], s75 offen offset:3072\n\t"
        "s_add_u32 s75, s75, 0x1000\n\t"
        "s_waitcnt vmcnt(32)\n\t"
        "buffer_load_dwordx4 v[64:67], %[al], %[rs], s75 offen offset:0\n\t"
        "buffer_load_dwordx4 v[68:71], %[al], %[rs], s75 offen offset:1024\n\t"
        "buffer_load_dwordx4 v[72:75], %[al], %[rs], s75 offen offset:2048\n\t"
        "buffer_load_dwordx4 v[76:79], %[al], %[rs], s75 offen offset:3072\n\t"
        "s_add_u32 s75, s75, 0x1000\n\t"
        "buffer_load_dwordx4 v[80:83], %[al], %[rs], s75 offen offset:0\n\t"
        "buffer_load_dwordx4 v[84:87], %[al], %[rs], s75 offen offset:1024\n\t"
        "buffer_load_dwordx4 v[88:91], %[al], %[rs], s75 offen offset:2048\n\t"
        "buffer_load_dwordx4 v[92:95], %[al], %[rs], s75 offen offset:3072\n\t"
        "s_add_u32 s75, s75, 0x1000\n\t"
        "s_waitcnt vmcnt(32)\n\t"
        "buffer_load_dwordx4 v[96:99], %[al], %[rs], s75 offen offset:0\n\t"
        "buffer_load_dwordx4 v[100:103], %[al], %[rs], s75 offen offset:1024\n\t"
        "buffer_load_dwordx4 v[104:107], %[al], %[rs], s75 offen offset:2048\n\t"
        "buffer_load_dwordx4 v[108:111], %[al], %[rs], s75 offen offset:3072\n\t"
        "s_add_u32 s75, s75, 0x1000\n\t"
        "buffer_load_dwordx4 v[112:115], %[al], %[rs], s75 offen offset:0\n\t"
        "buffer_load_dwordx4 v[116:119], %[al], %[rs], s75 offen offset:1024\n\t"
        "buffer_load_dwordx4 v[120:123], %[al], %[rs], s75 offen offset:2048\n\t"
        "buffer_load_dwordx4 v[124:127], %[al], %[rs], s75 offen offset:3072\n\t"
        "s_add_u32 s75, s75, 0x1000\n\t"
        "s_waitcnt vmcnt(32)\n\t"
        "buffer_load_dwordx4 v[128:131], %[al], %[rs], s75 offen offset:0\n\t"
        "buffer_load_dwordx4 v[132:135], %[al], %[rs], s75 offen offset:1024\n\t"
        "s_mov_b32 s64, %[g2o]\n\t"
        "s_mov_b32 s65, %[g1o]\n\t"
        "s_add_u32 s67, %[xo], 57344\n\t"
        "s_add_u32 s69, %[xo], 1806336\n\t"
        "s_mov_b32 s70, %[xl]\n\t"
        "s_mov_b32 s71, 28672\n\t"
        "s_mov_b32 s72, 63\n\t"
        "s_mov_b32 s73, %[cap]\n\t"
        "v_mov_b32_e32 v164, 0\n\t"
        "v_mov_b32_e32 v165, 0\n\t"
        "v_mov_b32_e32 v166, 0\n\t"
        "v_mov_b32_e32 v167, 0\n\t"
        "v_mov_b32_e32 v168, 0\n\t"
        "v_mov_b32_e32 v169, 0\n\t"
        "v_mov_b32_e32 v170, 0\n\t"
        "v_mov_b32_e32 v171, 0\n\t"
        "v_mov_b32_e32 v172, 0\n\t"
        "v_mov_b32_e32 v173, 0\n\t"
        "v_mov_b32_e32 v174, 0\n\t"
        "v_mov_b32_e32 v175, 0\n\t"
        "v_mov_b32_e32 v176, 0\n\t"
        "v_mov_b32_e32 v177, 0\n\t"
        "v_mov_b32_e32 v178, 0\n\t"
        "v_mov_b32_e32 v179, 0\n\t"
        "v_mov_b32_e32 v180, 0\n\t"
        "v_mov_b32_e32 v181, 0\n\t"
        "v_mov_b32_e32 v182, 0\n\t"
        "v_mov_b32_e32 v183, 0\n\t"
        "v_mov_b32_e32 v184, 0\n\t"
        "v_mov_b32_e32 v185, 0\n\t"
        "v_mov_b32_e32 v186, 0\n\t"
        "v_mov_b32_e32 v187, 0\n\t"
        "v_mov_b32_e32 v188, 0\n\t"
        "v_mov_b32_e32 v189, 0\n\t"
        "v_mov_b32_e32 v190, 0\n\t"
        "v_mov_b32_e32 v191, 0\n\t"
        "s_waitcnt vmcnt(0) lgkmcnt(0)\n\t"
        "s_barrier\n\t"
        "ds_read_b128 v[192:195], %[axb0] offset:0\n\t"
        "ds_read_b128 v[196:199], %[axb0] offset:1024\n\t"
        "ds_read_b128 v[200:203], %[axb0] offset:14336\n\t"
        "ds_read_b128 v[204:207], %[axb0] offset:15360\n\t"
        ".p2align 6\n\t"
        "LPOLL_%=:\n\t"
        "buffer_load_dwordx4 v[208:211], %[at], %[rs], s64 offen sc1\n\t"
        "buffer_load_dwordx4 v[212:215], %[ac], %[rs], s64 offen sc1\n\t"
        "s_sleep %[slq]\n\t"
        "buffer_load_dwordx4 v[216:219], %[at], %[rs], s64 offen sc1\n\t"
        "buffer_load_dwordx4 v[224:227], %[ac], %[rs], s64 offen sc1\n\t"
        "LP0_%=:\n\t"
        "s_waitcnt vmcnt(3)\n\t"
        "ds_write_b128 %[at], v[208:211]\n\t"
        "v_max_u32_e32 v220, v209, v211\n\t"
        "s_waitcnt vmcnt(2)\n\t"
        "ds_write_b128 %[ac], v[212:215]\n\t"
        "v_max3_u32 v220, v220, v213, v215\n\t"
        "v_cmp_eq_u32_e32 vcc, -1, v220\n\t"
        "s_cbranch_vccz LGOT_%=\n\t"
        "buffer_load_dwordx4 v[208:211], %[at], %[rs], s64 offen sc1\n\t"
        "buffer_load_dwordx4 v[212:215], %[ac], %[rs], s64 offen sc1\n\t"
        "s_waitcnt vmcnt(3)\n\t"
        "ds_write_b128 %[at], v[216:219]\n\t"
        "v_max_u32_e32 v220, v217, v219\n\t"
        "s_waitcnt vmcnt(2)\n\t"
        "ds_write_b128 %[ac], v[224:227]\n\t"
        "v_max3_u32 v220, v220, v225, v227\n\t"
        "v_cmp_eq_u32_e32 vcc, -1, v220\n\t"
        "s_cbranch_vccnz LFAIL_%=\n\t"
        "LGOT_%=:\n\t"
        "s_waitcnt lgkmcnt(0)\n\t"
        "s_barrier\n\t"
        "ds_read_b128 v[136:139], %[al] offset:0\n\t"
        "ds_read_b128 v[140:143], %[al] offset:1024\n\t"
        "ds_read_b128 v[144:147], %[al] offset:2048\n\t"
        "ds_read_b128 v[148:151], %[al] offset:3072\n\t"
        "ds_read_b128 v[152:155], %[al] offset:4096\n\t"
        "ds_read_b128 v[156:159], %[al] offset:5120\n\t"
        "ds_read_b128 v[160:163], %[al] offset:6144\n\t"
        "s_waitcnt lgkmcnt(6)\n\t"
        "v_smfmac_f32_16x16x64_bf16 v[164:167], v[136:139], a[0:7], %[idx]\n\t"
        "v_smfmac_f32_16x16x64_bf16 v[168:171], v[136:139], a[56:63], %[idx]\n\t"
        "v_smfmac_f32_16x16x64_bf16 v[172:175], v[136:139], a[112:119], %[idx]\n\t"
        "v_smfmac_f32_16x16x64_bf16 v[176:179], v[136:139], a[168:175], %[idx]\n\t"
        "s_waitcnt lgkmcnt(5)\n\t"
        "v_smfmac_f32_16x16x64_bf16 v[164:167], v[140:143], a[8:15], %[idx]\n\t"
        "v_smfmac_f32_16x16x64_bf16 v[168:171], v[140:143], a[64:71], %[idx]\n\t"
        "v_smfmac_f32_16x16x64_bf16 v[172:175], v[140:143], a[120:127], %[idx]\n\t"
        "v_smfmac_f32_16x16x64_bf16 v[176:179], v[140:143], a[176:183], %[idx]\n\t"
        "s_waitcnt lgkmcnt(4)\n\t"
        "v_smfmac_f32_16x16x64_bf16 v[164:167], v[144:147], a[16:23], %[idx]\n\t"
        "v_smfmac_f32_16x16x64_bf16 v[168:171], v[144:147], a[72:79], %[idx]\n\t"
        "v_smfmac_f32_16x16x64_bf16 v[172:175], v[144:147], a[128:135], %[idx]\n\t"
        "v_smfmac_f32_16x16x64_bf16 v[176:179], v[144:147], a[184:191], %[idx]\n\t"
        "s_waitcnt lgkmcnt(3)\n\t"
        "v_smfmac_f32_16x16x64_bf16 v[164:167], v[148:151], a[24:31], %[idx]\n\t"
        "v_smfmac_f32_16x16x64_bf16 v[168:171], v[148:151], a[80:87], %[idx]\n\t"
        "v_smfmac_f32_16x16x64_bf16 v[172:175], v[148:151], a[136:143], %[idx]\n\t"
        "v_smfmac_f32_16x16x64_bf16 v[176:179], v[148:151], a[192:199], %[idx]\n\t"
        "s_waitcnt lgkmcnt(2)\n\t"
        "v_smfmac_f32_16x16x64_bf16 v[164:167], v[152:155], a[32:39], %[idx]\n\t"
        "v_smfmac_f32_16x16x64_bf16 v[168:171], v[152:155], a[88:95], %[idx]\n\t"
        "v_smfmac_f32_16x16x64_bf16 v[172:175], v[152:155], a[144:151], %[idx]\n\t"
        "v_smfmac_f32_16x16x64_bf16 v[176:179], v[152:155], a[200:207], %[idx]\n\t"
        "s_waitcnt lgkmcnt(1)\n\t"
        "v_smfmac_f32_16x16x64_bf16 v[164:167], v[156:159], a[40:47], %[idx]\n\t"
        "v_smfmac_f32_16x16x64_bf16 v[168:171], v[156:159], a[96:103], %[idx]\n\t"
        "v_smfmac_f32_16x16x64_bf16 v[172:175], v[156:159], a[152:159], %[idx]\n\t"
        "v_smfmac_f32_16x16x64_bf16 v[176:179], v[156:159], a[208:215], %[idx]\n\t"
        "s_waitcnt lgkmcnt(0)\n\t"
        "v_smfmac_f32_16x16x64_bf16 v[164:167], v[160:163], a[48:55], %[idx]\n\t"
        "v_smfmac_f32_16x16x64_bf16 v[168:171], v[160:163], a[104:111], %[idx]\n\t"
        "v_smfmac_f32_16x16x64_bf16 v[172:175], v[160:163], a[160:167], %[idx]\n\t"
        "v_smfmac_f32_16x16x64_bf16 v[176:179], v[160:163], a[216:223], %[idx]\n\t"
        "v_permlane32_swap_b32_e32 v164, v168\n\t"
        "v_permlane32_swap_b32_e32 v165, v169\n\t"
        "v_smfmac_f32_16x16x64_bf16 v[180:183], v[136:139], a[224:231], %[idx]\n\t"
        "v_permlane32_swap_b32_e32 v166, v170\n\t"
        "v_permlane32_swap_b32_e32 v167, v171\n\t"
        "v_smfmac_f32_16x16x64_bf16 v[184:187], v[136:139], v[24:31], %[idx]\n\t"
        "v_pk_add_f32 v[164:165], v[164:165], v[168:169]\n\t"
        "v_pk_add_f32 v[166:167], v[166:167], v[170:171]\n\t"
        "v_smfmac_f32_16x16x64_bf16 v[180:183], v[140:143], a[232:239], %[idx]\n\t"
        "v_cvt_pk_bf16_f32 v168, v164, v165\n\t"
        "v_cvt_pk_bf16_f32 v169, v166, v167\n\t"
        "v_smfmac_f32_16x16x64_bf16 v[184:187], v[140:143], v[32:39], %[idx]\n\t"
        "v_permlane32_swap_b32_e32 v172, v176\n\t"
        "v_permlane32_swap_b32_e32 v173, v177\n\t"
        "v_mfma_f32_16x16x16_bf16 v[164:167], v[168:169], %[sel], 0\n\t"
        "v_permlane32_swap_b32_e32 v174, v178\n\t"
        "v_permlane32_swap_b32_e32 v175, v179\n\t"
        "v_smfmac_f32_16x16x64_bf16 v[180:183], v[144:147], a[240:247], %[idx]\n\t"
        "v_pk_add_f32 v[172:173], v[172:173], v[176:177]\n\t"
        "v_pk_add_f32 v[174:175], v[174:175], v[178:179]\n\t"
        "v_smfmac_f32_16x16x64_bf16 v[184:187], v[144:147], v[40:47], %[idx]\n\t"
        "v_cvt_pk_bf16_f32 v176, v172, v173\n\t"
        "v_cvt_pk_bf16_f32 v177, v174, v175\n\t"
        "v_smfmac_f32_16x16x64_bf16 v[180:183], v[148:151], a[248:255], %[idx]\n\t"
        "v_cvt_pk_bf16_f32 v220, v164, v165\n\t"
        "v_cvt_pk_bf16_f32 v221, v166, v167\n\t"
        "v_mfma_f32_16x16x16_bf16 v[172:175], v[176:177], %[sel], 0\n\t"
        "v_smfmac_f32_16x16x64_bf16 v[184:187], v[148:151], v[48:55], %[idx]\n\t"
        "ds_read_b128 v[164:167], %[axb1] offset:0\n\t"
        "ds_read_b128 v[168:171], %[axb1] offset:512\n\t"
        "v_smfmac_f32_16x16x64_bf16 v[180:183], v[152:155], v[0:7], %[idx]\n\t"
        "v_cvt_pk_bf16_f32 v222, v172, v173\n\t"
        "v_cvt_pk_bf16_f32 v223, v174, v175\n\t"
        "v_smfmac_f32_16x16x64_bf16 v[184:187], v[152:155], v[56:63], %[idx]\n\t"
        "ds_read_b128 v[172:175], %[axb1] offset:14336\n\t"
        "ds_read_b128 v[176:179], %[axb1] offset:14848\n\t"
        "v_smfmac_f32_16x16x64_bf16 v[180:183], v[156:159], v[8:15], %[idx]\n\t"
        "s_waitcnt vmcnt(0)\n\t"
        "ds_read_b128 v[228:231], %[at]\n\t"
        "v_smfmac_f32_16x16x64_bf16 v[184:187], v[156:159], v[64:71], %[idx]\n\t"
        "ds_read_b64 v[232:233], %[amk1]\n\t"
        "ds_read_b64 v[234:235], %[amk2]\n\t"
        "v_smfmac_f32_16x16x64_bf16 v[180:183], v[160:163], v[16:23], %[idx]\n\t"
        "v_mov_b32_e32 v208, 0\n\t"
        "v_mov_b32_e32 v209, 0\n\t"
        "v_smfmac_f32_16x16x64_bf16 v[184:187], v[160:163], v[72:79], %[idx]\n\t"
        "v_mov_b32_e32 v210, 0\n\t"
        "v_mov_b32_e32 v211, 0\n\t"
        "v_smfmac_f32_16x16x64_bf16 v[188:191], v[136:139], v[80:87], %[idx]\n\t"
        "v_mov_b32_e32 v212, 0\n\t"
        "v_mov_b32_e32 v213, 0\n\t"
        "v_smfmac_f32_16x16x64_bf16 v[188:191], v[140:143], v[88:95], %[idx]\n\t"
        "v_permlane32_swap_b32_e32 v180, v184\n\t"
        "v_permlane32_swap_b32_e32 v181, v185\n\t"
        "v_smfmac_f32_16x16x64_bf16 v[188:191], v[144:147], v[96:103], %[idx]\n\t"
        "v_permlane32_swap_b32_e32 v182, v186\n\t"
        "v_permlane32_swap_b32_e32 v183, v187\n\t"
        "v_smfmac_f32_16x16x64_bf16 v[188:191], v[148:151], v[104:111], %[idx]\n\t"
        "v_pk_add_f32 v[180:181], v[180:181], v[184:185]\n\t"
        "v_pk_add_f32 v[182:183], v[182:183], v[186:187]\n\t"
        "v_smfmac_f32_16x16x64_bf16 v[188:191], v[152:155], v[112:119], %[idx]\n\t"
        "v_cvt_pk_bf16_f32 v184, v180, v181\n\t"
        "v_cvt_pk_bf16_f32 v185, v182, v183\n\t"
        "v_smfmac_f32_16x16x64_bf16 v[188:191], v[156:159], v[120:127], %[idx]\n\t"
        "v_mov_b32_e32 v187, 0\n\t"
        "v_mov_b32_e32 v214, 0\n\t"
        "v_smfmac_f32_16x16x64_bf16 v[188:191], v[160:163], v[128:135], %[idx]\n\t"
        "v_mov_b32_e32 v215, 0\n\t"
        "v_mfma_f32_16x16x16_bf16 v[180:183], v[184:185], %[sel], 0\n\t"
        "s_waitcnt lgkmcnt(1)\n\t"
        "v_smfmac_f32_16x16x64_bf16 v[208:211], v[220:223], v[192:199], %[idx]\n\t"
        "v_permlane32_swap_b32_e32 v188, v190\n\t"
        "v_permlane32_swap_b32_e32 v189, v191\n\t"
        "v_pk_add_f32 v[188:189], v[188:189], v[190:191]\n\t"
        "v_cvt_pk_bf16_f32 v186, v188, v189\n\t"
        "v_cvt_pk_bf16_f32 v224, v180, v181\n\t"
        "v_cvt_pk_bf16_f32 v225, v182, v183\n\t"
        "v_mfma_f32_16x16x16_bf16 v[188:191], v[186:187], %[sel6], 0\n\t"
        "v_smfmac_f32_16x16x64_bf16 v[212:215], v[220:223], v[200:207], %[idx]\n\t"
        "v_mfma_f32_16x16x32_bf16 v[216:219], v[228:231], v[220:223], 0\n\t"
        "v_cvt_pk_bf16_f32 v226, v188, v189\n\t"
        "v_cvt_pk_bf16_f32 v227, v190, v191\n\t"
        "s_nop 1\n\t"
        "v_mfma_f32_16x16x32_bf16 v[216:219], v[232:235], v[224:227], v[216:219]\n\t"
        "v_smfmac_f32_16x16x64_bf16 v[208:211], v[224:227], v[164:171], %[idx]\n\t"
        "v_smfmac_f32_16x16x64_bf16 v[212:215], v[224:227], v[172:179], %[idx]\n\t"
        "v_permlane32_swap_b32_e32 v216, v218\n\t"
        "v_permlane32_swap_b32_e32 v217, v219\n\t"
        "s_nop 0\n\t"
        "v_add_f32_dpp v216, v218, v216 row_ror:8 row_mask:0xf bank_mask:0xf\n\t"
        "v_add_f32_dpp v217, v219, v217 row_ror:8 row_mask:0xf bank_mask:0xf\n\t"
        "v_permlane32_swap_b32_e32 v208, v210\n\t"
        "v_permlane32_swap_b32_e32 v209, v211\n\t"
        "ds_write_b64 %[aswm], v[216:217]\n\t"
        "v_pk_add_f32 v[208:209], v[208:209], v[210:211]\n\t"
        "v_permlane32_swap_b32_e32 v212, v214\n\t"
        "v_permlane32_swap_b32_e32 v213, v215\n\t"
        "ds_write2_b32 %[asw], v208, v209 offset0:0 offset1:44\n\t"
        "v_pk_add_f32 v[212:213], v[212:213], v[214:215]\n\t"
        "ds_write2_b32 %[asw], v212, v213 offset0:2 offset1:46\n\t"
        "s_waitcnt lgkmcnt(0)\n\t"
        "s_barrier\n\t"
        "ds_read_b128 v[136:139], %[asr] offset:0\n\t"
        "ds_read_b128 v[140:143], %[asr] offset:1408\n\t"
        "ds_read_b128 v[144:147], %[asr] offset:2816\n\t"
        "ds_read_b128 v[148:151], %[asr] offset:4224\n\t"
        "v_mov_b32_e32 v155, -2\n\t"
        "s_waitcnt lgkmcnt(2)\n\t"
        "v_pk_add_f32 v[136:137], v[136:137], v[140:141]\n\t"
        "v_pk_add_f32 v[138:139], v[138:139], v[142:143]\n\t"
        "s_waitcnt lgkmcnt(0)\n\t"
        "v_pk_add_f32 v[144:145], v[144:145], v[148:149]\n\t"
        "v_pk_add_f32 v[146:147], v[146:147], v[150:151]\n\t"
        "v_pk_add_f32 v[136:137], v[136:137], v[144:145]\n\t"
        "v_pk_add_f32 v[138:139], v[138:139], v[146:147]\n\t"
        "v_max3_f32 v152, v136, v137, v138\n\t"
        "v_max_f32_e32 v152, v152, v139\n\t"
        "s_nop 1\n\t"
        "v_max_f32_dpp v152, v152, v152 quad_perm:[1,0,3,2] row_mask:0xf bank_mask:0xf\n\t"
        "s_nop 1\n\t"
        "v_max_f32_dpp v152, v152, v152 quad_perm:[2,3,0,1] row_mask:0xf bank_mask:0xf\n\t"
        "s_nop 1\n\t"
        "v_max_f32_dpp v152, v152, v152 row_half_mirror row_mask:0xf bank_mask:0xf\n\t"
        "s_nop 1\n\t"
        "v_max_f32_dpp v152, v152, v152 row_mirror row_mask:0xf bank_mask:0xf\n\t"
        "v_pk_add_f32 v[136:137], v[136:137], v[152:153] op_sel_hi:[1,0] neg_lo:[0,1] neg_hi:[0,1]\n\t"
        "v_pk_add_f32 v[138:139], v[138:139], v[152:153] op_sel_hi:[1,0] neg_lo:[0,1] neg_hi:[0,1]\n\t"
        "v_exp_f32_e32 v136, v136\n\t"
        "v_exp_f32_e32 v137, v137\n\t"
        "v_exp_f32_e32 v138, v138\n\t"
        "v_exp_f32_e32 v139, v139\n\t"
        "v_add_f32_e32 v154, v136, v137\n\t"
        "v_add_f32_e32 v152, v138, v139\n\t"
        "v_add_f32_e32 v154, v154, v152\n\t"
        "s_nop 1\n\t"
        "v_add_f32_dpp v154, v154, v154 quad_perm:[1,0,3,2] row_mask:0xf bank_mask:0xf\n\t"
        "s_nop 1\n\t"
        "v_add_f32_dpp v154, v154, v154 quad_perm:[2,3,0,1] row_mask:0xf bank_mask:0xf\n\t"
        "s_nop 1\n\t"
        "v_add_f32_dpp v154, v154, v154 row_half_mirror row_mask:0xf bank_mask:0xf\n\t"
        "s_nop 1\n\t"
        "v_add_f32_dpp v154, v154, v154 row_mirror row_mask:0xf bank_mask:0xf\n\t"
        "v_rcp_f32_e32 v152, v154\n\t"
        "s_nop 0\n\t"
        "v_pk_mul_f32 v[136:137], v[136:137], v[152:153] op_sel_hi:[1,0]\n\t"
        "v_pk_mul_f32 v[138:139], v[138:139], v[152:153] op_sel_hi:[1,0]\n\t"
        "v_cvt_pk_bf16_f32 v156, v136, v137\n\t"
        "v_cvt_pk_bf16_f32 v154, v138, v139\n\t"
        "v_min_u32_e32 v157, v154, v155\n\t"
        "v_mov_b32_dpp v158, v156 quad_perm:[1,1,3,3] row_mask:0xf bank_mask:0xf\n\t"
        "v_min_u32_dpp v159, v154, v155 quad_perm:[1,1,3,3] row_mask:0xf bank_mask:0xf\n\t"
        "buffer_store_dwordx4 v[156:159], %[ag1], %[rs], s65 offen sc1\n\t"
        "v_add_u32_e32 %[axb0], s71, %[axb0]\n\t"
        "v_add_u32_e32 %[axb1], s71, %[axb1]\n\t"
        "ds_read_b128 v[192:195], %[axb0] offset:0\n\t"
        "ds_read_b128 v[196:199], %[axb0] offset:1024\n\t"
        "ds_read_b128 v[200:203], %[axb0] offset:14336\n\t"
        "ds_read_b128 v[204:207], %[axb0] offset:15360\n\t"
        "s_mov_b32 m0, s70\n\t"
        "s_add_u32 s68, s67, 0x1000\n\t"
        "buffer_load_dwordx4 %[al], %[rs], s67 offen offset:0 lds\n\t"
        "buffer_load_dwordx4 %[al], %[rs], s67 offen offset:1024 lds\n\t"
        "buffer_load_dwordx4 %[al], %[rs], s67 offen offset:2048 lds\n\t"
        "buffer_load_dwordx4 %[al], %[rs], s67 offen offset:3072 lds\n\t"
        "s_add_u32 m0, s70, 0x1000\n\t"
        "s_add_u32 s70, s70, s71\n\t"
        "buffer_load_dwordx4 %[al], %[rs], s68 offen offset:0 lds\n\t"
        "buffer_load_dwordx4 %[al], %[rs], s68 offen offset:1024 lds\n\t"
        "buffer_load_dwordx4 %[al], %[rs], s68 offen offset:2048 lds\n\t"
        "s_sub_u32 s71, 0, s71\n\t"
        "s_add_u32 s67, s67, 28672\n\t"
        "s_min_u32 s67, s67, s69\n\t"
        "s_add_u32 s64, s64, %[g2s]\n\t"
        "s_add_u32 s65, s65, %[g1s]\n\t"
        "v_mov_b32_e32 v164, 0\n\t"
        "v_mov_b32_e32 v165, 0\n\t"
        "v_mov_b32_e32 v166, 0\n\t"
        "v_mov_b32_e32 v167, 0\n\t"
        "v_mov_b32_e32 v168, 0\n\t"
        "v_mov_b32_e32 v169, 0\n\t"
        "v_mov_b32_e32 v170, 0\n\t"
        "v_mov_b32_e32 v171, 0\n\t"
        "v_mov_b32_e32 v172, 0\n\t"
        "v_mov_b32_e32 v173, 0\n\t"
        "v_mov_b32_e32 v174, 0\n\t"
        "v_mov_b32_e32 v175, 0\n\t"
        "v_mov_b32_e32 v176, 0\n\t"
        "v_mov_b32_e32 v177, 0\n\t"
        "v_mov_b32_e32 v178, 0\n\t"
        "v_mov_b32_e32 v179, 0\n\t"
        "v_mov_b32_e32 v180, 0\n\t"
        "v_mov_b32_e32 v181, 0\n\t"
        "v_mov_b32_e32 v182, 0\n\t"
        "v_mov_b32_e32 v183, 0\n\t"
        "v_mov_b32_e32 v184, 0\n\t"
        "v_mov_b32_e32 v185, 0\n\t"
        "v_mov_b32_e32 v186, 0\n\t"
        "v_mov_b32_e32 v187, 0\n\t"
        "v_mov_b32_e32 v188, 0\n\t"
        "v_mov_b32_e32 v189, 0\n\t"
        "v_mov_b32_e32 v190, 0\n\t"
        "v_mov_b32_e32 v191, 0\n\t"
        "s_sleep %[slp]\n\t"
        "s_sub_u32 s72, s72, 1\n\t"
        "s_cmp_lg_u32 s72, 0\n\t"
        "s_cbranch_scc1 LPOLL_%=\n\t"
        "s_branch LEND_%=\n\t"
        "LFAIL_%=:\n\t"
        "s_cmp_eq_u32 s73, 0\n\t"
        "s_cbranch_scc1 LGOT_%=\n\t"
        "s_sub_u32 s73, s73, 1\n\t"
        "buffer_load_dwordx4 v[216:219], %[at], %[rs], s64 offen sc1\n\t"
        "buffer_load_dwordx4 v[224:227], %[ac], %[rs], s64 offen sc1\n\t"
        "s_branch LP0_%=\n\t"
        "LEND_%=:\n\t"
        "s_waitcnt vmcnt(0) lgkmcnt(0)\n\t"
        "s_mov_b32 m0, s74\n\t"
        : [axb0] "+v"(axb0), [axb1] "+v"(axb1)
        : [rs] "s"(rs), [wo] "s"(wo), [g2o] "s"(g2o), [g1o] "s"(g1o), [xo] "s"(xo), [xl] "s"(xl), [al] "v"(al),
          [at] "v"(at), [ac] "v"(ac), [amk1] "v"(amk1), [amk2] "v"(amk2), [asw] "v"(asw), [aswm] "v"(aswm),
          [asr] "v"(asr), [ag1] "v"(ag1), [idx] "v"(idx), [sel] "v"(sel), [sel6] "v"(sel6), [g1cp] "n"(G1_CP * 2),
          [g2s] "n"(G2_SLOT * 2), [g1s] "n"(G1_SLOT * 2), [cap] "n"(SPIN_CAP), [slp] "n"(SLP_A), [slq] "n"(SLP_Q) RECA_DBG_IN
        :
          "v0", "v1", "v2", "v3", "v4", "v5", "v6", "v7", "v8", "v9", "v10", "v11", "v12", "v13", "v14", "v15",
          "v16", "v17", "v18", "v19", "v20", "v21", "v22", "v23", "v24", "v25", "v26", "v27", "v28", "v29", "v30",
          "v31", "v32", "v33", "v34", "v35", "v36", "v37", "v38", "v39", "v40", "v41", "v42", "v43", "v44", "v45",
          "v46", "v47", "v48", "v49", "v50", "v51", "v52", "v53", "v54", "v55", "v56", "v57", "v58", "v59", "v60",
          "v61", "v62", "v63", "v64", "v65", "v66", "v67", "v68", "v69", "v70", "v71", "v72", "v73", "v74", "v75",
          "v76", "v77", "v78", "v79", "v80", "v81", "v82", "v83", "v84", "v85", "v86", "v87", "v88", "v89", "v90",
          "v91", "v92", "v93", "v94", "v95", "v96", "v97", "v98", "v99", "v100", "v101", "v102", "v103", "v104",
          "v105", "v106", "v107", "v108", "v109", "v110", "v111", "v112", "v113", "v114", "v115", "v116", "v117",
          "v118", "v119", "v120", "v121", "v122", "v123", "v124", "v125", "v126", "v127", "v128", "v129", "v130",
          "v131", "v132", "v133", "v134", "v135", "v136", "v137", "v138", "v139", "v140", "v141", "v142", "v143",
          "v144", "v145", "v146", "v147", "v148", "v149", "v150", "v151", "v152", "v153", "v154", "v155", "v156",
          "v157", "v158", "v159", "v160", "v161", "v162", "v163", "v164", "v165", "v166", "v167", "v168", "v169",
          "v170", "v171", "v172", "v173", "v174", "v175", "v176", "v177", "v178", "v179", "v180", "v181", "v182",
          "v183", "v184", "v185", "v186", "v187", "v188", "v189", "v190", "v191", "v192", "v193", "v194", "v195",
          "v196", "v197", "v198", "v199", "v200", "v201", "v202", "v203", "v204", "v205", "v206", "v207", "v208",
          "v209", "v210", "v211", "v212", "v213", "v214", "v215", "v216", "v217", "v218", "v219", "v220", "v221",
          "v222", "v223", "v224", "v225", "v226", "v227", "v228", "v229", "v230", "v231", "v232", "v233", "v234",
          "v235", "a0", "a1", "a2", "a3", "a4", "a5", "a6", "a7", "a8", "a9", "a10", "a11", "a12", "a13", "a14",
          "a15", "a16", "a17", "a18", "a19", "a20", "a21", "a22", "a23", "a24", "a25", "a26", "a27", "a28", "a29",
          "a30", "a31", "a32", "a33", "a34", "a35", "a36", "a37", "a38", "a39", "a40", "a41", "a42", "a43", "a44",
          "a45", "a46", "a47", "a48", "a49", "a50", "a51", "a52", "a53", "a54", "a55", "a56", "a57", "a58", "a59",
          "a60", "a61", "a62", "a63", "a64", "a65", "a66", "a67", "a68", "a69", "a70", "a71", "a72", "a73", "a74",
          "a75", "a76", "a77", "a78", "a79", "a80", "a81", "a82", "a83", "a84", "a85", "a86", "a87", "a88", "a89",
          "a90", "a91", "a92", "a93", "a94", "a95", "a96", "a97", "a98", "a99", "a100", "a101", "a102", "a103",
          "a104", "a105", "a106", "a107", "a108", "a109", "a110", "a111", "a112", "a113", "a114", "a115", "a116",
          "a117", "a118", "a119", "a120", "a121", "a122", "a123", "a124", "a125", "a126", "a127", "a128", "a129",
          "a130", "a131", "a132", "a133", "a134", "a135", "a136", "a137", "a138", "a139", "a140", "a141", "a142",
          "a143", "a144", "a145", "a146", "a147", "a148", "a149", "a150", "a151", "a152", "a153", "a154", "a155",
          "a156", "a157", "a158", "a159", "a160", "a161", "a162", "a163", "a164", "a165", "a166", "a167", "a168",
          "a169", "a170", "a171", "a172", "a173", "a174", "a175", "a176", "a177", "a178", "a179", "a180", "a181",
          "a182", "a183", "a184", "a185", "a186", "a187", "a188", "a189", "a190", "a191", "a192", "a193", "a194",
          "a195", "a196", "a197", "a198", "a199", "a200", "a201", "a202", "a203", "a204", "a205", "a206", "a207",
          "a208", "a209", "a210", "a211", "a212", "a213", "a214", "a215", "a216", "a217", "a218", "a219", "a220",
          "a221", "a222", "a223", "a224", "a225", "a226", "a227", "a228", "a229", "a230", "a231", "a232", "a233",
          "a234", "a235", "a236", "a237", "a238", "a239", "a240", "a241", "a242", "a243", "a244", "a245", "a246",
          "a247", "a248", "a249", "a250", "a251", "a252", "a253", "a254", "a255", "s64", "s65", "s66", "s67", "s68",
          "s69", "s70", "s71", "s72", "s73", "s74", "s75", "s76", "s77", "s78", "s79", "s80", "s81", "s82", "s83",
          "vcc", "scc", "memory" );
}
constexpr int A2_IMG = 4 * XIMG, A2_SC = A2_IMG + 7168, A2_DUMP = A2_SC + 4 * 8 * 176;
__device__ __forceinline__ void rec_A_body(const RecParams& p, u16* smem, int h, int wid) {
    const int tid = tid_of(wid), lane = tid & 63;
    const int c16 = lane & 15, g = lane >> 4;
    const unsigned l0 = (unsigned)(uintptr_t)(LAS u16*)smem, img = l0 + A2_IMG, sc = l0 + A2_SC;
    dma_x(p.x, 0, (unsigned)lane * 16u, l0, wid);
    dma_x(p.x, 1, (unsigned)lane * 16u, l0 + XIMG * 2, wid);
    if (tid < 128) reinterpret_cast<float*>(smem + A2_SC / 2)[(tid >> 2) * 44 + 40 + (tid & 3)] = -INFINITY;
    const char* base = reinterpret_cast<const char*>(p.AmatT);
    const unsigned og1 = (unsigned)(reinterpret_cast<const char*>(p.G1) - base);
    const unsigned og2 = (unsigned)(reinterpret_cast<const char*>(p.G2) - base);
    const unsigned ox = (unsigned)(reinterpret_cast<const char*>(p.x) - base);
    __amdgpu_buffer_rsrc_t rs =
        __builtin_amdgcn_make_buffer_rsrc(const_cast<char*>(base) - img, 0, og2 + 64 * G2_SLOT * 2 + img, 0x00020000);
    const int ch1 = (tid < 192) ? (256 + tid) : tid;
    const int q8 = c16 & 7, hp = c16 >> 3, srow = 4 * (wid & 1) + g, r2 = 4 * (g & 1) + 2 * (g >> 1);
    const int e4 = q8 - 4 * (g & 1), e2 = q8 - r2;
    const unsigned one4 = (hp == (g >> 1) && e4 >= 0 && e4 < 4) ? (0x3f80u << (16 * (e4 & 1))) : 0u;
    const unsigned one2 = (hp == (wid & 1) && e2 >= 0 && e2 < 2) ? (0x3f80u << (16 * e2)) : 0u;
    const unsigned pst = hp ? 8192 + (h >> 3) * 1024 + (((h >> 1) & 3) * 16 + srow + 8 * (h & 1)) * 16
                            : (h >> 1) * 1024 + ((2 * (h & 1) + (c16 >> 2)) * 16 + srow + 8 * ((c16 >> 1) & 1)) * 16;
    reca_asm(rs, (unsigned)(h * 4 + wid) * (98 * 1024), og2,
             og1 + img, ox + wid * 7168, l0 + wid * 7168, img + lane * 16, img + tid * 16,
             img + ch1 * 16, img + (16 + 2 * wid + (g >> 1)) * 256 + c16 * 16 + 8 * (g & 1),
             img + (24 + 2 * (wid >> 1) + (g >> 1)) * 256 + c16 * 16 + 8 * (g & 1), l0 + wid * 2048 + lane * 16,
             l0 + ((g & 1) ? 12 + (wid >> 1) : 8 + wid) * 1024 + (g >> 1) * 256 + c16 * 16,
             sc + wid * 1408 + r2 * 176 + (16 * hp + 8 * ((c16 >> 1) & 1) + (c16 & 5)) * 4,
             hp ? l0 + A2_DUMP + lane * 8 : sc + wid * 1408 + q8 * 176 + (32 + r2) * 4,
             sc + srow * 176 + (c16 < 10 ? c16 : 10) * 16,
             (c16 < 10 && !(c16 & 1) && wid < 2) ? pst : 0x80000000u, hp ? 0xEEEEu : 0x4444u,
             u32x2{e4 < 2 ? one4 : 0u, e4 < 2 ? 0u : one4}, u32x2{one2, 0u}, RECA_DBG_PASS);
}

__device__ __forceinline__ void mfma_b0(f32x4& d, const u32x4& a, const bf16x8& w) {
    asm volatile("v_mfma_f32_16x16x32_bf16 %0, %1, %2, 0" : "=&v"(d) : "v"(a), "a"(w));
}
__device__ __forceinline__ void mfma_b(f32x4& d, const u32x4& a, const bf16x8& w) {
    asm volatile("v_mfma_f32_16x16x32_bf16 %0, %1, %2, %0" : "+v"(d) : "v"(a), "a"(w));
}
__device__ __forceinline__ void lds_rd16o(u32x4& d, unsigned addr, int off) {
    asm volatile("ds_read_b128 %0, %1 offset:%2" : "=v"(d) : "v"(addr), "n"(off));
}
__device__ __forceinline__ void chain5(f32x4& d, const u32x4& a0, const u32x4& a1, const u32x4& a2, const u32x4& a3,
                                       const u32x4& a4, const u32x4 (&b)[4], const u32x4& b4) {
    asm volatile(
        "s_nop 1\n\tv_mfma_f32_16x16x32_bf16 %0, %1, %6, 0\n\tv_mfma_f32_16x16x32_bf16 %0, %2, %7, %0\n\t"
        "v_mfma_f32_16x16x32_bf16 %0, %3, %8, %0\n\tv_mfma_f32_16x16x32_bf16 %0, %4, %9, %0\n\t"
        "v_mfma_f32_16x16x32_bf16 %0, %5, %10, %0\n\ts_nop 7"
        : "=&v"(d)
        : "v"(a0), "v"(a1), "v"(a2), "v"(a3), "v"(a4), "v"(b[0]), "v"(b[1]), "v"(b[2]), "v"(b[3]), "v"(b4));
}
constexpr int cmin(int a, int b) { return a < b ? a : b; }
#define SW14(F) switch (ks) { F(0) F(1) F(2) F(3) F(4) F(5) F(6) F(7) F(8) F(9) F(10) F(11) F(12) F(13) }

constexpr int CL_MEM = 0, CL_RED = CL_MEM + 8 * MROW, CL_ORED = CL_RED + 4 * 128 * 2;
constexpr int RECC_LDS = 2 * 128 * 68 * 4;
constexpr int DG_SEG = 28 * 4 * 4 * 512;
constexpr int UG_SEG = 28 * 4 * 512;

__device__ __forceinline__ void lds_bar() { asm volatile("s_waitcnt lgkmcnt(0)\n\ts_barrier" ::: "memory"); }
__device__ __forceinline__ void rec_D_body(const RecParams& p, u16* smem, int c, int wid) {
    const int tid = tid_of(wid), lane = tid & 63;
    const int c16 = lane & 15, g = lane >> 4;
    const unsigned xl_base = (unsigned)(uintptr_t)(LAS u16*)smem;
    const unsigned xvoff = (unsigned)lane * 16u;
    dma_x(p.x, 0, xvoff, xl_base, wid);
    dma_x(p.x, 1, xvoff, xl_base + XIMG * 2, wid);
    bf16x8 W[4][14];
#pragma unroll
    for (int hh = 0; hh < 4; ++hh) {
        const u16* cp = p.CmatT + (long)(c * 16 + wid * 4 + hh) * (14 * 512) + lane * 8;
#pragma unroll
        for (int ks = 0; ks < 14; ++ks) W[hh][ks] = *reinterpret_cast<const bf16x8*>(cp + 512 * ks);
    }
    __amdgpu_buffer_rsrc_t rsd = __builtin_amdgcn_make_buffer_rsrc(p.Dg, 0, 64 * DG_SEG * 2, 0x00020000);
    const unsigned dvoff = ((c * 4 + wid) * 2048 + lane * 8) * 2;
    unsigned* fl = p.dflag + (c * 4 + wid) * 64;
    __builtin_amdgcn_s_waitcnt(0x0F70);
    asm volatile("s_waitcnt vmcnt(0)" ::: "memory");
    for (int s = 0; s < NSEG; ++s) {
        lds_bar();
        const char* gb = reinterpret_cast<const char*>(p.x) + (long)min(s + 2, NSEG - 1) * (SEG * DM * 2) + wid * 7168;
        const unsigned xl = xl_base + ((s + 2) & 3) * (XIMG * 2) + (unsigned)(wid * 7) * 1024u;
        const unsigned xa = xl_base + (s & 3) * (XIMG * 2) + lane * 16;
        u32x4 fa[14], fb[14];
        f32x4 d0[4], d1[4];
        wait_lgkm<0>();
#pragma unroll
        for (int ks = 0; ks < 3; ++ks) {
            lds_rd16o(fa[ks], xa, ks * 1024);
            lds_rd16o(fb[ks], xa, (14 + ks) * 1024);
        }
#pragma unroll
        for (int ks = 0; ks < 14; ++ks) {
            if (ks + 3 < 14) {
                lds_rd16o(fa[ks + 3], xa, (ks + 3) * 1024);
                lds_rd16o(fb[ks + 3], xa, (17 + ks) * 1024);
            }
#define WD(Q) case Q: wait_lgkm<2 * cmin(Q + 4, 14) - 2 * (Q + 1)>(); break;
            SW14(WD)
#undef WD
#pragma unroll
            for (int hh = 0; hh < 4; ++hh) {
                if (ks == 0) {
                    mfma_b0(d0[hh], fa[ks], W[hh][ks]);
                    mfma_b0(d1[hh], fb[ks], W[hh][ks]);
                } else {
                    mfma_b(d0[hh], fa[ks], W[hh][ks]);
                    mfma_b(d1[hh], fb[ks], W[hh][ks]);
                }
            }
            if (ks < 7) dma16(gb + 1024 * ks, xvoff, xl + (unsigned)ks * 1024u);
        }
        asm volatile("s_nop 7"
                     : "+v"(d0[0]), "+v"(d0[1]), "+v"(d0[2]), "+v"(d0[3]), "+v"(d1[0]), "+v"(d1[1]), "+v"(d1[2]),
                       "+v"(d1[3]));
        if (s < D_NEAR) asm volatile("s_waitcnt vmcnt(7)" ::: "memory");
        else asm volatile("s_waitcnt vmcnt(11)" ::: "memory");
        if (s > 0 && lane == 0)
            __hip_atomic_store(fl, (unsigned)(s < D_NEAR ? s - 1 : s - 2), __ATOMIC_RELAXED, __HIP_MEMORY_SCOPE_AGENT);
        asm volatile("" ::: "memory");
#pragma unroll
        for (int hh = 0; hh < 4; ++hh)
            __builtin_amdgcn_raw_buffer_store_b128(u32x4{pack2(d0[hh][0], d0[hh][1]), pack2(d0[hh][2], d0[hh][3]),
                                                         pack2(d1[hh][0], d1[hh][1]), pack2(d1[hh][2], d1[hh][3])},
                                                   rsd, dvoff + hh * 1024, (unsigned)(s * DG_SEG * 2), 16);
    }
    asm volatile("s_waitcnt vmcnt(0)" ::: "memory");
    if (lane == 0) __hip_atomic_store(fl, (unsigned)(NSEG - 1), __ATOMIC_RELAXED, __HIP_MEMORY_SCOPE_AGENT);
}

typedef __attribute__((ext_vector_type(8))) unsigned u32x8;
__device__ __forceinline__ u32x8 cat8(const u32x4& a, const u32x4& b) {
    return __builtin_shufflevector(a, b, 0, 1, 2, 3, 4, 5, 6, 7);
}
__device__ __forceinline__ void comb_asm(__amdgpu_buffer_rsrc_t r1, __amdgpu_buffer_rsrc_t r2, unsigned so1, unsigned so2,
                                         unsigned& bud, const u32x8 (&b)[10], unsigned vo, unsigned idx, unsigned ps,
                                         unsigned ga, unsigned gr) {
    unsigned t1, t2;
    bud = (unsigned)__builtin_amdgcn_readfirstlane((int)bud);
    asm volatile(
        "s_add_u32 %[t1], %[so1], 0x1000\n\t"
        "s_add_u32 %[t2], %[so1], 0x2000\n\t"
        "LC0_%=:\n\t"
        "buffer_load_dwordx4 v[200:203], %[vo], %[r1], %[so1] offen offset:0 sc1\n\t"
        "buffer_load_dwordx4 v[204:207], %[vo], %[r1], %[so1] offen offset:1024 sc1\n\t"
        "buffer_load_dwordx4 v[208:211], %[vo], %[r1], %[so1] offen offset:2048 sc1\n\t"
        "buffer_load_dwordx4 v[212:215], %[vo], %[r1], %[so1] offen offset:3072 sc1\n\t"
        "buffer_load_dwordx4 v[216:219], %[vo], %[r1], %[t1] offen offset:0 sc1\n\t"
        "buffer_load_dwordx4 v[220:223], %[vo], %[r1], %[t1] offen offset:1024 sc1\n\t"
        "buffer_load_dwordx4 v[224:227], %[vo], %[r1], %[t1] offen offset:2048 sc1\n\t"
        "buffer_load_dwordx4 v[228:231], %[vo], %[r1], %[t1] offen offset:3072 sc1\n\t"
        "buffer_load_dwordx4 v[232:235], %[vo], %[r1], %[t2] offen offset:0 sc1\n\t"
        "buffer_load_dwordx4 v[236:239], %[vo], %[r1], %[t2] offen offset:1024 sc1\n\t"
        "v_mov_b32_e32 v240, 0\n\t"
        "v_mov_b32_e32 v241, 0\n\t"
        "v_mov_b32_e32 v242, 0\n\t"
        "v_mov_b32_e32 v243, 0\n\t"
        "v_mov_b32_e32 v244, 0\n\t"
        "s_nop 0\n\t"
        "s_waitcnt vmcnt(9)\n\t"
        "v_smfmac_f32_16x16x64_bf16 v[240:243], v[200:203], %[b0], %[idx]\n\t"
        "v_max3_u32 v244, v244, v201, v203\n\t"
        "s_waitcnt vmcnt(8)\n\t"
        "v_smfmac_f32_16x16x64_bf16 v[240:243], v[204:207], %[b1], %[idx]\n\t"
        "v_max3_u32 v244, v244, v205, v207\n\t"
        "s_waitcnt vmcnt(7)\n\t"
        "v_smfmac_f32_16x16x64_bf16 v[240:243], v[208:211], %[b2], %[idx]\n\t"
        "v_max3_u32 v244, v244, v209, v211\n\t"
        "s_waitcnt vmcnt(6)\n\t"
        "v_smfmac_f32_16x16x64_bf16 v[240:243], v[212:215], %[b3], %[idx]\n\t"
        "v_max3_u32 v244, v244, v213, v215\n\t"
        "s_waitcnt vmcnt(5)\n\t"
        "v_smfmac_f32_16x16x64_bf16 v[240:243], v[216:219], %[b4], %[idx]\n\t"
        "v_max3_u32 v244, v244, v217, v219\n\t"
        "s_waitcnt vmcnt(4)\n\t"
        "v_smfmac_f32_16x16x64_bf16 v[240:243], v[220:223], %[b5], %[idx]\n\t"
        "v_max3_u32 v244, v244, v221, v223\n\t"
        "s_waitcnt vmcnt(3)\n\t"
        "v_smfmac_f32_16x16x64_bf16 v[240:243], v[224:227], %[b6], %[idx]\n\t"
        "v_max3_u32 v244, v244, v225, v227\n\t"
        "s_waitcnt vmcnt(2)\n\t"
        "v_smfmac_f32_16x16x64_bf16 v[240:243], v[228:231], %[b7], %[idx]\n\t"
        "v_max3_u32 v244, v244, v229, v231\n\t"
        "s_waitcnt vmcnt(1)\n\t"
        "v_smfmac_f32_16x16x64_bf16 v[240:243], v[232:235], %[b8], %[idx]\n\t"
        "v_max3_u32 v244, v244, v233, v235\n\t"
        "s_waitcnt vmcnt(0)\n\t"
        "v_smfmac_f32_16x16x64_bf16 v[240:243], v[236:239], %[b9], %[idx]\n\t"
        "v_max3_u32 v244, v244, v237, v239\n\t"
        "v_cmp_eq_u32_e32 vcc, -1, v244\n\t"
        "s_cbranch_vccnz LC2_%=\n\t"
        "LC1_%=:\n\t"
        "v_mov_b32_e32 v244, -2\n\t"
        "s_nop 3\n\t"
        "v_permlane32_swap_b32_e32 v240, v242\n\t"
        "v_permlane32_swap_b32_e32 v241, v243\n\t"
        "v_pk_add_f32 v[240:241], v[240:241], v[242:243]\n\t"
        "v_cvt_pk_bf16_f32 v240, v240, v241\n\t"
        "s_nop 1\n\t"
        "v_mov_b32_dpp v241, v240 quad_perm:[1,0,3,2] row_mask:0xf bank_mask:0xf bound_ctrl:1\n\t"
        "v_perm_b32 v240, v241, v240, %[ps]\n\t"
        "s_nop 1\n\t"
        "v_min_u32_dpp v241, v240, v244 row_shl:4 row_mask:0xf bank_mask:0xf bound_ctrl:1\n\t"
        "v_mov_b32_dpp v242, v240 row_shl:8 row_mask:0xf bank_mask:0xf bound_ctrl:1\n\t"
        "v_min_u32_dpp v243, v240, v244 row_shl:12 row_mask:0xf bank_mask:0xf bound_ctrl:1\n\t"
        "buffer_store_dwordx4 v[240:243], %[ga], %[r2], %[so2] offen sc1\n\t"
        "v_mov_b32_e32 v248, v240\n\t"
        "v_min_u32_dpp v249, v240, v244 row_shl:2 row_mask:0xf bank_mask:0xf bound_ctrl:1\n\t"
        "v_mov_b32_dpp v250, v240 row_shl:4 row_mask:0xf bank_mask:0xf bound_ctrl:1\n\t"
        "v_min_u32_dpp v251, v240, v244 row_shl:6 row_mask:0xf bank_mask:0xf bound_ctrl:1\n\t"
        "s_add_u32 %[t1], %[so2], %[cp]\n\t"
        "buffer_store_dwordx4 v[248:251], %[gr], %[r2], %[so2] offen sc1\n\t"
        "buffer_store_dwordx4 v[248:251], %[gr], %[r2], %[t1] offen sc1\n\t"
        "s_branch LC3_%=\n\t"
        "LC2_%=:\n\t"
        "s_cmp_eq_u32 %[bud], 0\n\t"
        "s_cbranch_scc1 LC1_%=\n\t"
        "s_sub_u32 %[bud], %[bud], 1\n\t"
        "s_branch LC0_%=\n\t"
        "LC3_%=:\n\t"
        "s_nop 1\n\t"
        : [t1] "=&s"(t1), [t2] "=&s"(t2), [bud] "+s"(bud)
        : [r1] "s"(r1), [r2] "s"(r2), [so1] "s"(so1), [so2] "s"(so2), [b0] "v"(b[0]), [b1] "v"(b[1]), [b2] "v"(b[2]),
          [b3] "v"(b[3]), [b4] "v"(b[4]), [b5] "v"(b[5]), [b6] "v"(b[6]), [b7] "v"(b[7]), [b8] "v"(b[8]), [b9] "v"(b[9]),
          [vo] "v"(vo), [idx] "v"(idx), [ps] "v"(ps), [ga] "v"(ga), [gr] "v"(gr), [cp] "n"(G2_CP * 2)
        :
          "v200", "v201", "v202", "v203", "v204", "v205", "v206", "v207", "v208", "v209", "v210", "v211", "v212",
          "v213", "v214", "v215", "v216", "v217", "v218", "v219", "v220", "v221", "v222", "v223", "v224", "v225",
          "v226", "v227", "v228", "v229", "v230", "v231", "v232", "v233", "v234", "v235", "v236", "v237", "v238",
          "v239", "v240", "v241", "v242", "v243", "v244", "v245", "v246", "v247", "v248", "v249", "v250", "v251" , "vcc", "scc", "memory");
}
__device__ __forceinline__ void rec_C_body(const RecParams& p, u16* smem, int c, int wid) {
    u16* memL = smem + CL_MEM;
    u32x4* ufr = reinterpret_cast<u32x4*>(smem + CL_RED);
    const int tid = tid_of(wid), lane = tid & 63;
    const int c16 = lane & 15, g = lane >> 4;
    const int ch1 = (tid < 192) ? (256 + tid) : tid;
    if (tid < 96) {
        const int orow = (lane >> 2) & 7, oq = lane & 3;
        const bool ao = tid < 32;
        const unsigned goff =
            (tid >> 5) * G2_CP + 128 * c + (ao ? (orow + 8 * (oq >> 1)) * 8 + 4 * (oq & 1) : 4 * (lane & 31));
        const unsigned* m0 = reinterpret_cast<const unsigned*>(p.mem0 + orow * DM + 16 * c);
        const int p0 = ao ? 4 * (oq & 1) + (oq >> 1) : 2 * oq;
        st_gran4(p.G2 + goff, m0[p0], m0[p0 + (ao ? 2 : 1)]);
    }
    u16* mw0 = memL + ((tid >> 1) & 7) * MROW + 16 * (tid >> 4) + 8 * (tid & 1);
    u16* mw1 = memL + ((ch1 >> 1) & 7) * MROW + 16 * (ch1 >> 4) + 8 * (ch1 & 1);
    bf16x8 W[4][14];
#pragma unroll
    for (int hh = 0; hh < 4; ++hh) {
        const u16* cp = p.CmatT + (long)(c * 16 + wid * 4 + hh) * (14 * 512) + lane * 8;
#pragma unroll
        for (int ks = 0; ks < 14; ++ks) W[hh][ks] = *reinterpret_cast<const bf16x8*>(cp + 512 * ks);
    }
    u32x8 Bv[10];
    const int i8 = c16 & 7;
    const unsigned mfa = (unsigned)(uintptr_t)(LAS u16*)(memL + i8 * MROW + 8 * g);
    const int orow2 = 4 * (g & 1) + 2 * (g >> 1) + (c16 & 1);
    const unsigned goA = c16 < 4 ? 256 * c + (orow2 + 8 * (c16 >> 1)) * 16 : 0x80000000u;
    const unsigned goR = !(c16 & 6) ? G2_CP * 2 + 256 * c + orow2 * 32 + (c16 >> 3) * 16 : 0x80000000u;
    const unsigned psel = (c16 & 1) ? 0x03020706u : 0x05040100u;
    const unsigned sidx = c16 < 8 ? 0x4444u : 0xEEEEu;
    __amdgpu_buffer_rsrc_t rs1 = __builtin_amdgcn_make_buffer_rsrc(p.G1, 0, 63 * G1_SLOT * 2, 0x00020000);
    __amdgpu_buffer_rsrc_t rs2 = __builtin_amdgcn_make_buffer_rsrc(p.G2, 0, 64 * G2_SLOT * 2, 0x00020000);
    __amdgpu_buffer_rsrc_t rsd = __builtin_amdgcn_make_buffer_rsrc(p.Dg, 0, 64 * DG_SEG * 2, 0x00020000);
    __amdgpu_buffer_rsrc_t rsu = __builtin_amdgcn_make_buffer_rsrc(p.Ug, 0, 64 * UG_SEG * 2, 0x00020000);
    const unsigned dvoff = (c * 8192 + lane * 8) * 2;
    const unsigned uvoff = ((c * 4 + wid) * 512 + lane * 8) * 2;
    const unsigned* fl = p.dflag + (c * 4 + (lane & 3)) * 64;
    unsigned* ufl = p.dflag + (c * 4 + wid) * 64 + 32;
    bool dead = false;
    int dv = -1;
    unsigned budget = SPIN_CAP;
    auto load_D = [&](int s) {
        for (unsigned spin = 0; dv < s && !dead; ++spin) {
            const int v = (int)__hip_atomic_load(fl, __ATOMIC_RELAXED, __HIP_MEMORY_SCOPE_AGENT);
            dv = min(min(__builtin_amdgcn_readlane(v, 0), __builtin_amdgcn_readlane(v, 1)),
                     min(__builtin_amdgcn_readlane(v, 2), __builtin_amdgcn_readlane(v, 3)));
            if (spin > SPIN_CAP) dead = true;
        }
        asm volatile("" ::: "memory");
#pragma unroll
        for (int i = 0; i < 8; ++i)
            Bv[i] = cat8(ld16_sc1(rsd, dvoff + i * 2048, (unsigned)(s * DG_SEG * 2)),
                         ld16_sc1(rsd, dvoff + i * 2048 + 1024, (unsigned)(s * DG_SEG * 2)));
    };
    for (int s = 0;; ++s) {
        {
            u32x4 a, b;
            for (unsigned spin = 0;; ++spin) {
                asm volatile("" ::: "memory");
                a = ld16_sc1(rs2, tid * 16, (unsigned)((s * G2_SLOT + G2_CP) * 2));
                b = ld16_sc1(rs2, ch1 * 16, (unsigned)((s * G2_SLOT + G2_CP) * 2));
                unsigned mx = max(umax3(a[1], a[3], b[1]), b[3]);
                if (__all(mx != 0xffffffffu) || dead) break;
                if (spin > SPIN_CAP) dead = true;
                __builtin_amdgcn_s_sleep(SLP_C0);
            }
            *reinterpret_cast<u32x4*>(mw0) = a;
            *reinterpret_cast<u32x4*>(mw1) = b;
        }
        if (wid == 0 && s + 1 < NSEG) load_D(s);
        lds_bar();
        {
            TS(1);
            f32x4 U[4];
            u32x4 mf[14];
            wait_lgkm<0>();
#pragma unroll
            for (int ks = 0; ks < 5; ++ks) lds_rd16o(mf[ks], mfa, ks * 64);
#pragma unroll
            for (int ks = 0; ks < 14; ++ks) {
                if (ks + 5 < 14) lds_rd16o(mf[ks + 5], mfa, (ks + 5) * 64);
#define WU(Q) case Q: wait_lgkm<cmin(Q + 6, 14) - (Q + 1)>(); break;
                SW14(WU)
#undef WU
#pragma unroll
                for (int hh = 0; hh < 4; ++hh) {
                    if (ks == 0) mfma_b0(U[hh], mf[ks], W[hh][ks]);
                    else mfma_b(U[hh], mf[ks], W[hh][ks]);
                }
            }
            asm volatile("s_nop 7" : "+v"(U[0]), "+v"(U[1]), "+v"(U[2]), "+v"(U[3]));
            unsigned pk[4][2];
#pragma unroll
            for (int hh = 0; hh < 4; ++hh) {
                pk[hh][0] = pack2(U[hh][0], U[hh][1]);
                pk[hh][1] = pack2(U[hh][2], U[hh][3]);
            }
            const bool g1 = g & 1, g2 = g & 2;
            unsigned e0 = g2 ? pk[2][0] : pk[0][0], e1 = g2 ? pk[2][1] : pk[0][1];
            unsigned o0 = g2 ? pk[3][0] : pk[1][0], o1 = g2 ? pk[3][1] : pk[1][1];
            ufr[tid] = u32x4{e0, o0, e1, o1};
            unsigned m0 = g1 ? o0 : e0, m1 = g1 ? o1 : e1;
            unsigned t0 = g1 ? e0 : o0, t1 = g1 ? e1 : o1;
            unsigned r0 = (unsigned)__shfl_xor((int)t0, 16), r1 = (unsigned)__shfl_xor((int)t1, 16);
            u32x4 Uc = g1 ? u32x4{r0, r1, m0, m1} : u32x4{m0, m1, r0, r1};
            __builtin_amdgcn_raw_buffer_store_b128(Uc, rsu, uvoff, (unsigned)(s * UG_SEG * 2), 16);
            TS(7);
        }
        if (s == NSEG - 1) break;
        lds_bar();
        if (wid == 0) {
            Bv[8] = cat8(ufr[lane], ufr[64 + lane]);
            Bv[9] = cat8(ufr[128 + lane], ufr[192 + lane]);
            TS(6);
            __builtin_amdgcn_s_sleep(SLP_C1);
            comb_asm(rs1, rs2, (unsigned)(s * G1_SLOT * 2), (unsigned)((s + 1) * G2_SLOT * 2), budget, Bv, lane * 16, sidx, psel,
                     goA, goR);
            TS(3);
        } else {
            asm volatile("s_waitcnt vmcnt(0)" ::: "memory");
        }
        asm volatile("" ::: "memory");
        if (lane == 0) __hip_atomic_store(ufl, (unsigned)s, __ATOMIC_RELAXED, __HIP_MEMORY_SCOPE_AGENT);
        lds_bar();
        TS(5);
        TSC(4);
        __builtin_amdgcn_s_sleep(SLP_C2);
    }
    asm volatile("s_waitcnt vmcnt(0)" ::: "memory");
    if (lane == 0) __hip_atomic_store(ufl, (unsigned)(NSEG - 1), __ATOMIC_RELAXED, __HIP_MEMORY_SCOPE_AGENT);
}

constexpr int OROW = 68;
__device__ __forceinline__ void rec_O_body(const RecParams& p, u16* smem, int cg, int par, int wid) {
    float* ored = reinterpret_cast<float*>(smem);
    const int tid = tid_of(wid), lane = tid & 63;
    const int c16 = lane & 15, g = lane >> 4;
    const unsigned pxoff = ((wid * 4) * 1280 + c16 * 40 + 8 * g) * 2;
    const unsigned pxoffx = ((wid * 4 + g) * 1280 + c16 * 40 + 32) * 2;
    float* owr = ored + (wid * 32 + 4 * g) * OROW + c16;
    const float* ord = ored + (tid >> 3) * OROW + 8 * (tid & 7);
    u16* outp = p.out + (long)(tid >> 3) * 896 + 64 * cg + 8 * (tid & 7);
    __amdgpu_buffer_rsrc_t rs3 = __builtin_amdgcn_make_buffer_rsrc(p.G3, 0, 64 * G3_SLOT * 2, 0x00020000);
    __amdgpu_buffer_rsrc_t rsd = __builtin_amdgcn_make_buffer_rsrc(p.Dg, 0, 64 * DG_SEG * 2, 0x00020000);
    __amdgpu_buffer_rsrc_t rsu = __builtin_amdgcn_make_buffer_rsrc(p.Ug, 0, 64 * UG_SEG * 2, 0x00020000);
    const unsigned dvoff = ((cg * 16 + wid) * 2048 + lane * 8) * 2;
    const unsigned uvoff = ((cg * 16 + wid) * 512 + lane * 8) * 2;
    const unsigned* fl = p.dflag + ((cg * 4 + (lane & 3)) * 4 + wid) * 64;
    bool dead = false;
    auto wait_fl = [&](int w, int s) {
        for (unsigned spin = 0; !dead; ++spin) {
            int v = (int)__hip_atomic_load(fl + w, __ATOMIC_RELAXED, __HIP_MEMORY_SCOPE_AGENT);
            if (__all(v >= s)) break;
            if (spin > SPIN_CAP) dead = true;
            __builtin_amdgcn_s_sleep(8);
        }
        asm volatile("" ::: "memory");
    };
    for (int s = par; s < NSEG; s += 2) {
        u32x4 D[4][4], U[4], px[10];
        wait_fl(0, s);
#pragma unroll
        for (int cb = 0; cb < 4; ++cb)
#pragma unroll
            for (int hh = 0; hh < 4; ++hh)
                D[cb][hh] = ld16_sc1(rsd, dvoff + cb * 16384 + hh * 1024, (unsigned)(s * DG_SEG * 2));
        wait_fl(32, s);
#pragma unroll
        for (int cb = 0; cb < 4; ++cb) U[cb] = ld16_sc1(rsu, uvoff + cb * 4096, (unsigned)(s * UG_SEG * 2));
        for (unsigned spin = 0;; ++spin) {
            asm volatile("" ::: "memory");
            unsigned mx = 0;
#pragma unroll
            for (int q = 0; q < 10; ++q)
                px[q] = ld16_sc1(rs3, ((q % 5) < 4 ? pxoff + (q % 5) * 2560 : pxoffx) + (q / 5) * 1280,
                                 (unsigned)(s * G3_SLOT * 2));
#pragma unroll
            for (int q = 0; q < 10; ++q) mx = umax3(mx, px[q][1], px[q][3]);
            if (__all(mx != 0xffffffffu) || dead) break;
            if (spin > SPIN_CAP) dead = true;
            __builtin_amdgcn_s_sleep(4);
        }
        const int bo = ((s >> 1) & 1) * (128 * OROW);
#pragma unroll
        for (int cb = 0; cb < 4; ++cb)
#pragma unroll
            for (int qt = 0; qt < 2; ++qt) {
                f32x4 o;
                chain5(o, px[qt * 5], px[qt * 5 + 1], px[qt * 5 + 2], px[qt * 5 + 3], px[qt * 5 + 4], D[cb], U[cb]);
#pragma unroll
                for (int r = 0; r < 4; ++r) owr[bo + (qt * 16 + r) * OROW + cb * 16] = o[r];
            }
        __syncthreads();
        f32x4 a0 = f32x4{0.f, 0.f, 0.f, 0.f}, a1 = a0;
        {
            f32x4 y[4][2];
#pragma unroll
            for (int w = 0; w < 4; ++w) {
                y[w][0] = *reinterpret_cast<const f32x4*>(ord + bo + w * 32 * OROW);
                y[w][1] = *reinterpret_cast<const f32x4*>(ord + bo + w * 32 * OROW + 4);
            }
            a0 = (y[0][0] + y[1][0]) + (y[2][0] + y[3][0]);
            a1 = (y[0][1] + y[1][1]) + (y[2][1] + y[3][1]);
        }
        *reinterpret_cast<u32x4*>(outp + (long)s * (SEG * 896)) =
            u32x4{pack2(a0[0], a0[1]), pack2(a0[2], a0[3]), pack2(a1[0], a1[1]), pack2(a1[2], a1[3])};
    }
}

__device__ __forceinline__ void gemm0_asm(f32x16 (&c)[4][2], __amdgpu_buffer_rsrc_t ra, __amdgpu_buffer_rsrc_t rb,
                                          unsigned astr, unsigned nk, unsigned aoff, unsigned boff, unsigned ard,
                                          unsigned brd, unsigned aw, unsigned bw) {
    asm volatile(
        "v_mov_b32_e32 v236, %[aoff]\n\t"
        "v_add_u32_e32 v237, %[astr], v236\n\t"
        "v_add_u32_e32 v238, %[astr], v237\n\t"
        "v_add_u32_e32 v239, %[astr], v238\n\t"
        "v_add_u32_e32 v240, %[astr], v239\n\t"
        "v_add_u32_e32 v241, %[astr], v240\n\t"
        "v_add_u32_e32 v242, %[astr], v241\n\t"
        "v_add_u32_e32 v243, %[astr], v242\n\t"
        "v_mov_b32_e32 v244, %[boff]\n\t"
        "v_add_u32_e32 v245, 0x20000, v244\n\t"
        "v_add_u32_e32 v246, 0x20000, v245\n\t"
        "v_add_u32_e32 v247, 0x20000, v246\n\t"
        "s_sub_u32 s68, %[nk], 1\n\t"
        "s_mov_b32 s64, 0\n\t"
        "s_mov_b32 s65, 0\n\t"
        "s_mov_b32 s66, 0\n\t"
        "buffer_load_dwordx4 v[140:143], v236, %[ra], s65 offen\n\t"
        "buffer_load_dwordx4 v[144:147], v237, %[ra], s65 offen\n\t"
        "buffer_load_dwordx4 v[148:151], v238, %[ra], s65 offen\n\t"
        "buffer_load_dwordx4 v[152:155], v239, %[ra], s65 offen\n\t"
        "buffer_load_dwordx4 v[156:159], v240, %[ra], s65 offen\n\t"
        "buffer_load_dwordx4 v[160:163], v241, %[ra], s65 offen\n\t"
        "buffer_load_dwordx4 v[164:167], v242, %[ra], s65 offen\n\t"
        "buffer_load_dwordx4 v[168:171], v243, %[ra], s65 offen\n\t"
        "buffer_load_dwordx4 v[172:175], v244, %[rb], s66 offen\n\t"
        "buffer_load_dwordx4 v[176:179], v245, %[rb], s66 offen\n\t"
        "buffer_load_dwordx4 v[180:183], v246, %[rb], s66 offen\n\t"
        "buffer_load_dwordx4 v[184:187], v247, %[rb], s66 offen\n\t"
        "s_movk_i32 s65, 0x80\n\t"
        "s_mov_b32 s66, 0x80000\n\t"
        "buffer_load_dwordx4 v[188:191], v236, %[ra], s65 offen\n\t"
        "buffer_load_dwordx4 v[192:195], v237, %[ra], s65 offen\n\t"
        "buffer_load_dwordx4 v[196:199], v238, %[ra], s65 offen\n\t"
        "buffer_load_dwordx4 v[200:203], v239, %[ra], s65 offen\n\t"
        "buffer_load_dwordx4 v[204:207], v240, %[ra], s65 offen\n\t"
        "buffer_load_dwordx4 v[208:211], v241, %[ra], s65 offen\n\t"
        "buffer_load_dwordx4 v[212:215], v242, %[ra], s65 offen\n\t"
        "buffer_load_dwordx4 v[216:219], v243, %[ra], s65 offen\n\t"
        "buffer_load_dwordx4 v[220:223], v244, %[rb], s66 offen\n\t"
        "buffer_load_dwordx4 v[224:227], v245, %[rb], s66 offen\n\t"
        "buffer_load_dwordx4 v[228:231], v246, %[rb], s66 offen\n\t"
        "buffer_load_dwordx4 v[232:235], v247, %[rb], s66 offen\n\t"
        "v_add_u32_e32 v88, 57344, %[ard]\n\t"
        "v_add_u32_e32 v89, 57344, %[brd]\n\t"
        "v_add_u32_e32 v90, 57344, %[aw]\n\t"
        "v_add_u32_e32 v91, 57344, %[bw]\n\t"
        "v_mov_b32_e32 v108, 0\n\t"
        "v_mov_b32_e32 v109, 0\n\t"
        "v_mov_b32_e32 v110, 0\n\t"
        "v_mov_b32_e32 v111, 0\n\t"
        "v_mov_b32_e32 v112, 0\n\t"
        "v_mov_b32_e32 v113, 0\n\t"
        "v_mov_b32_e32 v114, 0\n\t"
        "v_mov_b32_e32 v115, 0\n\t"
        "v_mov_b32_e32 v116, 0\n\t"
        "v_mov_b32_e32 v117, 0\n\t"
        "v_mov_b32_e32 v118, 0\n\t"
        "v_mov_b32_e32 v119, 0\n\t"
        "v_mov_b32_e32 v120, 0\n\t"
        "v_mov_b32_e32 v121, 0\n\t"
        "v_mov_b32_e32 v122, 0\n\t"
        "v_mov_b32_e32 v123, 0\n\t"
        "v_mov_b32_e32 v132, 0\n\t"
        "v_mov_b32_e32 v133, 0\n\t"
        "v_mov_b32_e32 v134, 0\n\t"
        "v_mov_b32_e32 v135, 0\n\t"
        "v_mov_b32_e32 v136, 0\n\t"
        "v_mov_b32_e32 v137, 0\n\t"
        "v_mov_b32_e32 v138, 0\n\t"
        "v_mov_b32_e32 v139, 0\n\t"
        "s_nop 1\n\t"
        "v_mfma_f32_32x32x16_bf16 %[c0], v[132:135], v[108:111], 0\n\t"
        "v_mfma_f32_32x32x16_bf16 %[c1], v[136:139], v[108:111], 0\n\t"
        "v_mfma_f32_32x32x16_bf16 %[c2], v[132:135], v[112:115], 0\n\t"
        "v_mfma_f32_32x32x16_bf16 %[c3], v[136:139], v[112:115], 0\n\t"
        "v_mfma_f32_32x32x16_bf16 %[c4], v[132:135], v[116:119], 0\n\t"
        "v_mfma_f32_32x32x16_bf16 %[c5], v[136:139], v[116:119], 0\n\t"
        "v_mfma_f32_32x32x16_bf16 %[c6], v[132:135], v[120:123], 0\n\t"
        "v_mfma_f32_32x32x16_bf16 %[c7], v[136:139], v[120:123], 0\n\t"
        "s_movk_i32 s65, 0x100\n\t"
        "s_mov_b32 s66, 0x100000\n\t"
        "s_waitcnt vmcnt(23)\n\t"
        "ds_write_b128 %[aw], v[140:143] offset:0\n\t"
        "buffer_load_dwordx4 v[140:143], v236, %[ra], s65 offen\n\t"
        "s_waitcnt vmcnt(23)\n\t"
        "ds_write_b128 %[aw], v[144:147] offset:4608\n\t"
        "buffer_load_dwordx4 v[144:147], v237, %[ra], s65 offen\n\t"
        "s_waitcnt vmcnt(23)\n\t"
        "ds_write_b128 %[aw], v[148:151] offset:9216\n\t"
        "buffer_load_dwordx4 v[148:151], v238, %[ra], s65 offen\n\t"
        "s_waitcnt vmcnt(23)\n\t"
        "ds_write_b128 %[aw], v[152:155] offset:13824\n\t"
        "buffer_load_dwordx4 v[152:155], v239, %[ra], s65 offen\n\t"
        "s_waitcnt vmcnt(23)\n\t"
        "ds_write_b128 %[aw], v[156:159] offset:18432\n\t"
        "buffer_load_dwordx4 v[156:159], v240, %[ra], s65 offen\n\t"
        "s_waitcnt vmcnt(23)\n\t"
        "ds_write_b128 %[aw], v[160:163] offset:23040\n\t"
        "buffer_load_dwordx4 v[160:163], v241, %[ra], s65 offen\n\t"
        "s_waitcnt vmcnt(23)\n\t"
        "ds_write_b128 %[aw], v[164:167] offset:27648\n\t"
        "buffer_load_dwordx4 v[164:167], v242, %[ra], s65 offen\n\t"
        "s_waitcnt vmcnt(23)\n\t"
        "ds_write_b128 %[aw], v[168:171] offset:32256\n\t"
        "buffer_load_dwordx4 v[168:171], v243, %[ra], s65 offen\n\t"
        "s_waitcnt vmcnt(23)\n\t"
        "ds_write_b128 %[bw], v[172:175] offset:0\n\t"
        "buffer_load_dwordx4 v[172:175], v244, %[rb], s66 offen\n\t"
        "s_waitcnt vmcnt(23)\n\t"
        "ds_write_b128 %[bw], v[176:179] offset:5120\n\t"
        "buffer_load_dwordx4 v[176:179], v245, %[rb], s66 offen\n\t"
        "s_waitcnt vmcnt(23)\n\t"
        "ds_write_b128 %[bw], v[180:183] offset:10240\n\t"
        "buffer_load_dwordx4 v[180:183], v246, %[rb], s66 offen\n\t"
        "s_waitcnt vmcnt(23)\n\t"
        "ds_write_b128 %[bw], v[184:187] offset:15360\n\t"
        "buffer_load_dwordx4 v[184:187], v247, %[rb], s66 offen\n\t"
        "s_waitcnt lgkmcnt(0)\n\t"
        "s_barrier\n\t"
        ".p2align 6\n\t"
        "LG_%=:\n\t"
        "s_add_u32 s67, s64, 3\n\t"
        "s_min_u32 s67, s67, s68\n\t"
        "s_lshl_b32 s65, s67, 7\n\t"
        "s_lshl_b32 s66, s67, 19\n\t"
        "v_mfma_f32_32x32x16_bf16 %[c0], v[132:135], v[108:111], %[c0]\n\t"
        "ds_read_b64_tr_b16 v[124:125], %[brd] offset:0\n\t"
        "ds_read_b64_tr_b16 v[126:127], %[brd] offset:1280\n\t"
        "v_mfma_f32_32x32x16_bf16 %[c1], v[136:139], v[108:111], %[c1]\n\t"
        "ds_read_b128 v[92:95], %[ard] offset:0\n\t"
        "ds_read_b64_tr_b16 v[128:129], %[brd] offset:64\n\t"
        "v_mfma_f32_32x32x16_bf16 %[c2], v[132:135], v[112:115], %[c2]\n\t"
        "ds_read_b64_tr_b16 v[130:131], %[brd] offset:1344\n\t"
        "ds_read_b128 v[96:99], %[ard] offset:4608\n\t"
        "v_mfma_f32_32x32x16_bf16 %[c3], v[136:139], v[112:115], %[c3]\n\t"
        "ds_read_b128 v[100:103], %[ard] offset:9216\n\t"
        "ds_read_b128 v[104:107], %[ard] offset:13824\n\t"
        "v_mfma_f32_32x32x16_bf16 %[c4], v[132:135], v[116:119], %[c4]\n\t"
        "s_waitcnt vmcnt(23)\n\t"
        "ds_write_b128 v90, v[188:191] offset:0\n\t"
        "buffer_load_dwordx4 v[188:191], v236, %[ra], s65 offen\n\t"
        "v_mfma_f32_32x32x16_bf16 %[c5], v[136:139], v[116:119], %[c5]\n\t"
        "s_waitcnt vmcnt(23)\n\t"
        "ds_write_b128 v90, v[192:195] offset:4608\n\t"
        "buffer_load_dwordx4 v[192:195], v237, %[ra], s65 offen\n\t"
        "v_mfma_f32_32x32x16_bf16 %[c6], v[132:135], v[120:123], %[c6]\n\t"
        "s_waitcnt vmcnt(23)\n\t"
        "ds_write_b128 v90, v[196:199] offset:9216\n\t"
        "buffer_load_dwordx4 v[196:199], v238, %[ra], s65 offen\n\t"
        "v_mfma_f32_32x32x16_bf16 %[c7], v[136:139], v[120:123], %[c7]\n\t"
        "s_waitcnt vmcnt(23)\n\t"
        "ds_write_b128 v90, v[200:203] offset:13824\n\t"
        "buffer_load_dwordx4 v[200:203], v239, %[ra], s65 offen\n\t"
        "s_waitcnt lgkmcnt(9)\n\t"
        "v_mfma_f32_32x32x16_bf16 %[c0], v[124:127], v[92:95], %[c0]\n\t"
        "ds_read_b64_tr_b16 v[132:133], %[brd] offset:5120\n\t"
        "ds_read_b64_tr_b16 v[134:135], %[brd] offset:6400\n\t"
        "s_waitcnt lgkmcnt(9)\n\t"
        "v_mfma_f32_32x32x16_bf16 %[c1], v[128:131], v[92:95], %[c1]\n\t"
        "ds_read_b128 v[108:111], %[ard] offset:32\n\t"
        "ds_read_b64_tr_b16 v[136:137], %[brd] offset:5184\n\t"
        "s_waitcnt lgkmcnt(10)\n\t"
        "v_mfma_f32_32x32x16_bf16 %[c2], v[124:127], v[96:99], %[c2]\n\t"
        "ds_read_b64_tr_b16 v[138:139], %[brd] offset:6464\n\t"
        "ds_read_b128 v[112:115], %[ard] offset:4640\n\t"
        "v_mfma_f32_32x32x16_bf16 %[c3], v[128:131], v[96:99], %[c3]\n\t"
        "ds_read_b128 v[116:119], %[ard] offset:9248\n\t"
        "ds_read_b128 v[120:123], %[ard] offset:13856\n\t"
        "s_waitcnt lgkmcnt(13)\n\t"
        "v_mfma_f32_32x32x16_bf16 %[c4], v[124:127], v[100:103], %[c4]\n\t"
        "s_waitcnt vmcnt(23)\n\t"
        "ds_write_b128 v90, v[204:207] offset:18432\n\t"
        "buffer_load_dwordx4 v[204:207], v240, %[ra], s65 offen\n\t"
        "v_mfma_f32_32x32x16_bf16 %[c5], v[128:131], v[100:103], %[c5]\n\t"
        "s_waitcnt vmcnt(23)\n\t"
        "ds_write_b128 v90, v[208:211] offset:23040\n\t"
        "buffer_load_dwordx4 v[208:211], v241, %[ra], s65 offen\n\t"
        "s_waitcnt lgkmcnt(14)\n\t"
        "v_mfma_f32_32x32x16_bf16 %[c6], v[124:127], v[104:107], %[c6]\n\t"
        "s_waitcnt vmcnt(23)\n\t"
        "ds_write_b128 v90, v[212:215] offset:27648\n\t"
        "buffer_load_dwordx4 v[212:215], v242, %[ra], s65 offen\n\t"
        "v_mfma_f32_32x32x16_bf16 %[c7], v[128:131], v[104:107], %[c7]\n\t"
        "s_waitcnt vmcnt(23)\n\t"
        "ds_write_b128 v90, v[216:219] offset:32256\n\t"
        "buffer_load_dwordx4 v[216:219], v243, %[ra], s65 offen\n\t"
        "s_waitcnt lgkmcnt(9)\n\t"
        "v_mfma_f32_32x32x16_bf16 %[c0], v[132:135], v[108:111], %[c0]\n\t"
        "ds_read_b64_tr_b16 v[124:125], %[brd] offset:10240\n\t"
        "ds_read_b64_tr_b16 v[126:127], %[brd] offset:11520\n\t"
        "s_waitcnt lgkmcnt(9)\n\t"
        "v_mfma_f32_32x32x16_bf16 %[c1], v[136:139], v[108:111], %[c1]\n\t"
        "ds_read_b128 v[92:95], %[ard] offset:64\n\t"
        "ds_read_b64_tr_b16 v[128:129], %[brd] offset:10304\n\t"
        "s_waitcnt lgkmcnt(10)\n\t"
        "v_mfma_f32_32x32x16_bf16 %[c2], v[132:135], v[112:115], %[c2]\n\t"
        "ds_read_b64_tr_b16 v[130:131], %[brd] offset:11584\n\t"
        "ds_read_b128 v[96:99], %[ard] offset:4672\n\t"
        "v_mfma_f32_32x32x16_bf16 %[c3], v[136:139], v[112:115], %[c3]\n\t"
        "ds_read_b128 v[100:103], %[ard] offset:9280\n\t"
        "ds_read_b128 v[104:107], %[ard] offset:13888\n\t"
        "s_waitcnt lgkmcnt(13)\n\t"
        "v_mfma_f32_32x32x16_bf16 %[c4], v[132:135], v[116:119], %[c4]\n\t"
        "s_waitcnt vmcnt(23)\n\t"
        "ds_write_b128 v91, v[220:223] offset:0\n\t"
        "buffer_load_dwordx4 v[220:223], v244, %[rb], s66 offen\n\t"
        "v_mfma_f32_32x32x16_bf16 %[c5], v[136:139], v[116:119], %[c5]\n\t"
        "s_waitcnt vmcnt(23)\n\t"
        "ds_write_b128 v91, v[224:227] offset:5120\n\t"
        "buffer_load_dwordx4 v[224:227], v245, %[rb], s66 offen\n\t"
        "s_waitcnt lgkmcnt(14)\n\t"
        "v_mfma_f32_32x32x16_bf16 %[c6], v[132:135], v[120:123], %[c6]\n\t"
        "s_waitcnt vmcnt(23)\n\t"
        "ds_write_b128 v91, v[228:231] offset:10240\n\t"
        "buffer_load_dwordx4 v[228:231], v246, %[rb], s66 offen\n\t"
        "v_mfma_f32_32x32x16_bf16 %[c7], v[136:139], v[120:123], %[c7]\n\t"
        "s_waitcnt vmcnt(23)\n\t"
        "ds_write_b128 v91, v[232:235] offset:15360\n\t"
        "buffer_load_dwordx4 v[232:235], v247, %[rb], s66 offen\n\t"
        "s_waitcnt lgkmcnt(9)\n\t"
        "v_mfma_f32_32x32x16_bf16 %[c0], v[124:127], v[92:95], %[c0]\n\t"
        "ds_read_b64_tr_b16 v[132:133], %[brd] offset:15360\n\t"
        "ds_read_b64_tr_b16 v[134:135], %[brd] offset:16640\n\t"
        "s_waitcnt lgkmcnt(9)\n\t"
        "v_mfma_f32_32x32x16_bf16 %[c1], v[128:131], v[92:95], %[c1]\n\t"
        "ds_read_b128 v[108:111], %[ard] offset:96\n\t"
        "ds_read_b64_tr_b16 v[136:137], %[brd] offset:15424\n\t"
        "s_waitcnt lgkmcnt(10)\n\t"
        "v_mfma_f32_32x32x16_bf16 %[c2], v[124:127], v[96:99], %[c2]\n\t"
        "ds_read_b64_tr_b16 v[138:139], %[brd] offset:16704\n\t"
        "ds_read_b128 v[112:115], %[ard] offset:4704\n\t"
        "v_mfma_f32_32x32x16_bf16 %[c3], v[128:131], v[96:99], %[c3]\n\t"
        "ds_read_b128 v[116:119], %[ard] offset:9312\n\t"
        "ds_read_b128 v[120:123], %[ard] offset:13920\n\t"
        "s_waitcnt lgkmcnt(13)\n\t"
        "v_mfma_f32_32x32x16_bf16 %[c4], v[124:127], v[100:103], %[c4]\n\t"
        "v_mfma_f32_32x32x16_bf16 %[c5], v[128:131], v[100:103], %[c5]\n\t"
        "s_waitcnt lgkmcnt(12)\n\t"
        "v_mfma_f32_32x32x16_bf16 %[c6], v[124:127], v[104:107], %[c6]\n\t"
        "v_mfma_f32_32x32x16_bf16 %[c7], v[128:131], v[104:107], %[c7]\n\t"
        "s_add_u32 s64, s64, 1\n\t"
        "s_cmp_lt_u32 s64, %[nk]\n\t"
        "s_waitcnt lgkmcnt(0)\n\t"
        "s_barrier\n\t"
        "s_cbranch_scc0 LE_%=\n\t"
        "s_add_u32 s67, s64, 3\n\t"
        "s_min_u32 s67, s67, s68\n\t"
        "s_lshl_b32 s65, s67, 7\n\t"
        "s_lshl_b32 s66, s67, 19\n\t"
        "v_mfma_f32_32x32x16_bf16 %[c0], v[132:135], v[108:111], %[c0]\n\t"
        "ds_read_b64_tr_b16 v[124:125], v89 offset:0\n\t"
        "ds_read_b64_tr_b16 v[126:127], v89 offset:1280\n\t"
        "v_mfma_f32_32x32x16_bf16 %[c1], v[136:139], v[108:111], %[c1]\n\t"
        "ds_read_b128 v[92:95], v88 offset:0\n\t"
        "ds_read_b64_tr_b16 v[128:129], v89 offset:64\n\t"
        "v_mfma_f32_32x32x16_bf16 %[c2], v[132:135], v[112:115], %[c2]\n\t"
        "ds_read_b64_tr_b16 v[130:131], v89 offset:1344\n\t"
        "ds_read_b128 v[96:99], v88 offset:4608\n\t"
        "v_mfma_f32_32x32x16_bf16 %[c3], v[136:139], v[112:115], %[c3]\n\t"
        "ds_read_b128 v[100:103], v88 offset:9216\n\t"
        "ds_read_b128 v[104:107], v88 offset:13824\n\t"
        "v_mfma_f32_32x32x16_bf16 %[c4], v[132:135], v[116:119], %[c4]\n\t"
        "s_waitcnt vmcnt(23)\n\t"
        "ds_write_b128 %[aw], v[140:143] offset:0\n\t"
        "buffer_load_dwordx4 v[140:143], v236, %[ra], s65 offen\n\t"
        "v_mfma_f32_32x32x16_bf16 %[c5], v[136:139], v[116:119], %[c5]\n\t"
        "s_waitcnt vmcnt(23)\n\t"
        "ds_write_b128 %[aw], v[144:147] offset:4608\n\t"
        "buffer_load_dwordx4 v[144:147], v237, %[ra], s65 offen\n\t"
        "v_mfma_f32_32x32x16_bf16 %[c6], v[132:135], v[120:123], %[c6]\n\t"
        "s_waitcnt vmcnt(23)\n\t"
        "ds_write_b128 %[aw], v[148:151] offset:9216\n\t"
        "buffer_load_dwordx4 v[148:151], v238, %[ra], s65 offen\n\t"
        "v_mfma_f32_32x32x16_bf16 %[c7], v[136:139], v[120:123], %[c7]\n\t"
        "s_waitcnt vmcnt(23)\n\t"
        "ds_write_b128 %[aw], v[152:155] offset:13824\n\t"
        "buffer_load_dwordx4 v[152:155], v239, %[ra], s65 offen\n\t"
        "s_waitcnt lgkmcnt(9)\n\t"
        "v_mfma_f32_32x32x16_bf16 %[c0], v[124:127], v[92:95], %[c0]\n\t"
        "ds_read_b64_tr_b16 v[132:133], v89 offset:5120\n\t"
        "ds_read_b64_tr_b16 v[134:135], v89 offset:6400\n\t"
        "s_waitcnt lgkmcnt(9)\n\t"
        "v_mfma_f32_32x32x16_bf16 %[c1], v[128:131], v[92:95], %[c1]\n\t"
        "ds_read_b128 v[108:111], v88 offset:32\n\t"
        "ds_read_b64_tr_b16 v[136:137], v89 offset:5184\n\t"
        "s_waitcnt lgkmcnt(10)\n\t"
        "v_mfma_f32_32x32x16_bf16 %[c2], v[124:127], v[96:99], %[c2]\n\t"
        "ds_read_b64_tr_b16 v[138:139], v89 offset:6464\n\t"
        "ds_read_b128 v[112:115], v88 offset:4640\n\t"
        "v_mfma_f32_32x32x16_bf16 %[c3], v[128:131], v[96:99], %[c3]\n\t"
        "ds_read_b128 v[116:119], v88 offset:9248\n\t"
        "ds_read_b128 v[120:123], v88 offset:13856\n\t"
        "s_waitcnt lgkmcnt(13)\n\t"
        "v_mfma_f32_32x32x16_bf16 %[c4], v[124:127], v[100:103], %[c4]\n\t"
        "s_waitcnt vmcnt(23)\n\t"
        "ds_write_b128 %[aw], v[156:159] offset:18432\n\t"
        "buffer_load_dwordx4 v[156:159], v240, %[ra], s65 offen\n\t"
        "v_mfma_f32_32x32x16_bf16 %[c5], v[128:131], v[100:103], %[c5]\n\t"
        "s_waitcnt vmcnt(23)\n\t"
        "ds_write_b128 %[aw], v[160:163] offset:23040\n\t"
        "buffer_load_dwordx4 v[160:163], v241, %[ra], s65 offen\n\t"
        "s_waitcnt lgkmcnt(14)\n\t"
        "v_mfma_f32_32x32x16_bf16 %[c6], v[124:127], v[104:107], %[c6]\n\t"
        "s_waitcnt vmcnt(23)\n\t"
        "ds_write_b128 %[aw], v[164:167] offset:27648\n\t"
        "buffer_load_dwordx4 v[164:167], v242, %[ra], s65 offen\n\t"
        "v_mfma_f32_32x32x16_bf16 %[c7], v[128:131], v[104:107], %[c7]\n\t"
        "s_waitcnt vmcnt(23)\n\t"
        "ds_write_b128 %[aw], v[168:171] offset:32256\n\t"
        "buffer_load_dwordx4 v[168:171], v243, %[ra], s65 offen\n\t"
        "s_waitcnt lgkmcnt(9)\n\t"
        "v_mfma_f32_32x32x16_bf16 %[c0], v[132:135], v[108:111], %[c0]\n\t"
        "ds_read_b64_tr_b16 v[124:125], v89 offset:10240\n\t"
        "ds_read_b64_tr_b16 v[126:127], v89 offset:11520\n\t"
        "s_waitcnt lgkmcnt(9)\n\t"
        "v_mfma_f32_32x32x16_bf16 %[c1], v[136:139], v[108:111], %[c1]\n\t"
        "ds_read_b128 v[92:95], v88 offset:64\n\t"
        "ds_read_b64_tr_b16 v[128:129], v89 offset:10304\n\t"
        "s_waitcnt lgkmcnt(10)\n\t"
        "v_mfma_f32_32x32x16_bf16 %[c2], v[132:135], v[112:115], %[c2]\n\t"
        "ds_read_b64_tr_b16 v[130:131], v89 offset:11584\n\t"
        "ds_read_b128 v[96:99], v88 offset:4672\n\t"
        "v_mfma_f32_32x32x16_bf16 %[c3], v[136:139], v[112:115], %[c3]\n\t"
        "ds_read_b128 v[100:103], v88 offset:9280\n\t"
        "ds_read_b128 v[104:107], v88 offset:13888\n\t"
        "s_waitcnt lgkmcnt(13)\n\t"
        "v_mfma_f32_32x32x16_bf16 %[c4], v[132:135], v[116:119], %[c4]\n\t"
        "s_waitcnt vmcnt(23)\n\t"
        "ds_write_b128 %[bw], v[172:175] offset:0\n\t"
        "buffer_load_dwordx4 v[172:175], v244, %[rb], s66 offen\n\t"
        "v_mfma_f32_32x32x16_bf16 %[c5], v[136:139], v[116:119], %[c5]\n\t"
        "s_waitcnt vmcnt(23)\n\t"
        "ds_write_b128 %[bw], v[176:179] offset:5120\n\t"
        "buffer_load_dwordx4 v[176:179], v245, %[rb], s66 offen\n\t"
        "s_waitcnt lgkmcnt(14)\n\t"
        "v_mfma_f32_32x32x16_bf16 %[c6], v[132:135], v[120:123], %[c6]\n\t"
        "s_waitcnt vmcnt(23)\n\t"
        "ds_write_b128 %[bw], v[180:183] offset:10240\n\t"
        "buffer_load_dwordx4 v[180:183], v246, %[rb], s66 offen\n\t"
        "v_mfma_f32_32x32x16_bf16 %[c7], v[136:139], v[120:123], %[c7]\n\t"
        "s_waitcnt vmcnt(23)\n\t"
        "ds_write_b128 %[bw], v[184:187] offset:15360\n\t"
        "buffer_load_dwordx4 v[184:187], v247, %[rb], s66 offen\n\t"
        "s_waitcnt lgkmcnt(9)\n\t"
        "v_mfma_f32_32x32x16_bf16 %[c0], v[124:127], v[92:95], %[c0]\n\t"
        "ds_read_b64_tr_b16 v[132:133], v89 offset:15360\n\t"
        "ds_read_b64_tr_b16 v[134:135], v89 offset:16640\n\t"
        "s_waitcnt lgkmcnt(9)\n\t"
        "v_mfma_f32_32x32x16_bf16 %[c1], v[128:131], v[92:95], %[c1]\n\t"
        "ds_read_b128 v[108:111], v88 offset:96\n\t"
        "ds_read_b64_tr_b16 v[136:137], v89 offset:15424\n\t"
        "s_waitcnt lgkmcnt(10)\n\t"
        "v_mfma_f32_32x32x16_bf16 %[c2], v[124:127], v[96:99], %[c2]\n\t"
        "ds_read_b64_tr_b16 v[138:139], v89 offset:16704\n\t"
        "ds_read_b128 v[112:115], v88 offset:4704\n\t"
        "v_mfma_f32_32x32x16_bf16 %[c3], v[128:131], v[96:99], %[c3]\n\t"
        "ds_read_b128 v[116:119], v88 offset:9312\n\t"
        "ds_read_b128 v[120:123], v88 offset:13920\n\t"
        "s_waitcnt lgkmcnt(13)\n\t"
        "v_mfma_f32_32x32x16_bf16 %[c4], v[124:127], v[100:103], %[c4]\n\t"
        "v_mfma_f32_32x32x16_bf16 %[c5], v[128:131], v[100:103], %[c5]\n\t"
        "s_waitcnt lgkmcnt(12)\n\t"
        "v_mfma_f32_32x32x16_bf16 %[c6], v[124:127], v[104:107], %[c6]\n\t"
        "v_mfma_f32_32x32x16_bf16 %[c7], v[128:131], v[104:107], %[c7]\n\t"
        "s_add_u32 s64, s64, 1\n\t"
        "s_cmp_lt_u32 s64, %[nk]\n\t"
        "s_waitcnt lgkmcnt(0)\n\t"
        "s_barrier\n\t"
        "s_cbranch_scc1 LG_%=\n\t"
        "LE_%=:\n\t"
        "v_mfma_f32_32x32x16_bf16 %[c0], v[132:135], v[108:111], %[c0]\n\t"
        "v_mfma_f32_32x32x16_bf16 %[c1], v[136:139], v[108:111], %[c1]\n\t"
        "v_mfma_f32_32x32x16_bf16 %[c2], v[132:135], v[112:115], %[c2]\n\t"
        "v_mfma_f32_32x32x16_bf16 %[c3], v[136:139], v[112:115], %[c3]\n\t"
        "v_mfma_f32_32x32x16_bf16 %[c4], v[132:135], v[116:119], %[c4]\n\t"
        "v_mfma_f32_32x32x16_bf16 %[c5], v[136:139], v[116:119], %[c5]\n\t"
        "v_mfma_f32_32x32x16_bf16 %[c6], v[132:135], v[120:123], %[c6]\n\t"
        "v_mfma_f32_32x32x16_bf16 %[c7], v[136:139], v[120:123], %[c7]\n\t"
        "s_waitcnt vmcnt(0)\n\t"
        "s_nop 15\n\t"
        "s_nop 3\n\t"
        : [c0] "=&a"(c[0][0]), [c1] "=&a"(c[0][1]), [c2] "=&a"(c[1][0]), [c3] "=&a"(c[1][1]), [c4] "=&a"(c[2][0]),
          [c5] "=&a"(c[2][1]), [c6] "=&a"(c[3][0]), [c7] "=&a"(c[3][1])
        : [ra] "s"(ra), [rb] "s"(rb), [astr] "s"(astr), [nk] "s"(nk), [aoff] "v"(aoff), [boff] "v"(boff),
          [ard] "v"(ard), [brd] "v"(brd), [aw] "v"(aw), [bw] "v"(bw)
        :
          "v88", "v89", "v90", "v91", "v92", "v93", "v94", "v95", "v96", "v97", "v98", "v99", "v100", "v101",
          "v102", "v103", "v104", "v105", "v106", "v107", "v108", "v109", "v110", "v111", "v112", "v113", "v114",
          "v115", "v116", "v117", "v118", "v119", "v120", "v121", "v122", "v123", "v124", "v125", "v126", "v127",
          "v128", "v129", "v130", "v131", "v132", "v133", "v134", "v135", "v136", "v137", "v138", "v139", "v140",
          "v141", "v142", "v143", "v144", "v145", "v146", "v147", "v148", "v149", "v150", "v151", "v152", "v153",
          "v154", "v155", "v156", "v157", "v158", "v159", "v160", "v161", "v162", "v163", "v164", "v165", "v166",
          "v167", "v168", "v169", "v170", "v171", "v172", "v173", "v174", "v175", "v176", "v177", "v178", "v179",
          "v180", "v181", "v182", "v183", "v184", "v185", "v186", "v187", "v188", "v189", "v190", "v191", "v192",
          "v193", "v194", "v195", "v196", "v197", "v198", "v199", "v200", "v201", "v202", "v203", "v204", "v205",
          "v206", "v207", "v208", "v209", "v210", "v211", "v212", "v213", "v214", "v215", "v216", "v217", "v218",
          "v219", "v220", "v221", "v222", "v223", "v224", "v225", "v226", "v227", "v228", "v229", "v230", "v231",
          "v232", "v233", "v234", "v235", "v236", "v237", "v238", "v239", "v240", "v241", "v242", "v243", "v244",
          "v245", "v246", "v247", "s64", "s65", "s66", "s67", "s68", "s69", "scc", "memory" );
}
__device__ __forceinline__ void gemm1_asm(f32x16 (&c)[4][2], __amdgpu_buffer_rsrc_t ra, __amdgpu_buffer_rsrc_t rb,
                                          unsigned astr, unsigned nk, unsigned aoff, unsigned boff, unsigned ard,
                                          unsigned brd, unsigned aw, unsigned bw) {
    asm volatile(
        "v_mov_b32_e32 v236, %[aoff]\n\t"
        "v_add_u32_e32 v237, %[astr], v236\n\t"
        "v_add_u32_e32 v238, %[astr], v237\n\t"
        "v_add_u32_e32 v239, %[astr], v238\n\t"
        "v_add_u32_e32 v240, %[astr], v239\n\t"
        "v_add_u32_e32 v241, %[astr], v240\n\t"
        "v_add_u32_e32 v242, %[astr], v241\n\t"
        "v_add_u32_e32 v243, %[astr], v242\n\t"
        "v_mov_b32_e32 v244, %[boff]\n\t"
        "v_add_u32_e32 v245, 0x20000, v244\n\t"
        "v_add_u32_e32 v246, 0x20000, v245\n\t"
        "v_add_u32_e32 v247, 0x20000, v246\n\t"
        "s_sub_u32 s68, %[nk], 1\n\t"
        "s_mov_b32 s64, 0\n\t"
        "s_mov_b32 s65, 0\n\t"
        "s_mov_b32 s66, 0\n\t"
        "buffer_load_dwordx4 v[140:143], v236, %[ra], s65 offen\n\t"
        "buffer_load_dwordx4 v[144:147], v237, %[ra], s65 offen\n\t"
        "buffer_load_dwordx4 v[148:151], v238, %[ra], s65 offen\n\t"
        "buffer_load_dwordx4 v[152:155], v239, %[ra], s65 offen\n\t"
        "buffer_load_dwordx4 v[156:159], v240, %[ra], s65 offen\n\t"
        "buffer_load_dwordx4 v[160:163], v241, %[ra], s65 offen\n\t"
        "buffer_load_dwordx4 v[164:167], v242, %[ra], s65 offen\n\t"
        "buffer_load_dwordx4 v[168:171], v243, %[ra], s65 offen\n\t"
        "buffer_load_dwordx4 v[172:175], v244, %[rb], s66 offen\n\t"
        "buffer_load_dwordx4 v[176:179], v245, %[rb], s66 offen\n\t"
        "buffer_load_dwordx4 v[180:183], v246, %[rb], s66 offen\n\t"
        "buffer_load_dwordx4 v[184:187], v247, %[rb], s66 offen\n\t"
        "s_movk_i32 s65, 0x80\n\t"
        "s_mov_b32 s66, 0x80000\n\t"
        "buffer_load_dwordx4 v[188:191], v236, %[ra], s65 offen\n\t"
        "buffer_load_dwordx4 v[192:195], v237, %[ra], s65 offen\n\t"
        "buffer_load_dwordx4 v[196:199], v238, %[ra], s65 offen\n\t"
        "buffer_load_dwordx4 v[200:203], v239, %[ra], s65 offen\n\t"
        "buffer_load_dwordx4 v[204:207], v240, %[ra], s65 offen\n\t"
        "buffer_load_dwordx4 v[208:211], v241, %[ra], s65 offen\n\t"
        "buffer_load_dwordx4 v[212:215], v242, %[ra], s65 offen\n\t"
        "buffer_load_dwordx4 v[216:219], v243, %[ra], s65 offen\n\t"
        "buffer_load_dwordx4 v[220:223], v244, %[rb], s66 offen\n\t"
        "buffer_load_dwordx4 v[224:227], v245, %[rb], s66 offen\n\t"
        "buffer_load_dwordx4 v[228:231], v246, %[rb], s66 offen\n\t"
        "buffer_load_dwordx4 v[232:235], v247, %[rb], s66 offen\n\t"
        "v_add_u32_e32 v88, 57344, %[ard]\n\t"
        "v_add_u32_e32 v89, 57344, %[brd]\n\t"
        "v_add_u32_e32 v90, 57344, %[aw]\n\t"
        "v_add_u32_e32 v91, 57344, %[bw]\n\t"
        "v_mov_b32_e32 v108, 0\n\t"
        "v_mov_b32_e32 v109, 0\n\t"
        "v_mov_b32_e32 v110, 0\n\t"
        "v_mov_b32_e32 v111, 0\n\t"
        "v_mov_b32_e32 v112, 0\n\t"
        "v_mov_b32_e32 v113, 0\n\t"
        "v_mov_b32_e32 v114, 0\n\t"
        "v_mov_b32_e32 v115, 0\n\t"
        "v_mov_b32_e32 v116, 0\n\t"
        "v_mov_b32_e32 v117, 0\n\t"
        "v_mov_b32_e32 v118, 0\n\t"
        "v_mov_b32_e32 v119, 0\n\t"
        "v_mov_b32_e32 v120, 0\n\t"
        "v_mov_b32_e32 v121, 0\n\t"
        "v_mov_b32_e32 v122, 0\n\t"
        "v_mov_b32_e32 v123, 0\n\t"
        "v_mov_b32_e32 v132, 0\n\t"
        "v_mov_b32_e32 v133, 0\n\t"
        "v_mov_b32_e32 v134, 0\n\t"
        "v_mov_b32_e32 v135, 0\n\t"
        "v_mov_b32_e32 v136, 0\n\t"
        "v_mov_b32_e32 v137, 0\n\t"
        "v_mov_b32_e32 v138, 0\n\t"
        "v_mov_b32_e32 v139, 0\n\t"
        "s_nop 1\n\t"
        "v_mfma_f32_32x32x16_bf16 %[c0], v[108:111], v[132:135], 0\n\t"
        "v_mfma_f32_32x32x16_bf16 %[c1], v[108:111], v[136:139], 0\n\t"
        "v_mfma_f32_32x32x16_bf16 %[c2], v[112:115], v[132:135], 0\n\t"
        "v_mfma_f32_32x32x16_bf16 %[c3], v[112:115], v[136:139], 0\n\t"
        "v_mfma_f32_32x32x16_bf16 %[c4], v[116:119], v[132:135], 0\n\t"
        "v_mfma_f32_32x32x16_bf16 %[c5], v[116:119], v[136:139], 0\n\t"
        "v_mfma_f32_32x32x16_bf16 %[c6], v[120:123], v[132:135], 0\n\t"
        "v_mfma_f32_32x32x16_bf16 %[c7], v[120:123], v[136:139], 0\n\t"
        "s_movk_i32 s65, 0x100\n\t"
        "s_mov_b32 s66, 0x100000\n\t"
        "s_waitcnt vmcnt(23)\n\t"
        "ds_write_b128 %[aw], v[140:143] offset:0\n\t"
        "buffer_load_dwordx4 v[140:143], v236, %[ra], s65 offen\n\t"
        "s_waitcnt vmcnt(23)\n\t"
        "ds_write_b128 %[aw], v[144:147] offset:4608\n\t"
        "buffer_load_dwordx4 v[144:147], v237, %[ra], s65 offen\n\t"
        "s_waitcnt vmcnt(23)\n\t"
        "ds_write_b128 %[aw], v[148:151] offset:9216\n\t"
        "buffer_load_dwordx4 v[148:151], v238, %[ra], s65 offen\n\t"
        "s_waitcnt vmcnt(23)\n\t"
        "ds_write_b128 %[aw], v[152:155] offset:13824\n\t"
        "buffer_load_dwordx4 v[152:155], v239, %[ra], s65 offen\n\t"
        "s_waitcnt vmcnt(23)\n\t"
        "ds_write_b128 %[aw], v[156:159] offset:18432\n\t"
        "buffer_load_dwordx4 v[156:159], v240, %[ra], s65 offen\n\t"
        "s_waitcnt vmcnt(23)\n\t"
        "ds_write_b128 %[aw], v[160:163] offset:23040\n\t"
        "buffer_load_dwordx4 v[160:163], v241, %[ra], s65 offen\n\t"
        "s_waitcnt vmcnt(23)\n\t"
        "ds_write_b128 %[aw], v[164:167] offset:27648\n\t"
        "buffer_load_dwordx4 v[164:167], v242, %[ra], s65 offen\n\t"
        "s_waitcnt vmcnt(23)\n\t"
        "ds_write_b128 %[aw], v[168:171] offset:32256\n\t"
        "buffer_load_dwordx4 v[168:171], v243, %[ra], s65 offen\n\t"
        "s_waitcnt vmcnt(23)\n\t"
        "ds_write_b128 %[bw], v[172:175] offset:0\n\t"
        "buffer_load_dwordx4 v[172:175], v244, %[rb], s66 offen\n\t"
        "s_waitcnt vmcnt(23)\n\t"
        "ds_write_b128 %[bw], v[176:179] offset:5120\n\t"
        "buffer_load_dwordx4 v[176:179], v245, %[rb], s66 offen\n\t"
        "s_waitcnt vmcnt(23)\n\t"
        "ds_write_b128 %[bw], v[180:183] offset:10240\n\t"
        "buffer_load_dwordx4 v[180:183], v246, %[rb], s66 offen\n\t"
        "s_waitcnt vmcnt(23)\n\t"
        "ds_write_b128 %[bw], v[184:187] offset:15360\n\t"
        "buffer_load_dwordx4 v[184:187], v247, %[rb], s66 offen\n\t"
        "s_waitcnt lgkmcnt(0)\n\t"
        "s_barrier\n\t"
        ".p2align 6\n\t"
        "LG_%=:\n\t"
        "s_add_u32 s67, s64, 3\n\t"
        "s_min_u32 s67, s67, s68\n\t"
        "s_lshl_b32 s65, s67, 7\n\t"
        "s_lshl_b32 s66, s67, 19\n\t"
        "v_mfma_f32_32x32x16_bf16 %[c0], v[108:111], v[132:135], %[c0]\n\t"
        "ds_read_b64_tr_b16 v[124:125], %[brd] offset:0\n\t"
        "ds_read_b64_tr_b16 v[126:127], %[brd] offset:1280\n\t"
        "v_mfma_f32_32x32x16_bf16 %[c1], v[108:111], v[136:139], %[c1]\n\t"
        "ds_read_b128 v[92:95], %[ard] offset:0\n\t"
        "ds_read_b64_tr_b16 v[128:129], %[brd] offset:64\n\t"
        "v_mfma_f32_32x32x16_bf16 %[c2], v[112:115], v[132:135], %[c2]\n\t"
        "ds_read_b64_tr_b16 v[130:131], %[brd] offset:1344\n\t"
        "ds_read_b128 v[96:99], %[ard] offset:4608\n\t"
        "v_mfma_f32_32x32x16_bf16 %[c3], v[112:115], v[136:139], %[c3]\n\t"
        "ds_read_b128 v[100:103], %[ard] offset:9216\n\t"
        "ds_read_b128 v[104:107], %[ard] offset:13824\n\t"
        "v_mfma_f32_32x32x16_bf16 %[c4], v[116:119], v[132:135], %[c4]\n\t"
        "s_waitcnt vmcnt(23)\n\t"
        "ds_write_b128 v90, v[188:191] offset:0\n\t"
        "buffer_load_dwordx4 v[188:191], v236, %[ra], s65 offen\n\t"
        "v_mfma_f32_32x32x16_bf16 %[c5], v[116:119], v[136:139], %[c5]\n\t"
        "s_waitcnt vmcnt(23)\n\t"
        "ds_write_b128 v90, v[192:195] offset:4608\n\t"
        "buffer_load_dwordx4 v[192:195], v237, %[ra], s65 offen\n\t"
        "v_mfma_f32_32x32x16_bf16 %[c6], v[120:123], v[132:135], %[c6]\n\t"
        "s_waitcnt vmcnt(23)\n\t"
        "ds_write_b128 v90, v[196:199] offset:9216\n\t"
        "buffer_load_dwordx4 v[196:199], v238, %[ra], s65 offen\n\t"
        "v_mfma_f32_32x32x16_bf16 %[c7], v[120:123], v[136:139], %[c7]\n\t"
        "s_waitcnt vmcnt(23)\n\t"
        "ds_write_b128 v90, v[200:203] offset:13824\n\t"
        "buffer_load_dwordx4 v[200:203], v239, %[ra], s65 offen\n\t"
        "s_waitcnt lgkmcnt(9)\n\t"
        "v_mfma_f32_32x32x16_bf16 %[c0], v[92:95], v[124:127], %[c0]\n\t"
        "ds_read_b64_tr_b16 v[132:133], %[brd] offset:5120\n\t"
        "ds_read_b64_tr_b16 v[134:135], %[brd] offset:6400\n\t"
        "s_waitcnt lgkmcnt(9)\n\t"
        "v_mfma_f32_32x32x16_bf16 %[c1], v[92:95], v[128:131], %[c1]\n\t"
        "ds_read_b128 v[108:111], %[ard] offset:32\n\t"
        "ds_read_b64_tr_b16 v[136:137], %[brd] offset:5184\n\t"
        "s_waitcnt lgkmcnt(10)\n\t"
        "v_mfma_f32_32x32x16_bf16 %[c2], v[96:99], v[124:127], %[c2]\n\t"
        "ds_read_b64_tr_b16 v[138:139], %[brd] offset:6464\n\t"
        "ds_read_b128 v[112:115], %[ard] offset:4640\n\t"
        "v_mfma_f32_32x32x16_bf16 %[c3], v[96:99], v[128:131], %[c3]\n\t"
        "ds_read_b128 v[116:119], %[ard] offset:9248\n\t"
        "ds_read_b128 v[120:123], %[ard] offset:13856\n\t"
        "s_waitcnt lgkmcnt(13)\n\t"
        "v_mfma_f32_32x32x16_bf16 %[c4], v[100:103], v[124:127], %[c4]\n\t"
        "s_waitcnt vmcnt(23)\n\t"
        "ds_write_b128 v90, v[204:207] offset:18432\n\t"
        "buffer_load_dwordx4 v[204:207], v240, %[ra], s65 offen\n\t"
        "v_mfma_f32_32x32x16_bf16 %[c5], v[100:103], v[128:131], %[c5]\n\t"
        "s_waitcnt vmcnt(23)\n\t"
        "ds_write_b128 v90, v[208:211] offset:23040\n\t"
        "buffer_load_dwordx4 v[208:211], v241, %[ra], s65 offen\n\t"
        "s_waitcnt lgkmcnt(14)\n\t"
        "v_mfma_f32_32x32x16_bf16 %[c6], v[104:107], v[124:127], %[c6]\n\t"
        "s_waitcnt vmcnt(23)\n\t"
        "ds_write_b128 v90, v[212:215] offset:27648\n\t"
        "buffer_load_dwordx4 v[212:215], v242, %[ra], s65 offen\n\t"
        "v_mfma_f32_32x32x16_bf16 %[c7], v[104:107], v[128:131], %[c7]\n\t"
        "s_waitcnt vmcnt(23)\n\t"
        "ds_write_b128 v90, v[216:219] offset:32256\n\t"
        "buffer_load_dwordx4 v[216:219], v243, %[ra], s65 offen\n\t"
        "s_waitcnt lgkmcnt(9)\n\t"
        "v_mfma_f32_32x32x16_bf16 %[c0], v[108:111], v[132:135], %[c0]\n\t"
        "ds_read_b64_tr_b16 v[124:125], %[brd] offset:10240\n\t"
        "ds_read_b64_tr_b16 v[126:127], %[brd] offset:11520\n\t"
        "s_waitcnt lgkmcnt(9)\n\t"
        "v_mfma_f32_32x32x16_bf16 %[c1], v[108:111], v[136:139], %[c1]\n\t"
        "ds_read_b128 v[92:95], %[ard] offset:64\n\t"
        "ds_read_b64_tr_b16 v[128:129], %[brd] offset:10304\n\t"
        "s_waitcnt lgkmcnt(10)\n\t"
        "v_mfma_f32_32x32x16_bf16 %[c2], v[112:115], v[132:135], %[c2]\n\t"
        "ds_read_b64_tr_b16 v[130:131], %[brd] offset:11584\n\t"
        "ds_read_b128 v[96:99], %[ard] offset:4672\n\t"
        "v_mfma_f32_32x32x16_bf16 %[c3], v[112:115], v[136:139], %[c3]\n\t"
        "ds_read_b128 v[100:103], %[ard] offset:9280\n\t"
        "ds_read_b128 v[104:107], %[ard] offset:13888\n\t"
        "s_waitcnt lgkmcnt(13)\n\t"
        "v_mfma_f32_32x32x16_bf16 %[c4], v[116:119], v[132:135], %[c4]\n\t"
        "s_waitcnt vmcnt(23)\n\t"
        "ds_write_b128 v91, v[220:223] offset:0\n\t"
        "buffer_load_dwordx4 v[220:223], v244, %[rb], s66 offen\n\t"
        "v_mfma_f32_32x32x16_bf16 %[c5], v[116:119], v[136:139], %[c5]\n\t"
        "s_waitcnt vmcnt(23)\n\t"
        "ds_write_b128 v91, v[224:227] offset:5120\n\t"
        "buffer_load_dwordx4 v[224:227], v245, %[rb], s66 offen\n\t"
        "s_waitcnt lgkmcnt(14)\n\t"
        "v_mfma_f32_32x32x16_bf16 %[c6], v[120:123], v[132:135], %[c6]\n\t"
        "s_waitcnt vmcnt(23)\n\t"
        "ds_write_b128 v91, v[228:231] offset:10240\n\t"
        "buffer_load_dwordx4 v[228:231], v246, %[rb], s66 offen\n\t"
        "v_mfma_f32_32x32x16_bf16 %[c7], v[120:123], v[136:139], %[c7]\n\t"
        "s_waitcnt vmcnt(23)\n\t"
        "ds_write_b128 v91, v[232:235] offset:15360\n\t"
        "buffer_load_dwordx4 v[232:235], v247, %[rb], s66 offen\n\t"
        "s_waitcnt lgkmcnt(9)\n\t"
        "v_mfma_f32_32x32x16_bf16 %[c0], v[92:95], v[124:127], %[c0]\n\t"
        "ds_read_b64_tr_b16 v[132:133], %[brd] offset:15360\n\t"
        "ds_read_b64_tr_b16 v[134:135], %[brd] offset:16640\n\t"
        "s_waitcnt lgkmcnt(9)\n\t"
        "v_mfma_f32_32x32x16_bf16 %[c1], v[92:95], v[128:131], %[c1]\n\t"
        "ds_read_b128 v[108:111], %[ard] offset:96\n\t"
        "ds_read_b64_tr_b16 v[136:137], %[brd] offset:15424\n\t"
        "s_waitcnt lgkmcnt(10)\n\t"
        "v_mfma_f32_32x32x16_bf16 %[c2], v[96:99], v[124:127], %[c2]\n\t"
        "ds_read_b64_tr_b16 v[138:139], %[brd] offset:16704\n\t"
        "ds_read_b128 v[112:115], %[ard] offset:4704\n\t"
        "v_mfma_f32_32x32x16_bf16 %[c3], v[96:99], v[128:131], %[c3]\n\t"
        "ds_read_b128 v[116:119], %[ard] offset:9312\n\t"
        "ds_read_b128 v[120:123], %[ard] offset:13920\n\t"
        "s_waitcnt lgkmcnt(13)\n\t"
        "v_mfma_f32_32x32x16_bf16 %[c4], v[100:103], v[124:127], %[c4]\n\t"
        "v_mfma_f32_32x32x16_bf16 %[c5], v[100:103], v[128:131], %[c5]\n\t"
        "s_waitcnt lgkmcnt(12)\n\t"
        "v_mfma_f32_32x32x16_bf16 %[c6], v[104:107], v[124:127], %[c6]\n\t"
        "v_mfma_f32_32x32x16_bf16 %[c7], v[104:107], v[128:131], %[c7]\n\t"
        "s_add_u32 s64, s64, 1\n\t"
        "s_cmp_lt_u32 s64, %[nk]\n\t"
        "s_waitcnt lgkmcnt(0)\n\t"
        "s_barrier\n\t"
        "s_cbranch_scc0 LE_%=\n\t"
        "s_add_u32 s67, s64, 3\n\t"
        "s_min_u32 s67, s67, s68\n\t"
        "s_lshl_b32 s65, s67, 7\n\t"
        "s_lshl_b32 s66, s67, 19\n\t"
        "v_mfma_f32_32x32x16_bf16 %[c0], v[108:111], v[132:135], %[c0]\n\t"
        "ds_read_b64_tr_b16 v[124:125], v89 offset:0\n\t"
        "ds_read_b64_tr_b16 v[126:127], v89 offset:1280\n\t"
        "v_mfma_f32_32x32x16_bf16 %[c1], v[108:111], v[136:139], %[c1]\n\t"
        "ds_read_b128 v[92:95], v88 offset:0\n\t"
        "ds_read_b64_tr_b16 v[128:129], v89 offset:64\n\t"
        "v_mfma_f32_32x32x16_bf16 %[c2], v[112:115], v[132:135], %[c2]\n\t"
        "ds_read_b64_tr_b16 v[130:131], v89 offset:1344\n\t"
        "ds_read_b128 v[96:99], v88 offset:4608\n\t"
        "v_mfma_f32_32x32x16_bf16 %[c3], v[112:115], v[136:139], %[c3]\n\t"
        "ds_read_b128 v[100:103], v88 offset:9216\n\t"
        "ds_read_b128 v[104:107], v88 offset:13824\n\t"
        "v_mfma_f32_32x32x16_bf16 %[c4], v[116:119], v[132:135], %[c4]\n\t"
        "s_waitcnt vmcnt(23)\n\t"
        "ds_write_b128 %[aw], v[140:143] offset:0\n\t"
        "buffer_load_dwordx4 v[140:143], v236, %[ra], s65 offen\n\t"
        "v_mfma_f32_32x32x16_bf16 %[c5], v[116:119], v[136:139], %[c5]\n\t"
        "s_waitcnt vmcnt(23)\n\t"
        "ds_write_b128 %[aw], v[144:147] offset:4608\n\t"
        "buffer_load_dwordx4 v[144:147], v237, %[ra], s65 offen\n\t"
        "v_mfma_f32_32x32x16_bf16 %[c6], v[120:123], v[132:135], %[c6]\n\t"
        "s_waitcnt vmcnt(23)\n\t"
        "ds_write_b128 %[aw], v[148:151] offset:9216\n\t"
        "buffer_load_dwordx4 v[148:151], v238, %[ra], s65 offen\n\t"
        "v_mfma_f32_32x32x16_bf16 %[c7], v[120:123], v[136:139], %[c7]\n\t"
        "s_waitcnt vmcnt(23)\n\t"
        "ds_write_b128 %[aw], v[152:155] offset:13824\n\t"
        "buffer_load_dwordx4 v[152:155], v239, %[ra], s65 offen\n\t"
        "s_waitcnt lgkmcnt(9)\n\t"
        "v_mfma_f32_32x32x16_bf16 %[c0], v[92:95], v[124:127], %[c0]\n\t"
        "ds_read_b64_tr_b16 v[132:133], v89 offset:5120\n\t"
        "ds_read_b64_tr_b16 v[134:135], v89 offset:6400\n\t"
        "s_waitcnt lgkmcnt(9)\n\t"
        "v_mfma_f32_32x32x16_bf16 %[c1], v[92:95], v[128:131], %[c1]\n\t"
        "ds_read_b128 v[108:111], v88 offset:32\n\t"
        "ds_read_b64_tr_b16 v[136:137], v89 offset:5184\n\t"
        "s_waitcnt lgkmcnt(10)\n\t"
        "v_mfma_f32_32x32x16_bf16 %[c2], v[96:99], v[124:127], %[c2]\n\t"
        "ds_read_b64_tr_b16 v[138:139], v89 offset:6464\n\t"
        "ds_read_b128 v[112:115], v88 offset:4640\n\t"
        "v_mfma_f32_32x32x16_bf16 %[c3], v[96:99], v[128:131], %[c3]\n\t"
        "ds_read_b128 v[116:119], v88 offset:9248\n\t"
        "ds_read_b128 v[120:123], v88 offset:13856\n\t"
        "s_waitcnt lgkmcnt(13)\n\t"
        "v_mfma_f32_32x32x16_bf16 %[c4], v[100:103], v[124:127], %[c4]\n\t"
        "s_waitcnt vmcnt(23)\n\t"
        "ds_write_b128 %[aw], v[156:159] offset:18432\n\t"
        "buffer_load_dwordx4 v[156:159], v240, %[ra], s65 offen\n\t"
        "v_mfma_f32_32x32x16_bf16 %[c5], v[100:103], v[128:131], %[c5]\n\t"
        "s_waitcnt vmcnt(23)\n\t"
        "ds_write_b128 %[aw], v[160:163] offset:23040\n\t"
        "buffer_load_dwordx4 v[160:163], v241, %[ra], s65 offen\n\t"
        "s_waitcnt lgkmcnt(14)\n\t"
        "v_mfma_f32_32x32x16_bf16 %[c6], v[104:107], v[124:127], %[c6]\n\t"
        "s_waitcnt vmcnt(23)\n\t"
        "ds_write_b128 %[aw], v[164:167] offset:27648\n\t"
        "buffer_load_dwordx4 v[164:167], v242, %[ra], s65 offen\n\t"
        "v_mfma_f32_32x32x16_bf16 %[c7], v[104:107], v[128:131], %[c7]\n\t"
        "s_waitcnt vmcnt(23)\n\t"
        "ds_write_b128 %[aw], v[168:171] offset:32256\n\t"
        "buffer_load_dwordx4 v[168:171], v243, %[ra], s65 offen\n\t"
        "s_waitcnt lgkmcnt(9)\n\t"
        "v_mfma_f32_32x32x16_bf16 %[c0], v[108:111], v[132:135], %[c0]\n\t"
        "ds_read_b64_tr_b16 v[124:125], v89 offset:10240\n\t"
        "ds_read_b64_tr_b16 v[126:127], v89 offset:11520\n\t"
        "s_waitcnt lgkmcnt(9)\n\t"
        "v_mfma_f32_32x32x16_bf16 %[c1], v[108:111], v[136:139], %[c1]\n\t"
        "ds_read_b128 v[92:95], v88 offset:64\n\t"
        "ds_read_b64_tr_b16 v[128:129], v89 offset:10304\n\t"
        "s_waitcnt lgkmcnt(10)\n\t"
        "v_mfma_f32_32x32x16_bf16 %[c2], v[112:115], v[132:135], %[c2]\n\t"
        "ds_read_b64_tr_b16 v[130:131], v89 offset:11584\n\t"
        "ds_read_b128 v[96:99], v88 offset:4672\n\t"
        "v_mfma_f32_32x32x16_bf16 %[c3], v[112:115], v[136:139], %[c3]\n\t"
        "ds_read_b128 v[100:103], v88 offset:9280\n\t"
        "ds_read_b128 v[104:107], v88 offset:13888\n\t"
        "s_waitcnt lgkmcnt(13)\n\t"
        "v_mfma_f32_32x32x16_bf16 %[c4], v[116:119], v[132:135], %[c4]\n\t"
        "s_waitcnt vmcnt(23)\n\t"
        "ds_write_b128 %[bw], v[172:175] offset:0\n\t"
        "buffer_load_dwordx4 v[172:175], v244, %[rb], s66 offen\n\t"
        "v_mfma_f32_32x32x16_bf16 %[c5], v[116:119], v[136:139], %[c5]\n\t"
        "s_waitcnt vmcnt(23)\n\t"
        "ds_write_b128 %[bw], v[176:179] offset:5120\n\t"
        "buffer_load_dwordx4 v[176:179], v245, %[rb], s66 offen\n\t"
        "s_waitcnt lgkmcnt(14)\n\t"
        "v_mfma_f32_32x32x16_bf16 %[c6], v[120:123], v[132:135], %[c6]\n\t"
        "s_waitcnt vmcnt(23)\n\t"
        "ds_write_b128 %[bw], v[180:183] offset:10240\n\t"
        "buffer_load_dwordx4 v[180:183], v246, %[rb], s66 offen\n\t"
        "v_mfma_f32_32x32x16_bf16 %[c7], v[120:123], v[136:139], %[c7]\n\t"
        "s_waitcnt vmcnt(23)\n\t"
        "ds_write_b128 %[bw], v[184:187] offset:15360\n\t"
        "buffer_load_dwordx4 v[184:187], v247, %[rb], s66 offen\n\t"
        "s_waitcnt lgkmcnt(9)\n\t"
        "v_mfma_f32_32x32x16_bf16 %[c0], v[92:95], v[124:127], %[c0]\n\t"
        "ds_read_b64_tr_b16 v[132:133], v89 offset:15360\n\t"
        "ds_read_b64_tr_b16 v[134:135], v89 offset:16640\n\t"
        "s_waitcnt lgkmcnt(9)\n\t"
        "v_mfma_f32_32x32x16_bf16 %[c1], v[92:95], v[128:131], %[c1]\n\t"
        "ds_read_b128 v[108:111], v88 offset:96\n\t"
        "ds_read_b64_tr_b16 v[136:137], v89 offset:15424\n\t"
        "s_waitcnt lgkmcnt(10)\n\t"
        "v_mfma_f32_32x32x16_bf16 %[c2], v[96:99], v[124:127], %[c2]\n\t"
        "ds_read_b64_tr_b16 v[138:139], v89 offset:16704\n\t"
        "ds_read_b128 v[112:115], v88 offset:4704\n\t"
        "v_mfma_f32_32x32x16_bf16 %[c3], v[96:99], v[128:131], %[c3]\n\t"
        "ds_read_b128 v[116:119], v88 offset:9312\n\t"
        "ds_read_b128 v[120:123], v88 offset:13920\n\t"
        "s_waitcnt lgkmcnt(13)\n\t"
        "v_mfma_f32_32x32x16_bf16 %[c4], v[100:103], v[124:127], %[c4]\n\t"
        "v_mfma_f32_32x32x16_bf16 %[c5], v[100:103], v[128:131], %[c5]\n\t"
        "s_waitcnt lgkmcnt(12)\n\t"
        "v_mfma_f32_32x32x16_bf16 %[c6], v[104:107], v[124:127], %[c6]\n\t"
        "v_mfma_f32_32x32x16_bf16 %[c7], v[104:107], v[128:131], %[c7]\n\t"
        "s_add_u32 s64, s64, 1\n\t"
        "s_cmp_lt_u32 s64, %[nk]\n\t"
        "s_waitcnt lgkmcnt(0)\n\t"
        "s_barrier\n\t"
        "s_cbranch_scc1 LG_%=\n\t"
        "LE_%=:\n\t"
        "v_mfma_f32_32x32x16_bf16 %[c0], v[108:111], v[132:135], %[c0]\n\t"
        "v_mfma_f32_32x32x16_bf16 %[c1], v[108:111], v[136:139], %[c1]\n\t"
        "v_mfma_f32_32x32x16_bf16 %[c2], v[112:115], v[132:135], %[c2]\n\t"
        "v_mfma_f32_32x32x16_bf16 %[c3], v[112:115], v[136:139], %[c3]\n\t"
        "v_mfma_f32_32x32x16_bf16 %[c4], v[116:119], v[132:135], %[c4]\n\t"
        "v_mfma_f32_32x32x16_bf16 %[c5], v[116:119], v[136:139], %[c5]\n\t"
        "v_mfma_f32_32x32x16_bf16 %[c6], v[120:123], v[132:135], %[c6]\n\t"
        "v_mfma_f32_32x32x16_bf16 %[c7], v[120:123], v[136:139], %[c7]\n\t"
        "s_waitcnt vmcnt(0)\n\t"
        "s_nop 15\n\t"
        "s_nop 3\n\t"
        : [c0] "=&a"(c[0][0]), [c1] "=&a"(c[0][1]), [c2] "=&a"(c[1][0]), [c3] "=&a"(c[1][1]), [c4] "=&a"(c[2][0]),
          [c5] "=&a"(c[2][1]), [c6] "=&a"(c[3][0]), [c7] "=&a"(c[3][1])
        : [ra] "s"(ra), [rb] "s"(rb), [astr] "s"(astr), [nk] "s"(nk), [aoff] "v"(aoff), [boff] "v"(boff),
          [ard] "v"(ard), [brd] "v"(brd), [aw] "v"(aw), [bw] "v"(bw)
        :
          "v88", "v89", "v90", "v91", "v92", "v93", "v94", "v95", "v96", "v97", "v98", "v99", "v100", "v101",
          "v102", "v103", "v104", "v105", "v106", "v107", "v108", "v109", "v110", "v111", "v112", "v113", "v114",
          "v115", "v116", "v117", "v118", "v119", "v120", "v121", "v122", "v123", "v124", "v125", "v126", "v127",
          "v128", "v129", "v130", "v131", "v132", "v133", "v134", "v135", "v136", "v137", "v138", "v139", "v140",
          "v141", "v142", "v143", "v144", "v145", "v146", "v147", "v148", "v149", "v150", "v151", "v152", "v153",
          "v154", "v155", "v156", "v157", "v158", "v159", "v160", "v161", "v162", "v163", "v164", "v165", "v166",
          "v167", "v168", "v169", "v170", "v171", "v172", "v173", "v174", "v175", "v176", "v177", "v178", "v179",
          "v180", "v181", "v182", "v183", "v184", "v185", "v186", "v187", "v188", "v189", "v190", "v191", "v192",
          "v193", "v194", "v195", "v196", "v197", "v198", "v199", "v200", "v201", "v202", "v203", "v204", "v205",
          "v206", "v207", "v208", "v209", "v210", "v211", "v212", "v213", "v214", "v215", "v216", "v217", "v218",
          "v219", "v220", "v221", "v222", "v223", "v224", "v225", "v226", "v227", "v228", "v229", "v230", "v231",
          "v232", "v233", "v234", "v235", "v236", "v237", "v238", "v239", "v240", "v241", "v242", "v243", "v244",
          "v245", "v246", "v247", "s64", "s65", "s66", "s67", "s68", "s69", "scc", "memory" );
}
__device__ __forceinline__ void gemm2_asm(f32x16& c0, f32x16& c1, __amdgpu_buffer_rsrc_t ra, __amdgpu_buffer_rsrc_t rb,
                                          unsigned aoff, unsigned boff, unsigned brd, unsigned bw, unsigned rot,
                                          unsigned cnt) {
    asm volatile(
        "s_lshl_b32 s64, %[rot], 15\n\t"
        "s_mul_i32 s65, %[rot], 458752\n\t"
        "v_mov_b32_e32 v238, %[aoff]\n\t"
        "v_mov_b32_e32 v239, %[boff]\n\t"
        "s_mov_b32 s66, %[cnt]\n\t"
        "buffer_load_dwordx4 v[172:175], v239, %[rb], s65 offen\n\t"
        "s_add_u32 s65, s65, 28672\n\t"
        "buffer_load_dwordx4 v[176:179], v239, %[rb], s65 offen\n\t"
        "s_add_u32 s65, s65, 28672\n\t"
        "buffer_load_dwordx4 v[180:183], v239, %[rb], s65 offen\n\t"
        "s_add_u32 s65, s65, 28672\n\t"
        "buffer_load_dwordx4 v[184:187], v239, %[rb], s65 offen\n\t"
        "s_add_u32 s65, s65, 28672\n\t"
        "buffer_load_dwordx4 v[188:191], v239, %[rb], s65 offen\n\t"
        "s_add_u32 s65, s65, 28672\n\t"
        "buffer_load_dwordx4 v[192:195], v239, %[rb], s65 offen\n\t"
        "s_add_u32 s65, s65, 28672\n\t"
        "buffer_load_dwordx4 v[196:199], v239, %[rb], s65 offen\n\t"
        "s_add_u32 s65, s65, 28672\n\t"
        "buffer_load_dwordx4 v[200:203], v239, %[rb], s65 offen\n\t"
        "s_add_u32 s65, s65, 28672\n\t"
        "buffer_load_dwordx4 a[32:35], v238, %[ra], s64 offen offset:0\n\t"
        "buffer_load_dwordx4 v[204:207], v239, %[rb], s65 offen\n\t"
        "s_add_u32 s65, s65, 28672\n\t"
        "buffer_load_dwordx4 a[36:39], v238, %[ra], s64 offen offset:1024\n\t"
        "buffer_load_dwordx4 v[208:211], v239, %[rb], s65 offen\n\t"
        "s_add_u32 s65, s65, 28672\n\t"
        "buffer_load_dwordx4 a[40:43], v238, %[ra], s64 offen offset:2048\n\t"
        "buffer_load_dwordx4 v[212:215], v239, %[rb], s65 offen\n\t"
        "s_add_u32 s65, s65, 28672\n\t"
        "buffer_load_dwordx4 a[44:47], v238, %[ra], s64 offen offset:3072\n\t"
        "buffer_load_dwordx4 v[216:219], v239, %[rb], s65 offen\n\t"
        "s_add_u32 s65, s65, 28672\n\t"
        "s_add_u32 s64, s64, 0x1000\n\t"
        "buffer_load_dwordx4 a[48:51], v238, %[ra], s64 offen offset:0\n\t"
        "buffer_load_dwordx4 v[220:223], v239, %[rb], s65 offen\n\t"
        "s_add_u32 s65, s65, 28672\n\t"
        "buffer_load_dwordx4 a[52:55], v238, %[ra], s64 offen offset:1024\n\t"
        "buffer_load_dwordx4 v[224:227], v239, %[rb], s65 offen\n\t"
        "s_add_u32 s65, s65, 28672\n\t"
        "buffer_load_dwordx4 a[56:59], v238, %[ra], s64 offen offset:2048\n\t"
        "buffer_load_dwordx4 v[228:231], v239, %[rb], s65 offen\n\t"
        "s_add_u32 s65, s65, 28672\n\t"
        "buffer_load_dwordx4 a[60:63], v238, %[ra], s64 offen offset:3072\n\t"
        "buffer_load_dwordx4 v[232:235], v239, %[rb], s65 offen\n\t"
        "s_add_u32 s65, s65, 28672\n\t"
        "s_cmp_eq_u32 s65, 3670016\n\t"
        "s_cselect_b32 s65, 0, s65\n\t"
        "s_add_u32 s64, s64, 0x1000\n\t"
        "buffer_load_dwordx4 a[64:67], v238, %[ra], s64 offen offset:0\n\t"
        "buffer_load_dwordx4 a[68:71], v238, %[ra], s64 offen offset:1024\n\t"
        "buffer_load_dwordx4 a[72:75], v238, %[ra], s64 offen offset:2048\n\t"
        "buffer_load_dwordx4 a[76:79], v238, %[ra], s64 offen offset:3072\n\t"
        "s_add_u32 s64, s64, 0x1000\n\t"
        "buffer_load_dwordx4 a[80:83], v238, %[ra], s64 offen offset:0\n\t"
        "buffer_load_dwordx4 a[84:87], v238, %[ra], s64 offen offset:1024\n\t"
        "buffer_load_dwordx4 a[88:91], v238, %[ra], s64 offen offset:2048\n\t"
        "buffer_load_dwordx4 a[92:95], v238, %[ra], s64 offen offset:3072\n\t"
        "v_add_u32_e32 v236, 49152, %[brd]\n\t"
        "v_add_u32_e32 v237, 49152, %[bw]\n\t"
        "s_waitcnt vmcnt(31)\n\t"
        "ds_write_b128 %[bw], v[172:175] offset:0\n\t"
        "s_waitcnt vmcnt(30)\n\t"
        "ds_write_b128 %[bw], v[176:179] offset:6144\n\t"
        "s_waitcnt vmcnt(29)\n\t"
        "ds_write_b128 %[bw], v[180:183] offset:12288\n\t"
        "s_waitcnt vmcnt(28)\n\t"
        "ds_write_b128 %[bw], v[184:187] offset:18432\n\t"
        "s_waitcnt vmcnt(27)\n\t"
        "ds_write_b128 %[bw], v[188:191] offset:24576\n\t"
        "s_waitcnt vmcnt(26)\n\t"
        "ds_write_b128 %[bw], v[192:195] offset:30720\n\t"
        "s_waitcnt vmcnt(25)\n\t"
        "ds_write_b128 %[bw], v[196:199] offset:36864\n\t"
        "s_waitcnt vmcnt(24)\n\t"
        "ds_write_b128 %[bw], v[200:203] offset:43008\n\t"
        "s_add_u32 s64, s64, 0x1000\n\t"
        "buffer_load_dwordx4 a[96:99], v238, %[ra], s64 offen offset:0\n\t"
        "buffer_load_dwordx4 v[172:175], v239, %[rb], s65 offen\n\t"
        "s_add_u32 s65, s65, 28672\n\t"
        "buffer_load_dwordx4 a[100:103], v238, %[ra], s64 offen offset:1024\n\t"
        "buffer_load_dwordx4 v[176:179], v239, %[rb], s65 offen\n\t"
        "s_add_u32 s65, s65, 28672\n\t"
        "buffer_load_dwordx4 a[104:107], v238, %[ra], s64 offen offset:2048\n\t"
        "buffer_load_dwordx4 v[180:183], v239, %[rb], s65 offen\n\t"
        "s_add_u32 s65, s65, 28672\n\t"
        "buffer_load_dwordx4 a[108:111], v238, %[ra], s64 offen offset:3072\n\t"
        "buffer_load_dwordx4 v[184:187], v239, %[rb], s65 offen\n\t"
        "s_add_u32 s65, s65, 28672\n\t"
        "s_add_u32 s64, s64, 0x1000\n\t"
        "buffer_load_dwordx4 a[112:115], v238, %[ra], s64 offen offset:0\n\t"
        "buffer_load_dwordx4 v[188:191], v239, %[rb], s65 offen\n\t"
        "s_add_u32 s65, s65, 28672\n\t"
        "buffer_load_dwordx4 a[116:119], v238, %[ra], s64 offen offset:1024\n\t"
        "buffer_load_dwordx4 v[192:195], v239, %[rb], s65 offen\n\t"
        "s_add_u32 s65, s65, 28672\n\t"
        "buffer_load_dwordx4 a[120:123], v238, %[ra], s64 offen offset:2048\n\t"
        "buffer_load_dwordx4 v[196:199], v239, %[rb], s65 offen\n\t"
        "s_add_u32 s65, s65, 28672\n\t"
        "buffer_load_dwordx4 a[124:127], v238, %[ra], s64 offen offset:3072\n\t"
        "buffer_load_dwordx4 v[200:203], v239, %[rb], s65 offen\n\t"
        "s_add_u32 s65, s65, 28672\n\t"
        "s_add_u32 s64, s64, 0x1000\n\t"
        "buffer_load_dwordx4 a[128:131], v238, %[ra], s64 offen offset:0\n\t"
        "buffer_load_dwordx4 a[132:135], v238, %[ra], s64 offen offset:1024\n\t"
        "buffer_load_dwordx4 a[136:139], v238, %[ra], s64 offen offset:2048\n\t"
        "buffer_load_dwordx4 a[140:143], v238, %[ra], s64 offen offset:3072\n\t"
        "s_add_u32 s64, s64, 0x1000\n\t"
        "buffer_load_dwordx4 a[144:147], v238, %[ra], s64 offen offset:0\n\t"
        "buffer_load_dwordx4 a[148:151], v238, %[ra], s64 offen offset:1024\n\t"
        "buffer_load_dwordx4 a[152:155], v238, %[ra], s64 offen offset:2048\n\t"
        "buffer_load_dwordx4 a[156:159], v238, %[ra], s64 offen offset:3072\n\t"
        "s_waitcnt lgkmcnt(0)\n\t"
        "s_barrier\n\t"
        "ds_read_b64_tr_b16 v[140:141], %[brd] offset:0\n\t"
        "ds_read_b64_tr_b16 v[142:143], %[brd] offset:1536\n\t"
        "ds_read_b64_tr_b16 v[144:145], %[brd] offset:64\n\t"
        "ds_read_b64_tr_b16 v[146:147], %[brd] offset:1600\n\t"
        "ds_read_b64_tr_b16 v[148:149], %[brd] offset:3072\n\t"
        "ds_read_b64_tr_b16 v[150:151], %[brd] offset:4608\n\t"
        "ds_read_b64_tr_b16 v[152:153], %[brd] offset:3136\n\t"
        "ds_read_b64_tr_b16 v[154:155], %[brd] offset:4672\n\t"
        "ds_read_b64_tr_b16 v[156:157], %[brd] offset:6144\n\t"
        "ds_read_b64_tr_b16 v[158:159], %[brd] offset:7680\n\t"
        "ds_read_b64_tr_b16 v[160:161], %[brd] offset:6208\n\t"
        "ds_read_b64_tr_b16 v[162:163], %[brd] offset:7744\n\t"
        ".p2align 6\n\t"
        "LO_%=:\n\t"
        "s_cmp_eq_u32 s66, 1\n\t"
        "s_cselect_b32 s67, 0x80000000, 0\n\t"
        "v_or_b32_e32 v238, s67, v238\n\t"
        "v_or_b32_e32 v239, s67, v239\n\t"
        "s_waitcnt vmcnt(47)\n\t"
        "s_waitcnt lgkmcnt(10)\n\t"
        "v_mfma_f32_32x32x16_bf16 %[c0], v[140:143], a[32:35], %[c0]\n\t"
        "ds_read_b64_tr_b16 v[164:165], %[brd] offset:9216\n\t"
        "ds_read_b64_tr_b16 v[166:167], %[brd] offset:10752\n\t"
        "s_waitcnt lgkmcnt(10)\n\t"
        "v_mfma_f32_32x32x16_bf16 %[c1], v[144:147], a[32:35], %[c1]\n\t"
        "ds_read_b64_tr_b16 v[168:169], %[brd] offset:9280\n\t"
        "ds_read_b64_tr_b16 v[170:171], %[brd] offset:10816\n\t"
        "s_add_u32 s64, s64, 0x1000\n\t"
        "s_and_b32 s64, s64, 0x3ffff\n\t"
        "buffer_load_dwordx4 a[32:35], v238, %[ra], s64 offen offset:0\n\t"
        "s_waitcnt vmcnt(47)\n\t"
        "ds_write_b128 v237, v[204:207] offset:0\n\t"
        "buffer_load_dwordx4 v[204:207], v239, %[rb], s65 offen\n\t"
        "s_add_u32 s65, s65, 28672\n\t"
        "s_waitcnt vmcnt(47)\n\t"
        "s_waitcnt lgkmcnt(11)\n\t"
        "v_mfma_f32_32x32x16_bf16 %[c0], v[148:151], a[36:39], %[c0]\n\t"
        "ds_read_b64_tr_b16 v[140:141], %[brd] offset:12288\n\t"
        "ds_read_b64_tr_b16 v[142:143], %[brd] offset:13824\n\t"
        "s_waitcnt lgkmcnt(11)\n\t"
        "v_mfma_f32_32x32x16_bf16 %[c1], v[152:155], a[36:39], %[c1]\n\t"
        "ds_read_b64_tr_b16 v[144:145], %[brd] offset:12352\n\t"
        "ds_read_b64_tr_b16 v[146:147], %[brd] offset:13888\n\t"
        "buffer_load_dwordx4 a[36:39], v238, %[ra], s64 offen offset:1024\n\t"
        "s_waitcnt vmcnt(47)\n\t"
        "ds_write_b128 v237, v[208:211] offset:6144\n\t"
        "buffer_load_dwordx4 v[208:211], v239, %[rb], s65 offen\n\t"
        "s_add_u32 s65, s65, 28672\n\t"
        "s_waitcnt vmcnt(47)\n\t"
        "s_waitcnt lgkmcnt(12)\n\t"
        "v_mfma_f32_32x32x16_bf16 %[c0], v[156:159], a[40:43], %[c0]\n\t"
        "ds_read_b64_tr_b16 v[148:149], %[brd] offset:15360\n\t"
        "ds_read_b64_tr_b16 v[150:151], %[brd] offset:16896\n\t"
        "s_waitcnt lgkmcnt(12)\n\t"
        "v_mfma_f32_32x32x16_bf16 %[c1], v[160:163], a[40:43], %[c1]\n\t"
        "ds_read_b64_tr_b16 v[152:153], %[brd] offset:15424\n\t"
        "ds_read_b64_tr_b16 v[154:155], %[brd] offset:16960\n\t"
        "buffer_load_dwordx4 a[40:43], v238, %[ra], s64 offen offset:2048\n\t"
        "s_waitcnt vmcnt(47)\n\t"
        "ds_write_b128 v237, v[212:215] offset:12288\n\t"
        "buffer_load_dwordx4 v[212:215], v239, %[rb], s65 offen\n\t"
        "s_add_u32 s65, s65, 28672\n\t"
        "s_waitcnt vmcnt(47)\n\t"
        "s_waitcnt lgkmcnt(13)\n\t"
        "v_mfma_f32_32x32x16_bf16 %[c0], v[164:167], a[44:47], %[c0]\n\t"
        "ds_read_b64_tr_b16 v[156:157], %[brd] offset:18432\n\t"
        "ds_read_b64_tr_b16 v[158:159], %[brd] offset:19968\n\t"
        "s_waitcnt lgkmcnt(13)\n\t"
        "v_mfma_f32_32x32x16_bf16 %[c1], v[168:171], a[44:47], %[c1]\n\t"
        "ds_read_b64_tr_b16 v[160:161], %[brd] offset:18496\n\t"
        "ds_read_b64_tr_b16 v[162:163], %[brd] offset:20032\n\t"
        "buffer_load_dwordx4 a[44:47], v238, %[ra], s64 offen offset:3072\n\t"
        "s_waitcnt vmcnt(47)\n\t"
        "ds_write_b128 v237, v[216:219] offset:18432\n\t"
        "buffer_load_dwordx4 v[216:219], v239, %[rb], s65 offen\n\t"
        "s_add_u32 s65, s65, 28672\n\t"
        "s_waitcnt vmcnt(47)\n\t"
        "s_waitcnt lgkmcnt(13)\n\t"
        "v_mfma_f32_32x32x16_bf16 %[c0], v[140:143], a[48:51], %[c0]\n\t"
        "ds_read_b64_tr_b16 v[164:165], %[brd] offset:21504\n\t"
        "ds_read_b64_tr_b16 v[166:167], %[brd] offset:23040\n\t"
        "s_waitcnt lgkmcnt(13)\n\t"
        "v_mfma_f32_32x32x16_bf16 %[c1], v[144:147], a[48:51], %[c1]\n\t"
        "ds_read_b64_tr_b16 v[168:169], %[brd] offset:21568\n\t"
        "ds_read_b64_tr_b16 v[170:171], %[brd] offset:23104\n\t"
        "s_add_u32 s64, s64, 0x1000\n\t"
        "buffer_load_dwordx4 a[48:51], v238, %[ra], s64 offen offset:0\n\t"
        "s_waitcnt vmcnt(47)\n\t"
        "ds_write_b128 v237, v[220:223] offset:24576\n\t"
        "buffer_load_dwordx4 v[220:223], v239, %[rb], s65 offen\n\t"
        "s_add_u32 s65, s65, 28672\n\t"
        "s_waitcnt vmcnt(47)\n\t"
        "s_waitcnt lgkmcnt(13)\n\t"
        "v_mfma_f32_32x32x16_bf16 %[c0], v[148:151], a[52:55], %[c0]\n\t"
        "ds_read_b64_tr_b16 v[140:141], %[brd] offset:24576\n\t"
        "ds_read_b64_tr_b16 v[142:143], %[brd] offset:26112\n\t"
        "s_waitcnt lgkmcnt(13)\n\t"
        "v_mfma_f32_32x32x16_bf16 %[c1], v[152:155], a[52:55], %[c1]\n\t"
        "ds_read_b64_tr_b16 v[144:145], %[brd] offset:24640\n\t"
        "ds_read_b64_tr_b16 v[146:147], %[brd] offset:26176\n\t"
        "buffer_load_dwordx4 a[52:55], v238, %[ra], s64 offen offset:1024\n\t"
        "s_waitcnt vmcnt(47)\n\t"
        "ds_write_b128 v237, v[224:227] offset:30720\n\t"
        "buffer_load_dwordx4 v[224:227], v239, %[rb], s65 offen\n\t"
        "s_add_u32 s65, s65, 28672\n\t"
        "s_waitcnt vmcnt(47)\n\t"
        "s_waitcnt lgkmcnt(13)\n\t"
        "v_mfma_f32_32x32x16_bf16 %[c0], v[156:159], a[56:59], %[c0]\n\t"
        "ds_read_b64_tr_b16 v[148:149], %[brd] offset:27648\n\t"
        "ds_read_b64_tr_b16 v[150:151], %[brd] offset:29184\n\t"
        "s_waitcnt lgkmcnt(13)\n\t"
        "v_mfma_f32_32x32x16_bf16 %[c1], v[160:163], a[56:59], %[c1]\n\t"
        "ds_read_b64_tr_b16 v[152:153], %[brd] offset:27712\n\t"
        "ds_read_b64_tr_b16 v[154:155], %[brd] offset:29248\n\t"
        "buffer_load_dwordx4 a[56:59], v238, %[ra], s64 offen offset:2048\n\t"
        "s_waitcnt vmcnt(47)\n\t"
        "ds_write_b128 v237, v[228:231] offset:36864\n\t"
        "buffer_load_dwordx4 v[228:231], v239, %[rb], s65 offen\n\t"
        "s_add_u32 s65, s65, 28672\n\t"
        "s_waitcnt vmcnt(47)\n\t"
        "s_waitcnt lgkmcnt(13)\n\t"
        "v_mfma_f32_32x32x16_bf16 %[c0], v[164:167], a[60:63], %[c0]\n\t"
        "ds_read_b64_tr_b16 v[156:157], %[brd] offset:30720\n\t"
        "ds_read_b64_tr_b16 v[158:159], %[brd] offset:32256\n\t"
        "s_waitcnt lgkmcnt(13)\n\t"
        "v_mfma_f32_32x32x16_bf16 %[c1], v[168:171], a[60:63], %[c1]\n\t"
        "ds_read_b64_tr_b16 v[160:161], %[brd] offset:30784\n\t"
        "ds_read_b64_tr_b16 v[162:163], %[brd] offset:32320\n\t"
        "buffer_load_dwordx4 a[60:63], v238, %[ra], s64 offen offset:3072\n\t"
        "s_waitcnt vmcnt(47)\n\t"
        "ds_write_b128 v237, v[232:235] offset:43008\n\t"
        "buffer_load_dwordx4 v[232:235], v239, %[rb], s65 offen\n\t"
        "s_add_u32 s65, s65, 28672\n\t"
        "s_cmp_eq_u32 s65, 3670016\n\t"
        "s_cselect_b32 s65, 0, s65\n\t"
        "s_waitcnt vmcnt(47)\n\t"
        "s_waitcnt lgkmcnt(13)\n\t"
        "v_mfma_f32_32x32x16_bf16 %[c0], v[140:143], a[64:67], %[c0]\n\t"
        "ds_read_b64_tr_b16 v[164:165], %[brd] offset:33792\n\t"
        "ds_read_b64_tr_b16 v[166:167], %[brd] offset:35328\n\t"
        "s_waitcnt lgkmcnt(13)\n\t"
        "v_mfma_f32_32x32x16_bf16 %[c1], v[144:147], a[64:67], %[c1]\n\t"
        "ds_read_b64_tr_b16 v[168:169], %[brd] offset:33856\n\t"
        "ds_read_b64_tr_b16 v[170:171], %[brd] offset:35392\n\t"
        "s_add_u32 s64, s64, 0x1000\n\t"
        "buffer_load_dwordx4 a[64:67], v238, %[ra], s64 offen offset:0\n\t"
        "s_waitcnt vmcnt(47)\n\t"
        "s_waitcnt lgkmcnt(12)\n\t"
        "v_mfma_f32_32x32x16_bf16 %[c0], v[148:151], a[68:71], %[c0]\n\t"
        "ds_read_b64_tr_b16 v[140:141], %[brd] offset:36864\n\t"
        "ds_read_b64_tr_b16 v[142:143], %[brd] offset:38400\n\t"
        "s_waitcnt lgkmcnt(12)\n\t"
        "v_mfma_f32_32x32x16_bf16 %[c1], v[152:155], a[68:71], %[c1]\n\t"
        "ds_read_b64_tr_b16 v[144:145], %[brd] offset:36928\n\t"
        "ds_read_b64_tr_b16 v[146:147], %[brd] offset:38464\n\t"
        "buffer_load_dwordx4 a[68:71], v238, %[ra], s64 offen offset:1024\n\t"
        "s_waitcnt vmcnt(47)\n\t"
        "s_waitcnt lgkmcnt(11)\n\t"
        "v_mfma_f32_32x32x16_bf16 %[c0], v[156:159], a[72:75], %[c0]\n\t"
        "ds_read_b64_tr_b16 v[148:149], %[brd] offset:39936\n\t"
        "ds_read_b64_tr_b16 v[150:151], %[brd] offset:41472\n\t"
        "s_waitcnt lgkmcnt(11)\n\t"
        "v_mfma_f32_32x32x16_bf16 %[c1], v[160:163], a[72:75], %[c1]\n\t"
        "ds_read_b64_tr_b16 v[152:153], %[brd] offset:40000\n\t"
        "ds_read_b64_tr_b16 v[154:155], %[brd] offset:41536\n\t"
        "buffer_load_dwordx4 a[72:75], v238, %[ra], s64 offen offset:2048\n\t"
        "s_waitcnt vmcnt(47)\n\t"
        "s_waitcnt lgkmcnt(10)\n\t"
        "v_mfma_f32_32x32x16_bf16 %[c0], v[164:167], a[76:79], %[c0]\n\t"
        "ds_read_b64_tr_b16 v[156:157], %[brd] offset:43008\n\t"
        "ds_read_b64_tr_b16 v[158:159], %[brd] offset:44544\n\t"
        "s_waitcnt lgkmcnt(10)\n\t"
        "v_mfma_f32_32x32x16_bf16 %[c1], v[168:171], a[76:79], %[c1]\n\t"
        "ds_read_b64_tr_b16 v[160:161], %[brd] offset:43072\n\t"
        "ds_read_b64_tr_b16 v[162:163], %[brd] offset:44608\n\t"
        "buffer_load_dwordx4 a[76:79], v238, %[ra], s64 offen offset:3072\n\t"
        "s_waitcnt vmcnt(47)\n\t"
        "s_waitcnt lgkmcnt(10)\n\t"
        "v_mfma_f32_32x32x16_bf16 %[c0], v[140:143], a[80:83], %[c0]\n\t"
        "ds_read_b64_tr_b16 v[164:165], %[brd] offset:46080\n\t"
        "ds_read_b64_tr_b16 v[166:167], %[brd] offset:47616\n\t"
        "s_waitcnt lgkmcnt(10)\n\t"
        "v_mfma_f32_32x32x16_bf16 %[c1], v[144:147], a[80:83], %[c1]\n\t"
        "ds_read_b64_tr_b16 v[168:169], %[brd] offset:46144\n\t"
        "ds_read_b64_tr_b16 v[170:171], %[brd] offset:47680\n\t"
        "s_add_u32 s64, s64, 0x1000\n\t"
        "buffer_load_dwordx4 a[80:83], v238, %[ra], s64 offen offset:0\n\t"
        "s_waitcnt lgkmcnt(0)\n\t"
        "s_barrier\n\t"
        "s_waitcnt vmcnt(47)\n\t"
        "v_mfma_f32_32x32x16_bf16 %[c0], v[148:151], a[84:87], %[c0]\n\t"
        "ds_read_b64_tr_b16 v[140:141], v236 offset:0\n\t"
        "ds_read_b64_tr_b16 v[142:143], v236 offset:1536\n\t"
        "v_mfma_f32_32x32x16_bf16 %[c1], v[152:155], a[84:87], %[c1]\n\t"
        "ds_read_b64_tr_b16 v[144:145], v236 offset:64\n\t"
        "ds_read_b64_tr_b16 v[146:147], v236 offset:1600\n\t"
        "buffer_load_dwordx4 a[84:87], v238, %[ra], s64 offen offset:1024\n\t"
        "s_waitcnt vmcnt(47)\n\t"
        "v_mfma_f32_32x32x16_bf16 %[c0], v[156:159], a[88:91], %[c0]\n\t"
        "ds_read_b64_tr_b16 v[148:149], v236 offset:3072\n\t"
        "ds_read_b64_tr_b16 v[150:151], v236 offset:4608\n\t"
        "v_mfma_f32_32x32x16_bf16 %[c1], v[160:163], a[88:91], %[c1]\n\t"
        "ds_read_b64_tr_b16 v[152:153], v236 offset:3136\n\t"
        "ds_read_b64_tr_b16 v[154:155], v236 offset:4672\n\t"
        "buffer_load_dwordx4 a[88:91], v238, %[ra], s64 offen offset:2048\n\t"
        "s_waitcnt vmcnt(47)\n\t"
        "v_mfma_f32_32x32x16_bf16 %[c0], v[164:167], a[92:95], %[c0]\n\t"
        "ds_read_b64_tr_b16 v[156:157], v236 offset:6144\n\t"
        "ds_read_b64_tr_b16 v[158:159], v236 offset:7680\n\t"
        "v_mfma_f32_32x32x16_bf16 %[c1], v[168:171], a[92:95], %[c1]\n\t"
        "ds_read_b64_tr_b16 v[160:161], v236 offset:6208\n\t"
        "ds_read_b64_tr_b16 v[162:163], v236 offset:7744\n\t"
        "buffer_load_dwordx4 a[92:95], v238, %[ra], s64 offen offset:3072\n\t"
        "s_waitcnt vmcnt(47)\n\t"
        "s_waitcnt lgkmcnt(10)\n\t"
        "v_mfma_f32_32x32x16_bf16 %[c0], v[140:143], a[96:99], %[c0]\n\t"
        "ds_read_b64_tr_b16 v[164:165], v236 offset:9216\n\t"
        "ds_read_b64_tr_b16 v[166:167], v236 offset:10752\n\t"
        "s_waitcnt lgkmcnt(10)\n\t"
        "v_mfma_f32_32x32x16_bf16 %[c1], v[144:147], a[96:99], %[c1]\n\t"
        "ds_read_b64_tr_b16 v[168:169], v236 offset:9280\n\t"
        "ds_read_b64_tr_b16 v[170:171], v236 offset:10816\n\t"
        "s_add_u32 s64, s64, 0x1000\n\t"
        "buffer_load_dwordx4 a[96:99], v238, %[ra], s64 offen offset:0\n\t"
        "s_waitcnt vmcnt(47)\n\t"
        "ds_write_b128 %[bw], v[172:175] offset:0\n\t"
        "buffer_load_dwordx4 v[172:175], v239, %[rb], s65 offen\n\t"
        "s_add_u32 s65, s65, 28672\n\t"
        "s_waitcnt vmcnt(47)\n\t"
        "s_waitcnt lgkmcnt(11)\n\t"
        "v_mfma_f32_32x32x16_bf16 %[c0], v[148:151], a[100:103], %[c0]\n\t"
        "ds_read_b64_tr_b16 v[140:141], v236 offset:12288\n\t"
        "ds_read_b64_tr_b16 v[142:143], v236 offset:13824\n\t"
        "s_waitcnt lgkmcnt(11)\n\t"
        "v_mfma_f32_32x32x16_bf16 %[c1], v[152:155], a[100:103], %[c1]\n\t"
        "ds_read_b64_tr_b16 v[144:145], v236 offset:12352\n\t"
        "ds_read_b64_tr_b16 v[146:147], v236 offset:13888\n\t"
        "buffer_load_dwordx4 a[100:103], v238, %[ra], s64 offen offset:1024\n\t"
        "s_waitcnt vmcnt(47)\n\t"
        "ds_write_b128 %[bw], v[176:179] offset:6144\n\t"
        "buffer_load_dwordx4 v[176:179], v239, %[rb], s65 offen\n\t"
        "s_add_u32 s65, s65, 28672\n\t"
        "s_waitcnt vmcnt(47)\n\t"
        "s_waitcnt lgkmcnt(12)\n\t"
        "v_mfma_f32_32x32x16_bf16 %[c0], v[156:159], a[104:107], %[c0]\n\t"
        "ds_read_b64_tr_b16 v[148:149], v236 offset:15360\n\t"
        "ds_read_b64_tr_b16 v[150:151], v236 offset:16896\n\t"
        "s_waitcnt lgkmcnt(12)\n\t"
        "v_mfma_f32_32x32x16_bf16 %[c1], v[160:163], a[104:107], %[c1]\n\t"
        "ds_read_b64_tr_b16 v[152:153], v236 offset:15424\n\t"
        "ds_read_b64_tr_b16 v[154:155], v236 offset:16960\n\t"
        "buffer_load_dwordx4 a[104:107], v238, %[ra], s64 offen offset:2048\n\t"
        "s_waitcnt vmcnt(47)\n\t"
        "ds_write_b128 %[bw], v[180:183] offset:12288\n\t"
        "buffer_load_dwordx4 v[180:183], v239, %[rb], s65 offen\n\t"
        "s_add_u32 s65, s65, 28672\n\t"
        "s_waitcnt vmcnt(47)\n\t"
        "s_waitcnt lgkmcnt(13)\n\t"
        "v_mfma_f32_32x32x16_bf16 %[c0], v[164:167], a[108:111], %[c0]\n\t"
        "ds_read_b64_tr_b16 v[156:157], v236 offset:18432\n\t"
        "ds_read_b64_tr_b16 v[158:159], v236 offset:19968\n\t"
        "s_waitcnt lgkmcnt(13)\n\t"
        "v_mfma_f32_32x32x16_bf16 %[c1], v[168:171], a[108:111], %[c1]\n\t"
        "ds_read_b64_tr_b16 v[160:161], v236 offset:18496\n\t"
        "ds_read_b64_tr_b16 v[162:163], v236 offset:20032\n\t"
        "buffer_load_dwordx4 a[108:111], v238, %[ra], s64 offen offset:3072\n\t"
        "s_waitcnt vmcnt(47)\n\t"
        "ds_write_b128 %[bw], v[184:187] offset:18432\n\t"
        "buffer_load_dwordx4 v[184:187], v239, %[rb], s65 offen\n\t"
        "s_add_u32 s65, s65, 28672\n\t"
        "s_waitcnt vmcnt(47)\n\t"
        "s_waitcnt lgkmcnt(13)\n\t"
        "v_mfma_f32_32x32x16_bf16 %[c0], v[140:143], a[112:115], %[c0]\n\t"
        "ds_read_b64_tr_b16 v[164:165], v236 offset:21504\n\t"
        "ds_read_b64_tr_b16 v[166:167], v236 offset:23040\n\t"
        "s_waitcnt lgkmcnt(13)\n\t"
        "v_mfma_f32_32x32x16_bf16 %[c1], v[144:147], a[112:115], %[c1]\n\t"
        "ds_read_b64_tr_b16 v[168:169], v236 offset:21568\n\t"
        "ds_read_b64_tr_b16 v[170:171], v236 offset:23104\n\t"
        "s_add_u32 s64, s64, 0x1000\n\t"
        "buffer_load_dwordx4 a[112:115], v238, %[ra], s64 offen offset:0\n\t"
        "s_waitcnt vmcnt(47)\n\t"
        "ds_write_b128 %[bw], v[188:191] offset:24576\n\t"
        "buffer_load_dwordx4 v[188:191], v239, %[rb], s65 offen\n\t"
        "s_add_u32 s65, s65, 28672\n\t"
        "s_waitcnt vmcnt(47)\n\t"
        "s_waitcnt lgkmcnt(13)\n\t"
        "v_mfma_f32_32x32x16_bf16 %[c0], v[148:151], a[116:119], %[c0]\n\t"
        "ds_read_b64_tr_b16 v[140:141], v236 offset:24576\n\t"
        "ds_read_b64_tr_b16 v[142:143], v236 offset:26112\n\t"
        "s_waitcnt lgkmcnt(13)\n\t"
        "v_mfma_f32_32x32x16_bf16 %[c1], v[152:155], a[116:119], %[c1]\n\t"
        "ds_read_b64_tr_b16 v[144:145], v236 offset:24640\n\t"
        "ds_read_b64_tr_b16 v[146:147], v236 offset:26176\n\t"
        "buffer_load_dwordx4 a[116:119], v238, %[ra], s64 offen offset:1024\n\t"
        "s_waitcnt vmcnt(47)\n\t"
        "ds_write_b128 %[bw], v[192:195] offset:30720\n\t"
        "buffer_load_dwordx4 v[192:195], v239, %[rb], s65 offen\n\t"
        "s_add_u32 s65, s65, 28672\n\t"
        "s_waitcnt vmcnt(47)\n\t"
        "s_waitcnt lgkmcnt(13)\n\t"
        "v_mfma_f32_32x32x16_bf16 %[c0], v[156:159], a[120:123], %[c0]\n\t"
        "ds_read_b64_tr_b16 v[148:149], v236 offset:27648\n\t"
        "ds_read_b64_tr_b16 v[150:151], v236 offset:29184\n\t"
        "s_waitcnt lgkmcnt(13)\n\t"
        "v_mfma_f32_32x32x16_bf16 %[c1], v[160:163], a[120:123], %[c1]\n\t"
        "ds_read_b64_tr_b16 v[152:153], v236 offset:27712\n\t"
        "ds_read_b64_tr_b16 v[154:155], v236 offset:29248\n\t"
        "buffer_load_dwordx4 a[120:123], v238, %[ra], s64 offen offset:2048\n\t"
        "s_waitcnt vmcnt(47)\n\t"
        "ds_write_b128 %[bw], v[196:199] offset:36864\n\t"
        "buffer_load_dwordx4 v[196:199], v239, %[rb], s65 offen\n\t"
        "s_add_u32 s65, s65, 28672\n\t"
        "s_waitcnt vmcnt(47)\n\t"
        "s_waitcnt lgkmcnt(13)\n\t"
        "v_mfma_f32_32x32x16_bf16 %[c0], v[164:167], a[124:127], %[c0]\n\t"
        "ds_read_b64_tr_b16 v[156:157], v236 offset:30720\n\t"
        "ds_read_b64_tr_b16 v[158:159], v236 offset:32256\n\t"
        "s_waitcnt lgkmcnt(13)\n\t"
        "v_mfma_f32_32x32x16_bf16 %[c1], v[168:171], a[124:127], %[c1]\n\t"
        "ds_read_b64_tr_b16 v[160:161], v236 offset:30784\n\t"
        "ds_read_b64_tr_b16 v[162:163], v236 offset:32320\n\t"
        "buffer_load_dwordx4 a[124:127], v238, %[ra], s64 offen offset:3072\n\t"
        "s_waitcnt vmcnt(47)\n\t"
        "ds_write_b128 %[bw], v[200:203] offset:43008\n\t"
        "buffer_load_dwordx4 v[200:203], v239, %[rb], s65 offen\n\t"
        "s_add_u32 s65, s65, 28672\n\t"
        "s_waitcnt vmcnt(47)\n\t"
        "s_waitcnt lgkmcnt(13)\n\t"
        "v_mfma_f32_32x32x16_bf16 %[c0], v[140:143], a[128:131], %[c0]\n\t"
        "ds_read_b64_tr_b16 v[164:165], v236 offset:33792\n\t"
        "ds_read_b64_tr_b16 v[166:167], v236 offset:35328\n\t"
        "s_waitcnt lgkmcnt(13)\n\t"
        "v_mfma_f32_32x32x16_bf16 %[c1], v[144:147], a[128:131], %[c1]\n\t"
        "ds_read_b64_tr_b16 v[168:169], v236 offset:33856\n\t"
        "ds_read_b64_tr_b16 v[170:171], v236 offset:35392\n\t"
        "s_add_u32 s64, s64, 0x1000\n\t"
        "buffer_load_dwordx4 a[128:131], v238, %[ra], s64 offen offset:0\n\t"
        "s_waitcnt vmcnt(47)\n\t"
        "s_waitcnt lgkmcnt(12)\n\t"
        "v_mfma_f32_32x32x16_bf16 %[c0], v[148:151], a[132:135], %[c0]\n\t"
        "ds_read_b64_tr_b16 v[140:141], v236 offset:36864\n\t"
        "ds_read_b64_tr_b16 v[142:143], v236 offset:38400\n\t"
        "s_waitcnt lgkmcnt(12)\n\t"
        "v_mfma_f32_32x32x16_bf16 %[c1], v[152:155], a[132:135], %[c1]\n\t"
        "ds_read_b64_tr_b16 v[144:145], v236 offset:36928\n\t"
        "ds_read_b64_tr_b16 v[146:147], v236 offset:38464\n\t"
        "buffer_load_dwordx4 a[132:135], v238, %[ra], s64 offen offset:1024\n\t"
        "s_waitcnt vmcnt(47)\n\t"
        "s_waitcnt lgkmcnt(11)\n\t"
        "v_mfma_f32_32x32x16_bf16 %[c0], v[156:159], a[136:139], %[c0]\n\t"
        "ds_read_b64_tr_b16 v[148:149], v236 offset:39936\n\t"
        "ds_read_b64_tr_b16 v[150:151], v236 offset:41472\n\t"
        "s_waitcnt lgkmcnt(11)\n\t"
        "v_mfma_f32_32x32x16_bf16 %[c1], v[160:163], a[136:139], %[c1]\n\t"
        "ds_read_b64_tr_b16 v[152:153], v236 offset:40000\n\t"
        "ds_read_b64_tr_b16 v[154:155], v236 offset:41536\n\t"
        "buffer_load_dwordx4 a[136:139], v238, %[ra], s64 offen offset:2048\n\t"
        "s_waitcnt vmcnt(47)\n\t"
        "s_waitcnt lgkmcnt(10)\n\t"
        "v_mfma_f32_32x32x16_bf16 %[c0], v[164:167], a[140:143], %[c0]\n\t"
        "ds_read_b64_tr_b16 v[156:157], v236 offset:43008\n\t"
        "ds_read_b64_tr_b16 v[158:159], v236 offset:44544\n\t"
        "s_waitcnt lgkmcnt(10)\n\t"
        "v_mfma_f32_32x32x16_bf16 %[c1], v[168:171], a[140:143], %[c1]\n\t"
        "ds_read_b64_tr_b16 v[160:161], v236 offset:43072\n\t"
        "ds_read_b64_tr_b16 v[162:163], v236 offset:44608\n\t"
        "buffer_load_dwordx4 a[140:143], v238, %[ra], s64 offen offset:3072\n\t"
        "s_waitcnt vmcnt(47)\n\t"
        "s_waitcnt lgkmcnt(10)\n\t"
        "v_mfma_f32_32x32x16_bf16 %[c0], v[140:143], a[144:147], %[c0]\n\t"
        "ds_read_b64_tr_b16 v[164:165], v236 offset:46080\n\t"
        "ds_read_b64_tr_b16 v[166:167], v236 offset:47616\n\t"
        "s_waitcnt lgkmcnt(10)\n\t"
        "v_mfma_f32_32x32x16_bf16 %[c1], v[144:147], a[144:147], %[c1]\n\t"
        "ds_read_b64_tr_b16 v[168:169], v236 offset:46144\n\t"
        "ds_read_b64_tr_b16 v[170:171], v236 offset:47680\n\t"
        "s_add_u32 s64, s64, 0x1000\n\t"
        "buffer_load_dwordx4 a[144:147], v238, %[ra], s64 offen offset:0\n\t"
        "s_waitcnt lgkmcnt(0)\n\t"
        "s_barrier\n\t"
        "s_waitcnt vmcnt(47)\n\t"
        "v_mfma_f32_32x32x16_bf16 %[c0], v[148:151], a[148:151], %[c0]\n\t"
        "ds_read_b64_tr_b16 v[140:141], %[brd] offset:0\n\t"
        "ds_read_b64_tr_b16 v[142:143], %[brd] offset:1536\n\t"
        "v_mfma_f32_32x32x16_bf16 %[c1], v[152:155], a[148:151], %[c1]\n\t"
        "ds_read_b64_tr_b16 v[144:145], %[brd] offset:64\n\t"
        "ds_read_b64_tr_b16 v[146:147], %[brd] offset:1600\n\t"
        "buffer_load_dwordx4 a[148:151], v238, %[ra], s64 offen offset:1024\n\t"
        "s_waitcnt vmcnt(47)\n\t"
        "v_mfma_f32_32x32x16_bf16 %[c0], v[156:159], a[152:155], %[c0]\n\t"
        "ds_read_b64_tr_b16 v[148:149], %[brd] offset:3072\n\t"
        "ds_read_b64_tr_b16 v[150:151], %[brd] offset:4608\n\t"
        "v_mfma_f32_32x32x16_bf16 %[c1], v[160:163], a[152:155], %[c1]\n\t"
        "ds_read_b64_tr_b16 v[152:153], %[brd] offset:3136\n\t"
        "ds_read_b64_tr_b16 v[154:155], %[brd] offset:4672\n\t"
        "buffer_load_dwordx4 a[152:155], v238, %[ra], s64 offen offset:2048\n\t"
        "s_waitcnt vmcnt(47)\n\t"
        "v_mfma_f32_32x32x16_bf16 %[c0], v[164:167], a[156:159], %[c0]\n\t"
        "ds_read_b64_tr_b16 v[156:157], %[brd] offset:6144\n\t"
        "ds_read_b64_tr_b16 v[158:159], %[brd] offset:7680\n\t"
        "v_mfma_f32_32x32x16_bf16 %[c1], v[168:171], a[156:159], %[c1]\n\t"
        "ds_read_b64_tr_b16 v[160:161], %[brd] offset:6208\n\t"
        "ds_read_b64_tr_b16 v[162:163], %[brd] offset:7744\n\t"
        "buffer_load_dwordx4 a[156:159], v238, %[ra], s64 offen offset:3072\n\t"
        "s_sub_u32 s66, s66, 1\n\t"
        "s_cmp_lg_u32 s66, 0\n\t"
        "s_cbranch_scc1 LO_%=\n\t"
        "s_waitcnt vmcnt(0) lgkmcnt(0)\n\t"
        "s_nop 15\n\t"
        "s_barrier\n\t"
        : [c0] "+a"(c0), [c1] "+a"(c1)
        : [ra] "s"(ra), [rb] "s"(rb), [aoff] "v"(aoff), [boff] "v"(boff), [brd] "v"(brd), [bw] "v"(bw), [rot] "s"(rot),
          [cnt] "s"(cnt)
        :
          "v140", "v141", "v142", "v143", "v144", "v145", "v146", "v147", "v148", "v149", "v150", "v151", "v152",
          "v153", "v154", "v155", "v156", "v157", "v158", "v159", "v160", "v161", "v162", "v163", "v164", "v165",
          "v166", "v167", "v168", "v169", "v170", "v171", "v172", "v173", "v174", "v175", "v176", "v177", "v178",
          "v179", "v180", "v181", "v182", "v183", "v184", "v185", "v186", "v187", "v188", "v189", "v190", "v191",
          "v192", "v193", "v194", "v195", "v196", "v197", "v198", "v199", "v200", "v201", "v202", "v203", "v204",
          "v205", "v206", "v207", "v208", "v209", "v210", "v211", "v212", "v213", "v214", "v215", "v216", "v217",
          "v218", "v219", "v220", "v221", "v222", "v223", "v224", "v225", "v226", "v227", "v228", "v229", "v230",
          "v231", "v232", "v233", "v234", "v235", "v236", "v237", "v238", "v239", "s64", "s65", "s66", "s67", "scc",
          "memory",
          "a32", "a33", "a34", "a35", "a36", "a37", "a38", "a39", "a40", "a41", "a42", "a43", "a44", "a45", "a46", "a47",
          "a48", "a49", "a50", "a51", "a52", "a53", "a54", "a55", "a56", "a57", "a58", "a59", "a60", "a61", "a62", "a63",
          "a64", "a65", "a66", "a67", "a68", "a69", "a70", "a71", "a72", "a73", "a74", "a75", "a76", "a77", "a78", "a79",
          "a80", "a81", "a82", "a83", "a84", "a85", "a86", "a87", "a88", "a89", "a90", "a91", "a92", "a93", "a94", "a95",
          "a96", "a97", "a98", "a99", "a100", "a101", "a102", "a103", "a104", "a105", "a106", "a107", "a108", "a109", "a110", "a111",
          "a112", "a113", "a114", "a115", "a116", "a117", "a118", "a119", "a120", "a121", "a122", "a123", "a124", "a125", "a126", "a127",
          "a128", "a129", "a130", "a131", "a132", "a133", "a134", "a135", "a136", "a137", "a138", "a139", "a140", "a141", "a142", "a143",
          "a144", "a145", "a146", "a147", "a148", "a149", "a150", "a151", "a152", "a153", "a154", "a155", "a156", "a157", "a158", "a159");
}
template <int BM, int BN, int MODE>
__device__ __forceinline__ void gemm_tile(const u16* __restrict__ A, int lda, const u16* __restrict__ B, int ldb,
                                          u16* __restrict__ C, int ldc, int nk, u16* smem, int ws) {
    constexpr bool AF = MODE == 2;
    constexpr int AROW = AF ? 64 : 72, BROW = BN + 32;
    constexpr int ASZ = BM * AROW, STG = ASZ + 64 * BROW;
    constexpr int ACH = BM / 32, BCH = BN / 32, TM = BM / 64, TN = BN / 64;
    constexpr int BPR = BN / 8;
    constexpr int BRS = 256 / BPR;
    const int tid = tid_of(ws);
    const int lane = tid & 63, wid = ws;
    const int wm = wid >> 1, wn = wid & 1;
    const int r32 = lane & 31, hi = lane >> 5, g = lane >> 4, q4 = (lane & 15) >> 2, p4 = lane & 3;
    f32x16 acc[TM][TN];
#pragma unroll
    for (int i = 0; i < TM; ++i)
#pragma unroll
        for (int j = 0; j < TN; ++j) acc[i][j] = f32x16{};
    u32x4 ra0[ACH], rb0[BCH], ra1[ACH], rb1[BCH];
    const unsigned aoff = AF ? (unsigned)tid * 16u : ((unsigned)(tid >> 3) * lda + (tid & 7) * 8) * 2u;
    const unsigned boff = ((unsigned)(tid / BPR) * ldb + (tid % BPR) * 8) * 2u;
    const char* Ac = reinterpret_cast<const char*>(A);
    const char* Bc = reinterpret_cast<const char*>(B);
    u16* aw = smem + (tid >> 3) * AROW + (tid & 7) * 8;
    u16* bw = smem + ASZ + (tid / BPR) * BROW + (tid % BPR) * 8;
    const u16* ard = AF ? smem + wm * (BM / 2) * AROW + lane * 8 : smem + (wm * (BM / 2) + r32) * AROW + hi * 8;
    const u16* brd = smem + ASZ + ((AF ? 4 : 8) * (g >> 1) + q4) * BROW + wn * (BN / 2) + 16 * (g & 1) + 4 * p4;
    auto load = [&](u32x4(&ra)[ACH], u32x4(&rb)[BCH], int kt) {
#pragma unroll
        for (int i = 0; i < ACH; ++i)
            ra[i] = *reinterpret_cast<const u32x4*>(Ac + (aoff + (unsigned)(32 * i * lda + kt * (AF ? 2048 : 64)) * 2u));
#pragma unroll
        for (int i = 0; i < BCH; ++i)
            rb[i] = *reinterpret_cast<const u32x4*>(Bc + (boff + (unsigned)((kt * 64 + BRS * i) * ldb) * 2u));
    };
    auto store = [&](const u32x4(&ra)[ACH], const u32x4(&rb)[BCH], int st) {
#pragma unroll
        for (int i = 0; i < ACH; ++i) *reinterpret_cast<u32x4*>(aw + st * STG + 32 * i * AROW) = ra[i];
#pragma unroll
        for (int i = 0; i < BCH; ++i) *reinterpret_cast<u32x4*>(bw + st * STG + BRS * i * BROW) = rb[i];
    };
    auto compute = [&](int st, int ks) {
        bf16x8 af[TM], bfr[TN];
#pragma unroll
        for (int i = 0; i < TM; ++i)
            af[i] = *reinterpret_cast<const bf16x8*>(ard + st * STG + i * 32 * AROW + ks * (AF ? 512 : 16));
#pragma unroll
        for (int j = 0; j < TN; ++j) {
            const u16* pB = brd + st * STG + ks * 16 * BROW + j * 32;
            s16x4 lo = tr_read(pB);
            s16x4 hi4 = tr_read(pB + (AF ? 8 : 4) * BROW);
            bfr[j] = bf16x8{lo[0], lo[1], lo[2], lo[3], hi4[0], hi4[1], hi4[2], hi4[3]};
        }
#pragma unroll
        for (int i = 0; i < TM; ++i)
#pragma unroll
            for (int j = 0; j < TN; ++j)
                acc[i][j] = MODE == 1 ? MFMA32(af[i], bfr[j], acc[i][j]) : MFMA32(bfr[j], af[i], acc[i][j]);
    };
    if constexpr (MODE < 2) {
        __amdgpu_buffer_rsrc_t ra = __builtin_amdgcn_make_buffer_rsrc(const_cast<u16*>(A), 0, 0x7fffffff, 0x00020000);
        __amdgpu_buffer_rsrc_t rb = __builtin_amdgcn_make_buffer_rsrc(const_cast<u16*>(B), 0, 0x7fffffff, 0x00020000);
        __syncthreads();
#define LA(p) (unsigned)(uintptr_t)(LAS const u16*)(p)
        if constexpr (MODE == 0) gemm0_asm(acc, ra, rb, 64u * lda, nk, aoff, boff, LA(ard), LA(brd), LA(aw), LA(bw));
        else gemm1_asm(acc, ra, rb, 64u * lda, nk, aoff, boff, LA(ard), LA(brd), LA(aw), LA(bw));
    } else {
    load(ra0, rb0, 0);
    load(ra1, rb1, 1);
    __syncthreads();
    store(ra0, rb0, 0);
    load(ra0, rb0, 2);
    __syncthreads();
    for (int kt = 0; kt + 1 < nk; kt += 2) {
        compute(0, 0);
        compute(0, 1);
        store(ra1, rb1, 1);
        load(ra1, rb1, min(kt + 3, nk - 1));
        compute(0, 2);
        compute(0, 3);
        __syncthreads();
        compute(1, 0);
        compute(1, 1);
        store(ra0, rb0, 0);
        load(ra0, rb0, min(kt + 4, nk - 1));
        compute(1, 2);
        compute(1, 3);
        __syncthreads();
    }
    if (nk & 1) {
        compute(0, 0);
        compute(0, 1);
        compute(0, 2);
        compute(0, 3);
        __syncthreads();
    }
    }
    const int tid2 = tid_of(ws);
    __amdgpu_buffer_rsrc_t rs = __builtin_amdgcn_make_buffer_rsrc(C, 0, 0x7fffffff, 0x00020000);
    if constexpr (MODE < 2) {
        const bool isq = ldc != 0;
        const float sc = isq ? 0.0625f * 1.4426950408889634f : 1.f;
        const unsigned voff = (unsigned)(tid2 & 63) * 16u;
#pragma unroll
        for (int i = 0; i < TM; ++i)
#pragma unroll
            for (int j = 0; j < TN; ++j)
#pragma unroll
                for (int sh = 0; sh < 2; ++sh) {
                    u32x4 w;
#pragma unroll
                    for (int r = 0; r < 4; ++r)
                        w[r] = pack2(acc[i][j][8 * sh + 2 * r] * sc, acc[i][j][8 * sh + 2 * r + 1] * sc);
                    unsigned so;
                    if (MODE == 1) so = (wm * 2 + (i >> 1)) * 32768 + ((2 * (i & 1) + sh) * 8 + wn * 2 + j) * 1024;
                    else if (isq) so = (wm * 4 + i) * 16384 + (wn * 4 + 2 * j + sh) * 1024;
                    else so = (wm * 2 + (i >> 1)) * 32768 + ((wn * 4 + 2 * j + sh) * 2 + (i & 1)) * 1024;
                    __builtin_amdgcn_raw_buffer_store_b128(w, rs, voff, so, 16);
                }
    } else {
        constexpr int CROW = BN + 8;
#pragma unroll
        for (int i = 0; i < TM; ++i)
#pragma unroll
            for (int j = 0; j < TN; ++j)
#pragma unroll
                for (int rr = 0; rr < 4; ++rr) {
                    u32x2 w;
                    w[0] = pack2(acc[i][j][4 * rr], acc[i][j][4 * rr + 1]);
                    w[1] = pack2(acc[i][j][4 * rr + 2], acc[i][j][4 * rr + 3]);
                    *reinterpret_cast<u32x2*>(smem + (wm * (BM / 2) + 32 * i + r32) * CROW + wn * (BN / 2) + 32 * j +
                                              8 * rr + 4 * hi) = w;
                }
        __syncthreads();
        const unsigned coff = ((unsigned)(tid2 / BPR) * ldc + (tid2 % BPR) * 8) * 2u;
#pragma unroll
        for (int i = 0; i < BM * BN / 2048; ++i) {
            int row = tid2 / BPR + BRS * i, cc = tid2 % BPR;
            u32x4 v = *reinterpret_cast<const u32x4*>(smem + row * CROW + cc * 8);
            __builtin_amdgcn_raw_buffer_store_b128(v, rs, coff + (unsigned)(BRS * i * ldc) * 2u, 0, 0);
        }
    }
}

#define OROT cb
__device__ __forceinline__ void oproj_tile(const u16* __restrict__ OB, const u16* __restrict__ Wo, u16* __restrict__ out,
                                           int orb, int cb, u16* smem, int ws, const unsigned* cnt, int* slot) {
    const int tid = tid_of(ws), lane = tid & 63;
    const int r32 = lane & 31, hi = lane >> 5, g = lane >> 4, q4 = (lane & 15) >> 2, p4 = lane & 3;
    __amdgpu_buffer_rsrc_t ra = __builtin_amdgcn_make_buffer_rsrc(const_cast<u16*>(OB) + (long)(orb * 4 + ws) * (32 * HD),
                                                                  0, 32 * HD * 2, 0x00020000);
    __amdgpu_buffer_rsrc_t rb =
        __builtin_amdgcn_make_buffer_rsrc(const_cast<u16*>(Wo) + cb * 64, 0, (HD * DM - cb * 64) * 2, 0x00020000);
    const unsigned l0 = LA(smem);
    f32x16 acc[2];
#pragma unroll
    for (int i = 0; i < 16; ++i) acc[0][i] = acc[1][i] = 0.f;
    for (unsigned pos = 0; pos < 8u;) {
        const unsigned start = ((unsigned)cb + pos) & 7u;
        __syncthreads();
        if (ws == 0) {
            unsigned rdy = 0xffu;
            for (unsigned spin = 0; spin < WSPIN_CAP; ++spin) {
                const unsigned v = __hip_atomic_load(cnt + (lane & 7), __ATOMIC_RELAXED, __HIP_MEMORY_SCOPE_AGENT) + 1u;
                const unsigned ok = (unsigned)__ballot(v >= 2u) & 0xffu;
                if ((ok >> start) & 1u) { rdy = ok; break; }
                __builtin_amdgcn_s_sleep(16);
            }
            acquire_agent();
            if (lane == 0) *slot = (int)rdy;
        }
        __syncthreads();
        const unsigned rdy = (unsigned)__builtin_amdgcn_readfirstlane(*slot);
        const unsigned len = min((unsigned)__builtin_ctz(~((rdy | (rdy << 8)) >> start)), 8u - pos);
        gemm2_asm(acc[0], acc[1], ra, rb, lane * 16, (tid >> 3) * (DM * 2) + (tid & 7) * 16,
                  l0 + (4 * (g >> 1) + q4) * 192 + 32 * (g & 1) + 8 * p4, l0 + (tid >> 3) * 192 + (tid & 7) * 16, start, len);
        pos += len;
    }
    constexpr int CROW = 72;
#pragma unroll
    for (int j = 0; j < 2; ++j)
#pragma unroll
        for (int rr = 0; rr < 4; ++rr)
            *reinterpret_cast<u32x2*>(smem + (32 * ws + r32) * CROW + 32 * j + 8 * rr + 4 * hi) =
                u32x2{pack2(acc[j][4 * rr], acc[j][4 * rr + 1]), pack2(acc[j][4 * rr + 2], acc[j][4 * rr + 3])};
    __syncthreads();
    u16* op = out + (long)orb * 128 * 896 + 448 + cb * 64;
#pragma unroll
    for (int i = 0; i < 4; ++i) {
        const int row = (tid >> 3) + 32 * i;
        *reinterpret_cast<u32x4*>(op + row * 896 + (tid & 7) * 8) =
            *reinterpret_cast<const u32x4*>(smem + row * CROW + (tid & 7) * 8);
    }
}

struct MegaParams {
    const u16* B_x;
    const u16* B_ckv;
    const u16* B_wq;
    const u16* B_wuk;
    const u16* B_wuv;
    const u16* B_wo;
    u16* Qb;
    u16* Kb;
    u16* Vb;
    u16* OB;
    u16* out;
    unsigned* ctl;
};
constexpr int NWORK = 256 - NREC;
constexpr int CTL_QA = 0, CTL_KV = 1024, CTL_OC = 1280, CTL_DF = 1408, CTL_WORDS = 1408 + 112 * 64;
#define ORD 0
#define IDLE_SLP 32

__device__ __forceinline__ int next_ticket(unsigned* q, int x, int* slot, int ws) {
    __syncthreads();
    if (ws == 0) {
        const int lane = tid_of(0);
        unsigned t = 0;
        if (lane == 0) t = __hip_atomic_fetch_add(q + 64 * x, 1u, __ATOMIC_RELAXED, __HIP_MEMORY_SCOPE_AGENT) + 1u;
        const unsigned v = __hip_atomic_load(q + 64 * (lane & 7), __ATOMIC_RELAXED, __HIP_MEMORY_SCOPE_AGENT) + 1u;
        const unsigned left = (unsigned)__ballot(v < 128u) & 0xffu;
        if (lane == 0) *slot = (int)(min(t, 128u) | (left << 16));
    }
    __syncthreads();
    return *slot;
}
__device__ __forceinline__ void publish_count(unsigned* c, int ws) {
    asm volatile("s_waitcnt vmcnt(0)" ::: "memory");
    __syncthreads();
    if (tid_of(ws) == 0) __hip_atomic_fetch_add(c, 1u, __ATOMIC_RELAXED, __HIP_MEMORY_SCOPE_AGENT);
}
__device__ __forceinline__ void wait_counts(unsigned* c, int n, unsigned target, int ws) {
    if (ws == 0) {
        const int lane = tid_of(0);
        for (unsigned spin = 0; spin < WSPIN_CAP; ++spin) {
            unsigned v = __hip_atomic_load(c + (lane < n ? lane : 0), __ATOMIC_RELAXED, __HIP_MEMORY_SCOPE_AGENT) + 1u;
            if (__all(v >= target)) break;
            __builtin_amdgcn_s_sleep(32);
        }
        acquire_agent();
    }
    __syncthreads();
}

__device__ __forceinline__ void worker_body(const MegaParams& m, u16* smem, int* slot, int ws) {
    unsigned* ctl = m.ctl;
    WTS(0);
    const int myx = ((blockIdx.x >> 3) & 3) == 3 ? ((blockIdx.x & 7) < 5 ? 0 : 1) : (blockIdx.x & 7);
    int left = 0xff;
    for (int k = 0; k < 8; ++k) {
        const int x = (myx + k) & 7;
        if (!((left >> x) & 1)) continue;
        for (;;) {
            int t = __builtin_amdgcn_readfirstlane(next_ticket(ctl + CTL_QA, x, slot, ws));
            left = t >> 16;
            t &= 0xffff;
            if (t >= 128) break;
            KT0
            if (t >= 96) {
                const int at = t - 96, qb = 15 - (at >> 1), h = 2 * x + (at & 1);
                attnB_body(m.Qb, smem, qb, h, ws, ctl + CTL_KV + 16 * h);
                publish_count(ctl + CTL_OC + 8 * qb + x, ws);
                KT1(2);
                continue;
            }
            int rb, mat;
            if (t < 64) { rb = t >> 3; mat = (t >> 2) & 1; }
            else { rb = 7 - ((t - 64) >> 2); mat = 2; }
            const int cb = 4 * x + (t & 3);
            const int lda = (mat < 2) ? DC : DM;
            const u16* A = ((mat < 2) ? m.B_ckv : m.B_x) + (long)rb * 256 * lda;
            const u16* B = ((mat == 0) ? m.B_wuk : (mat == 1 ? m.B_wuv : m.B_wq)) + cb * 128;
            u16* C = ((mat == 0) ? m.Kb : (mat == 1 ? m.Vb : m.Qb)) + (long)(cb >> 1) * 524288 + rb * 65536 +
                     (cb & 1) * (mat == 0 ? 8192 : (mat == 1 ? 2048 : 4096));
            if (mat == 1) gemm_tile<256, 128, 1>(A, lda, B, HD, C, 0, lda / 64, smem, ws);
            else gemm_tile<256, 128, 0>(A, lda, B, HD, C, mat == 2, lda / 64, smem, ws);
            publish_count(ctl + CTL_KV + 16 * (cb >> 1) + (mat == 2 ? 8 : 0) + rb, ws);
            KT1(lda == DC ? 1 : 4);
        }
    }
    WTS(1);
    left = 0x3f;
    for (int k = 0; k < 6; ++k) {
        const int ox = ((int)(blockIdx.x & 7) + 4 + k) % 6;
        if (!((left >> ox) & 1)) continue;
        __syncthreads();
        if (ws == 0) {
            const int lane = tid_of(0);
            unsigned* q = ctl + CTL_QA + 32;
            unsigned t = 0;
            if (lane == 0) t = __hip_atomic_fetch_add(q + 64 * ox, 1u, __ATOMIC_RELAXED, __HIP_MEMORY_SCOPE_AGENT) + 1u;
            const unsigned v = __hip_atomic_load(q + 64 * (lane & 7), __ATOMIC_RELAXED, __HIP_MEMORY_SCOPE_AGENT) + 1u;
            const unsigned lf = (unsigned)__ballot(v < ((lane & 4) ? 14u : 21u)) & 0x3fu;
            if (lane == 0) *slot = (int)(min(t, 128u) | (lf << 16));
        }
        __syncthreads();
        int ot = __builtin_amdgcn_readfirstlane(*slot);
        left = ot >> 16;
        ot &= 0xffff;
        if (ot >= (ox < 4 ? 21 : 14)) continue;
        const int ort = ot / 7, orb = 15 - ox - 6 * ort;
        KT0
        oproj_tile(m.OB, m.B_wo, m.out, orb, ot - 7 * ort, smem, ws, ctl + CTL_OC + 8 * orb, slot);
        KT1(9);
        WTS(2);
        break;
    }
    WTS(6);
    WTS(7);
}

#define DLY_B 5
#define DLY_W 0
#define HLP_N 0
constexpr int GEMM_LDS = 2 * (256 * 72 + 64 * 160) * 2;
constexpr int cmax(int a, int b) { return a > b ? a : b; }
constexpr int FUSED_LDS = cmax(cmax(ATTN_LDS, RECA_LDS), cmax(GEMM_LDS, RECC_LDS));
static_assert(4 * XIMG * 2 <= FUSED_LDS, "x ring of role D");

__global__ __launch_bounds__(256) void mega_kernel(RecParams p, MegaParams m) {
    __shared__ __attribute__((aligned(16))) u16 smem[FUSED_LDS / 2 + 8];
    const int b = lid(), ws = __builtin_amdgcn_readfirstlane(threadIdx.x >> 6);
    if (b < NA_BLK) {
        rec_A_body(p, smem, b, ws);
    } else if (b < NA_BLK + NC_BLK) {
        rec_C_body(p, smem, b - NA_BLK, ws);
    } else if (b < NA_BLK + NC_BLK + NB_BLK) {
        for (int i = 0; i < DLY_B; ++i) __builtin_amdgcn_s_sleep(32);
        rec_B_body(p, smem, (b - NA_BLK - NC_BLK) & 15, (b - NA_BLK - NC_BLK) >> 4, ws);
    } else if (b < NA_BLK + NC_BLK + NB_BLK + NO_BLK) {
        rec_O_body(p, smem, (b - NA_BLK - NC_BLK - NB_BLK) >> 1, b & 1, ws);
    } else {
        if (b < NREC) {
            rec_D_body(p, smem, b - (NREC - ND_BLK), ws);
            const u32x4* wp = reinterpret_cast<const u32x4*>(m.B_wo) + (b - (NREC - ND_BLK)) * 8192 + threadIdx.x;
            u32x4 t[32];
#pragma unroll
            for (int f = 0; f < 32; ++f) t[f] = wp[f * 256];
#pragma unroll
            for (int f = 0; f < 32; ++f) asm volatile("" ::"v"(t[f]));
        } else
            for (int i = 0; i < DLY_W; ++i) __builtin_amdgcn_s_sleep(32);
        worker_body(m, smem, reinterpret_cast<int*>(smem + FUSED_LDS / 2), ws);
    }
}

extern "C" void kernel_launch(void* const* d_in, const int* in_sizes, int n_in, void* d_out, int out_size,
                              void* d_ws, size_t ws_size, hipStream_t stream) {
    const u16* A_x = (const u16*)d_in[0];
    const u16* A_mem0 = (const u16*)d_in[1];
    const u16* A_Wq = (const u16*)d_in[2];
    const u16* A_Wk = (const u16*)d_in[3];
    const u16* A_Wv = (const u16*)d_in[4];
    const u16* A_Wo = (const u16*)d_in[5];
    const u16* B_x = (const u16*)d_in[7];
    const u16* B_ckv = (const u16*)d_in[8];
    const u16* B_wq = (const u16*)d_in[9];
    const u16* B_wuk = (const u16*)d_in[10];
    const u16* B_wuv = (const u16*)d_in[11];
    const u16* B_wo = (const u16*)d_in[12];
    u16* out = (u16*)d_out;

    char* ws = (char*)d_ws;
    const size_t SZ = (size_t)S_ * HD * 2;
    u16* Qb = (u16*)(ws + 0 * SZ);
    u16* Kb = (u16*)(ws + 1 * SZ);
    u16* Vb = (u16*)(ws + 2 * SZ);
    u16* OB = (u16*)(ws + 3 * SZ);
    size_t off = 4 * SZ;
    u16* AmatT = (u16*)(ws + off); off += (size_t)H_ * DM * DM * 2;
    u16* CmatT = (u16*)(ws + off); off += (size_t)H_ * DM * DM * 2;
    u16* XP = (u16*)(ws + off); off += (size_t)S_ * DM * 2;
    u16* Dg = (u16*)(ws + off); off += (size_t)64 * DG_SEG * 2;
    u16* Ug = (u16*)(ws + off); off += (size_t)64 * UG_SEG * 2;
    char* gbase = ws + off;
    u16* G1 = (u16*)gbase;
    u16* G2 = G1 + 63 * G1_SLOT;
    u16* G3 = G2 + 64 * G2_SLOT;
    unsigned* ctl = (unsigned*)(G3 + 64 * G3_SLOT);
    size_t gbytes = (size_t)(63 * G1_SLOT + 64 * G2_SLOT + 64 * G3_SLOT) * 2 + (size_t)CTL_WORDS * 4;

    hipLaunchKernelGGL(pre_kernel, dim3(256), dim3(512), 0, stream, A_Wq, A_Wk, A_Wv, A_Wo, (u32x4*)AmatT, (u32x4*)CmatT,
                       (u32x4*)gbase, (int)(gbytes / 16), A_x, (u32x4*)XP);
    RecParams rp;
    rp.x = XP; rp.mem0 = A_mem0; rp.AmatT = AmatT; rp.CmatT = CmatT; rp.G1 = G1; rp.G2 = G2; rp.G3 = G3; rp.out = out;
    rp.Dg = Dg; rp.Ug = Ug; rp.dflag = ctl + CTL_DF;
    MegaParams mp;
    mp.B_x = B_x; mp.B_ckv = B_ckv; mp.B_wq = B_wq; mp.B_wuk = B_wuk; mp.B_wuv = B_wuv; mp.B_wo = B_wo;
    mp.Qb = Qb; mp.Kb = Kb; mp.Vb = Vb; mp.OB = OB;
    mp.out = out; mp.ctl = ctl;
    hipLaunchKernelGGL(mega_kernel, dim3(256), dim3(256), 0, stream, rp, mp);
}
```
